```python
import torch

def _softmax(x, axis=-1):
    return torch.softmax(x, dim=axis)
def _gelu(x):
    return torch.nn.functional.gelu(x, approximate="tanh")
def _silu(x):
    return torch.nn.functional.silu(x)
def _sigmoid(x):
    return torch.sigmoid(x)
def _layernorm(x, axis=-1, eps=1e-5):
    m = x.mean(dim=axis, keepdim=True)
    v = x.var(dim=axis, keepdim=True, unbiased=False)
    return (x - m) / torch.sqrt(v + eps)
def _scatter_add(tgt, idx, src, axis=0):
    return tgt.clone().index_add_(axis, idx, src)
def _segment_sum(src, seg, n):
    out = torch.zeros((n,) + src.shape[1:], dtype=src.dtype)
    return out.index_add_(0, seg, src)
def _pad(x, pw):
    flat = tuple(v for pair in reversed(pw) for v in pair)
    return torch.nn.functional.pad(x, flat)

kernel_name = 'recurrent_scan__grouped_kv_mqa__dense_activation_squared_relu__prompt'

def setup_inputs():

    _w = (lambda a: (lambda t: t.to(torch.bfloat16) if t.is_floating_point() else t)(torch.as_tensor(a)))
    def _A_setup():
        B, S, D, H = 1, 2048, 1024, 32
        Dh = D // H
        return {
            "x": _w((lambda *s: torch.randn(*s, dtype=torch.bfloat16) * 0.05)(B, S, D)),
            "wq": _w((lambda *s: torch.randn(*s, dtype=torch.bfloat16) * 0.05)(D, H * Dh)),
            "wk": _w((lambda *s: torch.randn(*s, dtype=torch.bfloat16) * 0.05)(D, Dh)),
            "wv": _w((lambda *s: torch.randn(*s, dtype=torch.bfloat16) * 0.05)(D, Dh)),
            "wo": _w((lambda *s: torch.randn(*s, dtype=torch.bfloat16) * 0.05)(H * Dh, D)),
        }
    _A_in = _A_setup()

    def _B_setup():
        B, S, D, F = 1, 2048, 1024, 3584
        return {
            "x": _w((lambda *s: torch.randn(*s, dtype=torch.bfloat16) * 0.05)(B, S, D)),
            "w1": _w((lambda *s: torch.randn(*s, dtype=torch.bfloat16) * 0.05)(D, F)),
            "b1": _w((lambda *s: torch.randn(*s, dtype=torch.bfloat16) * 0.8)(F)),
            "w2": _w((lambda *s: torch.randn(*s, dtype=torch.bfloat16) * 0.05)(F, D)),
            "b2": _w((lambda *s: torch.randn(*s, dtype=torch.bfloat16) * 6.4)(D)),
        }
    _B_in = _B_setup()

    return {
        "A_x": _A_in["x"],
        "A_wq": _A_in["wq"],
        "A_wk": _A_in["wk"],
        "A_wv": _A_in["wv"],
        "A_wo": _A_in["wo"],
        "B_x": _B_in["x"],
        "B_w1": _B_in["w1"],
        "B_b1": _B_in["b1"],
        "B_w2": _B_in["w2"],
        "B_b2": _B_in["b2"],
    }

def reference(A_x, A_wq, A_wk, A_wv, A_wo, B_x, B_w1, B_b1, B_w2, B_b2):
    def _A_ref(x, wq, wk, wv, wo):
        B, S, D = x.shape
        H = 32
        Dh = D // H
        q = (lambda x, *d: x.permute(*d))((x @ wq).reshape(B, S, H, Dh), 0, 2, 1, 3)
        k = x @ wk
        v = x @ wv
        scores = torch.einsum("bhqd,bkd->bhqk", q, k) * (Dh ** -0.5)
        p = _softmax(scores, axis=-1)
        o = torch.einsum("bhqk,bkd->bhqd", p, v)
        return (lambda x, *d: x.permute(*d))(o, 0, 2, 1, 3).reshape(B, S, H * Dh) @ wo

    def _B_ref(x, w1, b1, w2, b2):
        h = torch.einsum("bsd,df->bsf", x, w1) + b1
        zero = torch.zeros_like(h)
        r = torch.where(h > zero, h, zero)
        h = r * r
        out = torch.einsum("bsf,fd->bsd", h, w2) + b2
        return out

    x = A_x
    xn = _layernorm(x, axis=-1)
    s = _A_ref(xn, A_wq, A_wk, A_wv, A_wo)
    g = _silu(_B_ref(xn, B_w1, B_b1, B_w2, B_b2))
    return x + s * g
```

```cpp
#include <hip/hip_runtime.h>
#include <hip/hip_bf16.h>

typedef short bf16x8 __attribute__((ext_vector_type(8)));
typedef short s16x4 __attribute__((ext_vector_type(4)));
typedef float f32x16 __attribute__((ext_vector_type(16)));
typedef float f32x4 __attribute__((ext_vector_type(4)));
typedef float f32x2 __attribute__((ext_vector_type(2)));
typedef __bf16 bf16x2_t __attribute__((ext_vector_type(2)));
typedef unsigned u32x4 __attribute__((ext_vector_type(4)));
typedef unsigned u32x2 __attribute__((ext_vector_type(2)));
typedef unsigned short u16;
#define LAS __attribute__((address_space(3)))
#define DI __device__ __forceinline__
#define SBAR() __builtin_amdgcn_sched_barrier(0)
#define PIN(x) asm volatile("" : "+v"(x))
#define MFMA32(a, b, c) __builtin_amdgcn_mfma_f32_32x32x16_bf16((a), (b), (c), 0, 0, 0)
#define MFMA16(a, b, c) __builtin_amdgcn_mfma_f32_16x16x32_bf16((a), (b), (c), 0, 0, 0)

constexpr int SEQ = 2048, DM = 1024, NH = 32, FF = 3584;
constexpr size_t WS_XN = 0;
constexpr size_t WS_Q = 4u << 20;
constexpr size_t WS_KV = 8u << 20;
constexpr size_t WS_O = 9u << 20;
constexpr size_t WS_H = 13u << 20;

DI unsigned rfl(unsigned x) { return __builtin_amdgcn_readfirstlane(x); }
DI unsigned cvtpk(float lo, float hi) {
  f32x2 v = {lo, hi};
  bf16x2_t b = __builtin_convertvector(v, bf16x2_t);
  return __builtin_bit_cast(unsigned, b);
}
DI float bf_lo(unsigned u) { return __uint_as_float(u << 16); }
DI float bf_hi(unsigned u) { return __uint_as_float(u & 0xffff0000u); }
DI float bf_round(float x) { return bf_lo(cvtpk(x, x)); }

template <int IMM>
DI void glds16(unsigned voff, const void* sbase, unsigned ldsaddr) {
  asm volatile("s_mov_b32 m0, %2\n\ts_nop 0\n\tglobal_load_lds_dwordx4 %0, %1 offset:%3"
               :: "v"(voff), "s"(sbase), "s"(ldsaddr), "i"(IMM) : "memory");
}
DI bf16x8 lds_b128(const LAS char* p) { return *(const LAS bf16x8*)p; }
DI s16x4 lds_tr(const LAS char* p) {
  return __builtin_amdgcn_ds_read_tr16_b64_v4i16((LAS s16x4*)p);
}
DI f32x16 sel16(bool c, const f32x16& a, const f32x16& b) {
  f32x16 r;
#pragma unroll
  for (int i = 0; i < 16; ++i) r[i] = c ? a[i] : b[i];
  return r;
}
DI bf16x8 cat8(s16x4 lo, s16x4 hi) { return __builtin_shufflevector(lo, hi, 0, 1, 2, 3, 4, 5, 6, 7); }

__global__ __launch_bounds__(256) void ln_kernel(const u16* __restrict__ x, char* __restrict__ ws) {
  __shared__ float red[2][4][8];
  const int tid = threadIdx.x, w = tid >> 6, l = tid & 63;
  const int row = blockIdx.x * 8 + (l & 7);
  u32x4 v[4];
#pragma unroll
  for (int i = 0; i < 4; ++i) {
    const int c = (i * 4 + w) * 8 + (l >> 3);
    v[i] = *(const u32x4*)(x + (size_t)row * DM + c * 8);
  }
  float s = 0.f;
#pragma unroll
  for (int i = 0; i < 4; ++i)
#pragma unroll
    for (int j = 0; j < 4; ++j) s += bf_lo(v[i][j]) + bf_hi(v[i][j]);
  s += __shfl_xor(s, 8); s += __shfl_xor(s, 16); s += __shfl_xor(s, 32);
  if (l < 8) red[0][w][l] = s;
  __syncthreads();
  const float mean = (red[0][0][l & 7] + red[0][1][l & 7] + red[0][2][l & 7] + red[0][3][l & 7]) * (1.0f / DM);
  float q = 0.f;
#pragma unroll
  for (int i = 0; i < 4; ++i)
#pragma unroll
    for (int j = 0; j < 4; ++j) {
      const float a = bf_lo(v[i][j]) - mean, b = bf_hi(v[i][j]) - mean;
      q += a * a + b * b;
    }
  q += __shfl_xor(q, 8); q += __shfl_xor(q, 16); q += __shfl_xor(q, 32);
  if (l < 8) red[1][w][l] = q;
  __syncthreads();
  const float var = (red[1][0][l & 7] + red[1][1][l & 7] + red[1][2][l & 7] + red[1][3][l & 7]) * (1.0f / DM);
  const float mb = bf_round(mean);
  const float den = bf_round(__builtin_sqrtf(bf_round(bf_round(var) + 1e-5f)));
  const float rden = 1.0f / den;
  char* dst = ws + WS_XN + (size_t)(row >> 5) * 65536 + (row & 31) * 16;
#pragma unroll
  for (int i = 0; i < 4; ++i) {
    const int c = (i * 4 + w) * 8 + (l >> 3);
    u32x4 o;
#pragma unroll
    for (int j = 0; j < 4; ++j) {
      const unsigned d = cvtpk(bf_lo(v[i][j]) - mb, bf_hi(v[i][j]) - mb);
      o[j] = cvtpk(bf_lo(d) * rden, bf_hi(d) * rden);
    }
    *(u32x4*)(dst + (c >> 1) * 1024 + (c & 1) * 512) = o;
  }
}

constexpr int G1_STAGE = 53248, G1_BOFF = 32768, G1_LDS = 3 * G1_STAGE;

__global__ __launch_bounds__(512, 2) void gemm1_kernel(const u16* __restrict__ wq, const u16* __restrict__ wk,
                                                       const u16* __restrict__ wv, const u16* __restrict__ w1,
                                                       const u16* __restrict__ b1, char* __restrict__ ws) {
  extern __shared__ __attribute__((aligned(16))) char smem[];
  const int tid = threadIdx.x, l = tid & 63;
  const int w = rfl(tid >> 6);
  const int lin = (blockIdx.x & 7) * 30 + (blockIdx.x >> 3);
  const int mh = lin / 120, rem = lin % 120, nt = rem >> 2, mtile = mh * 4 + (rem & 3);
  const unsigned lds0 = (unsigned)(size_t)(LAS char*)smem;

  if (w >= 4) {
    const int lw = w - 4;
    const char* abase = ws + WS_XN + (size_t)(mtile * 8 + lw * 2) * 65536;
    const unsigned avoff = l * 16;
    const char* bbase[5];
    unsigned bvoff[5], bstep[5];
#pragma unroll
    for (int p = 0; p < 5; ++p) {
      const int cb = nt * 5 + p;
      const u16* src; int ld;
      if (cb < 32) { src = wq + cb * 32; ld = DM; }
      else if (cb == 33) { src = wv; ld = 32; }
      else if (cb < 35 || cb >= 147) { src = wk; ld = 32; }
      else { src = w1 + (cb - 35) * 32; ld = FF; }
      bbase[p] = (const char*)src;
      bvoff[p] = (unsigned)((lw * 16 + (l >> 2)) * ld * 2 + (l & 3) * 16);
      bstep[p] = (unsigned)(64 * ld * 2);
    }
    auto issue = [&](int kt, int slot) {
      const unsigned sa = lds0 + slot * G1_STAGE + lw * 8192;
      const char* a0 = abase + kt * 4096;
      glds16<0>(avoff, a0, sa); glds16<1024>(avoff, a0, sa); glds16<2048>(avoff, a0, sa); glds16<3072>(avoff, a0, sa);
      const char* a1 = a0 + 65536;
      glds16<0>(avoff, a1, sa + 4096); glds16<1024>(avoff, a1, sa + 4096); glds16<2048>(avoff, a1, sa + 4096); glds16<3072>(avoff, a1, sa + 4096);
      const unsigned sb = lds0 + slot * G1_STAGE + G1_BOFF + lw * 1024;
#pragma unroll
      for (int p = 0; p < 5; ++p) glds16<0>(bvoff[p], bbase[p] + (size_t)kt * bstep[p], sb + p * 4096);
    };
    issue(0, 0); issue(1, 1); issue(2, 2);
    asm volatile("s_waitcnt vmcnt(26)\n\ts_barrier" ::: "memory");
    int slot = 0;
    for (int i = 0; i < 13; ++i) {
      asm volatile("s_waitcnt vmcnt(13)\n\ts_barrier" ::: "memory");
      issue(i + 3, slot);
      slot = (slot == 2) ? 0 : slot + 1;
    }
    asm volatile("s_waitcnt vmcnt(13)\n\ts_barrier" ::: "memory");
    asm volatile("s_waitcnt vmcnt(0)\n\ts_barrier" ::: "memory");
    return;
  }

  const int h = l >> 5;
  f32x16 acc[2][5];
  if (nt >= 7) {
#pragma unroll
    for (int p = 0; p < 5; ++p) {
      int cb = nt * 5 + p - 35; cb = cb > 111 ? 111 : cb;
#pragma unroll
      for (int i = 0; i < 4; ++i) {
        const u32x2 bv = *(const u32x2*)(b1 + cb * 32 + 8 * i + 4 * h);
        acc[0][p][4 * i + 0] = bf_lo(bv[0]); acc[0][p][4 * i + 1] = bf_hi(bv[0]);
        acc[0][p][4 * i + 2] = bf_lo(bv[1]); acc[0][p][4 * i + 3] = bf_hi(bv[1]);
      }
      acc[1][p] = acc[0][p];
    }
  } else {
#pragma unroll
    for (int p = 0; p < 5; ++p) { acc[0][p] = f32x16{}; acc[1][p] = f32x16{}; }
  }
  const LAS char* lbase = (const LAS char*)smem;
  const int aoff = w * 8192 + l * 16;
  const int boff = G1_BOFF + (8 * h + ((l & 15) >> 2)) * 64 + ((l >> 4) & 1) * 32 + (l & 3) * 8;
  bf16x8 fa0[2], fb0[5], fa1[2], fb1[5];
#define G1_RD(FA, FB, base, ks) do { \
    FA[0] = lds_b128((base) + aoff + (ks) * 1024); FA[1] = lds_b128((base) + aoff + 4096 + (ks) * 1024); \
    _Pragma("unroll") for (int p_ = 0; p_ < 5; ++p_) \
      FB[p_] = cat8(lds_tr((base) + boff + p_ * 4096 + (ks) * 1024), lds_tr((base) + boff + p_ * 4096 + (ks) * 1024 + 256)); \
  } while (0)
#define G1_MM(FA, FB) do { \
    _Pragma("unroll") for (int p_ = 0; p_ < 5; ++p_) { \
      acc[0][p_] = MFMA32(FB[p_], FA[0], acc[0][p_]); acc[1][p_] = MFMA32(FB[p_], FA[1], acc[1][p_]); } \
  } while (0)
#define G1_SCHED() do { \
    _Pragma("unroll") for (int q_ = 0; q_ < 6; ++q_) { \
      __builtin_amdgcn_sched_group_barrier(0x008, 1, 0); __builtin_amdgcn_sched_group_barrier(0x100, 2, 0); } \
    __builtin_amdgcn_sched_group_barrier(0x008, 4, 0); SBAR(); \
  } while (0)

  asm volatile("s_barrier" ::: "memory");
  G1_RD(fa0, fb0, lbase, 0); SBAR();
  G1_RD(fa1, fb1, lbase, 1); G1_MM(fa0, fb0); G1_SCHED();
  G1_RD(fa0, fb0, lbase, 2); G1_MM(fa1, fb1); G1_SCHED();
  G1_RD(fa1, fb1, lbase, 3); G1_MM(fa0, fb0); G1_SCHED();
  int slot = 1;
  for (int i = 0; i < 15; ++i) {
    const LAS char* nb = lbase + slot * G1_STAGE;
    asm volatile("s_waitcnt lgkmcnt(0)\n\ts_barrier" ::: "memory"); SBAR();
    G1_RD(fa0, fb0, nb, 0); G1_MM(fa1, fb1); G1_SCHED();
    G1_RD(fa1, fb1, nb, 1); G1_MM(fa0, fb0); G1_SCHED();
    G1_RD(fa0, fb0, nb, 2); G1_MM(fa1, fb1); G1_SCHED();
    G1_RD(fa1, fb1, nb, 3); G1_MM(fa0, fb0); G1_SCHED();
    slot = (slot == 2) ? 0 : slot + 1;
  }
  G1_MM(fa1, fb1);

  if (nt >= 7) {
#pragma unroll
    for (int p = 0; p < 5; ++p) {
      const int cb = nt * 5 + p - 35;
      if (cb < 112) {
#pragma unroll
        for (int m = 0; m < 2; ++m) {
          char* dst = ws + WS_H + (size_t)(mtile * 8 + w * 2 + m) * (224 * 1024) + (size_t)(cb * 2) * 1024 + l * 16;
#pragma unroll
          for (int s = 0; s < 2; ++s) {
            u32x4 o;
#pragma unroll
            for (int j = 0; j < 4; ++j) {
              const float a = __builtin_fmaxf(acc[m][p][8 * s + 2 * j], 0.f), b = __builtin_fmaxf(acc[m][p][8 * s + 2 * j + 1], 0.f);
              o[j] = cvtpk(a * a, b * b);
            }
            *(u32x4*)(dst + s * 1024) = o;
          }
        }
      }
    }
  } else {
    const float qs = 0.17677669529663687f * 1.4426950408889634f;
#pragma unroll
    for (int p = 0; p < 5; ++p) {
      const int cb = nt * 5 + p;
#pragma unroll
      for (int m = 0; m < 2; ++m) {
        const int tt = mtile * 8 + w * 2 + m;
        if (cb <= 32) {
          char* dst = (cb < 32) ? ws + WS_Q + (size_t)(tt * 32 + cb) * 2048 + l * 16 : ws + WS_KV + (size_t)tt * 4096 + l * 16;
          const float sc = (cb < 32) ? qs : 1.0f;
#pragma unroll
          for (int s = 0; s < 2; ++s) {
            u32x4 o;
#pragma unroll
            for (int j = 0; j < 4; ++j) o[j] = cvtpk(acc[m][p][8 * s + 2 * j] * sc, acc[m][p][8 * s + 2 * j + 1] * sc);
            *(u32x4*)(dst + s * 1024) = o;
          }
        } else if (cb == 33) {
          const int key = l & 31;
          char* dst = ws + WS_KV + (size_t)tt * 4096 + 2048 + (key >> 4) * 1024 + ((key >> 2) & 1) * 512 + (((key >> 3) & 1) * 4 + (key & 3)) * 2;
#pragma unroll
          for (int r = 0; r < 16; ++r) {
            const int d = (r & 3) + 8 * (r >> 2) + 4 * h;
            *(u16*)(dst + d * 16) = (u16)cvtpk(acc[m][p][r], 0.f);
          }
        }
      }
    }
  }
#undef G1_RD
#undef G1_MM
#undef G1_SCHED
}

constexpr int AT_CHUNK = 8;
constexpr int AT_LDS = 2 * 2 * AT_CHUNK * 4096;

DI float xhalf_max(float m) {
  auto rr = __builtin_amdgcn_permlane32_swap(__float_as_uint(m), __float_as_uint(m), false, false);
  return __builtin_fmaxf(__uint_as_float(rr[0]), __uint_as_float(rr[1]));
}
DI float xhalf_sum(float m) {
  auto rr = __builtin_amdgcn_permlane32_swap(__float_as_uint(m), __float_as_uint(m), false, false);
  return __uint_as_float(rr[0]) + __uint_as_float(rr[1]);
}
DI float max16(const f32x16& s) {
  float a = __builtin_fmaxf(s[0], s[1]);
#pragma unroll
  for (int r = 2; r < 16; ++r) a = __builtin_fmaxf(a, s[r]);
  return a;
}
DI bf16x8 pack8(const f32x16& p, int s) {
  u32x4 o;
#pragma unroll
  for (int j = 0; j < 4; ++j) o[j] = cvtpk(p[8 * s + 2 * j], p[8 * s + 2 * j + 1]);
  return __builtin_bit_cast(bf16x8, o);
}

DI void attn_fallback(const char* __restrict__ kv, bf16x8 q0, bf16x8 q1, int l, f32x16& o_out, float& l_out) {
  f32x16 o = {};
  float m = -INFINITY, lsum = 0.f;
  for (int t = 0; t < 64; ++t) {
    const char* p = kv + (size_t)t * 4096 + l * 16;
    const bf16x8 k0 = *(const bf16x8*)p, k1 = *(const bf16x8*)(p + 1024), v0 = *(const bf16x8*)(p + 2048), v1 = *(const bf16x8*)(p + 3072);
    f32x16 s = {};
    s = MFMA32(k0, q0, s); s = MFMA32(k1, q1, s);
    const float mn = __builtin_fmaxf(m, xhalf_max(max16(s)));
    const float ms = (mn == -INFINITY) ? 0.f : mn;
    const float alpha = __builtin_amdgcn_exp2f(m - ms);
    m = mn;
    float ps = 0.f;
#pragma unroll
    for (int r = 0; r < 16; ++r) { s[r] = __builtin_amdgcn_exp2f(s[r] - ms); ps += s[r]; }
    lsum = lsum * alpha + ps;
#pragma unroll
    for (int r = 0; r < 16; ++r) o[r] *= alpha;
    o = MFMA32(v0, pack8(s, 0), o); o = MFMA32(v1, pack8(s, 1), o);
  }
  o_out = o;
  l_out = xhalf_sum(lsum);
}

__global__ __launch_bounds__(512, 2) void attn_kernel(char* __restrict__ ws) {
  extern __shared__ __attribute__((aligned(16))) char smem[];
  const int tid = threadIdx.x, l = tid & 63;
  const int w = rfl(tid >> 6);
  const int hp = w & 3, kh = w >> 2;
  const int tt = blockIdx.x >> 2, hg = blockIdx.x & 3;
  const int head0 = hg * 8 + hp * 2;
  const unsigned lds0 = (unsigned)(size_t)(LAS char*)smem;
  const char* kvg = ws + WS_KV;
  const unsigned voff = l * 16;
  auto issue = [&](int c) {
    const unsigned dst = lds0 + (c & 1) * (2 * AT_CHUNK * 4096) + w * 4096;
    const char* s0 = kvg + (size_t)(c * AT_CHUNK + w) * 4096;
    glds16<0>(voff, s0, dst); glds16<1024>(voff, s0, dst); glds16<2048>(voff, s0, dst); glds16<3072>(voff, s0, dst);
    const char* s1 = s0 + 32 * 4096;
    const unsigned dst1 = dst + AT_CHUNK * 4096;
    glds16<0>(voff, s1, dst1); glds16<1024>(voff, s1, dst1); glds16<2048>(voff, s1, dst1); glds16<3072>(voff, s1, dst1);
  };
  const char* qp = ws + WS_Q + (size_t)(tt * 32 + head0) * 2048 + l * 16;
  const bf16x8 qa0 = *(const bf16x8*)qp, qa1 = *(const bf16x8*)(qp + 1024), qb0 = *(const bf16x8*)(qp + 2048), qb1 = *(const bf16x8*)(qp + 3072);
  issue(0);
  asm volatile("s_waitcnt vmcnt(0)\n\ts_barrier" ::: "memory");
  issue(1);

  const LAS char* lb = (const LAS char*)smem + l * 16;
  f32x16 nma, nmb;
  {
    const bf16x8 k0 = lds_b128(lb), k1 = lds_b128(lb + 1024);
    f32x16 sa = {}, sb = {};
    sa = MFMA32(k0, qa0, sa); sa = MFMA32(k1, qa1, sa);
    sb = MFMA32(k0, qb0, sb); sb = MFMA32(k1, qb1, sb);
    const float ma = -xhalf_max(max16(sa)), mb = -xhalf_max(max16(sb));
#pragma unroll
    for (int r = 0; r < 16; ++r) { nma[r] = ma; nmb[r] = mb; }
    PIN(nma); PIN(nmb);
  }
  bf16x8 sel;
  {
    const short one = ((l & 15) == ((l >> 4) & 1)) ? (short)0x3f80 : (short)0;
#pragma unroll
    for (int j = 0; j < 8; ++j) sel[j] = one;
  }
  f32x16 oa = {}, ob = {}, sa, sb;
  f32x4 la = {}, lbs = {};
  bf16x8 pa0, pa1, pb0, pb1;

  for (int c = 0; c < 4; ++c) {
    const LAS char* cb = lb + (c & 1) * (2 * AT_CHUNK * 4096) + kh * (AT_CHUNK * 4096);
#pragma unroll 2
    for (int t = 0; t < AT_CHUNK; ++t) {
      const LAS char* tp = cb + t * 4096;
      const bf16x8 k0 = lds_b128(tp), k1 = lds_b128(tp + 1024), v0 = lds_b128(tp + 2048), v1 = lds_b128(tp + 3072);
      sa = MFMA32(k0, qa0, nma); sa = MFMA32(k1, qa1, sa);
      sb = MFMA32(k0, qb0, nmb); sb = MFMA32(k1, qb1, sb);
#pragma unroll
      for (int r = 0; r < 16; ++r) sa[r] = __builtin_amdgcn_exp2f(sa[r]);
      pa0 = pack8(sa, 0); pa1 = pack8(sa, 1);
      oa = MFMA32(v0, pa0, oa); oa = MFMA32(v1, pa1, oa);
      la = MFMA16(sel, pa0, la); la = MFMA16(sel, pa1, la);
#pragma unroll
      for (int r = 0; r < 16; ++r) sb[r] = __builtin_amdgcn_exp2f(sb[r]);
      pb0 = pack8(sb, 0); pb1 = pack8(sb, 1);
      ob = MFMA32(v0, pb0, ob); ob = MFMA32(v1, pb1, ob);
      lbs = MFMA16(sel, pb0, lbs); lbs = MFMA16(sel, pb1, lbs);
    }
    if (c < 3) {
      asm volatile("s_waitcnt vmcnt(0) lgkmcnt(0)\n\ts_barrier" ::: "memory");
      if (c < 2) issue(c + 2);
    }
  }
  asm volatile("s_waitcnt lgkmcnt(0)\n\ts_barrier" ::: "memory");
  {
    LAS char* xw = (LAS char*)smem + w * 8192;
    const f32x16 so = kh ? oa : ob;
    const f32x4 sl = kh ? la : lbs;
#pragma unroll
    for (int i = 0; i < 4; ++i) *(LAS f32x4*)(xw + i * 1024 + l * 16) = (f32x4){so[4 * i], so[4 * i + 1], so[4 * i + 2], so[4 * i + 3]};
    if (l < 16) *(LAS f32x2*)(xw + 4096 + l * 8) = (f32x2){sl[0], sl[1]};
    const f32x4 ml = kh ? lbs : la;
    if (l < 16) *(LAS f32x2*)(xw + 4096 + 128 + l * 8) = (f32x2){ml[0], ml[1]};
  }
  __syncthreads();
  f32x16 o = kh ? ob : oa;
  float lsum;
  {
    const LAS char* xr = (const LAS char*)smem + (w ^ 4) * 8192;
    const LAS char* xo = (const LAS char*)smem + w * 8192;
#pragma unroll
    for (int i = 0; i < 4; ++i) {
      const f32x4 t = *(const LAS f32x4*)(xr + i * 1024 + l * 16);
      o[4 * i] += t[0]; o[4 * i + 1] += t[1]; o[4 * i + 2] += t[2]; o[4 * i + 3] += t[3];
    }
    const int qo = (l & 15) * 8 + ((l >> 4) & 1) * 4;
    lsum = *(const LAS float*)(xr + 4096 + qo) + *(const LAS float*)(xo + 4096 + 128 + qo);
  }
  float omax = 0.f;
#pragma unroll
  for (int r = 0; r < 16; ++r) omax = __builtin_fmaxf(omax, __builtin_fabsf(o[r]));
  bool finite_o = true;
#pragma unroll
  for (int r = 0; r < 16; ++r) finite_o = finite_o && (o[r] == o[r]);
  const bool ok = (lsum >= 9.5367431640625e-07f) && (lsum <= 1.8446744073709552e19f) && (omax <= 3.0e38f) && finite_o;
  if (!__all(ok)) {
    attn_fallback(kvg, kh ? qb0 : qa0, kh ? qb1 : qa1, l, o, lsum);
  }
  const float rl = 1.0f / lsum;
  char* dst = ws + WS_O + (size_t)tt * 65536 + (size_t)((head0 + kh) * 2) * 1024 + l * 16;
#pragma unroll
  for (int s = 0; s < 2; ++s) {
    u32x4 ov;
#pragma unroll
    for (int j = 0; j < 4; ++j) ov[j] = cvtpk(o[8 * s + 2 * j] * rl, o[8 * s + 2 * j + 1] * rl);
    *(u32x4*)(dst + s * 1024) = ov;
  }
}

constexpr int G2_STAGE = 24576, G2_BOFF = 16384, G2_NST = 6, G2_LDS = G2_NST * G2_STAGE;

__global__ __launch_bounds__(512, 2) void gemm2_kernel(const u16* __restrict__ x, const u16* __restrict__ wo,
                                                       const u16* __restrict__ w2, const u16* __restrict__ b2,
                                                       const char* __restrict__ ws, u16* __restrict__ out) {
  extern __shared__ __attribute__((aligned(16))) char smem[];
  const int tid = threadIdx.x, l = tid & 63;
  const int w = rfl(tid >> 6);
  const int xcd = blockIdx.x & 7, idx = blockIdx.x >> 3;
  const int mtile = (xcd >> 1) * 4 + (idx & 3), ntile = (xcd & 1) * 8 + (idx >> 2);
  const unsigned lds0 = (unsigned)(size_t)(LAS char*)smem;
  constexpr int NKT = 72;

  if (w >= 4) {
    const int lw = w - 4;
    const char* hbase = ws + WS_H + (size_t)(mtile * 4 + lw) * (224 * 1024);
    const char* obase = ws + WS_O + (size_t)(mtile * 4 + lw) * 65536;
    const unsigned avoff = l * 16;
    const unsigned bvoff = (unsigned)((lw * 16 + (l >> 2)) * DM * 2 + (l & 3) * 16);
    const char* w2b = (const char*)(w2 + ntile * 64);
    const char* wob = (const char*)(wo + ntile * 64);
    auto issue = [&](int kt, int slot) {
      const char* a0 = (kt < 56) ? hbase + kt * 4096 : obase + (kt - 56) * 4096;
      const char* b0 = (kt < 56) ? w2b + (size_t)kt * (64 * DM * 2) : wob + (size_t)(kt - 56) * (64 * DM * 2);
      const unsigned sa = lds0 + slot * G2_STAGE + lw * 4096;
      glds16<0>(avoff, a0, sa); glds16<1024>(avoff, a0, sa); glds16<2048>(avoff, a0, sa); glds16<3072>(avoff, a0, sa);
      const unsigned sb = lds0 + slot * G2_STAGE + G2_BOFF + lw * 1024;
      glds16<0>(bvoff, b0, sb); glds16<0>(bvoff, b0 + 64, sb + 4096);
    };
#pragma unroll
    for (int i = 0; i < G2_NST; ++i) issue(i, i);
    asm volatile("s_waitcnt vmcnt(30)\n\ts_barrier" ::: "memory");
    int slot = 0;
    for (int i = 0; i < NKT - G2_NST; ++i) {
      asm volatile("s_waitcnt vmcnt(24)\n\ts_barrier" ::: "memory");
      issue(i + G2_NST, slot);
      slot = (slot == G2_NST - 1) ? 0 : slot + 1;
    }
    asm volatile("s_waitcnt vmcnt(24)\n\ts_barrier" ::: "memory");
    asm volatile("s_waitcnt vmcnt(18)\n\ts_barrier" ::: "memory");
    asm volatile("s_waitcnt vmcnt(12)\n\ts_barrier" ::: "memory");
    asm volatile("s_waitcnt vmcnt(6)\n\ts_barrier" ::: "memory");
    asm volatile("s_waitcnt vmcnt(0)\n\ts_barrier" ::: "memory");
    return;
  }

  const int h = l >> 5, wm = w & 1, wk = w >> 1;
  f32x16 accg[2][2] = {}, accs[2][2] = {};
  const LAS char* lbase = (const LAS char*)smem;
  const int aoff = wm * 8192 + wk * 2048 + l * 16;
  const int boff = G2_BOFF + wk * 2048 + (4 * h + ((l & 15) >> 2)) * 64 + ((l >> 4) & 1) * 32 + (l & 3) * 8;
  bf16x8 fa0[2], fb0[2], fa1[2], fb1[2];
#define G2_RD(FA, FB, base, ks) do { \
    FA[0] = lds_b128((base) + aoff + (ks) * 1024); FA[1] = lds_b128((base) + aoff + 4096 + (ks) * 1024); \
    _Pragma("unroll") for (int p_ = 0; p_ < 2; ++p_) \
      FB[p_] = cat8(lds_tr((base) + boff + p_ * 4096 + (ks) * 1024), lds_tr((base) + boff + p_ * 4096 + (ks) * 1024 + 512)); \
  } while (0)
#define G2_MM(ACC, FA, FB) do { \
    _Pragma("unroll") for (int p_ = 0; p_ < 2; ++p_) { \
      ACC[0][p_] = MFMA32(FB[p_], FA[0], ACC[0][p_]); ACC[1][p_] = MFMA32(FB[p_], FA[1], ACC[1][p_]); } \
  } while (0)
#define G2_SCHED() do { \
    _Pragma("unroll") for (int q_ = 0; q_ < 3; ++q_) { \
      __builtin_amdgcn_sched_group_barrier(0x008, 1, 0); __builtin_amdgcn_sched_group_barrier(0x100, 2, 0); } \
    __builtin_amdgcn_sched_group_barrier(0x008, 1, 0); SBAR(); \
  } while (0)

  asm volatile("s_barrier" ::: "memory");
  G2_RD(fa0, fb0, lbase, 0); SBAR();
  G2_RD(fa1, fb1, lbase, 1); G2_MM(accg, fa0, fb0); G2_SCHED();
  int slot = 1;
  for (int i = 0; i < 55; ++i) {
    const LAS char* nb = lbase + slot * G2_STAGE;
    asm volatile("s_waitcnt lgkmcnt(0)\n\ts_barrier" ::: "memory"); SBAR();
    G2_RD(fa0, fb0, nb, 0); G2_MM(accg, fa1, fb1); G2_SCHED();
    G2_RD(fa1, fb1, nb, 1); G2_MM(accg, fa0, fb0); G2_SCHED();
    slot = (slot == G2_NST - 1) ? 0 : slot + 1;
  }
  {
    const LAS char* nb = lbase + slot * G2_STAGE;
    asm volatile("s_waitcnt lgkmcnt(0)\n\ts_barrier" ::: "memory"); SBAR();
    G2_RD(fa0, fb0, nb, 0); G2_MM(accg, fa1, fb1); G2_SCHED();
    G2_RD(fa1, fb1, nb, 1); G2_MM(accs, fa0, fb0); G2_SCHED();
    slot = (slot == G2_NST - 1) ? 0 : slot + 1;
  }
  for (int i = 0; i < 15; ++i) {
    const LAS char* nb = lbase + slot * G2_STAGE;
    asm volatile("s_waitcnt lgkmcnt(0)\n\ts_barrier" ::: "memory"); SBAR();
    G2_RD(fa0, fb0, nb, 0); G2_MM(accs, fa1, fb1); G2_SCHED();
    G2_RD(fa1, fb1, nb, 1); G2_MM(accs, fa0, fb0); G2_SCHED();
    slot = (slot == G2_NST - 1) ? 0 : slot + 1;
  }
  G2_MM(accs, fa1, fb1);

  asm volatile("s_waitcnt lgkmcnt(0)\n\ts_barrier" ::: "memory");
  {
    LAS char* xw = (LAS char*)smem + w * 16384 + l * 16;
#pragma unroll
    for (int p = 0; p < 2; ++p) {
      const f32x16 g = sel16(wk, accg[0][p], accg[1][p]), s = sel16(wk, accs[0][p], accs[1][p]);
#pragma unroll
      for (int i = 0; i < 4; ++i) {
        *(LAS f32x4*)(xw + p * 8192 + i * 1024) = (f32x4){g[4 * i], g[4 * i + 1], g[4 * i + 2], g[4 * i + 3]};
        *(LAS f32x4*)(xw + p * 8192 + 4096 + i * 1024) = (f32x4){s[4 * i], s[4 * i + 1], s[4 * i + 2], s[4 * i + 3]};
      }
    }
  }
  asm volatile("s_waitcnt lgkmcnt(0)\n\ts_barrier" ::: "memory");
  {
    const LAS char* xr = (const LAS char*)smem + (w ^ 2) * 16384 + l * 16;
    const int row = mtile * 128 + wm * 64 + wk * 32 + (l & 31);
#pragma unroll
    for (int p = 0; p < 2; ++p) {
      const f32x16 g = sel16(wk, accg[1][p], accg[0][p]), s = sel16(wk, accs[1][p], accs[0][p]);
#pragma unroll
      for (int i = 0; i < 4; ++i) {
        const f32x4 tg = *(const LAS f32x4*)(xr + p * 8192 + i * 1024);
        const f32x4 ts = *(const LAS f32x4*)(xr + p * 8192 + 4096 + i * 1024);
        const int col = ntile * 64 + p * 32 + 8 * i + 4 * h;
        const u32x2 bv = *(const u32x2*)(b2 + col);
        const u32x2 xv = *(const u32x2*)(x + (size_t)row * DM + col);
        float r[4];
#pragma unroll
        for (int j = 0; j < 4; ++j) {
          const float bb = (j & 1) ? bf_hi(bv[j >> 1]) : bf_lo(bv[j >> 1]);
          const float xx = (j & 1) ? bf_hi(xv[j >> 1]) : bf_lo(xv[j >> 1]);
          const float z = g[4 * i + j] + tg[j] + bb;
          const float sg = z * __builtin_amdgcn_rcpf(1.0f + __builtin_amdgcn_exp2f(-1.4426950408889634f * z));
          r[j] = xx + (s[4 * i + j] + ts[j]) * sg;
        }
        *(u32x2*)(out + (size_t)row * DM + col) = (u32x2){cvtpk(r[0], r[1]), cvtpk(r[2], r[3])};
      }
    }
  }
#undef G2_RD
#undef G2_MM
#undef G2_SCHED
}

extern "C" void kernel_launch(void* const* d_in, const int* in_sizes, int n_in, void* d_out, int out_size, void* d_ws,
                              size_t ws_size, hipStream_t stream) {
  const u16* x = (const u16*)d_in[0];
  const u16* wq = (const u16*)d_in[1];
  const u16* wk = (const u16*)d_in[2];
  const u16* wv = (const u16*)d_in[3];
  const u16* wo = (const u16*)d_in[4];
  const u16* w1 = (const u16*)d_in[6];
  const u16* b1 = (const u16*)d_in[7];
  const u16* w2 = (const u16*)d_in[8];
  const u16* b2 = (const u16*)d_in[9];
  char* ws = (char*)d_ws;
  (void)hipFuncSetAttribute((const void*)gemm1_kernel, hipFuncAttributeMaxDynamicSharedMemorySize, G1_LDS);
  (void)hipFuncSetAttribute((const void*)attn_kernel, hipFuncAttributeMaxDynamicSharedMemorySize, AT_LDS);
  (void)hipFuncSetAttribute((const void*)gemm2_kernel, hipFuncAttributeMaxDynamicSharedMemorySize, G2_LDS);
  ln_kernel<<<256, 256, 0, stream>>>(x, ws);
  gemm1_kernel<<<240, 512, G1_LDS, stream>>>(wq, wk, wv, w1, b1, ws);
  attn_kernel<<<256, 512, AT_LDS, stream>>>(ws);
  gemm2_kernel<<<256, 512, G2_LDS, stream>>>(x, wo, w2, b2, ws, (u16*)d_out);
}
```

```cpp
#include <hip/hip_runtime.h>
#include <hip/hip_bf16.h>

typedef short bf16x8 __attribute__((ext_vector_type(8)));
typedef short s16x4 __attribute__((ext_vector_type(4)));
typedef float f32x16 __attribute__((ext_vector_type(16)));
typedef float f32x4 __attribute__((ext_vector_type(4)));
typedef float f32x2 __attribute__((ext_vector_type(2)));
typedef __bf16 bf16x2_t __attribute__((ext_vector_type(2)));
typedef unsigned u32x4 __attribute__((ext_vector_type(4)));
typedef unsigned u32x2 __attribute__((ext_vector_type(2)));
typedef unsigned short u16;
#define LAS __attribute__((address_space(3)))
#define DI __device__ __forceinline__
#define SBAR() __builtin_amdgcn_sched_barrier(0)
#define PIN(x) asm volatile("" : "+v"(x))
#define MFMA32(a, b, c) __builtin_amdgcn_mfma_f32_32x32x16_bf16((a), (b), (c), 0, 0, 0)
#define MFMA16(a, b, c) __builtin_amdgcn_mfma_f32_16x16x32_bf16((a), (b), (c), 0, 0, 0)

constexpr int SEQ = 2048, DM = 1024, NH = 32, FF = 3584;
constexpr size_t KT = 262144;
constexpr size_t WS_XN = 0;
constexpr size_t WS_Q = 4u << 20;
constexpr size_t WS_KV = 8u << 20;
constexpr size_t WS_O = 9u << 20;
constexpr size_t WS_H = 13u << 20;
constexpr size_t WS_WT = 28u << 20;
constexpr size_t WS_PART = 38u << 20;
constexpr size_t WS_FLAG = 47u << 20;
constexpr int FLAG_O = 256 * 32;
#define FUSE 1

#define STAMP(kid, idx) do {} while (0)
DI unsigned rfl(unsigned x) { return __builtin_amdgcn_readfirstlane(x); }
DI unsigned cvtpk(float lo, float hi) {
  f32x2 v = {lo, hi};
  bf16x2_t b = __builtin_convertvector(v, bf16x2_t);
  return __builtin_bit_cast(unsigned, b);
}
DI float fadd_s(float a, float b) { float r; asm("v_add_f32_e32 %0, %1, %2" : "=v"(r) : "v"(a), "v"(b)); return r; }
DI float bf_lo(unsigned u) { return __uint_as_float(u << 16); }
DI float bf_hi(unsigned u) { return __uint_as_float(u & 0xffff0000u); }
DI float bf_round(float x) { return bf_lo(cvtpk(x, x)); }

template <int IMM>
DI void glds16(unsigned voff, const void* sbase, unsigned ldsaddr) {
  asm volatile("s_mov_b32 m0, %2\n\ts_nop 0\n\tglobal_load_lds_dwordx4 %0, %1 offset:%3"
               :: "v"(voff), "s"(sbase), "s"(ldsaddr), "i"(IMM) : "memory");
}
template <int IMM>
DI void glds16_dev(unsigned voff, const void* sbase, unsigned ldsaddr) {
  asm volatile("s_mov_b32 m0, %2\n\ts_nop 0\n\tglobal_load_lds_dwordx4 %0, %1 offset:%3 sc1"
               :: "v"(voff), "s"(sbase), "s"(ldsaddr), "i"(IMM) : "memory");
}
DI void store16_dev(void* p, u32x4 v) {
  asm volatile("global_store_dwordx4 %0, %1, off sc1\n\ts_nop 1" :: "v"(p), "v"(v) : "memory");
}
DI void l2_touch(const void* p) { asm volatile("global_load_dword v255, %0, off" :: "v"(p) : "v255", "memory"); }
#define PFD 0
typedef __attribute__((address_space(1))) unsigned gu32;
#define RLX_AGENT __ATOMIC_RELAXED, __HIP_MEMORY_SCOPE_AGENT
DI void wait_ge(const unsigned* word, unsigned want) {
  for (unsigned spins = 0; spins < (1u << 22); ++spins) {
    if (rfl(__hip_atomic_load((gu32*)word, RLX_AGENT)) >= want) break;
    __builtin_amdgcn_s_sleep(2);
  }
  __builtin_amdgcn_fence(__ATOMIC_ACQUIRE, "wavefront");
}
DI float relu_s(float a) { float r; asm("v_max_f32_e32 %0, 0, %1" : "=v"(r) : "v"(a)); return r; }
DI bf16x8 lds_b128(const LAS char* p) { return *(const LAS bf16x8*)p; }
DI s16x4 lds_tr(const LAS char* p) {
  return __builtin_amdgcn_ds_read_tr16_b64_v4i16((LAS s16x4*)p);
}
DI f32x16 sel16(bool c, const f32x16& a, const f32x16& b) {
  f32x16 r;
#pragma unroll
  for (int i = 0; i < 16; ++i) r[i] = c ? a[i] : b[i];
  return r;
}
DI bf16x8 cat8(s16x4 lo, s16x4 hi) { return __builtin_shufflevector(lo, hi, 0, 1, 2, 3, 4, 5, 6, 7); }

__global__ __launch_bounds__(256) void ln_kernel(const u16* __restrict__ x, char* __restrict__ ws) {
  __shared__ float red[2][4][8];
  const int tid = threadIdx.x, w = tid >> 6, l = tid & 63;
  const int row = blockIdx.x * 8 + (l & 7);
  STAMP(0, 0);
  if (tid < 34) ((unsigned*)(ws + WS_FLAG))[blockIdx.x * 34 + tid] = 0u;
  u32x4 v[4];
#pragma unroll
  for (int i = 0; i < 4; ++i) {
    const int c = (i * 4 + w) * 8 + (l >> 3);
    v[i] = *(const u32x4*)(x + (size_t)row * DM + c * 8);
  }
  float s = 0.f;
#pragma unroll
  for (int i = 0; i < 4; ++i)
#pragma unroll
    for (int j = 0; j < 4; ++j) s += bf_lo(v[i][j]) + bf_hi(v[i][j]);
  s += __shfl_xor(s, 8); s += __shfl_xor(s, 16); s += __shfl_xor(s, 32);
  if (l < 8) red[0][w][l] = s;
  __syncthreads();
  const float mean = (red[0][0][l & 7] + red[0][1][l & 7] + red[0][2][l & 7] + red[0][3][l & 7]) * (1.0f / DM);
  float q = 0.f;
#pragma unroll
  for (int i = 0; i < 4; ++i)
#pragma unroll
    for (int j = 0; j < 4; ++j) {
      const float a = bf_lo(v[i][j]) - mean, b = bf_hi(v[i][j]) - mean;
      q += a * a + b * b;
    }
  q += __shfl_xor(q, 8); q += __shfl_xor(q, 16); q += __shfl_xor(q, 32);
  if (l < 8) red[1][w][l] = q;
  __syncthreads();
  const float var = (red[1][0][l & 7] + red[1][1][l & 7] + red[1][2][l & 7] + red[1][3][l & 7]) * (1.0f / DM);
  const float mb = bf_round(mean);
  const float den = bf_round(__builtin_sqrtf(bf_round(bf_round(var) + 1e-5f)));
  const float rden = 1.0f / den;
  char* dst = ws + WS_XN + (size_t)(row >> 5) * 4096 + (row & 31) * 16;
#pragma unroll
  for (int i = 0; i < 4; ++i) {
    const int c = (i * 4 + w) * 8 + (l >> 3);
    u32x4 o;
#pragma unroll
    for (int j = 0; j < 4; ++j) {
      const unsigned d = cvtpk(bf_lo(v[i][j]) - mb, bf_hi(v[i][j]) - mb);
      o[j] = cvtpk(bf_lo(d) * rden, bf_hi(d) * rden);
    }
    *(u32x4*)(dst + (size_t)(c >> 3) * KT + (c & 7) * 512) = o;
  }
  STAMP(0, 3);
}

constexpr int G1_STAGE = 53248, G1_BOFF = 32768, G1_LDS = 3 * G1_STAGE;

__global__ __launch_bounds__(512, 2) void gemm1_kernel(const u16* __restrict__ wq, const u16* __restrict__ wk,
                                                       const u16* __restrict__ wv, const u16* __restrict__ w1,
                                                       const u16* __restrict__ b1, const u16* __restrict__ w2,
                                                       const u16* __restrict__ wo, char* __restrict__ ws) {
  extern __shared__ __attribute__((aligned(16))) char smem[];
  const int tid = threadIdx.x, l = tid & 63;
  const int w = rfl(tid >> 6);
  if (blockIdx.x >= 240) {
    const int gw = (blockIdx.x - 240) * 8 + w;
    const unsigned lw0 = (unsigned)(size_t)(LAS char*)smem + w * 16384;
    const LAS char* lr = (const LAS char*)smem + w * 16384 + (4 * (l >> 5) + ((l & 15) >> 2)) * 64 + ((l >> 4) & 1) * 32 + (l & 3) * 8;
    const unsigned voff = (unsigned)((l >> 2) * DM * 2 + (l & 3) * 16);
    auto ld = [&](int u) {
      const int un = gw * 18 + u, ks = un >> 3, nt = un & 7;
      const char* src = (ks < 224) ? (const char*)(w2 + (size_t)ks * 16 * DM + nt * 128) : (const char*)(wo + (size_t)(ks - 224) * 16 * DM + nt * 128);
      const unsigned d = lw0 + (u & 3) * 4096;
      glds16<0>(voff, src, d); glds16<0>(voff, src + 64, d + 1024); glds16<0>(voff, src + 128, d + 2048); glds16<0>(voff, src + 192, d + 3072);
    };
    ld(0); ld(1); ld(2); ld(3);
#pragma unroll
    for (int u = 0; u < 18; ++u) {
      constexpr int dummy = 0; (void)dummy;
      if (u == 0 || u == 17) asm volatile("s_waitcnt vmcnt(12)" ::: "memory");
      else if (u == 1 || u == 16) asm volatile("s_waitcnt vmcnt(16)" ::: "memory");
      else if (u == 2 || u == 15) asm volatile("s_waitcnt vmcnt(20)" ::: "memory");
      else asm volatile("s_waitcnt vmcnt(24)" ::: "memory");
      const int un = gw * 18 + u, ks = un >> 3, nt = un & 7;
      char* dst = ws + WS_WT + (size_t)((ks >> 2) * 32 + nt * 4 + (ks & 3)) * 4096 + l * 16;
      bf16x8 f[4];
#pragma unroll
      for (int p = 0; p < 4; ++p) f[p] = cat8(lds_tr(lr + (u & 3) * 4096 + p * 1024), lds_tr(lr + (u & 3) * 4096 + p * 1024 + 512));
#pragma unroll
      for (int p = 0; p < 4; ++p) *(bf16x8*)(dst + p * 1024) = f[p];
      if (u + 4 < 18) ld(u + 4);
    }
    return;
  }
  const int lin = (blockIdx.x & 7) * 30 + (blockIdx.x >> 3);
  const int mh = lin / 120, rem = lin % 120, nt = rem >> 2, mtile = mh * 4 + (rem & 3);
  const unsigned lds0 = (unsigned)(size_t)(LAS char*)smem;

  if (w >= 4) {
    const int lw = w - 4;
    const char* abase = ws + WS_XN + (size_t)(mtile * 8 + lw * 2) * 4096;
    const unsigned avoff = l * 16;
    const char* bbase[5];
    unsigned bvoff[5], bstep[5];
#pragma unroll
    for (int p = 0; p < 5; ++p) {
      const int cb = nt * 5 + p;
      const u16* src; int ld;
      if (cb < 32) { src = wq + cb * 32; ld = DM; }
      else if (cb == 33) { src = wv; ld = 32; }
      else if (cb < 35 || cb >= 147) { src = wk; ld = 32; }
      else { src = w1 + (cb - 35) * 32; ld = FF; }
      bbase[p] = (const char*)src;
      bvoff[p] = (unsigned)((lw * 16 + (l >> 2)) * ld * 2 + (l & 3) * 16);
      bstep[p] = (unsigned)(64 * ld * 2);
    }
    auto issue = [&](int kt, int slot) {
      const unsigned sa = lds0 + slot * G1_STAGE + lw * 8192;
      const char* a0 = abase + (size_t)kt * KT;
      glds16<0>(avoff, a0, sa); glds16<1024>(avoff, a0, sa); glds16<2048>(avoff, a0, sa); glds16<3072>(avoff, a0, sa);
      const char* a1 = a0 + 4096;
      glds16<0>(avoff, a1, sa + 4096); glds16<1024>(avoff, a1, sa + 4096); glds16<2048>(avoff, a1, sa + 4096); glds16<3072>(avoff, a1, sa + 4096);
      const unsigned sb = lds0 + slot * G1_STAGE + G1_BOFF + lw * 1024;
#pragma unroll
      for (int p = 0; p < 5; ++p) glds16<0>(bvoff[p], bbase[p] + (size_t)kt * bstep[p], sb + p * 4096);
    };
    issue(0, 0); issue(1, 1); issue(2, 2);
    asm volatile("s_waitcnt vmcnt(26)\n\ts_barrier" ::: "memory");
    int slot = 0;
    for (int i = 0; i < 13; ++i) {
      asm volatile("s_waitcnt vmcnt(13)\n\ts_barrier" ::: "memory");
      issue(i + 3, slot);
      slot = (slot == 2) ? 0 : slot + 1;
    }
    asm volatile("s_waitcnt vmcnt(13)\n\ts_barrier" ::: "memory");
    asm volatile("s_waitcnt vmcnt(0)\n\ts_barrier" ::: "memory");
    return;
  }

  STAMP(1, 0);
  const int h = l >> 5;
  f32x16 acc[2][5];
  if (nt >= 7) {
#pragma unroll
    for (int p = 0; p < 5; ++p) {
      int cb = nt * 5 + p - 35; cb = cb > 111 ? 111 : cb;
#pragma unroll
      for (int i = 0; i < 4; ++i) {
        const u32x2 bv = *(const u32x2*)(b1 + cb * 32 + 8 * i + 4 * h);
        acc[0][p][4 * i + 0] = bf_lo(bv[0]); acc[0][p][4 * i + 1] = bf_hi(bv[0]);
        acc[0][p][4 * i + 2] = bf_lo(bv[1]); acc[0][p][4 * i + 3] = bf_hi(bv[1]);
      }
      acc[1][p] = acc[0][p];
    }
  } else {
#pragma unroll
    for (int p = 0; p < 5; ++p) { acc[0][p] = f32x16{}; acc[1][p] = f32x16{}; }
  }
  const LAS char* lbase = (const LAS char*)smem;
  const int aoff = w * 8192 + l * 16;
  const int boff = G1_BOFF + (8 * h + ((l & 15) >> 2)) * 64 + ((l >> 4) & 1) * 32 + (l & 3) * 8;
  bf16x8 fa0[2], fb0[5], fa1[2], fb1[5];
#define G1_RD(FA, FB, base, ks) do { \
    FA[0] = lds_b128((base) + aoff + (ks) * 1024); FA[1] = lds_b128((base) + aoff + 4096 + (ks) * 1024); \
    _Pragma("unroll") for (int p_ = 0; p_ < 5; ++p_) \
      FB[p_] = cat8(lds_tr((base) + boff + p_ * 4096 + (ks) * 1024), lds_tr((base) + boff + p_ * 4096 + (ks) * 1024 + 256)); \
  } while (0)
#define G1_MM(FA, FB) do { \
    _Pragma("unroll") for (int p_ = 0; p_ < 5; ++p_) { \
      acc[0][p_] = MFMA32(FB[p_], FA[0], acc[0][p_]); acc[1][p_] = MFMA32(FB[p_], FA[1], acc[1][p_]); } \
  } while (0)
#define G1_SCHED() do { \
    _Pragma("unroll") for (int q_ = 0; q_ < 6; ++q_) { \
      __builtin_amdgcn_sched_group_barrier(0x008, 1, 0); __builtin_amdgcn_sched_group_barrier(0x100, 2, 0); } \
    __builtin_amdgcn_sched_group_barrier(0x008, 4, 0); SBAR(); \
  } while (0)

  asm volatile("s_barrier" ::: "memory");
  STAMP(1, 1);
  G1_RD(fa0, fb0, lbase, 0); SBAR();
  G1_RD(fa1, fb1, lbase, 1); G1_MM(fa0, fb0); G1_SCHED();
  G1_RD(fa0, fb0, lbase, 2); G1_MM(fa1, fb1); G1_SCHED();
  G1_RD(fa1, fb1, lbase, 3); G1_MM(fa0, fb0); G1_SCHED();
  int slot = 1;
  for (int i = 0; i < 15; ++i) {
    const LAS char* nb = lbase + slot * G1_STAGE;
    asm volatile("s_waitcnt lgkmcnt(0)\n\ts_barrier" ::: "memory"); SBAR();
    G1_RD(fa0, fb0, nb, 0); G1_MM(fa1, fb1); G1_SCHED();
    G1_RD(fa1, fb1, nb, 1); G1_MM(fa0, fb0); G1_SCHED();
    G1_RD(fa0, fb0, nb, 2); G1_MM(fa1, fb1); G1_SCHED();
    G1_RD(fa1, fb1, nb, 3); G1_MM(fa0, fb0); G1_SCHED();
    slot = (slot == 2) ? 0 : slot + 1;
  }
  G1_MM(fa1, fb1);
  asm volatile("s_nop 15\n\ts_nop 3" ::: "memory"); SBAR();
  STAMP(1, 2);

  if (nt >= 7) {
#pragma unroll
    for (int p = 0; p < 5; ++p) {
      const int cb = nt * 5 + p - 35;
      if (cb < 112) {
#pragma unroll
        for (int m = 0; m < 2; ++m) {
          char* dst = ws + WS_H + (size_t)(cb >> 1) * KT + (size_t)(mtile * 8 + w * 2 + m) * 4096 + (cb & 1) * 2048 + l * 16;
#pragma unroll
          for (int s = 0; s < 2; ++s) {
            u32x4 o;
#pragma unroll
            for (int j = 0; j < 4; ++j) {
              const float a = relu_s(acc[m][p][8 * s + 2 * j]), b = relu_s(acc[m][p][8 * s + 2 * j + 1]);
              o[j] = cvtpk(a * a, b * b);
            }
            *(u32x4*)(dst + s * 1024) = o;
          }
        }
      }
    }
  } else {
    const float qs = 0.17677669529663687f * 1.4426950408889634f;
#pragma unroll
    for (int p = 0; p < 5; ++p) {
      const int cb = nt * 5 + p;
#pragma unroll
      for (int m = 0; m < 2; ++m) {
        const int tt = mtile * 8 + w * 2 + m;
        if (cb <= 32) {
          char* dst = (cb < 32) ? ws + WS_Q + (size_t)(tt * 32 + cb) * 2048 + l * 16 : ws + WS_KV + (size_t)tt * 4096 + l * 16;
          const float sc = (cb < 32) ? qs : 1.0f;
#pragma unroll
          for (int s = 0; s < 2; ++s) {
            u32x4 o;
#pragma unroll
            for (int j = 0; j < 4; ++j) o[j] = cvtpk(acc[m][p][8 * s + 2 * j] * sc, acc[m][p][8 * s + 2 * j + 1] * sc);
            *(u32x4*)(dst + s * 1024) = o;
          }
        } else if (cb == 33) {
          const int key = l & 31;
          char* dst = ws + WS_KV + (size_t)tt * 4096 + 2048 + (key >> 4) * 1024 + ((key >> 2) & 1) * 512 + (((key >> 3) & 1) * 4 + (key & 3)) * 2;
#pragma unroll
          for (int r = 0; r < 16; ++r) {
            const int d = (r & 3) + 8 * (r >> 2) + 4 * h;
            *(u16*)(dst + d * 16) = (u16)cvtpk(acc[m][p][r], 0.f);
          }
        }
      }
    }
  }
  STAMP(1, 3);
#undef G1_RD
#undef G1_MM
#undef G1_SCHED
}

constexpr int AT_CHUNK = 4, AT_NCH = 8;
constexpr int AT_BUF = 2 * AT_CHUNK * 4096;
constexpr int AT_BASE = 65536;
constexpr int K3_LDS = 163840;

DI float xhalf_max(float m) {
  auto rr = __builtin_amdgcn_permlane32_swap(__float_as_uint(m), __float_as_uint(m), false, false);
  return __builtin_fmaxf(__uint_as_float(rr[0]), __uint_as_float(rr[1]));
}
DI float xhalf_sum(float m) {
  auto rr = __builtin_amdgcn_permlane32_swap(__float_as_uint(m), __float_as_uint(m), false, false);
  return __uint_as_float(rr[0]) + __uint_as_float(rr[1]);
}
DI float max16(const f32x16& s) {
  float a = __builtin_fmaxf(s[0], s[1]);
#pragma unroll
  for (int r = 2; r < 16; ++r) a = __builtin_fmaxf(a, s[r]);
  return a;
}
DI bf16x8 pack8(const f32x16& p, int s) {
  u32x4 o;
#pragma unroll
  for (int j = 0; j < 4; ++j) o[j] = cvtpk(p[8 * s + 2 * j], p[8 * s + 2 * j + 1]);
  return __builtin_bit_cast(bf16x8, o);
}

DI void attn_fallback(const char* __restrict__ kv, bf16x8 q0, bf16x8 q1, int l, f32x16& o_out, float& l_out) {
  f32x16 o = {};
  float m = -INFINITY, lsum = 0.f;
  for (int t = 0; t < 64; ++t) {
    const char* p = kv + (size_t)t * 4096 + l * 16;
    const bf16x8 k0 = *(const bf16x8*)p, k1 = *(const bf16x8*)(p + 1024), v0 = *(const bf16x8*)(p + 2048), v1 = *(const bf16x8*)(p + 3072);
    f32x16 s = {};
    s = MFMA32(k0, q0, s); s = MFMA32(k1, q1, s);
    const float mn = __builtin_fmaxf(m, xhalf_max(max16(s)));
    const float ms = (mn == -INFINITY) ? 0.f : mn;
    const float alpha = __builtin_amdgcn_exp2f(m - ms);
    m = mn;
    float ps = 0.f;
#pragma unroll
    for (int r = 0; r < 16; ++r) { s[r] = __builtin_amdgcn_exp2f(s[r] - ms); ps += s[r]; }
    lsum = lsum * alpha + ps;
#pragma unroll
    for (int r = 0; r < 16; ++r) o[r] *= alpha;
    o = MFMA32(v0, pack8(s, 0), o); o = MFMA32(v1, pack8(s, 1), o);
  }
  o_out = o;
  l_out = xhalf_sum(lsum);
}

constexpr int G2_SLOT = 32768, G2_BOFF = 16384, G2_PART = 131072;
struct G2Ld {
  const char* hb; const char* ob; const char* wb; unsigned lds; unsigned voff; int kh;
  DI void init(const char* ws, unsigned lds0, int lw, int l) {
    const int xcd = blockIdx.x & 7, idx = blockIdx.x >> 3;
    kh = xcd & 1;
    const int mt = ((xcd >> 1) & 1) * 8 + (idx & 7), nt = (xcd >> 2) * 4 + (idx >> 3);
    hb = ws + WS_H + (size_t)(mt * 4 + lw) * 4096 + (size_t)kh * (28 * KT);
    ob = ws + WS_O + (size_t)(mt * 4 + lw) * 4096;
    wb = ws + WS_WT + (size_t)(nt * 4 + lw) * 4096;
    lds = lds0 + lw * 4096; voff = l * 16;
  }
  DI void ffn(int s) const {
    const unsigned d = lds + (s & 3) * G2_SLOT;
    const char* a = hb + (size_t)s * KT;
    const char* b = wb + (size_t)(kh * 28 + s) * 131072;
    glds16<0>(voff, a, d); glds16<1024>(voff, a, d); glds16<2048>(voff, a, d); glds16<3072>(voff, a, d);
    glds16<0>(voff, b, d + G2_BOFF); glds16<1024>(voff, b, d + G2_BOFF); glds16<2048>(voff, b, d + G2_BOFF); glds16<3072>(voff, b, d + G2_BOFF);
  }
  DI void wo(int s) const {
    const unsigned d = lds + (s & 3) * G2_SLOT;
    const char* a = ob + (size_t)(s - 28) * KT;
    const char* b = wb + (size_t)(28 + s) * 131072 + kh * 2048;
    glds16_dev<0>(voff, a, d); glds16_dev<1024>(voff, a, d); glds16_dev<2048>(voff, a, d); glds16_dev<3072>(voff, a, d);
    glds16<0>(voff, b, d + G2_BOFF); glds16<1024>(voff, b, d + G2_BOFF);
  }
};

DI void attn_phase(char* __restrict__ ws, char* smem, const int w, const int l) {
  const int hp = w & 3, kh = w >> 2;
  const int tt = blockIdx.x >> 2, hg = blockIdx.x & 3;
  const int head0 = hg * 8 + hp * 2;
  const unsigned lds0 = (unsigned)(size_t)(LAS char*)smem;
  const char* kvg = ws + WS_KV;
  const unsigned voff = l * 16;
  const char* ksrc = kvg + (size_t)(kh * 32 + hp) * 4096;
  const unsigned kdst = lds0 + AT_BASE + w * 4096;
#define AT_LD(c, slot, piece) glds16<(piece) * 1024>(voff, ksrc + (size_t)(c) * (AT_CHUNK * 4096), kdst + (slot))
  STAMP(2, 0);
  AT_LD(0, 0, 0); AT_LD(0, 0, 1); AT_LD(0, 0, 2); AT_LD(0, 0, 3);
  const char* qp = ws + WS_Q + (size_t)(tt * 32 + head0) * 2048 + l * 16;
  bf16x8 qa0 = *(const bf16x8*)qp, qa1 = *(const bf16x8*)(qp + 1024), qb0 = *(const bf16x8*)(qp + 2048), qb1 = *(const bf16x8*)(qp + 3072);
  PIN(qa0); PIN(qa1); PIN(qb0); PIN(qb1);
  asm volatile("s_waitcnt vmcnt(0)\n\ts_barrier" ::: "memory");
  AT_LD(1, AT_BUF, 0); AT_LD(1, AT_BUF, 1); AT_LD(1, AT_BUF, 2); AT_LD(1, AT_BUF, 3);
  AT_LD(2, 2 * AT_BUF, 0);
  STAMP(2, 1);

  const LAS char* lb = (const LAS char*)smem + AT_BASE + l * 16;
  f32x16 nma, nmb;
  {
    const bf16x8 k0 = lds_b128(lb), k1 = lds_b128(lb + 1024);
    f32x16 sa = {}, sb = {};
    sa = MFMA32(k0, qa0, sa); sa = MFMA32(k1, qa1, sa);
    sb = MFMA32(k0, qb0, sb); sb = MFMA32(k1, qb1, sb);
    const float ma = -xhalf_max(max16(sa)), mb = -xhalf_max(max16(sb));
#pragma unroll
    for (int r = 0; r < 16; ++r) { nma[r] = ma; nmb[r] = mb; }
    PIN(nma); PIN(nmb);
  }
  bf16x8 sel;
  {
    const short one = ((l & 15) == ((l >> 4) & 1)) ? (short)0x3f80 : (short)0;
#pragma unroll
    for (int j = 0; j < 8; ++j) sel[j] = one;
  }
  f32x16 oa = {}, ob = {}, sa, sb;
  f32x4 la = {}, lbs = {};
  u32x4 pa0, pa1, pb0 = {}, pb1 = {};
  bf16x8 kx0, kx1, vx0, vx1, ky0, ky1, vy0, vy1;
  const LAS char* kvp = lb + kh * (AT_CHUNK * 4096);
  kx0 = lds_b128(kvp); kx1 = lds_b128(kvp + 1024); vx0 = lds_b128(kvp + 2048); vx1 = lds_b128(kvp + 3072);
  vy0 = vx0; vy1 = vx1;
  sa = MFMA32(kx0, qa0, nma); sa = MFMA32(kx1, qa1, sa);
#define EX(S, i) S[i] = __builtin_amdgcn_exp2f(S[i])
#define CV(P, j, S, i) P[j] = cvtpk(S[i], S[(i) + 1])
#define B8(x) __builtin_bit_cast(bf16x8, x)
#define LSUM(L, P) L = MFMA16(sel, B8(P), L)
#define PV(O, V, P) O = MFMA32(V, B8(P), O)
#define HALF(S, P0, P1, M0, M1, M2, M3, M4, M5, LD0, LD1) do { \
    M0; SBAR(); EX(S, 0); EX(S, 1); EX(S, 2); EX(S, 3); PIN(S); SBAR(); \
    M1; SBAR(); CV(P0, 0, S, 0); CV(P0, 1, S, 2); EX(S, 4); EX(S, 5); EX(S, 6); PIN(S); PIN(P0); SBAR(); \
    M2; SBAR(); LD0; EX(S, 7); CV(P0, 2, S, 4); EX(S, 8); CV(P0, 3, S, 6); EX(S, 9); PIN(S); PIN(P0); SBAR(); \
    M3; SBAR(); EX(S, 10); EX(S, 11); PIN(S); SBAR(); \
    M4; SBAR(); LD1; CV(P1, 0, S, 8); CV(P1, 1, S, 10); EX(S, 12); EX(S, 13); EX(S, 14); PIN(S); PIN(P1); SBAR(); \
    M5; SBAR(); EX(S, 15); CV(P1, 2, S, 12); CV(P1, 3, S, 14); PIN(P1); SBAR(); \
  } while (0)
#define STEP(KC0, KC1, VC0, VC1, KN0, KN1, VP0, VP1, NP, DMA) do { \
    HALF(sa, pa0, pa1, sb = MFMA32(KC0, qb0, nmb), sb = MFMA32(KC1, qb1, sb), PV(ob, VP0, pb0), LSUM(lbs, pb0), \
         PV(ob, VP1, pb1), LSUM(lbs, pb1), KN0 = lds_b128(NP); KN1 = lds_b128((NP) + 1024), (void)0); \
    HALF(sb, pb0, pb1, sa = MFMA32(KN0, qa0, nma), sa = MFMA32(KN1, qa1, sa), PV(oa, VC0, pa0), LSUM(la, pa0), \
         PV(oa, VC1, pa1), LSUM(la, pa1), VP0 = lds_b128((NP) + 2048); VP1 = lds_b128((NP) + 3072), DMA); \
  } while (0)
  int s0 = 0, s1 = AT_BUF, s2 = 2 * AT_BUF;
  G2Ld g2; g2.init(ws, lds0, w & 3, l);
  for (int c = 0; c < AT_NCH; ++c) {
    const LAS char* cb = kvp + s0;
    const LAS char* nb = kvp + s1;
    const int c2 = c + 2 < AT_NCH ? c + 2 : AT_NCH - 1, c3 = c + 3 < AT_NCH ? c + 3 : AT_NCH - 1;
    STEP(kx0, kx1, vx0, vx1, ky0, ky1, vy0, vy1, cb + 4096, AT_LD(c2, s2, 1));
    STEP(ky0, ky1, vy0, vy1, kx0, kx1, vx0, vx1, cb + 2 * 4096, AT_LD(c2, s2, 2));
    STEP(kx0, kx1, vx0, vx1, ky0, ky1, vy0, vy1, cb + 3 * 4096, AT_LD(c2, s2, 3));
    asm volatile("s_waitcnt vmcnt(4) lgkmcnt(0)\n\ts_barrier" ::: "memory");
    SBAR();
    STEP(ky0, ky1, vy0, vy1, kx0, kx1, vx0, vx1, nb, AT_LD(c3, s0, 0));
    if (w >= 4 && (c == 3 || c == 4)) g2.ffn(c - 3);
    const int t_ = s0; s0 = s1; s1 = s2; s2 = t_;
  }
  PV(ob, vy0, pb0); LSUM(lbs, pb0);
  PV(ob, vy1, pb1); LSUM(lbs, pb1);
#undef EX
#undef CV
#undef B8
#undef HALF
#undef STEP
#undef LSUM
#undef PV
#undef AT_LD
  STAMP(2, 2);
  asm volatile("s_waitcnt vmcnt(0) lgkmcnt(0)\n\ts_barrier" ::: "memory");
  {
    LAS char* xw = (LAS char*)smem + AT_BASE + w * 8192;
    const f32x16 so = sel16(kh, oa, ob);
    const f32x4 sl = kh ? la : lbs;
#pragma unroll
    for (int i = 0; i < 4; ++i) *(LAS f32x4*)(xw + i * 1024 + l * 16) = (f32x4){so[4 * i], so[4 * i + 1], so[4 * i + 2], so[4 * i + 3]};
    const f32x4 ml = kh ? lbs : la;
    if (l < 16) *(LAS f32x4*)(xw + 4096 + l * 16) = (f32x4){sl[0], sl[1], ml[0], ml[1]};
  }
  asm volatile("s_waitcnt lgkmcnt(0)\n\ts_barrier" ::: "memory");
  f32x16 o = sel16(kh, ob, oa);
  float lsum;
  {
    const LAS char* xr = (const LAS char*)smem + AT_BASE + (w ^ 4) * 8192;
    const LAS char* xo = (const LAS char*)smem + AT_BASE + w * 8192;
#pragma unroll
    for (int i = 0; i < 4; ++i) {
      const f32x4 t = *(const LAS f32x4*)(xr + i * 1024 + l * 16);
      o[4 * i] += t[0]; o[4 * i + 1] += t[1]; o[4 * i + 2] += t[2]; o[4 * i + 3] += t[3];
    }
    const int qo = (l & 15) * 16 + ((l >> 4) & 1) * 4;
    lsum = *(const LAS float*)(xr + 4096 + qo) + *(const LAS float*)(xo + 4096 + 8 + qo);
  }
  bool ok = (lsum >= 9.5367431640625e-07f) && (lsum <= 1.8446744073709552e19f);
#pragma unroll
  for (int r = 0; r < 16; ++r) ok = ok && (__builtin_fabsf(o[r]) <= 3.0e38f);
  if (!__all(ok)) {
    attn_fallback(kvg, kh ? qb0 : qa0, kh ? qb1 : qa1, l, o, lsum);
  }
  const float rl = 1.0f / lsum;
  char* dst = ws + WS_O + (size_t)(head0 >> 1) * KT + (size_t)tt * 4096 + kh * 2048 + l * 16;
#pragma unroll
  for (int s = 0; s < 2; ++s) {
    u32x4 ov;
#pragma unroll
    for (int j = 0; j < 4; ++j) ov[j] = cvtpk(o[8 * s + 2 * j] * rl, o[8 * s + 2 * j + 1] * rl);
    store16_dev(dst + s * 1024, ov);
  }
  STAMP(2, 3);
}

DI void gemm2_phase(const u16* __restrict__ x, const u16* __restrict__ b2, char* __restrict__ ws, u16* __restrict__ out,
                    char* smem, const int w, const int l) {
  const int xcd = blockIdx.x & 7, idx = blockIdx.x >> 3;
  const int kh = xcd & 1, mt = ((xcd >> 1) & 1) * 8 + (idx & 7), nt = (xcd >> 2) * 4 + (idx >> 3);
  const unsigned lds0 = (unsigned)(size_t)(LAS char*)smem;
  unsigned* flags = (unsigned*)(ws + WS_FLAG);

  if (w >= 4) {
    G2Ld g; g.init(ws, lds0, w - 4, l);
    asm volatile("s_waitcnt lgkmcnt(0)\n\ts_barrier" ::: "memory");
    g.ffn(2); g.ffn(3);
    asm volatile("s_barrier" ::: "memory");
    g.ffn(4);
    asm volatile("s_waitcnt vmcnt(16)\n\ts_barrier" ::: "memory");
    if (l == 0) __hip_atomic_fetch_add((gu32*)(flags + FLAG_O + (blockIdx.x >> 4) * 32), 1u, RLX_AGENT);
    g.ffn(5);
    for (int i = 2; i < 24; ++i) {
      asm volatile("s_waitcnt vmcnt(16)\n\ts_barrier" ::: "memory");
      g.ffn(i + 4);
    }
    asm volatile("s_waitcnt vmcnt(16)\n\ts_barrier" ::: "memory");
    wait_ge(flags + FLAG_O + mt * 32, 128u);
    g.wo(28);
    for (int i = 25; i < 40; ++i) {
      asm volatile("s_waitcnt vmcnt(12)\n\ts_barrier" ::: "memory");
      g.wo(i + 4);
    }
    asm volatile("s_waitcnt vmcnt(12)\n\ts_barrier" ::: "memory");
    asm volatile("s_waitcnt vmcnt(6)\n\ts_barrier" ::: "memory");
    wait_ge(flags + blockIdx.x * 32, 2u);
    {
      const char* src = ws + WS_PART + (size_t)(blockIdx.x ^ 1) * 32768 + (w - 4) * 8192;
      const unsigned d = lds0 + G2_PART + (w - 4) * 8192;
      glds16_dev<0>(g.voff, src, d); glds16_dev<1024>(g.voff, src, d); glds16_dev<2048>(g.voff, src, d); glds16_dev<3072>(g.voff, src, d);
      glds16_dev<0>(g.voff, src + 4096, d + 4096); glds16_dev<1024>(g.voff, src + 4096, d + 4096); glds16_dev<2048>(g.voff, src + 4096, d + 4096); glds16_dev<3072>(g.voff, src + 4096, d + 4096);
    }
    asm volatile("s_waitcnt vmcnt(8)\n\ts_barrier" ::: "memory");
    asm volatile("s_waitcnt vmcnt(0)\n\ts_barrier" ::: "memory");
    return;
  }

  STAMP(3, 0);
  const int h = l >> 5, wm = w & 1, wn = w >> 1;
  const bool own = (wn == kh);
  f32x16 accg[2][2] = {}, accs[2][2] = {};
  const int col0 = nt * 128 + kh * 64;
  const int row = mt * 128 + wm * 64 + wn * 32 + (l & 31);
  u32x4 xv[4];
  u32x2 bv[8];
#pragma unroll
  for (int k = 0; k < 4; ++k) xv[k] = *(const u32x4*)(x + (size_t)row * DM + col0 + 16 * k + 8 * h);
#pragma unroll
  for (int k = 0; k < 8; ++k) bv[k] = *(const u32x2*)(b2 + col0 + 8 * k + 4 * h);
  const char* pfF = (l < 32) ? ws + WS_H + (size_t)(mt * 4 + (nt & 3)) * 4096 + (size_t)kh * (28 * KT) + l * 128
                             : ws + WS_WT + (size_t)(kh * 28) * 131072 + nt * 16384 + (mt & 7) * 2048 + (l & 15) * 128;
  const unsigned pfs = (l < 32) ? (unsigned)KT : 131072u;
  const char* pfW = (l < 32) ? ws + WS_O + (size_t)(mt * 4 + (nt & 3)) * 4096 + l * 128
                             : ws + WS_WT + (size_t)56 * 131072 + (nt * 4 + ((mt & 7) >> 1)) * 4096 + kh * 2048 + (mt & 1) * 1024 + (l & 7) * 128;
  auto pf = [&](int s_) { if (s_ < 28) l2_touch(pfF + (size_t)s_ * pfs); else if (s_ < 44) l2_touch(pfW + (size_t)(s_ - 28) * pfs); };
  const LAS char* lbase = (const LAS char*)smem + l * 16;
  const int aF = wm * 8192, bF = G2_BOFF + wn * 2048;
  const int aW = wm * 8192 + wn * 2048, bW = G2_BOFF + wn * 8192;
  bf16x8 fa0[2], fb0[2], fa1[2], fb1[2];
#define G2_RD(FA, FB, base, ao, bo, ks) do { \
    FA[0] = lds_b128((base) + (ao) + (ks) * 1024); FB[0] = lds_b128((base) + (bo) + (ks) * 4096); \
    FA[1] = lds_b128((base) + (ao) + 4096 + (ks) * 1024); FB[1] = lds_b128((base) + (bo) + (ks) * 4096 + 1024); \
  } while (0)
#define G2_MM(ACC, FA, FB) do { \
    ACC[0][0] = MFMA32(FB[0], FA[0], ACC[0][0]); ACC[1][0] = MFMA32(FB[0], FA[1], ACC[1][0]); \
    ACC[0][1] = MFMA32(FB[1], FA[0], ACC[0][1]); ACC[1][1] = MFMA32(FB[1], FA[1], ACC[1][1]); \
  } while (0)
#define G2_SCHED() do { \
    __builtin_amdgcn_sched_group_barrier(0x008, 1, 0); __builtin_amdgcn_sched_group_barrier(0x100, 2, 0); \
    __builtin_amdgcn_sched_group_barrier(0x008, 1, 0); __builtin_amdgcn_sched_group_barrier(0x100, 2, 0); \
    __builtin_amdgcn_sched_group_barrier(0x008, 2, 0); SBAR(); \
  } while (0)
#define G2_BAR() do { asm volatile("s_waitcnt lgkmcnt(0)\n\ts_barrier" ::: "memory"); SBAR(); } while (0)

  G2_BAR();
  STAMP(3, 1);
  G2_RD(fa0, fb0, lbase, aF, bF, 0); SBAR();
  G2_RD(fa1, fb1, lbase, aF, bF, 1); G2_MM(accg, fa0, fb0); G2_SCHED();
  G2_RD(fa0, fb0, lbase, aF, bF, 2); G2_MM(accg, fa1, fb1); G2_SCHED();
  G2_RD(fa1, fb1, lbase, aF, bF, 3); G2_MM(accg, fa0, fb0); G2_SCHED();
  for (int i = 0; i < 27; ++i) {
    const LAS char* nb = lbase + ((i + 1) & 3) * G2_SLOT;
    G2_BAR();
    G2_RD(fa0, fb0, nb, aF, bF, 0); G2_MM(accg, fa1, fb1); G2_SCHED();
    G2_RD(fa1, fb1, nb, aF, bF, 1); G2_MM(accg, fa0, fb0); G2_SCHED();
    G2_RD(fa0, fb0, nb, aF, bF, 2); G2_MM(accg, fa1, fb1); G2_SCHED();
    G2_RD(fa1, fb1, nb, aF, bF, 3); G2_MM(accg, fa0, fb0); G2_SCHED();
    if (i == 3) {
      asm volatile("s_waitcnt vmcnt(0)" ::: "memory");
      if (l == 0) __hip_atomic_fetch_add((gu32*)(flags + FLAG_O + (blockIdx.x >> 4) * 32), 1u, RLX_AGENT);
      SBAR();
    }
  }
  STAMP(3, 4);
  {
    G2_BAR();
    G2_RD(fa0, fb0, lbase, aW, bW, 0); G2_MM(accg, fa1, fb1); G2_SCHED();
    G2_RD(fa1, fb1, lbase, aW, bW, 1); G2_MM(accs, fa0, fb0); G2_SCHED();
  }
  if (!own) {
    char* dst = ws + WS_PART + (size_t)blockIdx.x * 32768 + wm * 16384 + l * 16;
    asm volatile("s_nop 7" ::: "memory");
#pragma unroll
    for (int m = 0; m < 2; ++m)
#pragma unroll
      for (int p = 0; p < 2; ++p)
#pragma unroll
        for (int i = 0; i < 4; ++i)
          store16_dev(dst + (m * 2 + p) * 4096 + i * 1024,
                      (u32x4){__float_as_uint(accg[m][p][4 * i]), __float_as_uint(accg[m][p][4 * i + 1]), __float_as_uint(accg[m][p][4 * i + 2]), __float_as_uint(accg[m][p][4 * i + 3])});
  }
  SBAR();
  STAMP(3, 5);
  for (int i = 28; i < 43; ++i) {
    if (i == 40) STAMP(3, 6);
    const LAS char* nb = lbase + ((i + 1) & 3) * G2_SLOT;
    G2_BAR();
    G2_RD(fa0, fb0, nb, aW, bW, 0); G2_MM(accs, fa1, fb1); G2_SCHED();
    G2_RD(fa1, fb1, nb, aW, bW, 1); G2_MM(accs, fa0, fb0); G2_SCHED();
    if (i == 33 && !own) {
      asm volatile("s_waitcnt vmcnt(0)" ::: "memory");
      if (l == 0) __hip_atomic_fetch_add((gu32*)(flags + (blockIdx.x ^ 1) * 32), 1u, RLX_AGENT);
      SBAR();
    }
  }
  G2_MM(accs, fa1, fb1);
  STAMP(3, 2);

  {
    LAS char* xw = (LAS char*)smem + w * 16384 + l * 16;
#pragma unroll
    for (int p = 0; p < 2; ++p) {
      const f32x16 s = sel16(wn, accs[0][p], accs[1][p]);
#pragma unroll
      for (int i = 0; i < 4; ++i) *(LAS f32x4*)(xw + p * 4096 + i * 1024) = (f32x4){s[4 * i], s[4 * i + 1], s[4 * i + 2], s[4 * i + 3]};
    }
    if (own) {
#pragma unroll
      for (int p = 0; p < 2; ++p) {
        const f32x16 g = sel16(wn, accg[0][p], accg[1][p]);
#pragma unroll
        for (int i = 0; i < 4; ++i) *(LAS f32x4*)(xw + 8192 + p * 4096 + i * 1024) = (f32x4){g[4 * i], g[4 * i + 1], g[4 * i + 2], g[4 * i + 3]};
      }
    }
  }
  G2_BAR();
  STAMP(3, 7);
  {
    const LAS char* xr = (const LAS char*)smem + (w ^ 2) * 16384 + l * 16;
    const LAS char* xp = (const LAS char*)smem + G2_PART + wm * 16384 + wn * 8192 + l * 16;
    f32x16 gf[2];
    if (own) {
      gf[0] = sel16(wn, accg[1][0], accg[0][0]); gf[1] = sel16(wn, accg[1][1], accg[0][1]);
    } else {
#pragma unroll
      for (int p = 0; p < 2; ++p)
#pragma unroll
        for (int i = 0; i < 4; ++i) {
          const f32x4 t = *(const LAS f32x4*)(xr + 8192 + p * 4096 + i * 1024);
          gf[p][4 * i] = t[0]; gf[p][4 * i + 1] = t[1]; gf[p][4 * i + 2] = t[2]; gf[p][4 * i + 3] = t[3];
        }
    }
#pragma unroll
    for (int p = 0; p < 2; ++p) {
      const f32x16 s = sel16(wn, accs[1][p], accs[0][p]);
      unsigned pk[8];
#pragma unroll
      for (int k = 0; k < 2; ++k) {
        u32x4 xx = xv[p * 2 + k];
        { auto r0 = __builtin_amdgcn_permlane32_swap(xx[0], xx[2], false, false); xx[0] = r0[0]; xx[2] = r0[1];
          auto r1 = __builtin_amdgcn_permlane32_swap(xx[1], xx[3], false, false); xx[1] = r1[0]; xx[3] = r1[1]; }
#pragma unroll
        for (int ii = 0; ii < 2; ++ii) {
          const int i = 2 * k + ii;
          const f32x4 ts = *(const LAS f32x4*)(xr + p * 4096 + i * 1024);
          const f32x4 tp = *(const LAS f32x4*)(xp + p * 4096 + i * 1024);
          float r[4];
#pragma unroll
          for (int j = 0; j < 4; ++j) {
            const unsigned xw_ = xx[2 * ii + (j >> 1)];
            const float xf = (j & 1) ? bf_hi(xw_) : bf_lo(xw_);
            const unsigned bw_ = bv[p * 4 + i][j >> 1];
            const float z = gf[p][4 * i + j] + tp[j] + ((j & 1) ? bf_hi(bw_) : bf_lo(bw_));
            const float sg = z * __builtin_amdgcn_rcpf(1.0f + __builtin_amdgcn_exp2f(-1.4426950408889634f * z));
            r[j] = xf + (s[4 * i + j] + ts[j]) * sg;
          }
          pk[4 * k + 2 * ii] = cvtpk(r[0], r[1]); pk[4 * k + 2 * ii + 1] = cvtpk(r[2], r[3]);
        }
        auto r0 = __builtin_amdgcn_permlane32_swap(pk[4 * k], pk[4 * k + 2], false, false);
        auto r1 = __builtin_amdgcn_permlane32_swap(pk[4 * k + 1], pk[4 * k + 3], false, false);
        *(u32x4*)(out + (size_t)row * DM + col0 + p * 32 + 16 * k + 8 * h) = (u32x4){r0[0], r1[0], r0[1], r1[1]};
      }
    }
  }
  STAMP(3, 3);
#undef G2_RD
#undef G2_MM
#undef G2_SCHED
#undef G2_BAR
}

__global__ __launch_bounds__(512, 2) void tail_kernel(const u16* __restrict__ x, const u16* __restrict__ b2, char* __restrict__ ws, u16* __restrict__ out) {
  extern __shared__ __attribute__((aligned(16))) char smem[];
  const int l = threadIdx.x & 63, w = rfl(threadIdx.x >> 6);
  attn_phase(ws, smem, w, l);
  gemm2_phase(x, b2, ws, out, smem, w, l);
}

extern "C" void kernel_launch(void* const* d_in, const int* in_sizes, int n_in, void* d_out, int out_size, void* d_ws,
                              size_t ws_size, hipStream_t stream) {
  const u16* x = (const u16*)d_in[0];
  const u16* wq = (const u16*)d_in[1];
  const u16* wk = (const u16*)d_in[2];
  const u16* wv = (const u16*)d_in[3];
  const u16* wo = (const u16*)d_in[4];
  const u16* w1 = (const u16*)d_in[6];
  const u16* b1 = (const u16*)d_in[7];
  const u16* w2 = (const u16*)d_in[8];
  const u16* b2 = (const u16*)d_in[9];
  char* ws = (char*)d_ws;
  (void)hipFuncSetAttribute((const void*)gemm1_kernel, hipFuncAttributeMaxDynamicSharedMemorySize, G1_LDS);
  ln_kernel<<<256, 256, 0, stream>>>(x, ws);
  gemm1_kernel<<<256, 512, G1_LDS, stream>>>(wq, wk, wv, w1, b1, w2, wo, ws);
  (void)hipFuncSetAttribute((const void*)tail_kernel, hipFuncAttributeMaxDynamicSharedMemorySize, K3_LDS);
  tail_kernel<<<256, 512, K3_LDS, stream>>>(x, b2, ws, (u16*)d_out);
}
```

```cpp
#include <hip/hip_runtime.h>
#include <hip/hip_bf16.h>

typedef short bf16x8 __attribute__((ext_vector_type(8)));
typedef short s16x4 __attribute__((ext_vector_type(4)));
typedef float f32x16 __attribute__((ext_vector_type(16)));
typedef float f32x4 __attribute__((ext_vector_type(4)));
typedef float f32x2 __attribute__((ext_vector_type(2)));
typedef __bf16 bf16x2_t __attribute__((ext_vector_type(2)));
typedef unsigned u32x4 __attribute__((ext_vector_type(4)));
typedef unsigned u32x2 __attribute__((ext_vector_type(2)));
typedef unsigned short u16;
#define LAS __attribute__((address_space(3)))
#define DI __device__ __forceinline__
#define SBAR() __builtin_amdgcn_sched_barrier(0)
#define PIN(x) asm volatile("" : "+v"(x))
#define MFMA32(a, b, c) __builtin_amdgcn_mfma_f32_32x32x16_bf16((a), (b), (c), 0, 0, 0)
#define MFMA16(a, b, c) __builtin_amdgcn_mfma_f32_16x16x32_bf16((a), (b), (c), 0, 0, 0)

constexpr int SEQ = 2048, DM = 1024, NH = 32, FF = 3584;
constexpr size_t KT = 262144;
constexpr size_t WS_XN = 0;
constexpr size_t WS_Q = 4u << 20;
constexpr size_t WS_KV = 8u << 20;
constexpr size_t WS_O = 9u << 20;
constexpr size_t WS_H = 13u << 20;
constexpr size_t WS_WT = 28u << 20;
constexpr size_t WS_PART = 38u << 20;
constexpr size_t WS_FLAG = 47u << 20;
constexpr int FLAG_O = 256 * 32;
#define FUSE 1

#define STAMP(kid, idx) do {} while (0)
DI unsigned rfl(unsigned x) { return __builtin_amdgcn_readfirstlane(x); }
DI unsigned cvtpk(float lo, float hi) {
  f32x2 v = {lo, hi};
  bf16x2_t b = __builtin_convertvector(v, bf16x2_t);
  return __builtin_bit_cast(unsigned, b);
}
DI float fadd_s(float a, float b) { float r; asm("v_add_f32_e32 %0, %1, %2" : "=v"(r) : "v"(a), "v"(b)); return r; }
DI float bf_lo(unsigned u) { return __uint_as_float(u << 16); }
DI float bf_hi(unsigned u) { return __uint_as_float(u & 0xffff0000u); }
DI float bf_round(float x) { return bf_lo(cvtpk(x, x)); }

template <int IMM>
DI void glds16(unsigned voff, const void* sbase, unsigned ldsaddr) {
  asm volatile("s_mov_b32 m0, %2\n\ts_nop 0\n\tglobal_load_lds_dwordx4 %0, %1 offset:%3"
               :: "v"(voff), "s"(sbase), "s"(ldsaddr), "i"(IMM) : "memory");
}
template <int IMM>
DI void glds16_dev(unsigned voff, const void* sbase, unsigned ldsaddr) {
  asm volatile("s_mov_b32 m0, %2\n\ts_nop 0\n\tglobal_load_lds_dwordx4 %0, %1 offset:%3 sc1"
               :: "v"(voff), "s"(sbase), "s"(ldsaddr), "i"(IMM) : "memory");
}
DI void store16_dev(void* p, u32x4 v) {
  asm volatile("global_store_dwordx4 %0, %1, off sc1\n\ts_nop 1" :: "v"(p), "v"(v) : "memory");
}
DI void l2_touch(const void* p) { asm volatile("global_load_dword v255, %0, off" :: "v"(p) : "v255", "memory"); }
#define PFH 4
#define PFD 0
typedef __attribute__((address_space(1))) unsigned gu32;
#define RLX_AGENT __ATOMIC_RELAXED, __HIP_MEMORY_SCOPE_AGENT
DI void wait_ge(const unsigned* word, unsigned want) {
  for (unsigned spins = 0; spins < (1u << 22); ++spins) {
    if (rfl(__hip_atomic_load((gu32*)word, RLX_AGENT)) >= want) break;
    __builtin_amdgcn_s_sleep(2);
  }
  __builtin_amdgcn_fence(__ATOMIC_ACQUIRE, "wavefront");
}
DI float relu_s(float a) { float r; asm("v_max_f32_e32 %0, 0, %1" : "=v"(r) : "v"(a)); return r; }
DI bf16x8 lds_b128(const LAS char* p) { return *(const LAS bf16x8*)p; }
DI s16x4 lds_tr(const LAS char* p) {
  return __builtin_amdgcn_ds_read_tr16_b64_v4i16((LAS s16x4*)p);
}
DI f32x16 sel16(bool c, const f32x16& a, const f32x16& b) {
  f32x16 r;
#pragma unroll
  for (int i = 0; i < 16; ++i) r[i] = c ? a[i] : b[i];
  return r;
}
DI bf16x8 cat8(s16x4 lo, s16x4 hi) { return __builtin_shufflevector(lo, hi, 0, 1, 2, 3, 4, 5, 6, 7); }

__global__ __launch_bounds__(256) void ln_kernel(const u16* __restrict__ x, char* __restrict__ ws) {
  __shared__ float red[2][4][8];
  const int tid = threadIdx.x, w = tid >> 6, l = tid & 63;
  const int row = blockIdx.x * 8 + (l & 7);
  STAMP(0, 0);
  if (tid < 34) ((unsigned*)(ws + WS_FLAG))[blockIdx.x * 34 + tid] = 0u;
  u32x4 v[4];
#pragma unroll
  for (int i = 0; i < 4; ++i) {
    const int c = (i * 4 + w) * 8 + (l >> 3);
    v[i] = *(const u32x4*)(x + (size_t)row * DM + c * 8);
  }
  float s = 0.f;
#pragma unroll
  for (int i = 0; i < 4; ++i)
#pragma unroll
    for (int j = 0; j < 4; ++j) s += bf_lo(v[i][j]) + bf_hi(v[i][j]);
  s += __shfl_xor(s, 8); s += __shfl_xor(s, 16); s += __shfl_xor(s, 32);
  if (l < 8) red[0][w][l] = s;
  __syncthreads();
  const float mean = (red[0][0][l & 7] + red[0][1][l & 7] + red[0][2][l & 7] + red[0][3][l & 7]) * (1.0f / DM);
  float q = 0.f;
#pragma unroll
  for (int i = 0; i < 4; ++i)
#pragma unroll
    for (int j = 0; j < 4; ++j) {
      const float a = bf_lo(v[i][j]) - mean, b = bf_hi(v[i][j]) - mean;
      q += a * a + b * b;
    }
  q += __shfl_xor(q, 8); q += __shfl_xor(q, 16); q += __shfl_xor(q, 32);
  if (l < 8) red[1][w][l] = q;
  __syncthreads();
  const float var = (red[1][0][l & 7] + red[1][1][l & 7] + red[1][2][l & 7] + red[1][3][l & 7]) * (1.0f / DM);
  const float mb = bf_round(mean);
  const float den = bf_round(__builtin_sqrtf(bf_round(bf_round(var) + 1e-5f)));
  const float rden = 1.0f / den;
  char* dst = ws + WS_XN + (size_t)(row >> 5) * 4096 + (row & 31) * 16;
#pragma unroll
  for (int i = 0; i < 4; ++i) {
    const int c = (i * 4 + w) * 8 + (l >> 3);
    u32x4 o;
#pragma unroll
    for (int j = 0; j < 4; ++j) {
      const unsigned d = cvtpk(bf_lo(v[i][j]) - mb, bf_hi(v[i][j]) - mb);
      o[j] = cvtpk(bf_lo(d) * rden, bf_hi(d) * rden);
    }
    *(u32x4*)(dst + (size_t)(c >> 3) * KT + (c & 7) * 512) = o;
  }
  STAMP(0, 3);
}

constexpr int G1_STAGE = 53248, G1_BOFF = 32768, G1_LDS = 3 * G1_STAGE;

__global__ __launch_bounds__(512, 2) void gemm1_kernel(const u16* __restrict__ wq, const u16* __restrict__ wk,
                                                       const u16* __restrict__ wv, const u16* __restrict__ w1,
                                                       const u16* __restrict__ b1, const u16* __restrict__ w2,
                                                       const u16* __restrict__ wo, char* __restrict__ ws) {
  extern __shared__ __attribute__((aligned(16))) char smem[];
  const int tid = threadIdx.x, l = tid & 63;
  const int w = rfl(tid >> 6);
  if (blockIdx.x >= 240) {
    const int gw = (blockIdx.x - 240) * 8 + w;
    const unsigned lw0 = (unsigned)(size_t)(LAS char*)smem + w * 16384;
    const LAS char* lr = (const LAS char*)smem + w * 16384 + (4 * (l >> 5) + ((l & 15) >> 2)) * 64 + ((l >> 4) & 1) * 32 + (l & 3) * 8;
    const unsigned voff = (unsigned)((l >> 2) * DM * 2 + (l & 3) * 16);
    auto ld = [&](int u) {
      const int un = gw * 18 + u, ks = un >> 3, nt = un & 7;
      const char* src = (ks < 224) ? (const char*)(w2 + (size_t)ks * 16 * DM + nt * 128) : (const char*)(wo + (size_t)(ks - 224) * 16 * DM + nt * 128);
      const unsigned d = lw0 + (u & 3) * 4096;
      glds16<0>(voff, src, d); glds16<0>(voff, src + 64, d + 1024); glds16<0>(voff, src + 128, d + 2048); glds16<0>(voff, src + 192, d + 3072);
    };
    ld(0); ld(1); ld(2); ld(3);
#pragma unroll
    for (int u = 0; u < 18; ++u) {
      constexpr int dummy = 0; (void)dummy;
      if (u == 0 || u == 17) asm volatile("s_waitcnt vmcnt(12)" ::: "memory");
      else if (u == 1 || u == 16) asm volatile("s_waitcnt vmcnt(16)" ::: "memory");
      else if (u == 2 || u == 15) asm volatile("s_waitcnt vmcnt(20)" ::: "memory");
      else asm volatile("s_waitcnt vmcnt(24)" ::: "memory");
      const int un = gw * 18 + u, ks = un >> 3, nt = un & 7;
      char* dst = ws + WS_WT + (size_t)((ks >> 2) * 32 + nt * 4 + (ks & 3)) * 4096 + l * 16;
      bf16x8 f[4];
#pragma unroll
      for (int p = 0; p < 4; ++p) f[p] = cat8(lds_tr(lr + (u & 3) * 4096 + p * 1024), lds_tr(lr + (u & 3) * 4096 + p * 1024 + 512));
#pragma unroll
      for (int p = 0; p < 4; ++p) *(bf16x8*)(dst + p * 1024) = f[p];
      if (u + 4 < 18) ld(u + 4);
    }
    return;
  }
  const int lin = (blockIdx.x & 7) * 30 + (blockIdx.x >> 3);
  const int mh = lin / 120, rem = lin % 120, nt = rem >> 2, mtile = mh * 4 + (rem & 3);
  const unsigned lds0 = (unsigned)(size_t)(LAS char*)smem;

  if (w >= 4) {
    const int lw = w - 4;
    const char* abase = ws + WS_XN + (size_t)(mtile * 8 + lw * 2) * 4096;
    const unsigned avoff = l * 16;
    const char* bbase[5];
    unsigned bvoff[5], bstep[5];
#pragma unroll
    for (int p = 0; p < 5; ++p) {
      const int cb = nt * 5 + p;
      const u16* src; int ld;
      if (cb < 32) { src = wq + cb * 32; ld = DM; }
      else if (cb == 33) { src = wv; ld = 32; }
      else if (cb < 35 || cb >= 147) { src = wk; ld = 32; }
      else { src = w1 + (cb - 35) * 32; ld = FF; }
      bbase[p] = (const char*)src;
      bvoff[p] = (unsigned)((lw * 16 + (l >> 2)) * ld * 2 + (l & 3) * 16);
      bstep[p] = (unsigned)(64 * ld * 2);
    }
    auto issue = [&](int kt, int slot) {
      const unsigned sa = lds0 + slot * G1_STAGE + lw * 8192;
      const char* a0 = abase + (size_t)kt * KT;
      glds16<0>(avoff, a0, sa); glds16<1024>(avoff, a0, sa); glds16<2048>(avoff, a0, sa); glds16<3072>(avoff, a0, sa);
      const char* a1 = a0 + 4096;
      glds16<0>(avoff, a1, sa + 4096); glds16<1024>(avoff, a1, sa + 4096); glds16<2048>(avoff, a1, sa + 4096); glds16<3072>(avoff, a1, sa + 4096);
      const unsigned sb = lds0 + slot * G1_STAGE + G1_BOFF + lw * 1024;
#pragma unroll
      for (int p = 0; p < 5; ++p) glds16<0>(bvoff[p], bbase[p] + (size_t)kt * bstep[p], sb + p * 4096);
    };
    issue(0, 0); issue(1, 1); issue(2, 2);
    asm volatile("s_waitcnt vmcnt(26)\n\ts_barrier" ::: "memory");
    int slot = 0;
    for (int i = 0; i < 13; ++i) {
      asm volatile("s_waitcnt vmcnt(13)\n\ts_barrier" ::: "memory");
      issue(i + 3, slot);
      slot = (slot == 2) ? 0 : slot + 1;
    }
    asm volatile("s_waitcnt vmcnt(13)\n\ts_barrier" ::: "memory");
    asm volatile("s_waitcnt vmcnt(0)\n\ts_barrier" ::: "memory");
    return;
  }

  STAMP(1, 0);
  const int h = l >> 5;
  f32x16 acc[2][5];
  if (nt >= 7) {
#pragma unroll
    for (int p = 0; p < 5; ++p) {
      int cb = nt * 5 + p - 35; cb = cb > 111 ? 111 : cb;
#pragma unroll
      for (int i = 0; i < 4; ++i) {
        const u32x2 bv = *(const u32x2*)(b1 + cb * 32 + 8 * i + 4 * h);
        acc[0][p][4 * i + 0] = bf_lo(bv[0]); acc[0][p][4 * i + 1] = bf_hi(bv[0]);
        acc[0][p][4 * i + 2] = bf_lo(bv[1]); acc[0][p][4 * i + 3] = bf_hi(bv[1]);
      }
      acc[1][p] = acc[0][p];
    }
  } else {
#pragma unroll
    for (int p = 0; p < 5; ++p) { acc[0][p] = f32x16{}; acc[1][p] = f32x16{}; }
  }
  const LAS char* lbase = (const LAS char*)smem;
  const int aoff = w * 8192 + l * 16;
  const int boff = G1_BOFF + (8 * h + ((l & 15) >> 2)) * 64 + ((l >> 4) & 1) * 32 + (l & 3) * 8;
  bf16x8 fa0[2], fb0[5], fa1[2], fb1[5];
#define G1_RD(FA, FB, base, ks) do { \
    FA[0] = lds_b128((base) + aoff + (ks) * 1024); FA[1] = lds_b128((base) + aoff + 4096 + (ks) * 1024); \
    _Pragma("unroll") for (int p_ = 0; p_ < 5; ++p_) \
      FB[p_] = cat8(lds_tr((base) + boff + p_ * 4096 + (ks) * 1024), lds_tr((base) + boff + p_ * 4096 + (ks) * 1024 + 256)); \
  } while (0)
#define G1_MM(FA, FB) do { \
    _Pragma("unroll") for (int p_ = 0; p_ < 5; ++p_) { \
      acc[0][p_] = MFMA32(FB[p_], FA[0], acc[0][p_]); acc[1][p_] = MFMA32(FB[p_], FA[1], acc[1][p_]); } \
  } while (0)
#define G1_SCHED() do { \
    _Pragma("unroll") for (int q_ = 0; q_ < 6; ++q_) { \
      __builtin_amdgcn_sched_group_barrier(0x008, 1, 0); __builtin_amdgcn_sched_group_barrier(0x100, 2, 0); } \
    __builtin_amdgcn_sched_group_barrier(0x008, 4, 0); SBAR(); \
  } while (0)

  asm volatile("s_barrier" ::: "memory");
  STAMP(1, 1);
  G1_RD(fa0, fb0, lbase, 0); SBAR();
  G1_RD(fa1, fb1, lbase, 1); G1_MM(fa0, fb0); G1_SCHED();
  G1_RD(fa0, fb0, lbase, 2); G1_MM(fa1, fb1); G1_SCHED();
  G1_RD(fa1, fb1, lbase, 3); G1_MM(fa0, fb0); G1_SCHED();
  int slot = 1;
  for (int i = 0; i < 15; ++i) {
    const LAS char* nb = lbase + slot * G1_STAGE;
    asm volatile("s_waitcnt lgkmcnt(0)\n\ts_barrier" ::: "memory"); SBAR();
    G1_RD(fa0, fb0, nb, 0); G1_MM(fa1, fb1); G1_SCHED();
    G1_RD(fa1, fb1, nb, 1); G1_MM(fa0, fb0); G1_SCHED();
    G1_RD(fa0, fb0, nb, 2); G1_MM(fa1, fb1); G1_SCHED();
    G1_RD(fa1, fb1, nb, 3); G1_MM(fa0, fb0); G1_SCHED();
    slot = (slot == 2) ? 0 : slot + 1;
  }
  G1_MM(fa1, fb1);
  asm volatile("s_nop 15\n\ts_nop 3" ::: "memory"); SBAR();
  STAMP(1, 2);

  if (nt >= 7) {
#pragma unroll
    for (int p = 0; p < 5; ++p) {
      const int cb = nt * 5 + p - 35;
      if (cb < 112) {
#pragma unroll
        for (int m = 0; m < 2; ++m) {
          char* dst = ws + WS_H + (size_t)(cb >> 1) * KT + (size_t)(mtile * 8 + w * 2 + m) * 4096 + (cb & 1) * 2048 + l * 16;
#pragma unroll
          for (int s = 0; s < 2; ++s) {
            u32x4 o;
#pragma unroll
            for (int j = 0; j < 4; ++j) {
              const float a = relu_s(acc[m][p][8 * s + 2 * j]), b = relu_s(acc[m][p][8 * s + 2 * j + 1]);
              o[j] = cvtpk(a * a, b * b);
            }
            *(u32x4*)(dst + s * 1024) = o;
          }
        }
      }
    }
  } else {
    const float qs = 0.17677669529663687f * 1.4426950408889634f;
#pragma unroll
    for (int p = 0; p < 5; ++p) {
      const int cb = nt * 5 + p;
#pragma unroll
      for (int m = 0; m < 2; ++m) {
        const int tt = mtile * 8 + w * 2 + m;
        if (cb <= 32) {
          char* dst = (cb < 32) ? ws + WS_Q + (size_t)(tt * 32 + cb) * 2048 + l * 16 : ws + WS_KV + (size_t)tt * 4096 + l * 16;
          const float sc = (cb < 32) ? qs : 1.0f;
#pragma unroll
          for (int s = 0; s < 2; ++s) {
            u32x4 o;
#pragma unroll
            for (int j = 0; j < 4; ++j) o[j] = cvtpk(acc[m][p][8 * s + 2 * j] * sc, acc[m][p][8 * s + 2 * j + 1] * sc);
            *(u32x4*)(dst + s * 1024) = o;
          }
        } else if (cb == 33) {
          const int key = l & 31;
          char* dst = ws + WS_KV + (size_t)tt * 4096 + 2048 + (key >> 4) * 1024 + ((key >> 2) & 1) * 512 + (((key >> 3) & 1) * 4 + (key & 3)) * 2;
#pragma unroll
          for (int r = 0; r < 16; ++r) {
            const int d = (r & 3) + 8 * (r >> 2) + 4 * h;
            *(u16*)(dst + d * 16) = (u16)cvtpk(acc[m][p][r], 0.f);
          }
        }
      }
    }
  }
  STAMP(1, 3);
#undef G1_RD
#undef G1_MM
#undef G1_SCHED
}

constexpr int AT_CHUNK = 4, AT_NCH = 8;
constexpr int AT_BUF = 2 * AT_CHUNK * 4096;
constexpr int AT_BASE = 65536;
constexpr int K3_LDS = 163840;

DI float xhalf_max(float m) {
  auto rr = __builtin_amdgcn_permlane32_swap(__float_as_uint(m), __float_as_uint(m), false, false);
  return __builtin_fmaxf(__uint_as_float(rr[0]), __uint_as_float(rr[1]));
}
DI float xhalf_sum(float m) {
  auto rr = __builtin_amdgcn_permlane32_swap(__float_as_uint(m), __float_as_uint(m), false, false);
  return __uint_as_float(rr[0]) + __uint_as_float(rr[1]);
}
DI float max16(const f32x16& s) {
  float a = __builtin_fmaxf(s[0], s[1]);
#pragma unroll
  for (int r = 2; r < 16; ++r) a = __builtin_fmaxf(a, s[r]);
  return a;
}
DI bf16x8 pack8(const f32x16& p, int s) {
  u32x4 o;
#pragma unroll
  for (int j = 0; j < 4; ++j) o[j] = cvtpk(p[8 * s + 2 * j], p[8 * s + 2 * j + 1]);
  return __builtin_bit_cast(bf16x8, o);
}

DI void attn_fallback(const char* __restrict__ kv, bf16x8 q0, bf16x8 q1, int l, f32x16& o_out, float& l_out) {
  f32x16 o = {};
  float m = -INFINITY, lsum = 0.f;
  for (int t = 0; t < 64; ++t) {
    const char* p = kv + (size_t)t * 4096 + l * 16;
    const bf16x8 k0 = *(const bf16x8*)p, k1 = *(const bf16x8*)(p + 1024), v0 = *(const bf16x8*)(p + 2048), v1 = *(const bf16x8*)(p + 3072);
    f32x16 s = {};
    s = MFMA32(k0, q0, s); s = MFMA32(k1, q1, s);
    const float mn = __builtin_fmaxf(m, xhalf_max(max16(s)));
    const float ms = (mn == -INFINITY) ? 0.f : mn;
    const float alpha = __builtin_amdgcn_exp2f(m - ms);
    m = mn;
    float ps = 0.f;
#pragma unroll
    for (int r = 0; r < 16; ++r) { s[r] = __builtin_amdgcn_exp2f(s[r] - ms); ps += s[r]; }
    lsum = lsum * alpha + ps;
#pragma unroll
    for (int r = 0; r < 16; ++r) o[r] *= alpha;
    o = MFMA32(v0, pack8(s, 0), o); o = MFMA32(v1, pack8(s, 1), o);
  }
  o_out = o;
  l_out = xhalf_sum(lsum);
}

constexpr int G2_SLOT = 32768, G2_BOFF = 16384, G2_PART = 131072;
#define BSTR 1
struct G2Ld {
  const char* hb; const char* ob; const char* wb; const char* wb2; unsigned lds; unsigned voff, voffb; int kh;
  DI void init(const char* ws, const u16* w2, const u16* wo, unsigned lds0, int lw, int l) {
    const int xcd = blockIdx.x & 7, idx = blockIdx.x >> 3;
    kh = xcd & 1;
    const int mt = ((xcd >> 1) & 1) * 8 + (idx & 7), nt = (xcd >> 2) * 4 + (idx >> 3);
    hb = ws + WS_H + (size_t)(mt * 4 + lw) * 4096 + (size_t)kh * (28 * KT);
    ob = ws + WS_O + (size_t)(mt * 4 + lw) * 4096;
    wb = (const char*)(w2 + (size_t)(kh * 1792 + lw * 16) * DM + nt * 128);
    wb2 = (const char*)(wo + (size_t)(lw * 16) * DM + nt * 128 + kh * 64);
    voffb = (unsigned)((l >> 2) * DM * 2 + (l & 3) * 16);
    lds = lds0 + lw * 4096; voff = l * 16;
  }
  DI void ffn(int s) const {
    const unsigned d = lds + (s & 3) * G2_SLOT;
    const char* a = hb + (size_t)s * KT;
    glds16<0>(voff, a, d); glds16<1024>(voff, a, d); glds16<2048>(voff, a, d); glds16<3072>(voff, a, d);
    const char* b = wb + (size_t)s * 131072;
    glds16<0>(voffb, b, d + G2_BOFF); glds16<0>(voffb, b + 64, d + G2_BOFF + 1024); glds16<0>(voffb, b + 128, d + G2_BOFF + 2048); glds16<0>(voffb, b + 192, d + G2_BOFF + 3072);
  }
  DI void wo(int s) const {
    const unsigned d = lds + (s & 3) * G2_SLOT;
    const char* a = ob + (size_t)(s - 28) * KT;
    glds16_dev<0>(voff, a, d); glds16_dev<1024>(voff, a, d); glds16_dev<2048>(voff, a, d); glds16_dev<3072>(voff, a, d);
    const char* b = wb2 + (size_t)(s - 28) * 131072;
    glds16<0>(voffb, b, d + G2_BOFF); glds16<0>(voffb, b + 64, d + G2_BOFF + 1024);
  }
};

DI void attn_phase(char* __restrict__ ws, const u16* w2, const u16* wo, char* smem, const int w, const int l) {
  const int hp = w & 3, kh = w >> 2;
  const int tt = blockIdx.x >> 2, hg = blockIdx.x & 3;
  const int head0 = hg * 8 + hp * 2;
  const unsigned lds0 = (unsigned)(size_t)(LAS char*)smem;
  const char* kvg = ws + WS_KV;
  const unsigned voff = l * 16;
  const char* ksrc = kvg + (size_t)(kh * 32 + hp) * 4096;
  const unsigned kdst = lds0 + AT_BASE + w * 4096;
#define AT_LD(c, slot, piece) glds16<(piece) * 1024>(voff, ksrc + (size_t)(c) * (AT_CHUNK * 4096), kdst + (slot))
  STAMP(2, 0);
  AT_LD(0, 0, 0); AT_LD(0, 0, 1); AT_LD(0, 0, 2); AT_LD(0, 0, 3);
  const char* qp = ws + WS_Q + (size_t)(tt * 32 + head0) * 2048 + l * 16;
  bf16x8 qa0 = *(const bf16x8*)qp, qa1 = *(const bf16x8*)(qp + 1024), qb0 = *(const bf16x8*)(qp + 2048), qb1 = *(const bf16x8*)(qp + 3072);
  PIN(qa0); PIN(qa1); PIN(qb0); PIN(qb1);
  asm volatile("s_waitcnt vmcnt(0)\n\ts_barrier" ::: "memory");
  AT_LD(1, AT_BUF, 0); AT_LD(1, AT_BUF, 1); AT_LD(1, AT_BUF, 2); AT_LD(1, AT_BUF, 3);
  AT_LD(2, 2 * AT_BUF, 0);
  STAMP(2, 1);

  const LAS char* lb = (const LAS char*)smem + AT_BASE + l * 16;
  f32x16 nma, nmb;
  {
    const bf16x8 k0 = lds_b128(lb), k1 = lds_b128(lb + 1024);
    f32x16 sa = {}, sb = {};
    sa = MFMA32(k0, qa0, sa); sa = MFMA32(k1, qa1, sa);
    sb = MFMA32(k0, qb0, sb); sb = MFMA32(k1, qb1, sb);
    const float ma = -xhalf_max(max16(sa)), mb = -xhalf_max(max16(sb));
#pragma unroll
    for (int r = 0; r < 16; ++r) { nma[r] = ma; nmb[r] = mb; }
    PIN(nma); PIN(nmb);
  }
  bf16x8 sel;
  {
    const short one = ((l & 15) == ((l >> 4) & 1)) ? (short)0x3f80 : (short)0;
#pragma unroll
    for (int j = 0; j < 8; ++j) sel[j] = one;
  }
  f32x16 oa = {}, ob = {}, sa, sb;
  f32x4 la = {}, lbs = {};
  u32x4 pa0, pa1, pb0 = {}, pb1 = {};
  bf16x8 kx0, kx1, vx0, vx1, ky0, ky1, vy0, vy1;
  const LAS char* kvp = lb + kh * (AT_CHUNK * 4096);
  kx0 = lds_b128(kvp); kx1 = lds_b128(kvp + 1024); vx0 = lds_b128(kvp + 2048); vx1 = lds_b128(kvp + 3072);
  vy0 = vx0; vy1 = vx1;
  sa = MFMA32(kx0, qa0, nma); sa = MFMA32(kx1, qa1, sa);
#define EX(S, i) S[i] = __builtin_amdgcn_exp2f(S[i])
#define CV(P, j, S, i) P[j] = cvtpk(S[i], S[(i) + 1])
#define B8(x) __builtin_bit_cast(bf16x8, x)
#define LSUM(L, P) L = MFMA16(sel, B8(P), L)
#define PV(O, V, P) O = MFMA32(V, B8(P), O)
#define HALF(S, P0, P1, M0, M1, M2, M3, M4, M5, LD0, LD1) do { \
    M0; SBAR(); EX(S, 0); EX(S, 1); EX(S, 2); EX(S, 3); PIN(S); SBAR(); \
    M1; SBAR(); CV(P0, 0, S, 0); CV(P0, 1, S, 2); EX(S, 4); EX(S, 5); EX(S, 6); PIN(S); PIN(P0); SBAR(); \
    M2; SBAR(); LD0; EX(S, 7); CV(P0, 2, S, 4); EX(S, 8); CV(P0, 3, S, 6); EX(S, 9); PIN(S); PIN(P0); SBAR(); \
    M3; SBAR(); EX(S, 10); EX(S, 11); PIN(S); SBAR(); \
    M4; SBAR(); LD1; CV(P1, 0, S, 8); CV(P1, 1, S, 10); EX(S, 12); EX(S, 13); EX(S, 14); PIN(S); PIN(P1); SBAR(); \
    M5; SBAR(); EX(S, 15); CV(P1, 2, S, 12); CV(P1, 3, S, 14); PIN(P1); SBAR(); \
  } while (0)
#define STEP(KC0, KC1, VC0, VC1, KN0, KN1, VP0, VP1, NP, DMA) do { \
    HALF(sa, pa0, pa1, sb = MFMA32(KC0, qb0, nmb), sb = MFMA32(KC1, qb1, sb), PV(ob, VP0, pb0), LSUM(lbs, pb0), \
         PV(ob, VP1, pb1), LSUM(lbs, pb1), KN0 = lds_b128(NP); KN1 = lds_b128((NP) + 1024), (void)0); \
    HALF(sb, pb0, pb1, sa = MFMA32(KN0, qa0, nma), sa = MFMA32(KN1, qa1, sa), PV(oa, VC0, pa0), LSUM(la, pa0), \
         PV(oa, VC1, pa1), LSUM(la, pa1), VP0 = lds_b128((NP) + 2048); VP1 = lds_b128((NP) + 3072), DMA); \
  } while (0)
  int s0 = 0, s1 = AT_BUF, s2 = 2 * AT_BUF;
  G2Ld g2; g2.init(ws, w2, wo, lds0, w & 3, l);
  int extra = 0;
  const char *pf0, *pf1, *pf2;
  {
    const int xcd = blockIdx.x & 7, idx = blockIdx.x >> 3, gk = xcd & 1;
    const int mt = ((xcd >> 1) & 1) * 8 + (idx & 7), nt = (xcd >> 2) * 4 + (idx >> 3);
    pf1 = (const char*)(wo + (size_t)((mt & 7) * 128 + l) * DM + nt * 128 + gk * 64);
    pf0 = (w < 7) ? (const char*)(w2 + (size_t)(gk * 1792 + (w * 8 + (mt & 7)) * 32 + (l >> 1)) * DM + nt * 128 + (l & 1) * 64) : pf1 + 64 * DM * 2;
    pf2 = ws + WS_H + (size_t)(gk * 28 + 2 + w * 2 + (l >> 5)) * KT + (size_t)(mt * 4 + (nt & 3)) * 4096 + (l & 31) * 128;
  }
  for (int c = 0; c < AT_NCH; ++c) {
    const LAS char* cb = kvp + s0;
    const LAS char* nb = kvp + s1;
    const int c2 = c + 2 < AT_NCH ? c + 2 : AT_NCH - 1, c3 = c + 3 < AT_NCH ? c + 3 : AT_NCH - 1;
    STEP(kx0, kx1, vx0, vx1, ky0, ky1, vy0, vy1, cb + 4096, AT_LD(c2, s2, 1));
    STEP(ky0, ky1, vy0, vy1, kx0, kx1, vx0, vx1, cb + 2 * 4096, AT_LD(c2, s2, 2));
    STEP(kx0, kx1, vx0, vx1, ky0, ky1, vy0, vy1, cb + 3 * 4096, AT_LD(c2, s2, 3));
    if (extra == 0) asm volatile("s_waitcnt vmcnt(4) lgkmcnt(0)\n\ts_barrier" ::: "memory");
    else if (extra == 1) asm volatile("s_waitcnt vmcnt(5) lgkmcnt(0)\n\ts_barrier" ::: "memory");
    else asm volatile("s_waitcnt vmcnt(12) lgkmcnt(0)\n\ts_barrier" ::: "memory");
    extra = 0;
    if (c == (w >> 1)) { l2_touch(pf0); extra = 1; }
    if (c == 4 && w == 0) { l2_touch(pf1); extra = 1; }
    if (c == 5 && w < PFH) { l2_touch(pf2); extra = 1; }
    if (w >= 4 && (c == 5 || c == 6)) { g2.ffn(c - 5); extra = 8; }
    SBAR();
    STEP(ky0, ky1, vy0, vy1, kx0, kx1, vx0, vx1, nb, AT_LD(c3, s0, 0));
    const int t_ = s0; s0 = s1; s1 = s2; s2 = t_;
  }
  PV(ob, vy0, pb0); LSUM(lbs, pb0);
  PV(ob, vy1, pb1); LSUM(lbs, pb1);
#undef EX
#undef CV
#undef B8
#undef HALF
#undef STEP
#undef LSUM
#undef PV
#undef AT_LD
  STAMP(2, 2);
  asm volatile("s_waitcnt vmcnt(0) lgkmcnt(0)\n\ts_barrier" ::: "memory");
  {
    LAS char* xw = (LAS char*)smem + AT_BASE + w * 8192;
    const f32x16 so = sel16(kh, oa, ob);
    const f32x4 sl = kh ? la : lbs;
#pragma unroll
    for (int i = 0; i < 4; ++i) *(LAS f32x4*)(xw + i * 1024 + l * 16) = (f32x4){so[4 * i], so[4 * i + 1], so[4 * i + 2], so[4 * i + 3]};
    const f32x4 ml = kh ? lbs : la;
    if (l < 16) *(LAS f32x4*)(xw + 4096 + l * 16) = (f32x4){sl[0], sl[1], ml[0], ml[1]};
  }
  asm volatile("s_waitcnt lgkmcnt(0)\n\ts_barrier" ::: "memory");
  f32x16 o = sel16(kh, ob, oa);
  float lsum;
  {
    const LAS char* xr = (const LAS char*)smem + AT_BASE + (w ^ 4) * 8192;
    const LAS char* xo = (const LAS char*)smem + AT_BASE + w * 8192;
#pragma unroll
    for (int i = 0; i < 4; ++i) {
      const f32x4 t = *(const LAS f32x4*)(xr + i * 1024 + l * 16);
      o[4 * i] += t[0]; o[4 * i + 1] += t[1]; o[4 * i + 2] += t[2]; o[4 * i + 3] += t[3];
    }
    const int qo = (l & 15) * 16 + ((l >> 4) & 1) * 4;
    lsum = *(const LAS float*)(xr + 4096 + qo) + *(const LAS float*)(xo + 4096 + 8 + qo);
  }
  bool ok = (lsum >= 9.5367431640625e-07f) && (lsum <= 1.8446744073709552e19f);
#pragma unroll
  for (int r = 0; r < 16; ++r) ok = ok && (__builtin_fabsf(o[r]) <= 3.0e38f);
  if (!__all(ok)) {
    attn_fallback(kvg, kh ? qb0 : qa0, kh ? qb1 : qa1, l, o, lsum);
  }
  const float rl = 1.0f / lsum;
  char* dst = ws + WS_O + (size_t)(head0 >> 1) * KT + (size_t)tt * 4096 + kh * 2048 + l * 16;
#pragma unroll
  for (int s = 0; s < 2; ++s) {
    u32x4 ov;
#pragma unroll
    for (int j = 0; j < 4; ++j) ov[j] = cvtpk(o[8 * s + 2 * j] * rl, o[8 * s + 2 * j + 1] * rl);
    store16_dev(dst + s * 1024, ov);
  }
  STAMP(2, 3);
}

DI void gemm2_phase(const u16* __restrict__ x, const u16* __restrict__ b2, const u16* w2, const u16* wo, char* __restrict__ ws,
                    u16* __restrict__ out, char* smem, const int w, const int l) {
  const int xcd = blockIdx.x & 7, idx = blockIdx.x >> 3;
  const int kh = xcd & 1, mt = ((xcd >> 1) & 1) * 8 + (idx & 7), nt = (xcd >> 2) * 4 + (idx >> 3);
  const unsigned lds0 = (unsigned)(size_t)(LAS char*)smem;
  unsigned* flags = (unsigned*)(ws + WS_FLAG);

  if (w >= 4) {
    G2Ld g; g.init(ws, w2, wo, lds0, w - 4, l);
    asm volatile("s_waitcnt lgkmcnt(0)\n\ts_barrier" ::: "memory");
    g.ffn(2); g.ffn(3);
    asm volatile("s_barrier" ::: "memory");
    g.ffn(4);
    asm volatile("s_waitcnt vmcnt(16)\n\ts_barrier" ::: "memory");
    if (l == 0) __hip_atomic_fetch_add((gu32*)(flags + FLAG_O + (blockIdx.x >> 4) * 32), 1u, RLX_AGENT);
    g.ffn(5);
    for (int i = 2; i < 24; ++i) {
      asm volatile("s_waitcnt vmcnt(16)\n\ts_barrier" ::: "memory");
      g.ffn(i + 4);
    }
    asm volatile("s_waitcnt vmcnt(16)\n\ts_barrier" ::: "memory");
    wait_ge(flags + FLAG_O + mt * 32, 128u);
    g.wo(28);
    for (int i = 25; i < 40; ++i) {
      asm volatile("s_waitcnt vmcnt(12)\n\ts_barrier" ::: "memory");
      g.wo(i + 4);
    }
    asm volatile("s_waitcnt vmcnt(12)\n\ts_barrier" ::: "memory");
    asm volatile("s_waitcnt vmcnt(6)\n\ts_barrier" ::: "memory");
    wait_ge(flags + blockIdx.x * 32, 2u);
    {
      const char* src = ws + WS_PART + (size_t)(blockIdx.x ^ 1) * 32768 + (w - 4) * 8192;
      const unsigned d = lds0 + G2_PART + (w - 4) * 8192;
      glds16_dev<0>(g.voff, src, d); glds16_dev<1024>(g.voff, src, d); glds16_dev<2048>(g.voff, src, d); glds16_dev<3072>(g.voff, src, d);
      glds16_dev<0>(g.voff, src + 4096, d + 4096); glds16_dev<1024>(g.voff, src + 4096, d + 4096); glds16_dev<2048>(g.voff, src + 4096, d + 4096); glds16_dev<3072>(g.voff, src + 4096, d + 4096);
    }
    asm volatile("s_waitcnt vmcnt(8)\n\ts_barrier" ::: "memory");
    asm volatile("s_waitcnt vmcnt(0)\n\ts_barrier" ::: "memory");
    return;
  }

  STAMP(3, 0);
  const int h = l >> 5, wm = w & 1, wn = w >> 1;
  const bool own = (wn == kh);
  f32x16 accg[2][2] = {}, accs[2][2] = {};
  const int col0 = nt * 128 + kh * 64;
  const int row = mt * 128 + wm * 64 + wn * 32 + (l & 31);
  u32x4 xv[4];
  u32x2 bv[8];
#pragma unroll
  for (int k = 0; k < 4; ++k) xv[k] = *(const u32x4*)(x + (size_t)row * DM + col0 + 16 * k + 8 * h);
#pragma unroll
  for (int k = 0; k < 8; ++k) bv[k] = *(const u32x2*)(b2 + col0 + 8 * k + 4 * h);
  const char* pfF = (l < 32) ? ws + WS_H + (size_t)(mt * 4 + (nt & 3)) * 4096 + (size_t)kh * (28 * KT) + l * 128
                             : ws + WS_WT + (size_t)(kh * 28) * 131072 + nt * 16384 + (mt & 7) * 2048 + (l & 15) * 128;
  const unsigned pfs = (l < 32) ? (unsigned)KT : 131072u;
  const char* pfW = (l < 32) ? ws + WS_O + (size_t)(mt * 4 + (nt & 3)) * 4096 + l * 128
                             : ws + WS_WT + (size_t)56 * 131072 + (nt * 4 + ((mt & 7) >> 1)) * 4096 + kh * 2048 + (mt & 1) * 1024 + (l & 7) * 128;
  auto pf = [&](int s_) { if (s_ < 28) l2_touch(pfF + (size_t)s_ * pfs); else if (s_ < 44) l2_touch(pfW + (size_t)(s_ - 28) * pfs); };
  const LAS char* lbase = (const LAS char*)smem + l * 16;
  const int btr = (4 * h + ((l & 15) >> 2)) * 64 + ((l >> 4) & 1) * 32 + (l & 3) * 8 - l * 16;
#define LDB(p_) cat8(lds_tr(p_), lds_tr((p_) + 512))
#define NRD 3
  const int aF = wm * 8192, bF = G2_BOFF + wn * 2048 + btr;
  const int aW = wm * 8192 + wn * 2048, bW = G2_BOFF + wn * 8192 + btr;
  bf16x8 fa0[2], fb0[2], fa1[2], fb1[2];
#define G2_RD(FA, FB, base, ao, bo, ks) do { \
    FA[0] = lds_b128((base) + (ao) + (ks) * 1024); FB[0] = LDB((base) + (bo) + (ks) * 4096); \
    FA[1] = lds_b128((base) + (ao) + 4096 + (ks) * 1024); FB[1] = LDB((base) + (bo) + (ks) * 4096 + 1024); \
  } while (0)
#define G2_MM(ACC, FA, FB) do { \
    ACC[0][0] = MFMA32(FB[0], FA[0], ACC[0][0]); ACC[1][0] = MFMA32(FB[0], FA[1], ACC[1][0]); \
    ACC[0][1] = MFMA32(FB[1], FA[0], ACC[0][1]); ACC[1][1] = MFMA32(FB[1], FA[1], ACC[1][1]); \
  } while (0)
#define G2_SCHED() do { \
    __builtin_amdgcn_sched_group_barrier(0x008, 1, 0); __builtin_amdgcn_sched_group_barrier(0x100, NRD, 0); \
    __builtin_amdgcn_sched_group_barrier(0x008, 1, 0); __builtin_amdgcn_sched_group_barrier(0x100, NRD, 0); \
    __builtin_amdgcn_sched_group_barrier(0x008, 2, 0); SBAR(); \
  } while (0)
#define G2_BAR() do { asm volatile("s_waitcnt lgkmcnt(0)\n\ts_barrier" ::: "memory"); SBAR(); } while (0)

  G2_BAR();
  STAMP(3, 1);
  G2_RD(fa0, fb0, lbase, aF, bF, 0); SBAR();
  G2_RD(fa1, fb1, lbase, aF, bF, 1); G2_MM(accg, fa0, fb0); G2_SCHED();
  G2_RD(fa0, fb0, lbase, aF, bF, 2); G2_MM(accg, fa1, fb1); G2_SCHED();
  G2_RD(fa1, fb1, lbase, aF, bF, 3); G2_MM(accg, fa0, fb0); G2_SCHED();
  for (int i = 0; i < 27; ++i) {
    const LAS char* nb = lbase + ((i + 1) & 3) * G2_SLOT;
    G2_BAR();
    G2_RD(fa0, fb0, nb, aF, bF, 0); G2_MM(accg, fa1, fb1); G2_SCHED();
    G2_RD(fa1, fb1, nb, aF, bF, 1); G2_MM(accg, fa0, fb0); G2_SCHED();
    G2_RD(fa0, fb0, nb, aF, bF, 2); G2_MM(accg, fa1, fb1); G2_SCHED();
    G2_RD(fa1, fb1, nb, aF, bF, 3); G2_MM(accg, fa0, fb0); G2_SCHED();
    if (i == 3) {
      asm volatile("s_waitcnt vmcnt(0)" ::: "memory");
      if (l == 0) __hip_atomic_fetch_add((gu32*)(flags + FLAG_O + (blockIdx.x >> 4) * 32), 1u, RLX_AGENT);
      SBAR();
    }
  }
  STAMP(3, 4);
  {
    G2_BAR();
    G2_RD(fa0, fb0, lbase, aW, bW, 0); G2_MM(accg, fa1, fb1); G2_SCHED();
    G2_RD(fa1, fb1, lbase, aW, bW, 1); G2_MM(accs, fa0, fb0); G2_SCHED();
  }
  if (!own) {
    char* dst = ws + WS_PART + (size_t)blockIdx.x * 32768 + wm * 16384 + l * 16;
    asm volatile("s_nop 7" ::: "memory");
#pragma unroll
    for (int m = 0; m < 2; ++m)
#pragma unroll
      for (int p = 0; p < 2; ++p)
#pragma unroll
        for (int i = 0; i < 4; ++i)
          store16_dev(dst + (m * 2 + p) * 4096 + i * 1024,
                      (u32x4){__float_as_uint(accg[m][p][4 * i]), __float_as_uint(accg[m][p][4 * i + 1]), __float_as_uint(accg[m][p][4 * i + 2]), __float_as_uint(accg[m][p][4 * i + 3])});
  }
  SBAR();
  STAMP(3, 5);
  for (int i = 28; i < 43; ++i) {
    if (i == 40) STAMP(3, 6);
    const LAS char* nb = lbase + ((i + 1) & 3) * G2_SLOT;
    G2_BAR();
    G2_RD(fa0, fb0, nb, aW, bW, 0); G2_MM(accs, fa1, fb1); G2_SCHED();
    G2_RD(fa1, fb1, nb, aW, bW, 1); G2_MM(accs, fa0, fb0); G2_SCHED();
    if (i == 33 && !own) {
      asm volatile("s_waitcnt vmcnt(0)" ::: "memory");
      if (l == 0) __hip_atomic_fetch_add((gu32*)(flags + (blockIdx.x ^ 1) * 32), 1u, RLX_AGENT);
      SBAR();
    }
  }
  G2_MM(accs, fa1, fb1);
  STAMP(3, 2);

  {
    LAS char* xw = (LAS char*)smem + w * 16384 + l * 16;
#pragma unroll
    for (int p = 0; p < 2; ++p) {
      const f32x16 s = sel16(wn, accs[0][p], accs[1][p]);
#pragma unroll
      for (int i = 0; i < 4; ++i) *(LAS f32x4*)(xw + p * 4096 + i * 1024) = (f32x4){s[4 * i], s[4 * i + 1], s[4 * i + 2], s[4 * i + 3]};
    }
    if (own) {
#pragma unroll
      for (int p = 0; p < 2; ++p) {
        const f32x16 g = sel16(wn, accg[0][p], accg[1][p]);
#pragma unroll
        for (int i = 0; i < 4; ++i) *(LAS f32x4*)(xw + 8192 + p * 4096 + i * 1024) = (f32x4){g[4 * i], g[4 * i + 1], g[4 * i + 2], g[4 * i + 3]};
      }
    }
  }
  G2_BAR();
  STAMP(3, 7);
  {
    const LAS char* xr = (const LAS char*)smem + (w ^ 2) * 16384 + l * 16;
    const LAS char* xp = (const LAS char*)smem + G2_PART + wm * 16384 + wn * 8192 + l * 16;
    f32x16 gf[2];
    if (own) {
      gf[0] = sel16(wn, accg[1][0], accg[0][0]); gf[1] = sel16(wn, accg[1][1], accg[0][1]);
    } else {
#pragma unroll
      for (int p = 0; p < 2; ++p)
#pragma unroll
        for (int i = 0; i < 4; ++i) {
          const f32x4 t = *(const LAS f32x4*)(xr + 8192 + p * 4096 + i * 1024);
          gf[p][4 * i] = t[0]; gf[p][4 * i + 1] = t[1]; gf[p][4 * i + 2] = t[2]; gf[p][4 * i + 3] = t[3];
        }
    }
#pragma unroll
    for (int p = 0; p < 2; ++p) {
      const f32x16 s = sel16(wn, accs[1][p], accs[0][p]);
      unsigned pk[8];
#pragma unroll
      for (int k = 0; k < 2; ++k) {
        u32x4 xx = xv[p * 2 + k];
        { auto r0 = __builtin_amdgcn_permlane32_swap(xx[0], xx[2], false, false); xx[0] = r0[0]; xx[2] = r0[1];
          auto r1 = __builtin_amdgcn_permlane32_swap(xx[1], xx[3], false, false); xx[1] = r1[0]; xx[3] = r1[1]; }
#pragma unroll
        for (int ii = 0; ii < 2; ++ii) {
          const int i = 2 * k + ii;
          const f32x4 ts = *(const LAS f32x4*)(xr + p * 4096 + i * 1024);
          const f32x4 tp = *(const LAS f32x4*)(xp + p * 4096 + i * 1024);
          float r[4];
#pragma unroll
          for (int j = 0; j < 4; ++j) {
            const unsigned xw_ = xx[2 * ii + (j >> 1)];
            const float xf = (j & 1) ? bf_hi(xw_) : bf_lo(xw_);
            const unsigned bw_ = bv[p * 4 + i][j >> 1];
            const float z = gf[p][4 * i + j] + tp[j] + ((j & 1) ? bf_hi(bw_) : bf_lo(bw_));
            const float sg = z * __builtin_amdgcn_rcpf(1.0f + __builtin_amdgcn_exp2f(-1.4426950408889634f * z));
            r[j] = xf + (s[4 * i + j] + ts[j]) * sg;
          }
          pk[4 * k + 2 * ii] = cvtpk(r[0], r[1]); pk[4 * k + 2 * ii + 1] = cvtpk(r[2], r[3]);
        }
        auto r0 = __builtin_amdgcn_permlane32_swap(pk[4 * k], pk[4 * k + 2], false, false);
        auto r1 = __builtin_amdgcn_permlane32_swap(pk[4 * k + 1], pk[4 * k + 3], false, false);
        *(u32x4*)(out + (size_t)row * DM + col0 + p * 32 + 16 * k + 8 * h) = (u32x4){r0[0], r1[0], r0[1], r1[1]};
      }
    }
  }
  STAMP(3, 3);
#undef G2_RD
#undef G2_MM
#undef G2_SCHED
#undef G2_BAR
}

__global__ __launch_bounds__(512, 2) void tail_kernel(const u16* __restrict__ x, const u16* __restrict__ b2, const u16* __restrict__ w2,
                                                      const u16* __restrict__ wo, char* __restrict__ ws, u16* __restrict__ out) {
  extern __shared__ __attribute__((aligned(16))) char smem[];
  const int l = threadIdx.x & 63, w = rfl(threadIdx.x >> 6);
  attn_phase(ws, w2, wo, smem, w, l);
  gemm2_phase(x, b2, w2, wo, ws, out, smem, w, l);
}

extern "C" void kernel_launch(void* const* d_in, const int* in_sizes, int n_in, void* d_out, int out_size, void* d_ws,
                              size_t ws_size, hipStream_t stream) {
  const u16* x = (const u16*)d_in[0];
  const u16* wq = (const u16*)d_in[1];
  const u16* wk = (const u16*)d_in[2];
  const u16* wv = (const u16*)d_in[3];
  const u16* wo = (const u16*)d_in[4];
  const u16* w1 = (const u16*)d_in[6];
  const u16* b1 = (const u16*)d_in[7];
  const u16* w2 = (const u16*)d_in[8];
  const u16* b2 = (const u16*)d_in[9];
  char* ws = (char*)d_ws;
  (void)hipFuncSetAttribute((const void*)gemm1_kernel, hipFuncAttributeMaxDynamicSharedMemorySize, G1_LDS);
  ln_kernel<<<256, 256, 0, stream>>>(x, ws);
  gemm1_kernel<<<BSTR ? 240 : 256, 512, G1_LDS, stream>>>(wq, wk, wv, w1, b1, w2, wo, ws);
  (void)hipFuncSetAttribute((const void*)tail_kernel, hipFuncAttributeMaxDynamicSharedMemorySize, K3_LDS);
  tail_kernel<<<256, 512, K3_LDS, stream>>>(x, b2, w2, wo, ws, (u16*)d_out);
}
```

```cpp
#include <hip/hip_runtime.h>
#include <hip/hip_bf16.h>

typedef short bf16x8 __attribute__((ext_vector_type(8)));
typedef short s16x4 __attribute__((ext_vector_type(4)));
typedef float f32x16 __attribute__((ext_vector_type(16)));
typedef float f32x4 __attribute__((ext_vector_type(4)));
typedef float f32x2 __attribute__((ext_vector_type(2)));
typedef __bf16 bf16x2_t __attribute__((ext_vector_type(2)));
typedef unsigned u32x4 __attribute__((ext_vector_type(4)));
typedef unsigned u32x2 __attribute__((ext_vector_type(2)));
typedef unsigned short u16;
#define LAS __attribute__((address_space(3)))
#define DI __device__ __forceinline__
#define SBAR() __builtin_amdgcn_sched_barrier(0)
#define PIN(x) asm volatile("" : "+v"(x))
#define MFMA32(a, b, c) __builtin_amdgcn_mfma_f32_32x32x16_bf16((a), (b), (c), 0, 0, 0)
#define MFMA16(a, b, c) __builtin_amdgcn_mfma_f32_16x16x32_bf16((a), (b), (c), 0, 0, 0)

constexpr int SEQ = 2048, DM = 1024, NH = 32, FF = 3584;
constexpr size_t KT = 262144;
constexpr size_t WS_XN = 0;
constexpr size_t WS_Q = 4u << 20;
constexpr size_t WS_KV = 8u << 20;
constexpr size_t WS_O = 9u << 20;
constexpr size_t WS_H = 13u << 20;
constexpr size_t WS_PART = 28u << 20;
constexpr size_t WS_FLAG = 37u << 20;
constexpr int FL_P = 0, FL_O = 256, FL_H = 272, FL_Q = 280, FL_KV = 344, FL_N = 345;

#define STAMP(kid, idx) do {} while (0)
DI unsigned rfl(unsigned x) { return __builtin_amdgcn_readfirstlane(x); }
DI unsigned cvtpk(float lo, float hi) {
  f32x2 v = {lo, hi};
  bf16x2_t b = __builtin_convertvector(v, bf16x2_t);
  return __builtin_bit_cast(unsigned, b);
}
DI float fadd_s(float a, float b) { float r; asm("v_add_f32_e32 %0, %1, %2" : "=v"(r) : "v"(a), "v"(b)); return r; }
DI float bf_lo(unsigned u) { return __uint_as_float(u << 16); }
DI float bf_hi(unsigned u) { return __uint_as_float(u & 0xffff0000u); }
DI float bf_round(float x) { return bf_lo(cvtpk(x, x)); }

template <int IMM>
DI void glds16(unsigned voff, const void* sbase, unsigned ldsaddr) {
  asm volatile("s_mov_b32 m0, %2\n\ts_nop 0\n\tglobal_load_lds_dwordx4 %0, %1 offset:%3"
               :: "v"(voff), "s"(sbase), "s"(ldsaddr), "i"(IMM) : "memory");
}
template <int IMM>
DI void glds16_dev(unsigned voff, const void* sbase, unsigned ldsaddr) {
  asm volatile("s_mov_b32 m0, %2\n\ts_nop 0\n\tglobal_load_lds_dwordx4 %0, %1 offset:%3 sc1"
               :: "v"(voff), "s"(sbase), "s"(ldsaddr), "i"(IMM) : "memory");
}
DI void store16_dev(void* p, u32x4 v) {
  asm volatile("global_store_dwordx4 %0, %1, off sc1\n\ts_nop 1" :: "v"(p), "v"(v) : "memory");
}
DI void l2_touch(const void* p) { asm volatile("global_load_dword v255, %0, off" :: "v"(p) : "v255", "memory"); }
typedef __attribute__((address_space(1))) unsigned gu32;
#define RLX_AGENT __ATOMIC_RELAXED, __HIP_MEMORY_SCOPE_AGENT
DI void wait_ge(const unsigned* word, unsigned want) {
  for (unsigned spins = 0; spins < (1u << 22); ++spins) {
    if (rfl(__hip_atomic_load((gu32*)word, RLX_AGENT)) >= want) break;
    __builtin_amdgcn_s_sleep(2);
  }
  __builtin_amdgcn_fence(__ATOMIC_ACQUIRE, "wavefront");
}
DI void publish(unsigned* word, int l) {
  asm volatile("s_waitcnt vmcnt(0)" ::: "memory");
  if (l == 0) __hip_atomic_fetch_add((gu32*)word, 1u, RLX_AGENT);
}
DI bf16x8 load16_dev(const char* p) {
  typedef __attribute__((address_space(1))) unsigned long long gu64;
  const unsigned long long a = __hip_atomic_load((gu64*)p, RLX_AGENT), b = __hip_atomic_load((gu64*)p + 1, RLX_AGENT);
  return __builtin_bit_cast(bf16x8, (u32x4){(unsigned)a, (unsigned)(a >> 32), (unsigned)b, (unsigned)(b >> 32)});
}
DI float relu_s(float a) { float r; asm("v_max_f32_e32 %0, 0, %1" : "=v"(r) : "v"(a)); return r; }
DI bf16x8 lds_b128(const LAS char* p) { return *(const LAS bf16x8*)p; }
DI s16x4 lds_tr(const LAS char* p) {
  return __builtin_amdgcn_ds_read_tr16_b64_v4i16((LAS s16x4*)p);
}
DI f32x16 sel16(bool c, const f32x16& a, const f32x16& b) {
  f32x16 r;
#pragma unroll
  for (int i = 0; i < 16; ++i) r[i] = c ? a[i] : b[i];
  return r;
}
DI bf16x8 cat8(s16x4 lo, s16x4 hi) { return __builtin_shufflevector(lo, hi, 0, 1, 2, 3, 4, 5, 6, 7); }

__global__ __launch_bounds__(256) void ln_kernel(const u16* __restrict__ x, char* __restrict__ ws) {
  __shared__ float red[2][4][8];
  const int tid = threadIdx.x, w = tid >> 6, l = tid & 63;
  const int row = blockIdx.x * 8 + (l & 7);
  STAMP(0, 0);
  if (tid < 2 && blockIdx.x + tid * 256 < FL_N) ((unsigned*)(ws + WS_FLAG))[(blockIdx.x + tid * 256) * 32] = 0u;
  u32x4 v[4];
#pragma unroll
  for (int i = 0; i < 4; ++i) {
    const int c = (i * 4 + w) * 8 + (l >> 3);
    v[i] = *(const u32x4*)(x + (size_t)row * DM + c * 8);
  }
  float s = 0.f;
#pragma unroll
  for (int i = 0; i < 4; ++i)
#pragma unroll
    for (int j = 0; j < 4; ++j) s += bf_lo(v[i][j]) + bf_hi(v[i][j]);
  s += __shfl_xor(s, 8); s += __shfl_xor(s, 16); s += __shfl_xor(s, 32);
  if (l < 8) red[0][w][l] = s;
  __syncthreads();
  const float mean = (red[0][0][l & 7] + red[0][1][l & 7] + red[0][2][l & 7] + red[0][3][l & 7]) * (1.0f / DM);
  float q = 0.f;
#pragma unroll
  for (int i = 0; i < 4; ++i)
#pragma unroll
    for (int j = 0; j < 4; ++j) {
      const float a = bf_lo(v[i][j]) - mean, b = bf_hi(v[i][j]) - mean;
      q += a * a + b * b;
    }
  q += __shfl_xor(q, 8); q += __shfl_xor(q, 16); q += __shfl_xor(q, 32);
  if (l < 8) red[1][w][l] = q;
  __syncthreads();
  const float var = (red[1][0][l & 7] + red[1][1][l & 7] + red[1][2][l & 7] + red[1][3][l & 7]) * (1.0f / DM);
  const float mb = bf_round(mean);
  const float den = bf_round(__builtin_sqrtf(bf_round(bf_round(var) + 1e-5f)));
  const float rden = 1.0f / den;
  char* dst = ws + WS_XN + (size_t)(row >> 5) * 4096 + (row & 31) * 16;
#pragma unroll
  for (int i = 0; i < 4; ++i) {
    const int c = (i * 4 + w) * 8 + (l >> 3);
    u32x4 o;
#pragma unroll
    for (int j = 0; j < 4; ++j) {
      const unsigned d = cvtpk(bf_lo(v[i][j]) - mb, bf_hi(v[i][j]) - mb);
      o[j] = cvtpk(bf_lo(d) * rden, bf_hi(d) * rden);
    }
    *(u32x4*)(dst + (size_t)(c >> 3) * KT + (c & 7) * 512) = o;
  }
  STAMP(0, 3);
}

constexpr int G1_STAGE = 53248, G1_BOFF = 32768;
enum { G1_FFN = 5, G1_Q = 4, G1_KV = 2 };

template <int NP>
DI void g1_load(const char* ws, const u16* wsrc, const u16* wv, int cg, int mtile, unsigned lds0, int lw, int l) {
  constexpr int LD = (NP == G1_FFN) ? FF : (NP == G1_Q) ? DM : 32;
  const char* abase = ws + WS_XN + (size_t)(mtile * 8 + lw * 2) * 4096;
  const unsigned avoff = l * 16;
  const unsigned bvoff = (unsigned)((lw * 16 + (l >> 2)) * LD * 2 + (l & 3) * 16);
  const char* bbase[NP];
#pragma unroll
  for (int p = 0; p < NP; ++p) {
    if (NP == G1_KV) bbase[p] = (const char*)(p ? wv : wsrc);
    else if (NP == G1_Q) bbase[p] = (const char*)(wsrc + (cg * 4 + p) * 32);
    else { const int cb = (cg - 9) * 5 + p; bbase[p] = (const char*)(wsrc + (cb > 111 ? 111 : cb) * 32); }
  }
  auto issue = [&](int kt, int slot) {
    const unsigned sa = lds0 + slot * G1_STAGE + lw * 8192;
    const char* a0 = abase + (size_t)kt * KT;
    glds16<0>(avoff, a0, sa); glds16<1024>(avoff, a0, sa); glds16<2048>(avoff, a0, sa); glds16<3072>(avoff, a0, sa);
    const char* a1 = a0 + 4096;
    glds16<0>(avoff, a1, sa + 4096); glds16<1024>(avoff, a1, sa + 4096); glds16<2048>(avoff, a1, sa + 4096); glds16<3072>(avoff, a1, sa + 4096);
    const unsigned sb = lds0 + slot * G1_STAGE + G1_BOFF + lw * 1024;
#pragma unroll
    for (int p = 0; p < NP; ++p) glds16<0>(bvoff, bbase[p] + (size_t)kt * (64 * LD * 2), sb + p * 4096);
  };
  issue(0, 0); issue(1, 1); issue(2, 2);
  asm volatile("s_waitcnt vmcnt(%0)\n\ts_barrier" :: "n"(2 * (8 + NP)) : "memory");
  int slot = 0;
  for (int i = 0; i < 13; ++i) {
    asm volatile("s_waitcnt vmcnt(%0)\n\ts_barrier" :: "n"(8 + NP) : "memory");
    issue(i + 3, slot);
    slot = (slot == 2) ? 0 : slot + 1;
  }
  asm volatile("s_waitcnt vmcnt(%0)\n\ts_barrier" :: "n"(8 + NP) : "memory");
  asm volatile("s_waitcnt vmcnt(0)\n\ts_barrier" ::: "memory");
}

template <int NP>
DI void g1_compute(char* __restrict__ ws, const u16* __restrict__ b1, int cg, int mtile, char* smem, int w, int l) {
  STAMP(1, 0);
  const int h = l >> 5;
  f32x16 acc[2][NP];
  if (NP == G1_FFN) {
#pragma unroll
    for (int p = 0; p < NP; ++p) {
      int cb = (cg - 9) * 5 + p; cb = cb > 111 ? 111 : cb;
#pragma unroll
      for (int i = 0; i < 4; ++i) {
        const u32x2 bv = *(const u32x2*)(b1 + cb * 32 + 8 * i + 4 * h);
        acc[0][p][4 * i + 0] = bf_lo(bv[0]); acc[0][p][4 * i + 1] = bf_hi(bv[0]);
        acc[0][p][4 * i + 2] = bf_lo(bv[1]); acc[0][p][4 * i + 3] = bf_hi(bv[1]);
      }
      acc[1][p] = acc[0][p];
    }
  } else {
#pragma unroll
    for (int p = 0; p < NP; ++p) { acc[0][p] = f32x16{}; acc[1][p] = f32x16{}; }
  }
  const LAS char* lbase = (const LAS char*)smem;
  const int aoff = w * 8192 + l * 16;
  const int boff = G1_BOFF + (8 * h + ((l & 15) >> 2)) * 64 + ((l >> 4) & 1) * 32 + (l & 3) * 8;
  bf16x8 fa0[2], fb0[NP], fa1[2], fb1[NP];
#define G1_RD(FA, FB, base, ks) do { \
    FA[0] = lds_b128((base) + aoff + (ks) * 1024); FA[1] = lds_b128((base) + aoff + 4096 + (ks) * 1024); \
    _Pragma("unroll") for (int p_ = 0; p_ < NP; ++p_) \
      FB[p_] = cat8(lds_tr((base) + boff + p_ * 4096 + (ks) * 1024), lds_tr((base) + boff + p_ * 4096 + (ks) * 1024 + 256)); \
  } while (0)
#define G1_MM(FA, FB) do { \
    _Pragma("unroll") for (int p_ = 0; p_ < NP; ++p_) { \
      if (NP == G1_KV && p_ == 1) { acc[0][p_] = MFMA32(FA[0], FB[p_], acc[0][p_]); acc[1][p_] = MFMA32(FA[1], FB[p_], acc[1][p_]); } \
      else { acc[0][p_] = MFMA32(FB[p_], FA[0], acc[0][p_]); acc[1][p_] = MFMA32(FB[p_], FA[1], acc[1][p_]); } } \
  } while (0)
#define G1_SCHED() do { \
    _Pragma("unroll") for (int q_ = 0; q_ < NP + 1; ++q_) { \
      __builtin_amdgcn_sched_group_barrier(0x008, 1, 0); __builtin_amdgcn_sched_group_barrier(0x100, 2, 0); } \
    __builtin_amdgcn_sched_group_barrier(0x008, NP - 1, 0); SBAR(); \
  } while (0)

  asm volatile("s_barrier" ::: "memory");
  STAMP(1, 1);
  G1_RD(fa0, fb0, lbase, 0); SBAR();
  G1_RD(fa1, fb1, lbase, 1); G1_MM(fa0, fb0); G1_SCHED();
  G1_RD(fa0, fb0, lbase, 2); G1_MM(fa1, fb1); G1_SCHED();
  G1_RD(fa1, fb1, lbase, 3); G1_MM(fa0, fb0); G1_SCHED();
  int slot = 1;
  for (int i = 0; i < 15; ++i) {
    const LAS char* nb = lbase + slot * G1_STAGE;
    asm volatile("s_waitcnt lgkmcnt(0)\n\ts_barrier" ::: "memory"); SBAR();
    G1_RD(fa0, fb0, nb, 0); G1_MM(fa1, fb1); G1_SCHED();
    G1_RD(fa1, fb1, nb, 1); G1_MM(fa0, fb0); G1_SCHED();
    G1_RD(fa0, fb0, nb, 2); G1_MM(fa1, fb1); G1_SCHED();
    G1_RD(fa1, fb1, nb, 3); G1_MM(fa0, fb0); G1_SCHED();
    slot = (slot == 2) ? 0 : slot + 1;
  }
  G1_MM(fa1, fb1);
  asm volatile("s_nop 15\n\ts_nop 3" ::: "memory"); SBAR();
  STAMP(1, 2);

#pragma unroll
  for (int p = 0; p < NP; ++p) {
    const int cb = (NP == G1_FFN) ? (cg - 9) * 5 + p : cg * 4 + p;
    if (NP != G1_FFN || cb < 112) {
#pragma unroll
      for (int m = 0; m < 2; ++m) {
        const int tt = mtile * 8 + w * 2 + m;
        char* dst = (NP == G1_FFN) ? ws + WS_H + (size_t)(cb >> 1) * KT + (size_t)tt * 4096 + (cb & 1) * 2048 + l * 16
                  : (NP == G1_Q)   ? ws + WS_Q + (size_t)(tt * 32 + cb) * 2048 + l * 16
                                   : ws + WS_KV + (size_t)tt * 4096 + p * 2048 + l * 16;
#pragma unroll
        for (int s = 0; s < 2; ++s) {
          u32x4 o;
#pragma unroll
          for (int j = 0; j < 4; ++j) {
            float a = acc[m][p][8 * s + 2 * j], b = acc[m][p][8 * s + 2 * j + 1];
            if (NP == G1_FFN) { a = relu_s(a); b = relu_s(b); a *= a; b *= b; }
            if (NP == G1_Q) { const float qs = 0.17677669529663687f * 1.4426950408889634f; a *= qs; b *= qs; }
            o[j] = cvtpk(a, b);
          }
          store16_dev(dst + s * 1024, o);
        }
      }
    }
  }
  STAMP(1, 3);
#undef G1_RD
#undef G1_MM
#undef G1_SCHED
}

constexpr int AT_NCH = 16;
constexpr int AT_BUF = 16384;
constexpr int AT_BASE = 114688;
constexpr int AT_Q = 65536;
constexpr int MAIN_LDS = 163840;

DI float xhalf_max(float m) {
  auto rr = __builtin_amdgcn_permlane32_swap(__float_as_uint(m), __float_as_uint(m), false, false);
  return __builtin_fmaxf(__uint_as_float(rr[0]), __uint_as_float(rr[1]));
}
DI float xhalf_sum(float m) {
  auto rr = __builtin_amdgcn_permlane32_swap(__float_as_uint(m), __float_as_uint(m), false, false);
  return __uint_as_float(rr[0]) + __uint_as_float(rr[1]);
}
DI float max16(const f32x16& s) {
  float a = __builtin_fmaxf(s[0], s[1]);
#pragma unroll
  for (int r = 2; r < 16; ++r) a = __builtin_fmaxf(a, s[r]);
  return a;
}
DI bf16x8 pack8(const f32x16& p, int s) {
  u32x4 o;
#pragma unroll
  for (int j = 0; j < 4; ++j) o[j] = cvtpk(p[8 * s + 2 * j], p[8 * s + 2 * j + 1]);
  return __builtin_bit_cast(bf16x8, o);
}

DI void attn_fallback(const char* __restrict__ kv, bf16x8 q0, bf16x8 q1, int l, f32x16& o_out, float& l_out) {
  f32x16 o = {};
  float m = -INFINITY, lsum = 0.f;
  for (int t = 0; t < 64; ++t) {
    const char* p = kv + (size_t)t * 4096 + l * 16;
    const bf16x8 k0 = load16_dev(p), k1 = load16_dev(p + 1024), v0 = load16_dev(p + 2048), v1 = load16_dev(p + 3072);
    f32x16 s = {};
    s = MFMA32(k0, q0, s); s = MFMA32(k1, q1, s);
    const float mn = __builtin_fmaxf(m, xhalf_max(max16(s)));
    const float ms = (mn == -INFINITY) ? 0.f : mn;
    const float alpha = __builtin_amdgcn_exp2f(m - ms);
    m = mn;
    float ps = 0.f;
#pragma unroll
    for (int r = 0; r < 16; ++r) { s[r] = __builtin_amdgcn_exp2f(s[r] - ms); ps += s[r]; }
    lsum = lsum * alpha + ps;
#pragma unroll
    for (int r = 0; r < 16; ++r) o[r] *= alpha;
    o = MFMA32(v0, pack8(s, 0), o); o = MFMA32(v1, pack8(s, 1), o);
  }
  o_out = o;
  l_out = xhalf_sum(lsum);
}

constexpr int G2_SLOT = 32768, G2_BOFF = 16384, G2_PART = 131072;
struct G2Ld {
  const char* hb; const char* ob; const char* wb; const char* wb2; unsigned lds; unsigned voff, voffb;
  DI void init(const char* ws, const u16* w2, const u16* wo, unsigned lds0, int lw, int l) {
    const int xcd = blockIdx.x & 7, idx = blockIdx.x >> 3, kh = xcd & 1;
    const int mt = ((xcd >> 1) & 1) * 8 + (idx & 7), nt = (xcd >> 2) * 4 + (idx >> 3);
    hb = ws + WS_H + (size_t)(mt * 4 + lw) * 4096 + (size_t)kh * (28 * KT);
    ob = ws + WS_O + (size_t)(mt * 4 + lw) * 4096;
    wb = (const char*)(w2 + (size_t)(kh * 1792 + lw * 16) * DM + nt * 128);
    wb2 = (const char*)(wo + (size_t)(lw * 16) * DM + nt * 128 + kh * 64);
    voffb = (unsigned)((l >> 2) * DM * 2 + (l & 3) * 16);
    lds = lds0 + lw * 4096; voff = l * 16;
  }
  DI void ffn(int s) const {
    const unsigned d = lds + (s & 3) * G2_SLOT;
    const char* a = hb + (size_t)s * KT;
    glds16_dev<0>(voff, a, d); glds16_dev<1024>(voff, a, d); glds16_dev<2048>(voff, a, d); glds16_dev<3072>(voff, a, d);
    const char* b = wb + (size_t)s * 131072;
    glds16<0>(voffb, b, d + G2_BOFF); glds16<0>(voffb, b + 64, d + G2_BOFF + 1024); glds16<0>(voffb, b + 128, d + G2_BOFF + 2048); glds16<0>(voffb, b + 192, d + G2_BOFF + 3072);
  }
  DI void wo(int s) const {
    const unsigned d = lds + (s & 3) * G2_SLOT;
    const char* a = ob + (size_t)(s - 28) * KT;
    glds16_dev<0>(voff, a, d); glds16_dev<1024>(voff, a, d); glds16_dev<2048>(voff, a, d); glds16_dev<3072>(voff, a, d);
    const char* b = wb2 + (size_t)(s - 28) * 131072;
    glds16<0>(voffb, b, d + G2_BOFF); glds16<0>(voffb, b + 64, d + G2_BOFF + 1024);
  }
};

DI void attn_finish(char* __restrict__ ws, f32x16 o, f32x4 lacc, bf16x8 q0, bf16x8 q1, int l, char* dst) {
  const float l0 = __shfl(lacc[0], l & 15), l1 = __shfl(lacc[1], l & 15);
  float lsum = (l & 16) ? l1 : l0;
  bool ok = (lsum >= 9.5367431640625e-07f) && (lsum <= 1.8446744073709552e19f);
#pragma unroll
  for (int r = 0; r < 16; ++r) ok = ok && (__builtin_fabsf(o[r]) <= 3.0e38f);
  if (!__all(ok)) attn_fallback(ws + WS_KV, q0, q1, l, o, lsum);
  const float rl = 1.0f / lsum;
#pragma unroll
  for (int s = 0; s < 2; ++s) {
    u32x4 ov;
#pragma unroll
    for (int j = 0; j < 4; ++j) ov[j] = cvtpk(o[8 * s + 2 * j] * rl, o[8 * s + 2 * j + 1] * rl);
    store16_dev(dst + s * 1024, ov);
  }
}

DI void attn_load(char* __restrict__ ws, const u16* w2, const u16* wo, unsigned lds0, const int lw, const int l) {
  const int tt = blockIdx.x >> 2, hg = blockIdx.x & 3;
  unsigned* flags = (unsigned*)(ws + WS_FLAG);
  const unsigned voff = l * 16;
  const char* ksrc = ws + WS_KV + (size_t)lw * 4096;
  const unsigned kdst = lds0 + AT_BASE + lw * 4096;
  auto ld = [&](int c, int slot) {
    const char* s_ = ksrc + (size_t)c * AT_BUF;
    glds16_dev<0>(voff, s_, kdst + slot); glds16_dev<1024>(voff, s_, kdst + slot); glds16_dev<2048>(voff, s_, kdst + slot); glds16_dev<3072>(voff, s_, kdst + slot);
  };
  {
    const unsigned* word = flags + ((l & 1) ? FL_Q + (tt >> 3) * 8 + hg * 2 + (lw >> 1) : FL_KV) * 32;
    const unsigned need = (l & 1) ? 4u : 32u;
    for (unsigned spins = 0; spins < (1u << 22); ++spins) {
      if (__all(__hip_atomic_load((gu32*)word, RLX_AGENT) >= need)) break;
      __builtin_amdgcn_s_sleep(2);
    }
    __builtin_amdgcn_fence(__ATOMIC_ACQUIRE, "wavefront");
    const char* qs = ws + WS_Q + (size_t)(tt * 32 + hg * 8 + lw * 2) * 2048;
    const unsigned qd = lds0 + AT_Q + lw * 4096;
    glds16_dev<0>(voff, qs, qd); glds16_dev<1024>(voff, qs, qd); glds16_dev<2048>(voff, qs, qd); glds16_dev<3072>(voff, qs, qd);
  }
  ld(0, 0); ld(1, AT_BUF); ld(2, 2 * AT_BUF);
  G2Ld g2; g2.init(ws, w2, wo, lds0, lw, l);
  const int xcd = blockIdx.x & 7, idx = blockIdx.x >> 3, gk = xcd & 1;
  const int mt = ((xcd >> 1) & 1) * 8 + (idx & 7), nt = (xcd >> 2) * 4 + (idx >> 3);
  const char* pfw = (const char*)(w2 + (size_t)(gk * 1792 + (mt & 7) * 32 + (l >> 1)) * DM + nt * 128 + (l & 1) * 64);
  const char* pfo = (const char*)(wo + (size_t)((mt & 7) * 128 + l) * DM + nt * 128 + gk * 64);
  const char* pfh = ws + WS_H + (size_t)(gk * 28 + 3 + (l >> 5)) * KT + (size_t)(mt * 4 + (nt & 3)) * 4096 + (l & 31) * 128;
  asm volatile("s_waitcnt vmcnt(8)\n\ts_barrier" ::: "memory");
  int slot = 0, extra = 0;
  for (int c = 0; c < AT_NCH - 1; ++c) {
    if (c >= AT_NCH - 2) asm volatile("s_waitcnt vmcnt(8)\n\ts_barrier" ::: "memory");
    else if (extra == 0) asm volatile("s_waitcnt vmcnt(4)\n\ts_barrier" ::: "memory");
    else if (extra == 1) asm volatile("s_waitcnt vmcnt(5)\n\ts_barrier" ::: "memory");
    else asm volatile("s_waitcnt vmcnt(12)\n\ts_barrier" ::: "memory");
    if (c + 3 < AT_NCH) ld(c + 3, slot);
    slot = (slot == 2 * AT_BUF) ? 0 : slot + AT_BUF;
    extra = 0;
    if (c < 9 && (c & 3) == lw) { l2_touch(c < 7 ? pfw + (size_t)c * (256 * DM * 2) : pfo + (size_t)(c - 7) * (64 * DM * 2)); extra = 1; }
    if (c == 10) { wait_ge(flags + (FL_H + (mt >> 1)) * 32, 92u); l2_touch(pfh + (size_t)lw * (2 * KT)); extra = 1; }
    if (c >= 11 && c < 14) { g2.ffn(c - 11); extra = 8; }
  }
}

DI void attn_compute(char* __restrict__ ws, char* smem, const int w, const int l, unsigned* late_pub) {
  const int tt = blockIdx.x >> 2, hg = blockIdx.x & 3;
  const int head0 = hg * 8 + w * 2;
  STAMP(2, 0);
  bf16x8 sel;
  {
    const short one = ((l & 15) == ((l >> 4) & 1)) ? (short)0x3f80 : (short)0;
#pragma unroll
    for (int j = 0; j < 8; ++j) sel[j] = one;
  }
  f32x16 oa = {}, ob = {}, sa, sb;
  f32x4 la = {}, lbs = {};
  u32x4 pa0, pa1, pb0 = {}, pb1 = {};
  PIN(oa); PIN(ob);
  asm volatile("s_barrier" ::: "memory");
  STAMP(2, 1);

  const LAS char* qp = (const LAS char*)smem + AT_Q + w * 4096 + l * 16;
  const bf16x8 qa0 = lds_b128(qp), qa1 = lds_b128(qp + 1024), qb0 = lds_b128(qp + 2048), qb1 = lds_b128(qp + 3072);
  const LAS char* kvp = (const LAS char*)smem + AT_BASE + l * 16;
  bf16x8 kx0, kx1, vx0, vx1, ky0, ky1, vy0, vy1;
  kx0 = lds_b128(kvp); kx1 = lds_b128(kvp + 1024); vx0 = lds_b128(kvp + 2048); vx1 = lds_b128(kvp + 3072);
  vy0 = vx0; vy1 = vx1;
  f32x16 nma, nmb;
  {
    sa = MFMA32(kx0, qa0, oa); sb = MFMA32(kx0, qb0, ob);
    sa = MFMA32(kx1, qa1, sa); sb = MFMA32(kx1, qb1, sb);
    const float ma = -xhalf_max(max16(sa)), mb = -xhalf_max(max16(sb));
#pragma unroll
    for (int r = 0; r < 16; ++r) { nma[r] = ma; nmb[r] = mb; sa[r] += ma; }
    PIN(nma); PIN(nmb);
  }
#define EX(S, i) S[i] = __builtin_amdgcn_exp2f(S[i])
#define CV(P, j, S, i) P[j] = cvtpk(S[i], S[(i) + 1])
#define B8(x) __builtin_bit_cast(bf16x8, x)
#define LSUM(L, P) L = MFMA16(sel, B8(P), L)
#define PV(O, V, P) O = MFMA32(V, B8(P), O)
#define HALF(S, P0, P1, M0, M1, M2, M3, M4, M5, LD0, LD1) do { \
    M0; SBAR(); LD0; EX(S, 0); EX(S, 1); EX(S, 2); EX(S, 3); PIN(S); SBAR(); \
    M1; SBAR(); LD1; CV(P0, 0, S, 0); CV(P0, 1, S, 2); EX(S, 4); EX(S, 5); EX(S, 6); PIN(S); PIN(P0); SBAR(); \
    M2; SBAR(); EX(S, 7); CV(P0, 2, S, 4); EX(S, 8); CV(P0, 3, S, 6); EX(S, 9); PIN(S); PIN(P0); SBAR(); \
    M3; SBAR(); EX(S, 10); EX(S, 11); PIN(S); SBAR(); \
    M4; SBAR(); CV(P1, 0, S, 8); CV(P1, 1, S, 10); EX(S, 12); EX(S, 13); EX(S, 14); PIN(S); PIN(P1); SBAR(); \
    M5; SBAR(); EX(S, 15); CV(P1, 2, S, 12); CV(P1, 3, S, 14); PIN(P1); SBAR(); \
  } while (0)
#define STEP(KC0, KC1, VC0, VC1, KN0, KN1, VP0, VP1, NP) do { \
    HALF(sa, pa0, pa1, sb = MFMA32(KC0, qb0, nmb), sb = MFMA32(KC1, qb1, sb), PV(ob, VP0, pb0), LSUM(lbs, pb0), \
         PV(ob, VP1, pb1), LSUM(lbs, pb1), KN0 = lds_b128(NP), KN1 = lds_b128((NP) + 1024)); \
    HALF(sb, pb0, pb1, sa = MFMA32(KN0, qa0, nma), sa = MFMA32(KN1, qa1, sa), PV(oa, VC0, pa0), LSUM(la, pa0), \
         PV(oa, VC1, pa1), LSUM(la, pa1), VP0 = lds_b128((NP) + 2048), VP1 = lds_b128((NP) + 3072)); \
  } while (0)
  int s0 = 0;
  for (int c = 0; c < AT_NCH; ++c) {
    const int s1 = (s0 == 2 * AT_BUF) ? 0 : s0 + AT_BUF;
    const LAS char* cb = kvp + s0;
    const LAS char* nb = kvp + s1;
    STEP(kx0, kx1, vx0, vx1, ky0, ky1, vy0, vy1, cb + 4096);
    STEP(ky0, ky1, vy0, vy1, kx0, kx1, vx0, vx1, cb + 2 * 4096);
    STEP(kx0, kx1, vx0, vx1, ky0, ky1, vy0, vy1, cb + 3 * 4096);
    if (c < AT_NCH - 1) asm volatile("s_waitcnt lgkmcnt(0)\n\ts_barrier" ::: "memory");
    if (c == 2 && late_pub) publish(late_pub, l);
    SBAR();
    STEP(ky0, ky1, vy0, vy1, kx0, kx1, vx0, vx1, nb);
    s0 = s1;
  }
  PV(ob, vy0, pb0); LSUM(lbs, pb0);
  PV(ob, vy1, pb1); LSUM(lbs, pb1);
#undef EX
#undef CV
#undef B8
#undef HALF
#undef STEP
#undef LSUM
#undef PV
  STAMP(2, 2);
  char* dst = ws + WS_O + (size_t)(head0 >> 1) * KT + (size_t)tt * 4096 + l * 16;
  attn_finish(ws, oa, la, qa0, qa1, l, dst);
  attn_finish(ws, ob, lbs, qb0, qb1, l, dst + 2048);
  STAMP(2, 3);
}

DI void g2_load(const u16* w2, const u16* wo, char* __restrict__ ws, unsigned lds0, const int lw, const int l) {
  const int xcd = blockIdx.x & 7, idx = blockIdx.x >> 3;
  const int mt = ((xcd >> 1) & 1) * 8 + (idx & 7);
  unsigned* flags = (unsigned*)(ws + WS_FLAG);
  G2Ld g; g.init(ws, w2, wo, lds0, lw, l);
  asm volatile("s_waitcnt vmcnt(16)\n\ts_barrier" ::: "memory");
  g.ffn(3);
  for (int i = 0; i < 24; ++i) {
    asm volatile("s_waitcnt vmcnt(16)\n\ts_barrier" ::: "memory");
    g.ffn(i + 4);
  }
  asm volatile("s_waitcnt vmcnt(16)\n\ts_barrier" ::: "memory");
  wait_ge(flags + (FL_O + mt) * 32, 64u);
  g.wo(28);
  for (int i = 25; i < 40; ++i) {
    asm volatile("s_waitcnt vmcnt(12)\n\ts_barrier" ::: "memory");
    g.wo(i + 4);
  }
  asm volatile("s_waitcnt vmcnt(12)\n\ts_barrier" ::: "memory");
  asm volatile("s_waitcnt vmcnt(6)\n\ts_barrier" ::: "memory");
  wait_ge(flags + (FL_P + blockIdx.x) * 32, 2u);
  {
    const char* src = ws + WS_PART + (size_t)(blockIdx.x ^ 1) * 32768 + lw * 8192;
    const unsigned d = lds0 + G2_PART + lw * 8192;
    glds16_dev<0>(g.voff, src, d); glds16_dev<1024>(g.voff, src, d); glds16_dev<2048>(g.voff, src, d); glds16_dev<3072>(g.voff, src, d);
    glds16_dev<0>(g.voff, src + 4096, d + 4096); glds16_dev<1024>(g.voff, src + 4096, d + 4096); glds16_dev<2048>(g.voff, src + 4096, d + 4096); glds16_dev<3072>(g.voff, src + 4096, d + 4096);
  }
  asm volatile("s_waitcnt vmcnt(8)\n\ts_barrier" ::: "memory");
  asm volatile("s_waitcnt vmcnt(0)\n\ts_barrier" ::: "memory");
}

DI void g2_compute(const u16* __restrict__ x, const u16* __restrict__ b2, char* __restrict__ ws, u16* __restrict__ out,
                   char* smem, const int w, const int l) {
  const int xcd = blockIdx.x & 7, idx = blockIdx.x >> 3;
  const int kh = xcd & 1, mt = ((xcd >> 1) & 1) * 8 + (idx & 7), nt = (xcd >> 2) * 4 + (idx >> 3);
  unsigned* flags = (unsigned*)(ws + WS_FLAG);
  STAMP(3, 0);
  const int h = l >> 5, wm = w & 1, wn = w >> 1;
  const bool own = (wn == kh);
  f32x16 accg[2][2] = {}, accs[2][2] = {};
  const int col0 = nt * 128 + kh * 64;
  const int row = mt * 128 + wm * 64 + wn * 32 + (l & 31);
  u32x4 xv[4];
  u32x2 bv[8];
#pragma unroll
  for (int k = 0; k < 4; ++k) xv[k] = *(const u32x4*)(x + (size_t)row * DM + col0 + 16 * k + 8 * h);
#pragma unroll
  for (int k = 0; k < 8; ++k) bv[k] = *(const u32x2*)(b2 + col0 + 8 * k + 4 * h);
  const LAS char* lbase = (const LAS char*)smem + l * 16;
  const int btr = (4 * h + ((l & 15) >> 2)) * 64 + ((l >> 4) & 1) * 32 + (l & 3) * 8 - l * 16;
#define LDB(p_) cat8(lds_tr(p_), lds_tr((p_) + 512))
#define NRD 3
  const int aF = wm * 8192, bF = G2_BOFF + wn * 2048 + btr;
  const int aW = wm * 8192 + wn * 2048, bW = G2_BOFF + wn * 8192 + btr;
  bf16x8 fa0[2], fb0[2], fa1[2], fb1[2];
#define G2_RD(FA, FB, base, ao, bo, ks) do { \
    FA[0] = lds_b128((base) + (ao) + (ks) * 1024); FB[0] = LDB((base) + (bo) + (ks) * 4096); \
    FA[1] = lds_b128((base) + (ao) + 4096 + (ks) * 1024); FB[1] = LDB((base) + (bo) + (ks) * 4096 + 1024); \
  } while (0)
#define G2_MM(ACC, FA, FB) do { \
    ACC[0][0] = MFMA32(FB[0], FA[0], ACC[0][0]); ACC[1][0] = MFMA32(FB[0], FA[1], ACC[1][0]); \
    ACC[0][1] = MFMA32(FB[1], FA[0], ACC[0][1]); ACC[1][1] = MFMA32(FB[1], FA[1], ACC[1][1]); \
  } while (0)
#define G2_SCHED() do { \
    __builtin_amdgcn_sched_group_barrier(0x008, 1, 0); __builtin_amdgcn_sched_group_barrier(0x100, NRD, 0); \
    __builtin_amdgcn_sched_group_barrier(0x008, 1, 0); __builtin_amdgcn_sched_group_barrier(0x100, NRD, 0); \
    __builtin_amdgcn_sched_group_barrier(0x008, 2, 0); SBAR(); \
  } while (0)
#define G2_BAR() do { asm volatile("s_waitcnt lgkmcnt(0)\n\ts_barrier" ::: "memory"); SBAR(); } while (0)

  G2_BAR();
  STAMP(3, 1);
  G2_RD(fa0, fb0, lbase, aF, bF, 0); SBAR();
  G2_RD(fa1, fb1, lbase, aF, bF, 1); G2_MM(accg, fa0, fb0); G2_SCHED();
  G2_RD(fa0, fb0, lbase, aF, bF, 2); G2_MM(accg, fa1, fb1); G2_SCHED();
  G2_RD(fa1, fb1, lbase, aF, bF, 3); G2_MM(accg, fa0, fb0); G2_SCHED();
  for (int i = 0; i < 27; ++i) {
    const LAS char* nb = lbase + ((i + 1) & 3) * G2_SLOT;
    G2_BAR();
    G2_RD(fa0, fb0, nb, aF, bF, 0); G2_MM(accg, fa1, fb1); G2_SCHED();
    G2_RD(fa1, fb1, nb, aF, bF, 1); G2_MM(accg, fa0, fb0); G2_SCHED();
    G2_RD(fa0, fb0, nb, aF, bF, 2); G2_MM(accg, fa1, fb1); G2_SCHED();
    G2_RD(fa1, fb1, nb, aF, bF, 3); G2_MM(accg, fa0, fb0); G2_SCHED();
    if (i == 3) { publish(flags + (FL_O + (blockIdx.x >> 4)) * 32, l); SBAR(); }
  }
  STAMP(3, 4);
  {
    G2_BAR();
    G2_RD(fa0, fb0, lbase, aW, bW, 0); G2_MM(accg, fa1, fb1); G2_SCHED();
    G2_RD(fa1, fb1, lbase, aW, bW, 1); G2_MM(accs, fa0, fb0); G2_SCHED();
  }
  if (!own) {
    char* dst = ws + WS_PART + (size_t)blockIdx.x * 32768 + wm * 16384 + l * 16;
    asm volatile("s_nop 7" ::: "memory");
#pragma unroll
    for (int m = 0; m < 2; ++m)
#pragma unroll
      for (int p = 0; p < 2; ++p)
#pragma unroll
        for (int i = 0; i < 4; ++i)
          store16_dev(dst + (m * 2 + p) * 4096 + i * 1024,
                      (u32x4){__float_as_uint(accg[m][p][4 * i]), __float_as_uint(accg[m][p][4 * i + 1]), __float_as_uint(accg[m][p][4 * i + 2]), __float_as_uint(accg[m][p][4 * i + 3])});
  }
  SBAR();
  STAMP(3, 5);
  for (int i = 28; i < 43; ++i) {
    if (i == 40) STAMP(3, 6);
    const LAS char* nb = lbase + ((i + 1) & 3) * G2_SLOT;
    G2_BAR();
    G2_RD(fa0, fb0, nb, aW, bW, 0); G2_MM(accs, fa1, fb1); G2_SCHED();
    G2_RD(fa1, fb1, nb, aW, bW, 1); G2_MM(accs, fa0, fb0); G2_SCHED();
    if (i == 33 && !own) { publish(flags + (FL_P + (blockIdx.x ^ 1)) * 32, l); SBAR(); }
  }
  G2_MM(accs, fa1, fb1);
  STAMP(3, 2);

  {
    LAS char* xw = (LAS char*)smem + w * 16384 + l * 16;
#pragma unroll
    for (int p = 0; p < 2; ++p) {
      const f32x16 s = sel16(wn, accs[0][p], accs[1][p]);
#pragma unroll
      for (int i = 0; i < 4; ++i) *(LAS f32x4*)(xw + p * 4096 + i * 1024) = (f32x4){s[4 * i], s[4 * i + 1], s[4 * i + 2], s[4 * i + 3]};
    }
    if (own) {
#pragma unroll
      for (int p = 0; p < 2; ++p) {
        const f32x16 g = sel16(wn, accg[0][p], accg[1][p]);
#pragma unroll
        for (int i = 0; i < 4; ++i) *(LAS f32x4*)(xw + 8192 + p * 4096 + i * 1024) = (f32x4){g[4 * i], g[4 * i + 1], g[4 * i + 2], g[4 * i + 3]};
      }
    }
  }
  G2_BAR();
  STAMP(3, 7);
  {
    const LAS char* xr = (const LAS char*)smem + (w ^ 2) * 16384 + l * 16;
    const LAS char* xp = (const LAS char*)smem + G2_PART + wm * 16384 + wn * 8192 + l * 16;
    f32x16 gf[2];
    if (own) {
      gf[0] = sel16(wn, accg[1][0], accg[0][0]); gf[1] = sel16(wn, accg[1][1], accg[0][1]);
    } else {
#pragma unroll
      for (int p = 0; p < 2; ++p)
#pragma unroll
        for (int i = 0; i < 4; ++i) {
          const f32x4 t = *(const LAS f32x4*)(xr + 8192 + p * 4096 + i * 1024);
          gf[p][4 * i] = t[0]; gf[p][4 * i + 1] = t[1]; gf[p][4 * i + 2] = t[2]; gf[p][4 * i + 3] = t[3];
        }
    }
#pragma unroll
    for (int p = 0; p < 2; ++p) {
      const f32x16 s = sel16(wn, accs[1][p], accs[0][p]);
      unsigned pk[8];
#pragma unroll
      for (int k = 0; k < 2; ++k) {
        u32x4 xx = xv[p * 2 + k];
        { auto r0 = __builtin_amdgcn_permlane32_swap(xx[0], xx[2], false, false); xx[0] = r0[0]; xx[2] = r0[1];
          auto r1 = __builtin_amdgcn_permlane32_swap(xx[1], xx[3], false, false); xx[1] = r1[0]; xx[3] = r1[1]; }
#pragma unroll
        for (int ii = 0; ii < 2; ++ii) {
          const int i = 2 * k + ii;
          const f32x4 ts = *(const LAS f32x4*)(xr + p * 4096 + i * 1024);
          const f32x4 tp = *(const LAS f32x4*)(xp + p * 4096 + i * 1024);
          float r[4];
#pragma unroll
          for (int j = 0; j < 4; ++j) {
            const unsigned xw_ = xx[2 * ii + (j >> 1)];
            const float xf = (j & 1) ? bf_hi(xw_) : bf_lo(xw_);
            const unsigned bw_ = bv[p * 4 + i][j >> 1];
            const float z = gf[p][4 * i + j] + tp[j] + ((j & 1) ? bf_hi(bw_) : bf_lo(bw_));
            const float sg = z * __builtin_amdgcn_rcpf(1.0f + __builtin_amdgcn_exp2f(-1.4426950408889634f * z));
            r[j] = xf + (s[4 * i + j] + ts[j]) * sg;
          }
          pk[4 * k + 2 * ii] = cvtpk(r[0], r[1]); pk[4 * k + 2 * ii + 1] = cvtpk(r[2], r[3]);
        }
        auto r0 = __builtin_amdgcn_permlane32_swap(pk[4 * k], pk[4 * k + 2], false, false);
        auto r1 = __builtin_amdgcn_permlane32_swap(pk[4 * k + 1], pk[4 * k + 3], false, false);
        *(u32x4*)(out + (size_t)row * DM + col0 + p * 32 + 16 * k + 8 * h) = (u32x4){r0[0], r1[0], r0[1], r1[1]};
      }
    }
  }
  STAMP(3, 3);
#undef G2_RD
#undef G2_MM
#undef G2_SCHED
#undef G2_BAR
}

__global__ __launch_bounds__(512, 2) void main_kernel(const u16* __restrict__ x, const u16* __restrict__ wq, const u16* __restrict__ wk,
                                                      const u16* __restrict__ wv, const u16* __restrict__ wo, const u16* __restrict__ w1,
                                                      const u16* __restrict__ b1, const u16* __restrict__ w2, const u16* __restrict__ b2,
                                                      char* __restrict__ ws, u16* __restrict__ out) {
  extern __shared__ __attribute__((aligned(16))) char smem[];
  const int l = threadIdx.x & 63, w = rfl(threadIdx.x >> 6);
  const unsigned lds0 = (unsigned)(size_t)(LAS char*)smem;
  const int xcd = blockIdx.x & 7, idx = blockIdx.x >> 3;
  const int mtile = (xcd & 1) * 4 + (idx & 3), cg = (idx >> 2) * 4 + (xcd >> 1);
  unsigned* flags = (unsigned*)(ws + WS_FLAG);
  if (w >= 4) {
    if (cg > 8) g1_load<G1_FFN>(ws, w1, nullptr, cg, mtile, lds0, w - 4, l);
    else if (cg < 8) g1_load<G1_Q>(ws, wq, nullptr, cg, mtile, lds0, w - 4, l);
    else g1_load<G1_KV>(ws, wk, wv, cg, mtile, lds0, w - 4, l);
    attn_load(ws, w2, wo, lds0, w - 4, l);
    g2_load(w2, wo, ws, lds0, w - 4, l);
  } else {
    unsigned* late = nullptr;
    if (cg > 8) { g1_compute<G1_FFN>(ws, b1, cg, mtile, smem, w, l); late = flags + (FL_H + mtile) * 32; }
    else if (cg < 8) { g1_compute<G1_Q>(ws, b1, cg, mtile, smem, w, l); publish(flags + (FL_Q + mtile * 8 + cg) * 32, l); }
    else { g1_compute<G1_KV>(ws, b1, cg, mtile, smem, w, l); publish(flags + FL_KV * 32, l); }
    attn_compute(ws, smem, w, l, late);
    g2_compute(x, b2, ws, out, smem, w, l);
  }
}

extern "C" void kernel_launch(void* const* d_in, const int* in_sizes, int n_in, void* d_out, int out_size, void* d_ws,
                              size_t ws_size, hipStream_t stream) {
  const u16* x = (const u16*)d_in[0];
  const u16* wq = (const u16*)d_in[1];
  const u16* wk = (const u16*)d_in[2];
  const u16* wv = (const u16*)d_in[3];
  const u16* wo = (const u16*)d_in[4];
  const u16* w1 = (const u16*)d_in[6];
  const u16* b1 = (const u16*)d_in[7];
  const u16* w2 = (const u16*)d_in[8];
  const u16* b2 = (const u16*)d_in[9];
  char* ws = (char*)d_ws;
  (void)hipFuncSetAttribute((const void*)main_kernel, hipFuncAttributeMaxDynamicSharedMemorySize, MAIN_LDS);
  ln_kernel<<<256, 256, 0, stream>>>(x, ws);
  main_kernel<<<256, 512, MAIN_LDS, stream>>>(x, wq, wk, wv, wo, w1, b1, w2, b2, ws, (u16*)d_out);
}
```

```cpp
#include <hip/hip_runtime.h>
#include <hip/hip_bf16.h>

typedef short bf16x8 __attribute__((ext_vector_type(8)));
typedef short s16x4 __attribute__((ext_vector_type(4)));
typedef float f32x16 __attribute__((ext_vector_type(16)));
typedef float f32x4 __attribute__((ext_vector_type(4)));
typedef float f32x2 __attribute__((ext_vector_type(2)));
typedef __bf16 bf16x2_t __attribute__((ext_vector_type(2)));
typedef unsigned u32x4 __attribute__((ext_vector_type(4)));
typedef unsigned u32x2 __attribute__((ext_vector_type(2)));
typedef unsigned short u16;
#define LAS __attribute__((address_space(3)))
#define DI __device__ __forceinline__
#define SBAR() __builtin_amdgcn_sched_barrier(0)
#define PIN(x) asm volatile("" : "+v"(x))
#define MFMA32(a, b, c) __builtin_amdgcn_mfma_f32_32x32x16_bf16((a), (b), (c), 0, 0, 0)
#define MFMA16(a, b, c) __builtin_amdgcn_mfma_f32_16x16x32_bf16((a), (b), (c), 0, 0, 0)

constexpr int SEQ = 2048, DM = 1024, NH = 32, FF = 3584;
constexpr size_t KT = 262144;
constexpr size_t WS_STAT = 0;
constexpr size_t WS_Q = 4u << 20;
constexpr size_t WS_KV = 8u << 20;
constexpr size_t WS_O = 9u << 20;
constexpr size_t WS_H = 13u << 20;
constexpr size_t WS_PART = 28u << 20;
constexpr size_t WS_FLAG = 37u << 20;
constexpr int FL_P = 0, FL_O = 256, FL_H = 272, FL_Q = 280, FL_KV = 344, FL_S = 345, FL_N = 353;
constexpr size_t WS_HSA = WS_FLAG + 65536, WS_HSB = WS_HSA + 4096, WS_HSC = WS_HSB + 4096;
constexpr int COORD_BLOCK = 64;

#define STAMP(kid, idx) do {} while (0)
DI unsigned rfl(unsigned x) { return __builtin_amdgcn_readfirstlane(x); }
DI unsigned cvtpk(float lo, float hi) {
  f32x2 v = {lo, hi};
  bf16x2_t b = __builtin_convertvector(v, bf16x2_t);
  return __builtin_bit_cast(unsigned, b);
}
DI float fadd_s(float a, float b) { float r; asm("v_add_f32_e32 %0, %1, %2" : "=v"(r) : "v"(a), "v"(b)); return r; }
DI float bf_lo(unsigned u) { return __uint_as_float(u << 16); }
DI float bf_hi(unsigned u) { return __uint_as_float(u & 0xffff0000u); }
DI float bf_round(float x) { return bf_lo(cvtpk(x, x)); }

template <int IMM>
DI void glds16(unsigned voff, const void* sbase, unsigned ldsaddr) {
  asm volatile("s_mov_b32 m0, %2\n\ts_nop 0\n\tglobal_load_lds_dwordx4 %0, %1 offset:%3"
               :: "v"(voff), "s"(sbase), "s"(ldsaddr), "i"(IMM) : "memory");
}
template <int IMM>
DI void glds16_dev(unsigned voff, const void* sbase, unsigned ldsaddr) {
  asm volatile("s_mov_b32 m0, %2\n\ts_nop 0\n\tglobal_load_lds_dwordx4 %0, %1 offset:%3 sc1"
               :: "v"(voff), "s"(sbase), "s"(ldsaddr), "i"(IMM) : "memory");
}
DI void store16_dev(void* p, u32x4 v) {
  asm volatile("global_store_dwordx4 %0, %1, off sc1\n\ts_nop 1" :: "v"(p), "v"(v) : "memory");
}
DI void l2_touch(const void* p) { asm volatile("global_load_dword v255, %0, off" :: "v"(p) : "v255", "memory"); }
typedef __attribute__((address_space(1))) unsigned gu32;
#define RLX_AGENT __ATOMIC_RELAXED, __HIP_MEMORY_SCOPE_AGENT
DI void wait_ge(const unsigned* word, unsigned want) {
  for (unsigned spins = 0; spins < (1u << 22); ++spins) {
    if (rfl(__hip_atomic_load((gu32*)word, RLX_AGENT)) >= want) break;
    __builtin_amdgcn_s_sleep(2);
  }
  __builtin_amdgcn_fence(__ATOMIC_ACQUIRE, "wavefront");
}
DI void publish(unsigned* word, int l) {
  asm volatile("s_waitcnt vmcnt(0)" ::: "memory");
  if (l == 0) __hip_atomic_fetch_add((gu32*)word, 1u, RLX_AGENT);
}
DI bf16x8 load16_dev(const char* p) {
  typedef __attribute__((address_space(1))) unsigned long long gu64;
  const unsigned long long a = __hip_atomic_load((gu64*)p, RLX_AGENT), b = __hip_atomic_load((gu64*)p + 1, RLX_AGENT);
  return __builtin_bit_cast(bf16x8, (u32x4){(unsigned)a, (unsigned)(a >> 32), (unsigned)b, (unsigned)(b >> 32)});
}
DI float relu_s(float a) { float r; asm("v_max_f32_e32 %0, 0, %1" : "=v"(r) : "v"(a)); return r; }
DI bf16x8 lds_b128(const LAS char* p) { return *(const LAS bf16x8*)p; }
DI s16x4 lds_tr(const LAS char* p) {
  return __builtin_amdgcn_ds_read_tr16_b64_v4i16((LAS s16x4*)p);
}
DI f32x16 sel16(bool c, const f32x16& a, const f32x16& b) {
  f32x16 r;
#pragma unroll
  for (int i = 0; i < 16; ++i) r[i] = c ? a[i] : b[i];
  return r;
}
DI bf16x8 cat8(s16x4 lo, s16x4 hi) { return __builtin_shufflevector(lo, hi, 0, 1, 2, 3, 4, 5, 6, 7); }

DI void row_stats(const u32x4 (&v)[4], char* __restrict__ ws, int row, int l) {
  float s = 0.f;
#pragma unroll
  for (int i = 0; i < 4; ++i)
#pragma unroll
    for (int j = 0; j < 4; ++j) s += bf_lo(v[i][j]) + bf_hi(v[i][j]);
#pragma unroll
  for (int m = 1; m < 32; m <<= 1) s += __shfl_xor(s, m);
  const float mean = s * (1.0f / DM);
  float q = 0.f;
#pragma unroll
  for (int i = 0; i < 4; ++i)
#pragma unroll
    for (int j = 0; j < 4; ++j) {
      const float a = bf_lo(v[i][j]) - mean, b = bf_hi(v[i][j]) - mean;
      q += a * a + b * b;
    }
#pragma unroll
  for (int m = 1; m < 32; m <<= 1) q += __shfl_xor(q, m);
  const float den = __builtin_sqrtf(bf_round(bf_round(q * (1.0f / DM)) + 1e-5f));
  if ((l & 31) == 0) __hip_atomic_store((gu32*)(ws + WS_STAT) + row, cvtpk(-mean, den), RLX_AGENT);
}

constexpr int G1_STAGE = 53248, G1_BOFF = 32768;
enum { G1_FFN = 5, G1_Q = 4, G1_KV = 2 };

constexpr int G1_ST = 103424;
constexpr int G1_HS = 159744;
constexpr int G1_CS = 98304;
#define KROT 0
DI float pair_sum(unsigned v) { return fadd_s(bf_lo(v), bf_hi(v)); }

template <int NP>
DI void g1_load(char* ws, const u16* x, const u16* wsrc, const u16* wv, int cg, int mtile, char* smem, int lw, int l) {
  constexpr int LD = (NP == G1_FFN) ? FF : (NP == G1_Q) ? DM : 32;
  const unsigned lds0 = (unsigned)(size_t)(LAS char*)smem;
  const char* abase = (const char*)(x + (size_t)(mtile * 256 + lw * 64) * DM);
  const unsigned ave = (unsigned)((l >> 3) * 2048 + (((l & 7) ^ (l >> 4)) << 4)), avo = ave ^ 64u;
  const unsigned bvoff = (unsigned)((lw * 16 + (l >> 2)) * LD * 2 + (l & 3) * 16);
  const int krot = (mtile & 3) * KROT;
  const char* bbase[NP];
#pragma unroll
  for (int p = 0; p < NP; ++p) {
    if (NP == G1_KV) bbase[p] = (const char*)(p ? wv : wsrc);
    else if (NP == G1_Q) bbase[p] = (const char*)(wsrc + (cg * 4 + p) * 32);
    else { const int cb = (cg - 9) * 5 + p; bbase[p] = (const char*)(wsrc + (cb > 111 ? 111 : cb) * 32); }
  }
  float cs[NP];
#pragma unroll
  for (int p = 0; p < NP; ++p) cs[p] = 0.f;
  const LAS char* cb0 = (const LAS char*)smem + G1_BOFF + lw * 1024 + (8 * (l >> 5) + ((l & 15) >> 2)) * 64 + ((l >> 4) & 1) * 32 + (l & 3) * 8;
  u32x2 t0[NP], t1[NP];
  auto cs_read = [&](int slot) {
#pragma unroll
    for (int p = 0; p < NP; ++p) {
      t0[p] = __builtin_bit_cast(u32x2, lds_tr(cb0 + slot * G1_STAGE + p * 4096));
      t1[p] = __builtin_bit_cast(u32x2, lds_tr(cb0 + slot * G1_STAGE + p * 4096 + 256));
    }
    SBAR();
  };
  auto cs_add = [&](int p) {
    cs[p] = fadd_s(cs[p], fadd_s(fadd_s(pair_sum(t0[p][0]), pair_sum(t0[p][1])), fadd_s(pair_sum(t1[p][0]), pair_sum(t1[p][1]))));
    PIN(cs[p]); SBAR();
  };
  auto issue = [&](int st, int slot, bool sum) {
    const int kt = (st + krot) & 15;
    const unsigned sa = lds0 + slot * G1_STAGE + lw * 8192;
    const char* a0 = abase + kt * 128;
    const unsigned sb = lds0 + slot * G1_STAGE + G1_BOFF + lw * 1024;
#pragma unroll
    for (int p = 0; p < NP; ++p) glds16<0>(bvoff, bbase[p] + (size_t)kt * (64 * LD * 2), sb + p * 4096);
#pragma unroll
    for (int i = 0; i < 8; ++i) {
      if (sum && i < NP) { SBAR(); cs_add(i); }
      glds16<0>((i & 1) ? avo : ave, a0 + i * 16384, sa + i * 1024);
    }
  };
  auto colsum = [&](int slot) {
    cs_read(slot);
#pragma unroll
    for (int p = 0; p < NP; ++p) cs_add(p);
  };
  unsigned* flags = (unsigned*)(ws + WS_FLAG);
  const bool coord = (NP == G1_KV) && blockIdx.x == COORD_BLOCK && lw == 1;
  if (coord) {
#pragma unroll
    for (int i = 0; i < 6; ++i) if (i * 64 + l < FL_N) __hip_atomic_store((gu32*)flags + (i * 64 + l) * 32, 0u, RLX_AGENT);
    store16_dev(ws + WS_HSC + l * 16, (u32x4){0u, 0u, 0u, 0u});
  }
  issue(0, 0, false); issue(1, 1, false); issue(2, 2, false);
  asm volatile("s_waitcnt vmcnt(%0)\n\ts_barrier" :: "n"(2 * (8 + NP)) : "memory");
  colsum(0);
  int state = (lw == 0 || coord) ? 0 : 2;
  bool pend = false;
  const unsigned found = rfl(*(const LAS unsigned*)((const LAS char*)smem + G1_HS));
  auto poll_issue = [&]() {
    if (NP == G1_KV && coord) {
      glds16_dev<0>(l * 16, ws + WS_HSA, lds0 + G1_HS + 1024); glds16_dev<0>(l * 16, ws + WS_HSC, lds0 + G1_HS + 2048);
    } else {
      const char* a_ = state == 0 ? ws + WS_HSB + blockIdx.x * 4 : (const char*)(flags + (FL_S + mtile) * 32);
      asm volatile("s_mov_b32 m0, %2\n\ts_nop 0\n\tglobal_load_lds_dword %0, %1 sc1" :: "v"(0u), "s"(a_), "s"(lds0 + G1_HS + 256) : "memory");
    }
  };
  auto poll_check = [&]() {
    if (NP == G1_KV && coord) {
      const u32x4 pa = *(const LAS u32x4*)((const LAS char*)smem + G1_HS + 1024 + l * 16), pc = *(const LAS u32x4*)((const LAS char*)smem + G1_HS + 2048 + l * 16);
      store16_dev(ws + WS_HSB + l * 16, pa);
      if (__all((pc[0] & pc[1] & pc[2] & pc[3]) == 1u)) state = 2;
    } else {
      const unsigned v = rfl(*(const LAS unsigned*)((const LAS char*)smem + G1_HS + 256));
      if (state == 0) {
        if (v != found) {
          if (l == 0) {
            __hip_atomic_store((gu32*)(ws + WS_HSC) + blockIdx.x, 1u, RLX_AGENT);
            __hip_atomic_fetch_add((gu32*)flags + (FL_S + mtile) * 32, 1u, RLX_AGENT);
          }
          state = 1;
        }
      } else if (v >= 32u) state = 2;
    }
    state = rfl(state);
  };
  int slot = 0;
  for (int i = 0; i < 13; ++i) {
    asm volatile("s_waitcnt vmcnt(%0)\n\ts_barrier" :: "n"(8 + NP) : "memory");
    if (state < 2) {
      if (pend) poll_check();
      pend = state < 2;
      if (pend) poll_issue();
    }
    const int nslot = (slot == 2) ? 0 : slot + 1;
    cs_read(nslot);
    issue(i + 3, slot, true);
    slot = nslot;
  }
  for (unsigned spins = 0; state < 2 && spins < (1u << 22); ++spins) {
    if (!pend) poll_issue();
    asm volatile("s_waitcnt vmcnt(0)" ::: "memory");
    poll_check();
    pend = false;
    __builtin_amdgcn_s_sleep(1);
  }
  asm volatile("s_waitcnt vmcnt(%0)\n\ts_barrier" :: "n"(8 + NP) : "memory");
  if (lw == 0) glds16_dev<0>(l * 16, ws + WS_STAT + mtile * 1024, lds0 + G1_ST);
  colsum(2);
  asm volatile("s_waitcnt vmcnt(0)" ::: "memory");
  colsum(0);
#pragma unroll
  for (int p = 0; p < NP; ++p) *(LAS float*)((LAS char*)smem + G1_CS + p * 1024 + (l & 31) * 32 + (lw * 2 + (l >> 5)) * 4) = cs[p];
  asm volatile("s_waitcnt lgkmcnt(0)\n\ts_barrier" ::: "memory");
}

template <int NP>
DI void g1_compute(char* __restrict__ ws, const u16* __restrict__ x, const u16* __restrict__ b1, int cg, int mtile, char* smem, int w, int l) {
  STAMP(1, 0);
  const int h = l >> 5;
  {
    const int row = mtile * 256 + cg * 8 + w * 2 + h;
    const unsigned found = __hip_atomic_load((gu32*)(ws + WS_HSB) + blockIdx.x, RLX_AGENT);
    u32x4 v[4];
#pragma unroll
    for (int i = 0; i < 4; ++i) v[i] = *(const u32x4*)(x + (size_t)row * DM + (i * 32 + (l & 31)) * 8);
    SBAR();
    if (w == 0 && l == 0) {
      __hip_atomic_store((gu32*)(ws + WS_HSA) + blockIdx.x, found + 1u, RLX_AGENT);
      *(LAS unsigned*)((LAS char*)smem + G1_HS) = found;
    }
    row_stats(v, ws, row, l);
  }
  f32x16 acc[2][NP];
#pragma unroll
  for (int p = 0; p < NP; ++p) { acc[0][p] = f32x16{}; acc[1][p] = f32x16{}; }
  unsigned st[2], bias[NP];
#pragma unroll
  for (int p = 0; p < NP; ++p) {
    int cb = (cg - 9) * 5 + p; cb = cb > 111 ? 111 : cb;
    bias[p] = (NP == G1_FFN) ? (unsigned)b1[cb * 32 + (l & 31)] : 0u;
  }
  const LAS char* lbase = (const LAS char*)smem;
  int aoff[4];
#pragma unroll
  for (int ks = 0; ks < 4; ++ks) aoff[ks] = (w * 64 + (l & 31)) * 128 + (((2 * ks + h) ^ ((l >> 1) & 7)) << 4);
  const int boff = G1_BOFF + (8 * h + ((l & 15) >> 2)) * 64 + ((l >> 4) & 1) * 32 + (l & 3) * 8;
  bf16x8 fa0[2], fb0[NP], fa1[2], fb1[NP];
#define G1_RD(FA, FB, base, ks) do { \
    FA[0] = lds_b128((base) + aoff[ks]); FA[1] = lds_b128((base) + aoff[ks] + 4096); \
    _Pragma("unroll") for (int p_ = 0; p_ < NP; ++p_) \
      FB[p_] = cat8(lds_tr((base) + boff + p_ * 4096 + (ks) * 1024), lds_tr((base) + boff + p_ * 4096 + (ks) * 1024 + 256)); \
  } while (0)
#define G1_MM(FA, FB) do { \
    _Pragma("unroll") for (int p_ = 0; p_ < NP; ++p_) { \
      if (NP == G1_KV && p_ == 1) { acc[0][p_] = MFMA32(FA[0], FB[p_], acc[0][p_]); acc[1][p_] = MFMA32(FA[1], FB[p_], acc[1][p_]); } \
      else { acc[0][p_] = MFMA32(FB[p_], FA[0], acc[0][p_]); acc[1][p_] = MFMA32(FB[p_], FA[1], acc[1][p_]); } } \
  } while (0)
#define G1_SCHED() do { \
    _Pragma("unroll") for (int q_ = 0; q_ < NP + 1; ++q_) { \
      __builtin_amdgcn_sched_group_barrier(0x008, 1, 0); __builtin_amdgcn_sched_group_barrier(0x100, 2, 0); } \
    __builtin_amdgcn_sched_group_barrier(0x008, NP - 1, 0); SBAR(); \
  } while (0)

  asm volatile("s_waitcnt lgkmcnt(0)\n\ts_barrier" ::: "memory");
  STAMP(1, 1);
  G1_RD(fa0, fb0, lbase, 0); SBAR();
  G1_RD(fa1, fb1, lbase, 1); G1_MM(fa0, fb0); G1_SCHED();
  G1_RD(fa0, fb0, lbase, 2); G1_MM(fa1, fb1); G1_SCHED();
  G1_RD(fa1, fb1, lbase, 3); G1_MM(fa0, fb0); G1_SCHED();
  int slot = 1;
  for (int i = 0; i < 15; ++i) {
    const LAS char* nb = lbase + slot * G1_STAGE;
    asm volatile("s_waitcnt vmcnt(0) lgkmcnt(0)\n\ts_barrier" ::: "memory"); SBAR();
    G1_RD(fa0, fb0, nb, 0); G1_MM(fa1, fb1); G1_SCHED();
    G1_RD(fa1, fb1, nb, 1); G1_MM(fa0, fb0); G1_SCHED();
    G1_RD(fa0, fb0, nb, 2); G1_MM(fa1, fb1); G1_SCHED();
    G1_RD(fa1, fb1, nb, 3); G1_MM(fa0, fb0); G1_SCHED();
    slot = (slot == 2) ? 0 : slot + 1;
  }
  STAMP(1, 4);
  {
    f32x4 t[NP][2];
#pragma unroll
    for (int m = 0; m < 2; ++m) st[m] = *(const LAS unsigned*)(lbase + G1_ST + (w * 64 + m * 32 + (l & 31)) * 4);
#pragma unroll
    for (int p = 0; p < NP; ++p) {
      t[p][0] = *(const LAS f32x4*)(lbase + G1_CS + p * 1024 + (l & 31) * 32);
      t[p][1] = *(const LAS f32x4*)(lbase + G1_CS + p * 1024 + (l & 31) * 32 + 16);
    }
    SBAR();
    G1_MM(fa1, fb1);
    const unsigned msk = h ? 0u : ~0u;
#pragma unroll
    for (int m = 0; m < 2; ++m) {
      const unsigned nm = st[m] & 0xffffu;
      fa0[m] = __builtin_bit_cast(bf16x8, (u32x4){(nm | (nm << 16)) & msk, st[m] & msk, 0u, 0u});
    }
#pragma unroll
    for (int p = 0; p < NP; ++p) {
      const f32x4 u = t[p][0] + t[p][1];
      const float c = (u[0] + u[1]) + (u[2] + u[3]);
      const unsigned c0 = cvtpk(c, c) & 0xffffu; const float r1 = c - bf_lo(c0);
      const unsigned c1 = cvtpk(r1, r1) & 0xffffu; const float r2 = r1 - bf_lo(c1);
      const unsigned c2 = cvtpk(r2, r2) & 0xffffu;
      fb0[p] = __builtin_bit_cast(bf16x8, (u32x4){c0 | (c1 << 16), c2 | (bias[p] << 16), 0u, 0u});
    }
  }
  G1_MM(fa0, fb0);
  float rden[2];
#pragma unroll
  for (int m = 0; m < 2; ++m) rden[m] = 1.0f / bf_hi(st[m]);
  asm volatile("s_nop 15\n\ts_nop 3" ::: "memory"); SBAR();
  STAMP(1, 2);

#pragma unroll
  for (int p = 0; p < NP; ++p) {
    const int cb = (NP == G1_FFN) ? (cg - 9) * 5 + p : cg * 4 + p;
    if (NP != G1_FFN || cb < 112) {
#pragma unroll
      for (int m = 0; m < 2; ++m) {
        const int tt = mtile * 8 + w * 2 + m;
        char* dst = (NP == G1_FFN) ? ws + WS_H + (size_t)(cb >> 1) * KT + (size_t)tt * 4096 + (cb & 1) * 2048 + l * 16
                  : (NP == G1_Q)   ? ws + WS_Q + (size_t)(tt * 32 + cb) * 2048 + l * 16
                                   : ws + WS_KV + (size_t)tt * 4096 + p * 2048 + l * 16;
        const float sc = (NP == G1_Q) ? rden[m] * (0.17677669529663687f * 1.4426950408889634f) : rden[m];
#pragma unroll
        for (int s = 0; s < 2; ++s) {
          u32x4 o;
#pragma unroll
          for (int j = 0; j < 4; ++j) {
            float a = acc[m][p][8 * s + 2 * j], b = acc[m][p][8 * s + 2 * j + 1];
            if (NP == G1_FFN) { a = relu_s(a) * sc; b = relu_s(b) * sc; a *= a; b *= b; }
            else if (NP == G1_KV && p == 1) {
              const int r = 8 * s + 2 * j, tk = (r & 3) + 8 * (r >> 2) + 4 * h;
              a *= __shfl(rden[m], tk); b *= __shfl(rden[m], tk + 1);
            } else { a *= sc; b *= sc; }
            o[j] = cvtpk(a, b);
          }
          store16_dev(dst + s * 1024, o);
        }
      }
    }
  }
  STAMP(1, 3);
#undef G1_RD
#undef G1_MM
#undef G1_SCHED
}

constexpr int AT_NCH = 16;
constexpr int AT_BUF = 16384;
constexpr int AT_BASE = 114688;
constexpr int AT_Q = 65536;
constexpr int MAIN_LDS = 163840;

DI float xhalf_max(float m) {
  auto rr = __builtin_amdgcn_permlane32_swap(__float_as_uint(m), __float_as_uint(m), false, false);
  return __builtin_fmaxf(__uint_as_float(rr[0]), __uint_as_float(rr[1]));
}
DI float xhalf_sum(float m) {
  auto rr = __builtin_amdgcn_permlane32_swap(__float_as_uint(m), __float_as_uint(m), false, false);
  return __uint_as_float(rr[0]) + __uint_as_float(rr[1]);
}
DI float max16(const f32x16& s) {
  float a = __builtin_fmaxf(s[0], s[1]);
#pragma unroll
  for (int r = 2; r < 16; ++r) a = __builtin_fmaxf(a, s[r]);
  return a;
}
DI bf16x8 pack8(const f32x16& p, int s) {
  u32x4 o;
#pragma unroll
  for (int j = 0; j < 4; ++j) o[j] = cvtpk(p[8 * s + 2 * j], p[8 * s + 2 * j + 1]);
  return __builtin_bit_cast(bf16x8, o);
}

DI void attn_fallback(const char* __restrict__ kv, bf16x8 q0, bf16x8 q1, int l, f32x16& o_out, float& l_out) {
  f32x16 o = {};
  float m = -INFINITY, lsum = 0.f;
  for (int t = 0; t < 64; ++t) {
    const char* p = kv + (size_t)t * 4096 + l * 16;
    const bf16x8 k0 = load16_dev(p), k1 = load16_dev(p + 1024), v0 = load16_dev(p + 2048), v1 = load16_dev(p + 3072);
    f32x16 s = {};
    s = MFMA32(k0, q0, s); s = MFMA32(k1, q1, s);
    const float mn = __builtin_fmaxf(m, xhalf_max(max16(s)));
    const float ms = (mn == -INFINITY) ? 0.f : mn;
    const float alpha = __builtin_amdgcn_exp2f(m - ms);
    m = mn;
    float ps = 0.f;
#pragma unroll
    for (int r = 0; r < 16; ++r) { s[r] = __builtin_amdgcn_exp2f(s[r] - ms); ps += s[r]; }
    lsum = lsum * alpha + ps;
#pragma unroll
    for (int r = 0; r < 16; ++r) o[r] *= alpha;
    o = MFMA32(v0, pack8(s, 0), o); o = MFMA32(v1, pack8(s, 1), o);
  }
  o_out = o;
  l_out = xhalf_sum(lsum);
}

constexpr int G2_SLOT = 32768, G2_BOFF = 16384, G2_PART = 131072;
struct G2Ld {
  const char* hb; const char* ob; const char* wb; const char* wb2; unsigned lds; unsigned voff, voffb;
  DI void init(const char* ws, const u16* w2, const u16* wo, unsigned lds0, int lw, int l) {
    const int xcd = blockIdx.x & 7, idx = blockIdx.x >> 3, kh = xcd & 1;
    const int mt = ((xcd >> 1) & 1) * 8 + (idx & 7), nt = (xcd >> 2) * 4 + (idx >> 3);
    hb = ws + WS_H + (size_t)(mt * 4 + lw) * 4096 + (size_t)kh * (28 * KT);
    ob = ws + WS_O + (size_t)(mt * 4 + lw) * 4096;
    wb = (const char*)(w2 + (size_t)(kh * 1792 + lw * 16) * DM + nt * 128);
    wb2 = (const char*)(wo + (size_t)(lw * 16) * DM + nt * 128 + kh * 64);
    voffb = (unsigned)((l >> 2) * DM * 2 + (l & 3) * 16);
    lds = lds0 + lw * 4096; voff = l * 16;
  }
  DI void ffn(int s) const {
    const unsigned d = lds + (s & 3) * G2_SLOT;
    const char* a = hb + (size_t)s * KT;
    glds16_dev<0>(voff, a, d); glds16_dev<1024>(voff, a, d); glds16_dev<2048>(voff, a, d); glds16_dev<3072>(voff, a, d);
    const char* b = wb + (size_t)s * 131072;
    glds16<0>(voffb, b, d + G2_BOFF); glds16<0>(voffb, b + 64, d + G2_BOFF + 1024); glds16<0>(voffb, b + 128, d + G2_BOFF + 2048); glds16<0>(voffb, b + 192, d + G2_BOFF + 3072);
  }
  DI void wo(int s) const {
    const unsigned d = lds + (s & 3) * G2_SLOT;
    const char* a = ob + (size_t)(s - 28) * KT;
    glds16_dev<0>(voff, a, d); glds16_dev<1024>(voff, a, d); glds16_dev<2048>(voff, a, d); glds16_dev<3072>(voff, a, d);
    const char* b = wb2 + (size_t)(s - 28) * 131072;
    glds16<0>(voffb, b, d + G2_BOFF); glds16<0>(voffb, b + 64, d + G2_BOFF + 1024);
  }
};

DI void attn_finish(char* __restrict__ ws, f32x16 o, f32x4 lacc, bf16x8 q0, bf16x8 q1, int l, char* dst) {
  const float l0 = __shfl(lacc[0], l & 15), l1 = __shfl(lacc[1], l & 15);
  float lsum = (l & 16) ? l1 : l0;
  bool ok = (lsum >= 9.5367431640625e-07f) && (lsum <= 1.8446744073709552e19f);
#pragma unroll
  for (int r = 0; r < 16; ++r) ok = ok && (__builtin_fabsf(o[r]) <= 3.0e38f);
  if (!__all(ok)) attn_fallback(ws + WS_KV, q0, q1, l, o, lsum);
  const float rl = 1.0f / lsum;
#pragma unroll
  for (int s = 0; s < 2; ++s) {
    u32x4 ov;
#pragma unroll
    for (int j = 0; j < 4; ++j) ov[j] = cvtpk(o[8 * s + 2 * j] * rl, o[8 * s + 2 * j + 1] * rl);
    store16_dev(dst + s * 1024, ov);
  }
}

DI void attn_load(char* __restrict__ ws, const u16* w2, const u16* wo, unsigned lds0, const int lw, const int l) {
  const int tt = blockIdx.x >> 2, hg = blockIdx.x & 3;
  unsigned* flags = (unsigned*)(ws + WS_FLAG);
  const unsigned voff = l * 16;
  const char* ksrc = ws + WS_KV + (size_t)lw * 4096;
  const unsigned kdst = lds0 + AT_BASE + lw * 4096;
  auto ld = [&](int c, int slot) {
    const char* s_ = ksrc + (size_t)c * AT_BUF;
    glds16_dev<0>(voff, s_, kdst + slot); glds16_dev<1024>(voff, s_, kdst + slot); glds16_dev<2048>(voff, s_, kdst + slot); glds16_dev<3072>(voff, s_, kdst + slot);
  };
  {
    const unsigned* word = flags + ((l & 1) ? FL_Q + (tt >> 3) * 8 + hg * 2 + (lw >> 1) : FL_KV) * 32;
    const unsigned need = (l & 1) ? 4u : 32u;
    for (unsigned spins = 0; spins < (1u << 22); ++spins) {
      if (__all(__hip_atomic_load((gu32*)word, RLX_AGENT) >= need)) break;
      __builtin_amdgcn_s_sleep(2);
    }
    __builtin_amdgcn_fence(__ATOMIC_ACQUIRE, "wavefront");
    const char* qs = ws + WS_Q + (size_t)(tt * 32 + hg * 8 + lw * 2) * 2048;
    const unsigned qd = lds0 + AT_Q + lw * 4096;
    glds16_dev<0>(voff, qs, qd); glds16_dev<1024>(voff, qs, qd); glds16_dev<2048>(voff, qs, qd); glds16_dev<3072>(voff, qs, qd);
  }
  ld(0, 0); ld(1, AT_BUF); ld(2, 2 * AT_BUF);
  G2Ld g2; g2.init(ws, w2, wo, lds0, lw, l);
  const int xcd = blockIdx.x & 7, idx = blockIdx.x >> 3, gk = xcd & 1;
  const int mt = ((xcd >> 1) & 1) * 8 + (idx & 7), nt = (xcd >> 2) * 4 + (idx >> 3);
  const char* pfw = (const char*)(w2 + (size_t)(gk * 1792 + (mt & 7) * 32 + (l >> 1)) * DM + nt * 128 + (l & 1) * 64);
  const char* pfo = (const char*)(wo + (size_t)((mt & 7) * 128 + l) * DM + nt * 128 + gk * 64);
  const char* pfh = ws + WS_H + (size_t)(gk * 28 + 3 + (l >> 5)) * KT + (size_t)(mt * 4 + (nt & 3)) * 4096 + (l & 31) * 128;
  asm volatile("s_waitcnt vmcnt(8)\n\ts_barrier" ::: "memory");
  int slot = 0, extra = 0;
  for (int c = 0; c < AT_NCH - 1; ++c) {
    if (c >= AT_NCH - 2) asm volatile("s_waitcnt vmcnt(8)\n\ts_barrier" ::: "memory");
    else if (extra == 0) asm volatile("s_waitcnt vmcnt(4)\n\ts_barrier" ::: "memory");
    else if (extra == 1) asm volatile("s_waitcnt vmcnt(5)\n\ts_barrier" ::: "memory");
    else asm volatile("s_waitcnt vmcnt(12)\n\ts_barrier" ::: "memory");
    if (c + 3 < AT_NCH) ld(c + 3, slot);
    slot = (slot == 2 * AT_BUF) ? 0 : slot + AT_BUF;
    extra = 0;
    if (c < 9 && (c & 3) == lw) { l2_touch(c < 7 ? pfw + (size_t)c * (256 * DM * 2) : pfo + (size_t)(c - 7) * (64 * DM * 2)); extra = 1; }
    if (c == 10) { wait_ge(flags + (FL_H + (mt >> 1)) * 32, 92u); l2_touch(pfh + (size_t)lw * (2 * KT)); extra = 1; }
    if (c >= 11 && c < 14) { g2.ffn(c - 11); extra = 8; }
  }
}

DI void attn_compute(char* __restrict__ ws, char* smem, const int w, const int l, unsigned* late_pub) {
  const int tt = blockIdx.x >> 2, hg = blockIdx.x & 3;
  const int head0 = hg * 8 + w * 2;
  STAMP(2, 0);
  bf16x8 sel;
  {
    const short one = ((l & 15) == ((l >> 4) & 1)) ? (short)0x3f80 : (short)0;
#pragma unroll
    for (int j = 0; j < 8; ++j) sel[j] = one;
  }
  f32x16 oa = {}, ob = {}, sa, sb;
  f32x4 la = {}, lbs = {};
  u32x4 pa0, pa1, pb0 = {}, pb1 = {};
  PIN(oa); PIN(ob);
  asm volatile("s_barrier" ::: "memory");
  STAMP(2, 1);

  const LAS char* qp = (const LAS char*)smem + AT_Q + w * 4096 + l * 16;
  const bf16x8 qa0 = lds_b128(qp), qa1 = lds_b128(qp + 1024), qb0 = lds_b128(qp + 2048), qb1 = lds_b128(qp + 3072);
  const LAS char* kvp = (const LAS char*)smem + AT_BASE + l * 16;
  bf16x8 kx0, kx1, vx0, vx1, ky0, ky1, vy0, vy1;
  kx0 = lds_b128(kvp); kx1 = lds_b128(kvp + 1024); vx0 = lds_b128(kvp + 2048); vx1 = lds_b128(kvp + 3072);
  vy0 = vx0; vy1 = vx1;
  f32x16 nma, nmb;
  {
    sa = MFMA32(kx0, qa0, oa); sb = MFMA32(kx0, qb0, ob);
    sa = MFMA32(kx1, qa1, sa); sb = MFMA32(kx1, qb1, sb);
    const float ma = -xhalf_max(max16(sa)), mb = -xhalf_max(max16(sb));
#pragma unroll
    for (int r = 0; r < 16; ++r) { nma[r] = ma; nmb[r] = mb; sa[r] += ma; }
    PIN(nma); PIN(nmb);
  }
#define EX(S, i) S[i] = __builtin_amdgcn_exp2f(S[i])
#define CV(P, j, S, i) P[j] = cvtpk(S[i], S[(i) + 1])
#define B8(x) __builtin_bit_cast(bf16x8, x)
#define LSUM(L, P) L = MFMA16(sel, B8(P), L)
#define PV(O, V, P) O = MFMA32(V, B8(P), O)
#define HALF(S, P0, P1, M0, M1, M2, M3, M4, M5, LD0, LD1) do { \
    M0; SBAR(); LD0; EX(S, 0); EX(S, 1); EX(S, 2); EX(S, 3); PIN(S); SBAR(); \
    M1; SBAR(); LD1; CV(P0, 0, S, 0); CV(P0, 1, S, 2); EX(S, 4); EX(S, 5); EX(S, 6); PIN(S); PIN(P0); SBAR(); \
    M2; SBAR(); EX(S, 7); CV(P0, 2, S, 4); EX(S, 8); CV(P0, 3, S, 6); EX(S, 9); PIN(S); PIN(P0); SBAR(); \
    M3; SBAR(); EX(S, 10); EX(S, 11); PIN(S); SBAR(); \
    M4; SBAR(); CV(P1, 0, S, 8); CV(P1, 1, S, 10); EX(S, 12); EX(S, 13); EX(S, 14); PIN(S); PIN(P1); SBAR(); \
    M5; SBAR(); EX(S, 15); CV(P1, 2, S, 12); CV(P1, 3, S, 14); PIN(P1); SBAR(); \
  } while (0)
#define STEP(KC0, KC1, VC0, VC1, KN0, KN1, VP0, VP1, NP) do { \
    HALF(sa, pa0, pa1, sb = MFMA32(KC0, qb0, nmb), sb = MFMA32(KC1, qb1, sb), PV(ob, VP0, pb0), LSUM(lbs, pb0), \
         PV(ob, VP1, pb1), LSUM(lbs, pb1), KN0 = lds_b128(NP), KN1 = lds_b128((NP) + 1024)); \
    HALF(sb, pb0, pb1, sa = MFMA32(KN0, qa0, nma), sa = MFMA32(KN1, qa1, sa), PV(oa, VC0, pa0), LSUM(la, pa0), \
         PV(oa, VC1, pa1), LSUM(la, pa1), VP0 = lds_b128((NP) + 2048), VP1 = lds_b128((NP) + 3072)); \
  } while (0)
  int s0 = 0;
  for (int c = 0; c < AT_NCH; ++c) {
    const int s1 = (s0 == 2 * AT_BUF) ? 0 : s0 + AT_BUF;
    const LAS char* cb = kvp + s0;
    const LAS char* nb = kvp + s1;
    STEP(kx0, kx1, vx0, vx1, ky0, ky1, vy0, vy1, cb + 4096);
    STEP(ky0, ky1, vy0, vy1, kx0, kx1, vx0, vx1, cb + 2 * 4096);
    STEP(kx0, kx1, vx0, vx1, ky0, ky1, vy0, vy1, cb + 3 * 4096);
    if (c < AT_NCH - 1) asm volatile("s_waitcnt lgkmcnt(0)\n\ts_barrier" ::: "memory");
    if (c == 2 && late_pub) publish(late_pub, l);
    SBAR();
    STEP(ky0, ky1, vy0, vy1, kx0, kx1, vx0, vx1, nb);
    s0 = s1;
  }
  PV(ob, vy0, pb0); LSUM(lbs, pb0);
  PV(ob, vy1, pb1); LSUM(lbs, pb1);
#undef EX
#undef CV
#undef B8
#undef HALF
#undef STEP
#undef LSUM
#undef PV
  STAMP(2, 2);
  char* dst = ws + WS_O + (size_t)(head0 >> 1) * KT + (size_t)tt * 4096 + l * 16;
  attn_finish(ws, oa, la, qa0, qa1, l, dst);
  attn_finish(ws, ob, lbs, qb0, qb1, l, dst + 2048);
  STAMP(2, 3);
}

DI void g2_load(const u16* w2, const u16* wo, char* __restrict__ ws, unsigned lds0, const int lw, const int l) {
  const int xcd = blockIdx.x & 7, idx = blockIdx.x >> 3;
  const int mt = ((xcd >> 1) & 1) * 8 + (idx & 7);
  unsigned* flags = (unsigned*)(ws + WS_FLAG);
  G2Ld g; g.init(ws, w2, wo, lds0, lw, l);
  asm volatile("s_waitcnt vmcnt(16)\n\ts_barrier" ::: "memory");
  g.ffn(3);
  for (int i = 0; i < 24; ++i) {
    asm volatile("s_waitcnt vmcnt(16)\n\ts_barrier" ::: "memory");
    g.ffn(i + 4);
  }
  asm volatile("s_waitcnt vmcnt(16)\n\ts_barrier" ::: "memory");
  wait_ge(flags + (FL_O + mt) * 32, 64u);
  g.wo(28);
  for (int i = 25; i < 40; ++i) {
    asm volatile("s_waitcnt vmcnt(12)\n\ts_barrier" ::: "memory");
    g.wo(i + 4);
  }
  asm volatile("s_waitcnt vmcnt(12)\n\ts_barrier" ::: "memory");
  asm volatile("s_waitcnt vmcnt(6)\n\ts_barrier" ::: "memory");
  wait_ge(flags + (FL_P + blockIdx.x) * 32, 2u);
  {
    const char* src = ws + WS_PART + (size_t)(blockIdx.x ^ 1) * 32768 + lw * 8192;
    const unsigned d = lds0 + G2_PART + lw * 8192;
    glds16_dev<0>(g.voff, src, d); glds16_dev<1024>(g.voff, src, d); glds16_dev<2048>(g.voff, src, d); glds16_dev<3072>(g.voff, src, d);
    glds16_dev<0>(g.voff, src + 4096, d + 4096); glds16_dev<1024>(g.voff, src + 4096, d + 4096); glds16_dev<2048>(g.voff, src + 4096, d + 4096); glds16_dev<3072>(g.voff, src + 4096, d + 4096);
  }
  asm volatile("s_waitcnt vmcnt(8)\n\ts_barrier" ::: "memory");
  asm volatile("s_waitcnt vmcnt(0)\n\ts_barrier" ::: "memory");
}

DI void g2_compute(const u16* __restrict__ x, const u16* __restrict__ b2, char* __restrict__ ws, u16* __restrict__ out,
                   char* smem, const int w, const int l) {
  const int xcd = blockIdx.x & 7, idx = blockIdx.x >> 3;
  const int kh = xcd & 1, mt = ((xcd >> 1) & 1) * 8 + (idx & 7), nt = (xcd >> 2) * 4 + (idx >> 3);
  unsigned* flags = (unsigned*)(ws + WS_FLAG);
  STAMP(3, 0);
  const int h = l >> 5, wm = w & 1, wn = w >> 1;
  const bool own = (wn == kh);
  f32x16 accg[2][2] = {}, accs[2][2] = {};
  const int col0 = nt * 128 + kh * 64;
  const int row = mt * 128 + wm * 64 + wn * 32 + (l & 31);
  u32x4 xv[4];
  u32x2 bv[8];
#pragma unroll
  for (int k = 0; k < 4; ++k) xv[k] = *(const u32x4*)(x + (size_t)row * DM + col0 + 16 * k + 8 * h);
#pragma unroll
  for (int k = 0; k < 8; ++k) bv[k] = *(const u32x2*)(b2 + col0 + 8 * k + 4 * h);
  const LAS char* lbase = (const LAS char*)smem + l * 16;
  const int btr = (4 * h + ((l & 15) >> 2)) * 64 + ((l >> 4) & 1) * 32 + (l & 3) * 8 - l * 16;
#define LDB(p_) cat8(lds_tr(p_), lds_tr((p_) + 512))
#define NRD 3
  const int aF = wm * 8192, bF = G2_BOFF + wn * 2048 + btr;
  const int aW = wm * 8192 + wn * 2048, bW = G2_BOFF + wn * 8192 + btr;
  bf16x8 fa0[2], fb0[2], fa1[2], fb1[2];
#define G2_RD(FA, FB, base, ao, bo, ks) do { \
    FA[0] = lds_b128((base) + (ao) + (ks) * 1024); FB[0] = LDB((base) + (bo) + (ks) * 4096); \
    FA[1] = lds_b128((base) + (ao) + 4096 + (ks) * 1024); FB[1] = LDB((base) + (bo) + (ks) * 4096 + 1024); \
  } while (0)
#define G2_MM(ACC, FA, FB) do { \
    ACC[0][0] = MFMA32(FB[0], FA[0], ACC[0][0]); ACC[1][0] = MFMA32(FB[0], FA[1], ACC[1][0]); \
    ACC[0][1] = MFMA32(FB[1], FA[0], ACC[0][1]); ACC[1][1] = MFMA32(FB[1], FA[1], ACC[1][1]); \
  } while (0)
#define G2_SCHED() do { \
    __builtin_amdgcn_sched_group_barrier(0x008, 1, 0); __builtin_amdgcn_sched_group_barrier(0x100, NRD, 0); \
    __builtin_amdgcn_sched_group_barrier(0x008, 1, 0); __builtin_amdgcn_sched_group_barrier(0x100, NRD, 0); \
    __builtin_amdgcn_sched_group_barrier(0x008, 2, 0); SBAR(); \
  } while (0)
#define G2_BAR() do { asm volatile("s_waitcnt lgkmcnt(0)\n\ts_barrier" ::: "memory"); SBAR(); } while (0)

  G2_BAR();
  STAMP(3, 1);
  G2_RD(fa0, fb0, lbase, aF, bF, 0); SBAR();
  G2_RD(fa1, fb1, lbase, aF, bF, 1); G2_MM(accg, fa0, fb0); G2_SCHED();
  G2_RD(fa0, fb0, lbase, aF, bF, 2); G2_MM(accg, fa1, fb1); G2_SCHED();
  G2_RD(fa1, fb1, lbase, aF, bF, 3); G2_MM(accg, fa0, fb0); G2_SCHED();
  for (int i = 0; i < 27; ++i) {
    const LAS char* nb = lbase + ((i + 1) & 3) * G2_SLOT;
    G2_BAR();
    G2_RD(fa0, fb0, nb, aF, bF, 0); G2_MM(accg, fa1, fb1); G2_SCHED();
    G2_RD(fa1, fb1, nb, aF, bF, 1); G2_MM(accg, fa0, fb0); G2_SCHED();
    G2_RD(fa0, fb0, nb, aF, bF, 2); G2_MM(accg, fa1, fb1); G2_SCHED();
    G2_RD(fa1, fb1, nb, aF, bF, 3); G2_MM(accg, fa0, fb0); G2_SCHED();
    if (i == 3) { publish(flags + (FL_O + (blockIdx.x >> 4)) * 32, l); SBAR(); }
  }
  STAMP(3, 4);
  {
    G2_BAR();
    G2_RD(fa0, fb0, lbase, aW, bW, 0); G2_MM(accg, fa1, fb1); G2_SCHED();
    G2_RD(fa1, fb1, lbase, aW, bW, 1); G2_MM(accs, fa0, fb0); G2_SCHED();
  }
  if (!own) {
    char* dst = ws + WS_PART + (size_t)blockIdx.x * 32768 + wm * 16384 + l * 16;
    asm volatile("s_nop 7" ::: "memory");
#pragma unroll
    for (int m = 0; m < 2; ++m)
#pragma unroll
      for (int p = 0; p < 2; ++p)
#pragma unroll
        for (int i = 0; i < 4; ++i)
          store16_dev(dst + (m * 2 + p) * 4096 + i * 1024,
                      (u32x4){__float_as_uint(accg[m][p][4 * i]), __float_as_uint(accg[m][p][4 * i + 1]), __float_as_uint(accg[m][p][4 * i + 2]), __float_as_uint(accg[m][p][4 * i + 3])});
  }
  SBAR();
  STAMP(3, 5);
  for (int i = 28; i < 43; ++i) {
    if (i == 40) STAMP(3, 6);
    const LAS char* nb = lbase + ((i + 1) & 3) * G2_SLOT;
    G2_BAR();
    G2_RD(fa0, fb0, nb, aW, bW, 0); G2_MM(accs, fa1, fb1); G2_SCHED();
    G2_RD(fa1, fb1, nb, aW, bW, 1); G2_MM(accs, fa0, fb0); G2_SCHED();
    if (i == 33 && !own) { publish(flags + (FL_P + (blockIdx.x ^ 1)) * 32, l); SBAR(); }
  }
  G2_MM(accs, fa1, fb1);
  STAMP(3, 2);

  {
    LAS char* xw = (LAS char*)smem + w * 16384 + l * 16;
#pragma unroll
    for (int p = 0; p < 2; ++p) {
      const f32x16 s = sel16(wn, accs[0][p], accs[1][p]);
#pragma unroll
      for (int i = 0; i < 4; ++i) *(LAS f32x4*)(xw + p * 4096 + i * 1024) = (f32x4){s[4 * i], s[4 * i + 1], s[4 * i + 2], s[4 * i + 3]};
    }
    if (own) {
#pragma unroll
      for (int p = 0; p < 2; ++p) {
        const f32x16 g = sel16(wn, accg[0][p], accg[1][p]);
#pragma unroll
        for (int i = 0; i < 4; ++i) *(LAS f32x4*)(xw + 8192 + p * 4096 + i * 1024) = (f32x4){g[4 * i], g[4 * i + 1], g[4 * i + 2], g[4 * i + 3]};
      }
    }
  }
  G2_BAR();
  STAMP(3, 7);
  {
    const LAS char* xr = (const LAS char*)smem + (w ^ 2) * 16384 + l * 16;
    const LAS char* xp = (const LAS char*)smem + G2_PART + wm * 16384 + wn * 8192 + l * 16;
    f32x16 gf[2];
    if (own) {
      gf[0] = sel16(wn, accg[1][0], accg[0][0]); gf[1] = sel16(wn, accg[1][1], accg[0][1]);
    } else {
#pragma unroll
      for (int p = 0; p < 2; ++p)
#pragma unroll
        for (int i = 0; i < 4; ++i) {
          const f32x4 t = *(const LAS f32x4*)(xr + 8192 + p * 4096 + i * 1024);
          gf[p][4 * i] = t[0]; gf[p][4 * i + 1] = t[1]; gf[p][4 * i + 2] = t[2]; gf[p][4 * i + 3] = t[3];
        }
    }
#pragma unroll
    for (int p = 0; p < 2; ++p) {
      const f32x16 s = sel16(wn, accs[1][p], accs[0][p]);
      unsigned pk[8];
#pragma unroll
      for (int k = 0; k < 2; ++k) {
        u32x4 xx = xv[p * 2 + k];
        { auto r0 = __builtin_amdgcn_permlane32_swap(xx[0], xx[2], false, false); xx[0] = r0[0]; xx[2] = r0[1];
          auto r1 = __builtin_amdgcn_permlane32_swap(xx[1], xx[3], false, false); xx[1] = r1[0]; xx[3] = r1[1]; }
#pragma unroll
        for (int ii = 0; ii < 2; ++ii) {
          const int i = 2 * k + ii;
          const f32x4 ts = *(const LAS f32x4*)(xr + p * 4096 + i * 1024);
          const f32x4 tp = *(const LAS f32x4*)(xp + p * 4096 + i * 1024);
          float r[4];
#pragma unroll
          for (int j = 0; j < 4; ++j) {
            const unsigned xw_ = xx[2 * ii + (j >> 1)];
            const float xf = (j & 1) ? bf_hi(xw_) : bf_lo(xw_);
            const unsigned bw_ = bv[p * 4 + i][j >> 1];
            const float z = gf[p][4 * i + j] + tp[j] + ((j & 1) ? bf_hi(bw_) : bf_lo(bw_));
            const float sg = z * __builtin_amdgcn_rcpf(1.0f + __builtin_amdgcn_exp2f(-1.4426950408889634f * z));
            r[j] = xf + (s[4 * i + j] + ts[j]) * sg;
          }
          pk[4 * k + 2 * ii] = cvtpk(r[0], r[1]); pk[4 * k + 2 * ii + 1] = cvtpk(r[2], r[3]);
        }
        auto r0 = __builtin_amdgcn_permlane32_swap(pk[4 * k], pk[4 * k + 2], false, false);
        auto r1 = __builtin_amdgcn_permlane32_swap(pk[4 * k + 1], pk[4 * k + 3], false, false);
        *(u32x4*)(out + (size_t)row * DM + col0 + p * 32 + 16 * k + 8 * h) = (u32x4){r0[0], r1[0], r0[1], r1[1]};
      }
    }
  }
  STAMP(3, 3);
#undef G2_RD
#undef G2_MM
#undef G2_SCHED
#undef G2_BAR
}

__global__ __launch_bounds__(512, 2) void main_kernel(const u16* __restrict__ x, const u16* __restrict__ wq, const u16* __restrict__ wk,
                                                      const u16* __restrict__ wv, const u16* __restrict__ wo, const u16* __restrict__ w1,
                                                      const u16* __restrict__ b1, const u16* __restrict__ w2, const u16* __restrict__ b2,
                                                      char* __restrict__ ws, u16* __restrict__ out) {
  extern __shared__ __attribute__((aligned(16))) char smem[];
  const int l = threadIdx.x & 63, w = rfl(threadIdx.x >> 6);
  const unsigned lds0 = (unsigned)(size_t)(LAS char*)smem;
  const int xcd = blockIdx.x & 7, idx = blockIdx.x >> 3;
  const int mtile = (xcd & 1) * 4 + (idx & 3), cg = (idx >> 2) * 4 + (xcd >> 1);
  unsigned* flags = (unsigned*)(ws + WS_FLAG);
  if (w >= 4) {
    if (cg > 8) g1_load<G1_FFN>(ws, x, w1, nullptr, cg, mtile, smem, w - 4, l);
    else if (cg < 8) g1_load<G1_Q>(ws, x, wq, nullptr, cg, mtile, smem, w - 4, l);
    else g1_load<G1_KV>(ws, x, wk, wv, cg, mtile, smem, w - 4, l);
    attn_load(ws, w2, wo, lds0, w - 4, l);
    g2_load(w2, wo, ws, lds0, w - 4, l);
  } else {
    unsigned* late = nullptr;
    if (cg > 8) { g1_compute<G1_FFN>(ws, x, b1, cg, mtile, smem, w, l); late = flags + (FL_H + mtile) * 32; }
    else if (cg < 8) { g1_compute<G1_Q>(ws, x, b1, cg, mtile, smem, w, l); publish(flags + (FL_Q + mtile * 8 + cg) * 32, l); }
    else { g1_compute<G1_KV>(ws, x, b1, cg, mtile, smem, w, l); publish(flags + FL_KV * 32, l); }
    attn_compute(ws, smem, w, l, late);
    g2_compute(x, b2, ws, out, smem, w, l);
  }
}

extern "C" void kernel_launch(void* const* d_in, const int* in_sizes, int n_in, void* d_out, int out_size, void* d_ws,
                              size_t ws_size, hipStream_t stream) {
  const u16* x = (const u16*)d_in[0];
  const u16* wq = (const u16*)d_in[1];
  const u16* wk = (const u16*)d_in[2];
  const u16* wv = (const u16*)d_in[3];
  const u16* wo = (const u16*)d_in[4];
  const u16* w1 = (const u16*)d_in[6];
  const u16* b1 = (const u16*)d_in[7];
  const u16* w2 = (const u16*)d_in[8];
  const u16* b2 = (const u16*)d_in[9];
  char* ws = (char*)d_ws;
  (void)hipFuncSetAttribute((const void*)main_kernel, hipFuncAttributeMaxDynamicSharedMemorySize, MAIN_LDS);
  main_kernel<<<256, 512, MAIN_LDS, stream>>>(x, wq, wk, wv, wo, w1, b1, w2, b2, ws, (u16*)d_out);
}
```

```cpp
#include <hip/hip_runtime.h>
#include <hip/hip_bf16.h>

typedef short bf16x8 __attribute__((ext_vector_type(8)));
typedef short s16x4 __attribute__((ext_vector_type(4)));
typedef float f32x16 __attribute__((ext_vector_type(16)));
typedef float f32x4 __attribute__((ext_vector_type(4)));
typedef float f32x2 __attribute__((ext_vector_type(2)));
typedef __bf16 bf16x2_t __attribute__((ext_vector_type(2)));
typedef unsigned u32x4 __attribute__((ext_vector_type(4)));
typedef unsigned u32x2 __attribute__((ext_vector_type(2)));
typedef unsigned short u16;
#define LAS __attribute__((address_space(3)))
#define DI __device__ __forceinline__
#define SBAR() __builtin_amdgcn_sched_barrier(0)
#define PIN(x) asm volatile("" : "+v"(x))
#define MFMA32(a, b, c) __builtin_amdgcn_mfma_f32_32x32x16_bf16((a), (b), (c), 0, 0, 0)
#define MFMA16(a, b, c) __builtin_amdgcn_mfma_f32_16x16x32_bf16((a), (b), (c), 0, 0, 0)

constexpr int SEQ = 2048, DM = 1024, NH = 32, FF = 3584;
constexpr size_t KT = 262144;
constexpr size_t WS_STAT = 0;
constexpr size_t WS_Q = 4u << 20;
constexpr size_t WS_KV = 8u << 20;
constexpr size_t WS_O = 9u << 20;
constexpr size_t WS_H = 13u << 20;
constexpr size_t WS_PART = 28u << 20;
constexpr size_t WS_FLAG = 37u << 20;
constexpr int FL_P = 0, FL_O = 256, FL_H = 272, FL_Q = 280, FL_KV = 344, FL_S = 345, FL_N = 353;
constexpr size_t WS_HSA = WS_FLAG + 65536, WS_HSB = WS_HSA + 4096, WS_HSC = WS_HSB + 4096;
constexpr int COORD_BLOCK = 64;

#define STAMP(kid, idx) do {} while (0)
#define TICK(v) do {} while (0)
#define TOUT(idx, v) do {} while (0)
DI unsigned rfl(unsigned x) { return __builtin_amdgcn_readfirstlane(x); }
DI unsigned cvtpk(float lo, float hi) {
  f32x2 v = {lo, hi};
  bf16x2_t b = __builtin_convertvector(v, bf16x2_t);
  return __builtin_bit_cast(unsigned, b);
}
DI float fadd_s(float a, float b) { float r; asm("v_add_f32_e32 %0, %1, %2" : "=v"(r) : "v"(a), "v"(b)); return r; }
DI float bf_lo(unsigned u) { return __uint_as_float(u << 16); }
DI float bf_hi(unsigned u) { return __uint_as_float(u & 0xffff0000u); }
DI float bf_round(float x) { return bf_lo(cvtpk(x, x)); }

template <int IMM>
DI void glds16(unsigned voff, const void* sbase, unsigned ldsaddr) {
  asm volatile("s_mov_b32 m0, %2\n\ts_nop 0\n\tglobal_load_lds_dwordx4 %0, %1 offset:%3"
               :: "v"(voff), "s"(sbase), "s"(ldsaddr), "i"(IMM) : "memory");
}
template <int IMM>
DI void glds16_dev(unsigned voff, const void* sbase, unsigned ldsaddr) {
  asm volatile("s_mov_b32 m0, %2\n\ts_nop 0\n\tglobal_load_lds_dwordx4 %0, %1 offset:%3 sc1"
               :: "v"(voff), "s"(sbase), "s"(ldsaddr), "i"(IMM) : "memory");
}
DI void peek_dev(const void* word, unsigned ldsaddr) {
  asm volatile("s_mov_b32 m0, %2\n\ts_nop 0\n\tglobal_load_lds_dword %0, %1 sc1" :: "v"(0u), "s"(word), "s"(ldsaddr) : "memory");
}
DI void store16_dev(void* p, u32x4 v) {
  asm volatile("global_store_dwordx4 %0, %1, off sc1\n\ts_nop 1" :: "v"(p), "v"(v) : "memory");
}
DI void l2_touch(const void* p) { asm volatile("global_load_dword v255, %0, off" :: "v"(p) : "v255", "memory"); }
typedef __attribute__((address_space(1))) unsigned gu32;
#define RLX_AGENT __ATOMIC_RELAXED, __HIP_MEMORY_SCOPE_AGENT
DI void wait_ge(const unsigned* word, unsigned want) {
#pragma unroll 1
  for (unsigned spins = 0; spins < (1u << 22); ++spins) {
    if (rfl(__hip_atomic_load((gu32*)word, RLX_AGENT)) >= want) break;
    __builtin_amdgcn_s_sleep(2);
  }
  __builtin_amdgcn_fence(__ATOMIC_ACQUIRE, "wavefront");
}
DI void publish(unsigned* word, int l) {
  asm volatile("s_waitcnt vmcnt(0)" ::: "memory");
  if (l == 0) __hip_atomic_fetch_add((gu32*)word, 1u, RLX_AGENT);
}
DI bf16x8 load16_dev(const char* p) {
  typedef __attribute__((address_space(1))) unsigned long long gu64;
  const unsigned long long a = __hip_atomic_load((gu64*)p, RLX_AGENT), b = __hip_atomic_load((gu64*)p + 1, RLX_AGENT);
  return __builtin_bit_cast(bf16x8, (u32x4){(unsigned)a, (unsigned)(a >> 32), (unsigned)b, (unsigned)(b >> 32)});
}
DI float relu_s(float a) { float r; asm("v_max_f32_e32 %0, 0, %1" : "=v"(r) : "v"(a)); return r; }
DI bf16x8 lds_b128(const LAS char* p) { return *(const LAS bf16x8*)p; }
DI s16x4 lds_tr(const LAS char* p) {
  return __builtin_amdgcn_ds_read_tr16_b64_v4i16((LAS s16x4*)p);
}
DI f32x16 sel16(bool c, const f32x16& a, const f32x16& b) {
  f32x16 r;
#pragma unroll
  for (int i = 0; i < 16; ++i) r[i] = c ? a[i] : b[i];
  return r;
}
DI bf16x8 cat8(s16x4 lo, s16x4 hi) { return __builtin_shufflevector(lo, hi, 0, 1, 2, 3, 4, 5, 6, 7); }

DI float half_sum(float v) {
  v += __uint_as_float(__builtin_amdgcn_update_dpp(0u, __float_as_uint(v), 0xB1, 0xf, 0xf, true));
  v += __uint_as_float(__builtin_amdgcn_update_dpp(0u, __float_as_uint(v), 0x4E, 0xf, 0xf, true));
  v += __uint_as_float(__builtin_amdgcn_update_dpp(0u, __float_as_uint(v), 0x141, 0xf, 0xf, true));
  v += __uint_as_float(__builtin_amdgcn_update_dpp(0u, __float_as_uint(v), 0x140, 0xf, 0xf, true));
  auto r = __builtin_amdgcn_permlane16_swap(__float_as_uint(v), __float_as_uint(v), false, false);
  return __uint_as_float(r[0]) + __uint_as_float(r[1]);
}
DI unsigned row_stats(const u32x4 (&v)[4]) {
  f32x2 e[16], s2 = {0.f, 0.f};
#pragma unroll
  for (int i = 0; i < 16; ++i) { e[i] = (f32x2){bf_lo(v[i >> 2][i & 3]), bf_hi(v[i >> 2][i & 3])}; s2 += e[i]; }
  const float mean = half_sum(s2[0] + s2[1]) * (1.0f / DM);
  f32x2 q2 = {0.f, 0.f};
#pragma unroll
  for (int i = 0; i < 16; ++i) { const f32x2 d = e[i] - mean; q2 += d * d; }
  const float q = half_sum(q2[0] + q2[1]);
  const float den = __builtin_sqrtf(bf_round(bf_round(q * (1.0f / DM)) + 1e-5f));
  return cvtpk(-mean, den);
}

constexpr int G1_STAGE = 53248, G1_BOFF = 32768;
enum { G1_FFN = 5, G1_Q = 4, G1_KV = 2 };

constexpr int G1_ST = 87040;
constexpr int G1_HS = 159744;
constexpr int G1_CS = 81920;
#define PSTART 9
#define ESTART 3
#define WROT 0
#define KROT 0
DI float pair_sum(unsigned v) { return fadd_s(bf_lo(v), bf_hi(v)); }

template <int NP>
DI void g1_load(char* ws, const u16* x, const u16* wsrc, const u16* wv, int cg, int mtile, char* smem, int lw, int l) {
  constexpr int LD = (NP == G1_FFN) ? FF : (NP == G1_Q) ? DM : 32;
  const unsigned lds0 = (unsigned)(size_t)(LAS char*)smem;
  const char* abase = (const char*)(x + (size_t)(mtile * 256 + lw * 64) * DM);
  const unsigned ave = (unsigned)((l >> 3) * 2048 + (((l & 7) ^ (l >> 4)) << 4)), avo = ave ^ 64u;
  const unsigned bvoff = (unsigned)((lw * 16 + (l >> 2)) * LD * 2 + (l & 3) * 16);
  const int krot = KROT == 8 ? mtile * 2 : (mtile & 3) * KROT;
  const char* bbase[NP];
#pragma unroll
  for (int p = 0; p < NP; ++p) {
    if (NP == G1_KV) bbase[p] = (const char*)(p ? wv : wsrc);
    else if (NP == G1_Q) bbase[p] = (const char*)(wsrc + (cg * 4 + p) * 32);
    else { const int cb = (cg - 9) * 5 + p; bbase[p] = (const char*)(wsrc + (cb > 111 ? 111 : cb) * 32); }
  }
  float cs[NP];
#pragma unroll
  for (int p = 0; p < NP; ++p) cs[p] = 0.f;
  const LAS char* cb0 = (const LAS char*)smem + G1_BOFF + lw * 1024 + (8 * (l >> 5) + ((l & 15) >> 2)) * 64 + ((l >> 4) & 1) * 32 + (l & 3) * 8;
  u32x2 t0[NP], t1[NP];
  auto cs_read = [&](int slot) {
#pragma unroll
    for (int p = 0; p < NP; ++p) {
      t0[p] = __builtin_bit_cast(u32x2, lds_tr(cb0 + slot * G1_STAGE + p * 4096));
      t1[p] = __builtin_bit_cast(u32x2, lds_tr(cb0 + slot * G1_STAGE + p * 4096 + 256));
    }
    SBAR();
  };
  auto cs_add = [&](int p) {
    cs[p] = fadd_s(cs[p], fadd_s(fadd_s(pair_sum(t0[p][0]), pair_sum(t0[p][1])), fadd_s(pair_sum(t1[p][0]), pair_sum(t1[p][1]))));
    PIN(cs[p]); SBAR();
  };
  auto issue = [&](int st, int slot, bool sum) {
    const int kt = (st + krot) & 15;
    const unsigned sa = lds0 + slot * G1_STAGE + lw * 8192;
    const char* a0 = abase + kt * 128;
    const unsigned sb = lds0 + slot * G1_STAGE + G1_BOFF + lw * 1024;
#pragma unroll
    for (int p = 0; p < NP; ++p) glds16<0>(bvoff, bbase[p] + (size_t)kt * (64 * LD * 2), sb + p * 4096);
#pragma unroll
    for (int i = 0; i < 8; ++i) {
      if (sum && i < NP) { SBAR(); cs_add(i); }
      glds16<0>((i & 1) ? avo : ave, a0 + i * 16384, sa + i * 1024);
    }
  };
  auto colsum = [&](int slot) {
    cs_read(slot);
#pragma unroll
    for (int p = 0; p < NP; ++p) cs_add(p);
  };
  unsigned* flags = (unsigned*)(ws + WS_FLAG);
  const bool coord = (NP == G1_KV) && blockIdx.x == COORD_BLOCK && lw == 1;
  if (coord) {
#pragma unroll
    for (int i = 0; i < 6; ++i) if (i * 64 + l < FL_N) __hip_atomic_store((gu32*)flags + (i * 64 + l) * 32, 0u, RLX_AGENT);
    store16_dev(ws + WS_HSC + l * 16, (u32x4){0u, 0u, 0u, 0u});
  }
  TICK(t1_);
  issue(0, 0, false);
  TICK(t2_);
  issue(1, 1, false); issue(2, 2, false);
  TICK(t3_);
  asm volatile("s_waitcnt vmcnt(%0)" :: "n"(2 * (8 + NP)) : "memory");
  TICK(t4_);
  asm volatile("s_barrier" ::: "memory");
  TICK(t5_);
  TOUT(1, t1_); TOUT(2, t2_); TOUT(3, t3_); TOUT(4, t4_); TOUT(5, t5_);
  int state = (lw == 0 || coord) ? 0 : 2;
  bool pend = false, first = true;
  u32x4 last = {0u, 0u, 0u, 0u};
  const unsigned found = rfl(*(const LAS unsigned*)((const LAS char*)smem + G1_HS));
  if (lw == 0 && l < 9) {
    const unsigned v = *(const LAS unsigned*)((const LAS char*)smem + G1_HS + 16 + (l & 7) * 4);
    __hip_atomic_store(l < 8 ? (gu32*)(ws + WS_STAT) + mtile * 256 + cg * 8 + l : (gu32*)(ws + WS_HSA) + blockIdx.x, l < 8 ? v : found + 1u, RLX_AGENT);
  }
  colsum(0);
  auto poll_issue = [&]() {
    if (NP == G1_KV && coord) {
      glds16_dev<0>(l * 16, ws + WS_HSA, lds0 + G1_HS + 1024); glds16_dev<0>(l * 16, ws + WS_HSC, lds0 + G1_HS + 2048);
    } else {
      const char* a_ = state == 0 ? ws + WS_HSB + blockIdx.x * 4 : (const char*)(flags + (FL_S + mtile) * 32);
      peek_dev(a_, lds0 + G1_HS + 256);
    }
  };
  auto poll_check = [&]() {
    if (NP == G1_KV && coord) {
      const u32x4 pa = *(const LAS u32x4*)((const LAS char*)smem + G1_HS + 1024 + l * 16), pc = *(const LAS u32x4*)((const LAS char*)smem + G1_HS + 2048 + l * 16);
      if (first || pa[0] != last[0] || pa[1] != last[1] || pa[2] != last[2] || pa[3] != last[3]) store16_dev(ws + WS_HSB + l * 16, pa);
      last = pa; first = false;
      if (__all((pc[0] & pc[1] & pc[2] & pc[3]) == 1u)) state = 2;
    } else {
      const unsigned v = rfl(*(const LAS unsigned*)((const LAS char*)smem + G1_HS + 256));
      if (state == 0) {
        if (v != found) {
          if (l == 0) {
            __hip_atomic_store((gu32*)(ws + WS_HSC) + blockIdx.x, 1u, RLX_AGENT);
            __hip_atomic_fetch_add((gu32*)flags + (FL_S + mtile) * 32, 1u, RLX_AGENT);
          }
          state = 1;
        }
      } else if (v >= 32u) state = 2;
    }
    state = rfl(state);
  };
  int slot = 0;
#pragma unroll 1
  for (int i = 0; i < 13; ++i) {
    asm volatile("s_waitcnt vmcnt(%0)\n\ts_barrier" :: "n"(8 + NP) : "memory");
    if (state < 2 && (i >= ESTART || coord)) {
      if (pend) poll_check();
      pend = state == 0 || (state == 1 && i >= PSTART);
      if (pend) poll_issue();
    }
    const int nslot = (slot == 2) ? 0 : slot + 1;
    cs_read(nslot);
    issue(i + 3, slot, true);
    slot = nslot;
  }
#pragma unroll 1
  for (unsigned spins = 0; state < 2 && spins < (1u << 22); ++spins) {
    if (!pend) poll_issue();
    asm volatile("s_waitcnt vmcnt(0)" ::: "memory");
    poll_check();
    pend = false;
    __builtin_amdgcn_s_sleep(1);
  }
  asm volatile("s_waitcnt vmcnt(%0)\n\ts_barrier" :: "n"(8 + NP) : "memory");
  if (lw == 0) glds16_dev<0>(l * 16, ws + WS_STAT + mtile * 1024, lds0 + G1_ST);
  colsum(2);
  asm volatile("s_waitcnt vmcnt(0)" ::: "memory");
  colsum(0);
#pragma unroll
  for (int p = 0; p < NP; ++p) *(LAS float*)((LAS char*)smem + G1_CS + p * 1024 + (l & 31) * 32 + (lw * 2 + (l >> 5)) * 4) = cs[p];
  asm volatile("s_waitcnt lgkmcnt(0)\n\ts_barrier" ::: "memory");
}

template <int NP>
DI void g1_compute(char* __restrict__ ws, const u16* __restrict__ x, const u16* __restrict__ b1, int cg, int mtile, char* smem, int w, int l,
                   u32x4 (&hp)[20]) {
  STAMP(1, 0);
  const int h = l >> 5;
  unsigned st[2], bias[NP];
  {
    const int row = mtile * 256 + cg * 8 + w * 2 + h;
    u32x4 v[4];
#pragma unroll
    for (int i = 0; i < 4; ++i) v[i] = *(const u32x4*)(x + (size_t)row * DM + (i * 32 + (l & 31)) * 8);
    const unsigned found = __hip_atomic_load((gu32*)(ws + WS_HSB) + blockIdx.x, RLX_AGENT);
#pragma unroll
    for (int p = 0; p < NP; ++p) {
      int cb = (cg - 9) * 5 + p; cb = cb > 111 ? 111 : cb;
      bias[p] = (NP == G1_FFN) ? (unsigned)b1[cb * 32 + (l & 31)] : 0u;
    }
    SBAR();
    const unsigned rs = row_stats(v);
    if ((l & 31) == 0) *(LAS unsigned*)((LAS char*)smem + G1_HS + 16 + (w * 2 + h) * 4) = rs;
    if (w == 0 && l == 0) *(LAS unsigned*)((LAS char*)smem + G1_HS) = found;
  }
  f32x16 acc[2][NP];
#pragma unroll
  for (int p = 0; p < NP; ++p) { acc[0][p] = f32x16{}; acc[1][p] = f32x16{}; }
  const LAS char* lbase = (const LAS char*)smem;
  int aoff[4];
#pragma unroll
  for (int ks = 0; ks < 4; ++ks) aoff[ks] = (w * 64 + (l & 31)) * 128 + (((2 * ks + h) ^ ((l >> 1) & 7)) << 4);
  const int boff = G1_BOFF + (8 * h + ((l & 15) >> 2)) * 64 + ((l >> 4) & 1) * 32 + (l & 3) * 8;
  bf16x8 fa0[2], fb0[NP], fa1[2], fb1[NP];
#define G1_RD(FA, FB, base, ks) do { \
    FA[0] = lds_b128((base) + aoff[ks]); FA[1] = lds_b128((base) + aoff[ks] + 4096); \
    _Pragma("unroll") for (int p_ = 0; p_ < NP; ++p_) \
      FB[p_] = cat8(lds_tr((base) + boff + p_ * 4096 + (ks) * 1024), lds_tr((base) + boff + p_ * 4096 + (ks) * 1024 + 256)); \
  } while (0)
#define G1_MM(FA, FB) do { \
    _Pragma("unroll") for (int p_ = 0; p_ < NP; ++p_) { \
      if (NP == G1_KV && p_ == 1) { acc[0][p_] = MFMA32(FA[0], FB[p_], acc[0][p_]); acc[1][p_] = MFMA32(FA[1], FB[p_], acc[1][p_]); } \
      else { acc[0][p_] = MFMA32(FB[p_], FA[0], acc[0][p_]); acc[1][p_] = MFMA32(FB[p_], FA[1], acc[1][p_]); } } \
  } while (0)
#define G1_SCHED() do { \
    _Pragma("unroll") for (int q_ = 0; q_ < NP + 1; ++q_) { \
      __builtin_amdgcn_sched_group_barrier(0x008, 1, 0); __builtin_amdgcn_sched_group_barrier(0x100, 2, 0); } \
    __builtin_amdgcn_sched_group_barrier(0x008, NP - 1, 0); SBAR(); \
  } while (0)

  asm volatile("s_waitcnt lgkmcnt(0)\n\ts_barrier" ::: "memory");
  STAMP(1, 1);
  G1_RD(fa0, fb0, lbase, 0); SBAR();
  G1_RD(fa1, fb1, lbase, 1); G1_MM(fa0, fb0); G1_SCHED();
  G1_RD(fa0, fb0, lbase, 2); G1_MM(fa1, fb1); G1_SCHED();
  G1_RD(fa1, fb1, lbase, 3); G1_MM(fa0, fb0); G1_SCHED();
  int slot = 1;
#pragma unroll 1
  for (int i = 0; i < 15; ++i) {
    const LAS char* nb = lbase + slot * G1_STAGE;
    asm volatile("s_waitcnt lgkmcnt(0)\n\ts_barrier" ::: "memory"); SBAR();
    G1_RD(fa0, fb0, nb, 0); G1_MM(fa1, fb1); G1_SCHED();
    G1_RD(fa1, fb1, nb, 1); G1_MM(fa0, fb0); G1_SCHED();
    G1_RD(fa0, fb0, nb, 2); G1_MM(fa1, fb1); G1_SCHED();
    G1_RD(fa1, fb1, nb, 3); G1_MM(fa0, fb0); G1_SCHED();
    slot = (slot == 2) ? 0 : slot + 1;
  }
  STAMP(1, 4);
  {
    f32x4 t[NP][2];
#pragma unroll
    for (int m = 0; m < 2; ++m) st[m] = *(const LAS unsigned*)(lbase + G1_ST + (w * 64 + m * 32 + (l & 31)) * 4);
#pragma unroll
    for (int p = 0; p < NP; ++p) {
      t[p][0] = *(const LAS f32x4*)(lbase + G1_CS + p * 1024 + (l & 31) * 32);
      t[p][1] = *(const LAS f32x4*)(lbase + G1_CS + p * 1024 + (l & 31) * 32 + 16);
    }
    SBAR();
    G1_MM(fa1, fb1);
    const unsigned msk = h ? 0u : ~0u;
#pragma unroll
    for (int m = 0; m < 2; ++m) {
      const unsigned nm = st[m] & 0xffffu;
      fa0[m] = __builtin_bit_cast(bf16x8, (u32x4){(nm | (nm << 16)) & msk, st[m] & msk, 0u, 0u});
    }
#pragma unroll
    for (int p = 0; p < NP; ++p) {
      const f32x4 u = t[p][0] + t[p][1];
      const float c = (u[0] + u[1]) + (u[2] + u[3]);
      const unsigned c0 = cvtpk(c, c) & 0xffffu; const float r1 = c - bf_lo(c0);
      const unsigned c1 = cvtpk(r1, r1) & 0xffffu; const float r2 = r1 - bf_lo(c1);
      const unsigned c2 = cvtpk(r2, r2) & 0xffffu;
      fb0[p] = __builtin_bit_cast(bf16x8, (u32x4){c0 | (c1 << 16), c2 | (bias[p] << 16), 0u, 0u});
    }
  }
  G1_MM(fa0, fb0);
  float rden[2];
#pragma unroll
  for (int m = 0; m < 2; ++m) rden[m] = 1.0f / bf_hi(st[m]);
  asm volatile("s_nop 15\n\ts_nop 3" ::: "memory"); SBAR();
  STAMP(1, 2);

#pragma unroll
  for (int p = 0; p < NP; ++p) {
#pragma unroll
    for (int m = 0; m < 2; ++m) {
      const int tt = mtile * 8 + w * 2 + m;
      char* dst = (NP == G1_Q) ? ws + WS_Q + (size_t)(tt * 32 + cg * 4 + p) * 2048 + l * 16 : ws + WS_KV + (size_t)tt * 4096 + p * 2048 + l * 16;
      const float sc = (NP == G1_Q) ? rden[m] * (0.17677669529663687f * 1.4426950408889634f) : rden[m];
#pragma unroll
      for (int s = 0; s < 2; ++s) {
        u32x4 o;
#pragma unroll
        for (int j = 0; j < 4; ++j) {
          float a = acc[m][p][8 * s + 2 * j], b = acc[m][p][8 * s + 2 * j + 1];
          if (NP == G1_FFN) { a = relu_s(a); b = relu_s(b); a *= a; b *= b; }
          else if (NP == G1_KV && p == 1) {
            const int r = 8 * s + 2 * j, tk = (r & 3) + 8 * (r >> 2) + 4 * h;
            a *= __shfl(rden[m], tk); b *= __shfl(rden[m], tk + 1);
          } else { a *= sc; b *= sc; }
          o[j] = cvtpk(a, b);
        }
        if (NP == G1_FFN) { hp[p * 4 + m * 2 + s] = o; PIN(hp[p * 4 + m * 2 + s]); }
        else store16_dev(dst + s * 1024, o);
      }
    }
  }
  if (NP != G1_FFN) {
#pragma unroll
    for (int i = 0; i < 20; ++i) hp[i] = (u32x4){0u, 0u, 0u, 0u};
  }
  STAMP(1, 3);
#undef G1_RD
#undef G1_MM
#undef G1_SCHED
}

constexpr int AT_NCH = 8;
constexpr int AT_BUF = 32768;
constexpr int AT_BASE = 98304;
constexpr int AT_Q = 65536;
constexpr int MAIN_LDS = 163840;

DI float xhalf_max(float m) {
  auto rr = __builtin_amdgcn_permlane32_swap(__float_as_uint(m), __float_as_uint(m), false, false);
  return __builtin_fmaxf(__uint_as_float(rr[0]), __uint_as_float(rr[1]));
}
DI float xhalf_sum(float m) {
  auto rr = __builtin_amdgcn_permlane32_swap(__float_as_uint(m), __float_as_uint(m), false, false);
  return __uint_as_float(rr[0]) + __uint_as_float(rr[1]);
}
DI float max16(const f32x16& s) {
  float a = __builtin_fmaxf(s[0], s[1]);
#pragma unroll
  for (int r = 2; r < 16; ++r) a = __builtin_fmaxf(a, s[r]);
  return a;
}
DI bf16x8 pack8(const f32x16& p, int s) {
  u32x4 o;
#pragma unroll
  for (int j = 0; j < 4; ++j) o[j] = cvtpk(p[8 * s + 2 * j], p[8 * s + 2 * j + 1]);
  return __builtin_bit_cast(bf16x8, o);
}

DI void attn_fallback(const char* __restrict__ kv, bf16x8 q0, bf16x8 q1, int l, f32x16& o_out, float& l_out) {
  f32x16 o = {};
  float m = -INFINITY, lsum = 0.f;
  for (int t = 0; t < 64; ++t) {
    const char* p = kv + (size_t)t * 4096 + l * 16;
    const bf16x8 k0 = load16_dev(p), k1 = load16_dev(p + 1024), v0 = load16_dev(p + 2048), v1 = load16_dev(p + 3072);
    f32x16 s = {};
    s = MFMA32(k0, q0, s); s = MFMA32(k1, q1, s);
    const float mn = __builtin_fmaxf(m, xhalf_max(max16(s)));
    const float ms = (mn == -INFINITY) ? 0.f : mn;
    const float alpha = __builtin_amdgcn_exp2f(m - ms);
    m = mn;
    float ps = 0.f;
#pragma unroll
    for (int r = 0; r < 16; ++r) { s[r] = __builtin_amdgcn_exp2f(s[r] - ms); ps += s[r]; }
    lsum = lsum * alpha + ps;
#pragma unroll
    for (int r = 0; r < 16; ++r) o[r] *= alpha;
    o = MFMA32(v0, pack8(s, 0), o); o = MFMA32(v1, pack8(s, 1), o);
  }
  o_out = o;
  l_out = xhalf_sum(lsum);
}

constexpr int G2_SLOT = 32768, G2_BOFF = 16384, G2_PART = 131072;
struct G2Ld {
  const char* hb; const char* ob; const char* wb; const char* wb2; unsigned lds; unsigned voff, voffb; int rot;
  DI void init(const char* ws, const u16* w2, const u16* wo, unsigned lds0, int lw, int l) {
    const int xcd = blockIdx.x & 7, idx = blockIdx.x >> 3, kh = xcd & 1;
    const int mt = ((xcd >> 1) & 1) * 8 + (idx & 7), nt = (xcd >> 2) * 4 + (idx >> 3);
    hb = ws + WS_H + (size_t)(mt * 4 + lw) * 4096 + (size_t)kh * (28 * KT);
    ob = ws + WS_O + (size_t)(mt * 4 + lw) * 4096;
    wb = (const char*)(w2 + (size_t)(kh * 1792 + lw * 16) * DM + nt * 128);
    wb2 = (const char*)(wo + (size_t)(lw * 16) * DM + nt * 128 + kh * 64);
    voffb = (unsigned)((l >> 2) * DM * 2 + (l & 3) * 16);
    lds = lds0 + lw * 4096; voff = l * 16;
    rot = WROT * (nt & 3);
  }
  DI void ffn(int s) const {
    const unsigned d = lds + (s & 3) * G2_SLOT;
    const char* a = hb + (size_t)s * KT;
    glds16_dev<0>(voff, a, d); glds16_dev<1024>(voff, a, d); glds16_dev<2048>(voff, a, d); glds16_dev<3072>(voff, a, d);
    const char* b = wb + (size_t)s * 131072;
    glds16<0>(voffb, b, d + G2_BOFF); glds16<0>(voffb, b + 64, d + G2_BOFF + 1024); glds16<0>(voffb, b + 128, d + G2_BOFF + 2048); glds16<0>(voffb, b + 192, d + G2_BOFF + 3072);
  }
  DI void wo(int s) const {
    const unsigned d = lds + (s & 3) * G2_SLOT;
    const int k = (s - 28 + rot) & 15;
    const char* a = ob + (size_t)k * KT;
    glds16_dev<0>(voff, a, d); glds16_dev<1024>(voff, a, d); glds16_dev<2048>(voff, a, d); glds16_dev<3072>(voff, a, d);
    const char* b = wb2 + (size_t)k * 131072;
    glds16<0>(voffb, b, d + G2_BOFF); glds16<0>(voffb, b + 64, d + G2_BOFF + 1024);
  }
};

DI void attn_finish(char* __restrict__ ws, f32x16 o, f32x4 lacc, bf16x8 q0, bf16x8 q1, int l, char* dst) {
  const float l0 = __shfl(lacc[0], l & 15), l1 = __shfl(lacc[1], l & 15);
  float lsum = (l & 16) ? l1 : l0;
  bool ok = (lsum >= 9.5367431640625e-07f) && (lsum <= 1.8446744073709552e19f);
#pragma unroll
  for (int r = 0; r < 16; ++r) ok = ok && (__builtin_fabsf(o[r]) <= 3.0e38f);
  if (!__all(ok)) attn_fallback(ws + WS_KV, q0, q1, l, o, lsum);
  const float rl = 1.0f / lsum;
#pragma unroll
  for (int s = 0; s < 2; ++s) {
    u32x4 ov;
#pragma unroll
    for (int j = 0; j < 4; ++j) ov[j] = cvtpk(o[8 * s + 2 * j] * rl, o[8 * s + 2 * j + 1] * rl);
    store16_dev(dst + s * 1024, ov);
  }
}

template <bool LOADER>
DI void attn_phase(char* __restrict__ ws, const u16* w2, const u16* wo, char* smem, const int w, const int l, unsigned* late_pub,
                   const u32x4 (&hp)[20], int hcb, int htt) {
  const int tt = blockIdx.x >> 2, hg = blockIdx.x & 3;
  const int head = hg * 8 + w, lw = w & 3;
  const unsigned lds0 = (unsigned)(size_t)(LAS char*)smem;
  unsigned* flags = (unsigned*)(ws + WS_FLAG);
  const unsigned voff = l * 16;
  const char* ksrc = ws + WS_KV + (size_t)lw * 4096;
  const unsigned kdst = lds0 + AT_BASE + lw * 4096;
  auto ld = [&](int c, int half) {
    const char* s_ = ksrc + (size_t)c * AT_BUF + half * 16384;
    const unsigned d_ = kdst + (c & 1) * AT_BUF + half * 16384;
    glds16_dev<0>(voff, s_, d_); glds16_dev<1024>(voff, s_, d_); glds16_dev<2048>(voff, s_, d_); glds16_dev<3072>(voff, s_, d_);
  };
  G2Ld g2;
  const int xcd = blockIdx.x & 7, idx = blockIdx.x >> 3, gk = xcd & 1;
  const int mt = ((xcd >> 1) & 1) * 8 + (idx & 7), nt = (xcd >> 2) * 4 + (idx >> 3);
  STAMP(2, 0);
  if (LOADER) {
    const unsigned* word = flags + ((l & 1) ? FL_Q + (tt >> 3) * 8 + hg * 2 + (lw >> 1) : FL_KV) * 32;
    const unsigned need = (l & 1) ? 4u : 32u;
#pragma unroll 1
    for (unsigned spins = 0; spins < (1u << 22); ++spins) {
      if (__all(__hip_atomic_load((gu32*)word, RLX_AGENT) >= need)) break;
      __builtin_amdgcn_s_sleep(2);
    }
    __builtin_amdgcn_fence(__ATOMIC_ACQUIRE, "wavefront");
    const char* qs = ws + WS_Q + (size_t)(tt * 32 + hg * 8 + lw * 2) * 2048;
    const unsigned qd = lds0 + AT_Q + lw * 4096;
    glds16_dev<0>(voff, qs, qd); glds16_dev<1024>(voff, qs, qd); glds16_dev<2048>(voff, qs, qd); glds16_dev<3072>(voff, qs, qd);
    ld(0, 0); ld(0, 1); ld(1, 0); ld(1, 1);
    l2_touch(ksrc);
    g2.init(ws, w2, wo, lds0, lw, l);
  }
  bf16x8 sel;
  {
    const short one = ((l & 15) == ((l >> 4) & 1)) ? (short)0x3f80 : (short)0;
#pragma unroll
    for (int j = 0; j < 8; ++j) sel[j] = one;
  }
  f32x16 o = {}, sa, sb;
  f32x4 ls = {};
  u32x4 pa0, pa1, pb0 = {}, pb1 = {};
  PIN(o);
#define APRIO 1
  if (LOADER) { asm volatile("s_waitcnt vmcnt(9)\n\ts_barrier" ::: "memory"); __builtin_amdgcn_s_setprio(APRIO); }
  else asm volatile("s_barrier" ::: "memory");
  STAMP(2, 1);

  const LAS char* qp = (const LAS char*)smem + AT_Q + w * 2048 + l * 16;
  const bf16x8 q0 = lds_b128(qp), q1 = lds_b128(qp + 1024);
  const LAS char* kvp = (const LAS char*)smem + AT_BASE + l * 16;
  bf16x8 kx0, kx1, ky0, ky1, va0, va1, vb0, vb1, vc0, vc1, vd0, vd1;
  kx0 = lds_b128(kvp); kx1 = lds_b128(kvp + 1024); ky0 = lds_b128(kvp + 4096); ky1 = lds_b128(kvp + 4096 + 1024);
  va0 = lds_b128(kvp + 2048); va1 = lds_b128(kvp + 3072); vb0 = lds_b128(kvp + 4096 + 2048); vb1 = lds_b128(kvp + 4096 + 3072);
  vd0 = va0; vd1 = va1;
  f32x16 nm;
  {
    sa = MFMA32(kx0, q0, o); sa = MFMA32(kx1, q1, sa);
    const float ma = -xhalf_max(max16(sa));
#pragma unroll
    for (int r = 0; r < 16; ++r) { nm[r] = ma; sa[r] += ma; }
    PIN(nm);
  }
#define EX(S, i) S[i] = __builtin_amdgcn_exp2f(S[i])
#define CV(P, j, S, i) P[j] = cvtpk(S[i], S[(i) + 1])
#define B8(x) __builtin_bit_cast(bf16x8, x)
#define STEP(S, P0, P1, SN, PP0, PP1, KN0, KN1, VP0, VP1, KL0, KL1, VL0, VL1, NP) do { \
    SN = MFMA32(KN0, q0, nm); SBAR(); KL0 = lds_b128(NP); EX(S, 0); EX(S, 1); EX(S, 2); EX(S, 3); PIN(S); SBAR(); \
    SN = MFMA32(KN1, q1, SN); SBAR(); KL1 = lds_b128((NP) + 1024); CV(P0, 0, S, 0); CV(P0, 1, S, 2); EX(S, 4); EX(S, 5); EX(S, 6); PIN(S); PIN(P0); SBAR(); \
    o = MFMA32(VP0, B8(PP0), o); SBAR(); VL0 = lds_b128((NP) + 2048); EX(S, 7); CV(P0, 2, S, 4); EX(S, 8); CV(P0, 3, S, 6); EX(S, 9); PIN(S); PIN(P0); SBAR(); \
    ls = MFMA16(sel, B8(PP0), ls); SBAR(); EX(S, 10); EX(S, 11); PIN(S); SBAR(); \
    o = MFMA32(VP1, B8(PP1), o); SBAR(); VL1 = lds_b128((NP) + 3072); CV(P1, 0, S, 8); CV(P1, 1, S, 10); EX(S, 12); EX(S, 13); EX(S, 14); PIN(S); PIN(P1); SBAR(); \
    ls = MFMA16(sel, B8(PP1), ls); SBAR(); EX(S, 15); CV(P1, 2, S, 12); CV(P1, 3, S, 14); PIN(P1); SBAR(); \
  } while (0)
#define HST(i) do { if (!LOADER && late_pub && hcb + ((i) >> 2) < 112) { const int cb_ = hcb + ((i) >> 2); \
      const char* b_ = ws + WS_H + (size_t)(cb_ >> 1) * KT + (size_t)(htt + 1) * 4096 + (cb_ & 1) * 2048; \
      asm volatile("global_store_dwordx4 %0, %1, %2 offset:%3 sc1\n\ts_nop 1" :: "v"(voff), "v"(hp[i]), "s"(b_), "i"((((i) >> 1) & 1) * 4096 - 4096 + ((i) & 1) * 1024) : "memory"); } SBAR(); } while (0)
#define MID(c) do { \
    if (LOADER) { \
        \
      if ((c) < 6) asm volatile("s_waitcnt vmcnt(1) lgkmcnt(0)\n\ts_barrier" ::: "memory"); \
      else asm volatile("s_waitcnt vmcnt(16) lgkmcnt(0)\n\ts_barrier" ::: "memory"); \
      if ((c) == 4) peek_dev(flags + (FL_H + (mt >> 1)) * 32, lds0 + G1_CS + lw * 256);     \
      if ((c) + 2 < AT_NCH) { ld((c) + 2, 0); ld((c) + 2, 1); } \
      if ((c) < 2) {            \
        const int j_ = (c) * 4 + lw; \
        l2_touch(j_ < 7 ? (const char*)(w2 + (size_t)(gk * 1792 + j_ * 256 + (mt & 7) * 32 + (l >> 1)) * DM + nt * 128 + (l & 1) * 64) \
                        : (const char*)(wo + (size_t)((mt & 7) * 128 + l) * DM + nt * 128 + gk * 64)); \
      } else if ((c) < 5) {     \
        l2_touch(((c) == 2 && lw == 0) ? (const char*)(wo + (size_t)((mt & 7) * 128 + 64 + l) * DM + nt * 128 + gk * 64) : ksrc); \
      } else if ((c) == 5) { \
        if (rfl(*(const LAS unsigned*)((const LAS char*)smem + G1_CS + lw * 256)) < 92u) wait_ge(flags + (FL_H + (mt >> 1)) * 32, 92u); \
        l2_touch(ws + WS_H + (size_t)(gk * 28 + 3 + lw * 2 + (l >> 5)) * KT + (size_t)(mt * 4 + (nt & 3)) * 4096 + (l & 31) * 128); \
        g2.ffn(0); g2.ffn(1); \
      } else g2.ffn(2); \
    } else { \
      asm volatile("s_waitcnt lgkmcnt(0)\n\ts_barrier" ::: "memory"); \
      if ((c) == 2 && late_pub) publish(late_pub, l); \
      if (!((c) & 1)) STAMP(2, 4 + ((c) >> 1)); \
    } \
  } while (0)
#define CHUNK(c, X0, X1, X2, X3, X4, X5, X6, X7) do { \
    const LAS char* cb = kvp + ((c) & 1) * AT_BUF; \
    const LAS char* nb = kvp + (((c) + 1) & 1) * AT_BUF;          \
    STEP(sa, pa0, pa1, sb, pb0, pb1, ky0, ky1, vd0, vd1, kx0, kx1, vc0, vc1, cb + 2 * 4096); X0; \
    STEP(sb, pb0, pb1, sa, pa0, pa1, kx0, kx1, va0, va1, ky0, ky1, vd0, vd1, cb + 3 * 4096); X1; \
    STEP(sa, pa0, pa1, sb, pb0, pb1, ky0, ky1, vb0, vb1, kx0, kx1, va0, va1, cb + 4 * 4096); X2; \
    STEP(sb, pb0, pb1, sa, pa0, pa1, kx0, kx1, vc0, vc1, ky0, ky1, vb0, vb1, cb + 5 * 4096); X3; \
    STEP(sa, pa0, pa1, sb, pb0, pb1, ky0, ky1, vd0, vd1, kx0, kx1, vc0, vc1, cb + 6 * 4096); X4; \
    STEP(sb, pb0, pb1, sa, pa0, pa1, kx0, kx1, va0, va1, ky0, ky1, vd0, vd1, cb + 7 * 4096); X5; \
    if ((c) < AT_NCH - 1) MID(c); \
    SBAR(); \
    STEP(sa, pa0, pa1, sb, pb0, pb1, ky0, ky1, vb0, vb1, kx0, kx1, va0, va1, nb); X6; \
    STEP(sb, pb0, pb1, sa, pa0, pa1, kx0, kx1, vc0, vc1, ky0, ky1, vb0, vb1, nb + 4096); X7; \
  } while (0)
  if (!LOADER) {
    CHUNK(0, HST(0); HST(1), HST(2), HST(3), HST(4); HST(5), HST(6), HST(7), HST(8), HST(9));
    CHUNK(1, HST(10); HST(11), HST(12), HST(13), HST(14); HST(15), HST(16), HST(17), HST(18), HST(19));
  }
#pragma unroll 1
  for (int c = LOADER ? 0 : 2; c < AT_NCH; ++c) CHUNK(c, (void)0, (void)0, (void)0, (void)0, (void)0, (void)0, (void)0, (void)0);
#undef HST
#undef MID
#undef CHUNK
  o = MFMA32(vd0, B8(pb0), o); ls = MFMA16(sel, B8(pb0), ls);
  o = MFMA32(vd1, B8(pb1), o); ls = MFMA16(sel, B8(pb1), ls);
#undef EX
#undef CV
#undef B8
#undef STEP
  STAMP(2, 2);
  if (LOADER) __builtin_amdgcn_s_setprio(0);
  attn_finish(ws, o, ls, q0, q1, l, ws + WS_O + (size_t)(head >> 1) * KT + (size_t)tt * 4096 + (head & 1) * 2048 + l * 16);
  STAMP(2, 3);
}

DI void g2_load(const u16* w2, const u16* wo, char* __restrict__ ws, unsigned lds0, const int lw, const int l) {
  const int xcd = blockIdx.x & 7, idx = blockIdx.x >> 3;
  const int mt = ((xcd >> 1) & 1) * 8 + (idx & 7);
  unsigned* flags = (unsigned*)(ws + WS_FLAG);
  G2Ld g; g.init(ws, w2, wo, lds0, lw, l);
  asm volatile("s_waitcnt vmcnt(18) lgkmcnt(0)\n\ts_barrier" ::: "memory");
  g.ffn(3);
  asm volatile("s_waitcnt vmcnt(18)\n\ts_barrier" ::: "memory");
  g.ffn(4);
  asm volatile("s_waitcnt vmcnt(16)\n\ts_barrier" ::: "memory");
  if (l == 0) __hip_atomic_fetch_add((gu32*)flags + (FL_O + (blockIdx.x >> 4)) * 32, 1u, RLX_AGENT);
  g.ffn(5);
  const LAS unsigned* peek = (const LAS unsigned*)(size_t)(lds0 + G2_PART);
#pragma unroll 1
  for (int i = 2; i < 24; ++i) {
    asm volatile("s_waitcnt vmcnt(16)\n\ts_barrier" ::: "memory");
    if (i == 20 && lw == 0) peek_dev(flags + (FL_O + mt) * 32, lds0 + G2_PART);
    g.ffn(i + 4);
  }
  asm volatile("s_waitcnt vmcnt(16)\n\ts_barrier" ::: "memory");
  if (rfl(peek[0]) < 128u) wait_ge(flags + (FL_O + mt) * 32, 128u);
  g.wo(28);
  peek = (const LAS unsigned*)(size_t)(lds0 + G2_BOFF + 2048);
#pragma unroll 1
  for (int i = 25; i < 40; ++i) {
    asm volatile("s_waitcnt vmcnt(12)\n\ts_barrier" ::: "memory");
    if (i == 38 && lw == 0) peek_dev(flags + (FL_P + blockIdx.x) * 32, lds0 + G2_BOFF + 2048);
    g.wo(i + 4);
  }
  auto part = [&]() {
    const char* src = ws + WS_PART + (size_t)(blockIdx.x ^ 1) * 32768 + lw * 8192;
    const unsigned d = lds0 + G2_PART + lw * 8192;
    glds16_dev<0>(g.voff, src, d); glds16_dev<1024>(g.voff, src, d); glds16_dev<2048>(g.voff, src, d); glds16_dev<3072>(g.voff, src, d);
    glds16_dev<0>(g.voff, src + 4096, d + 4096); glds16_dev<1024>(g.voff, src + 4096, d + 4096); glds16_dev<2048>(g.voff, src + 4096, d + 4096); glds16_dev<3072>(g.voff, src + 4096, d + 4096);
  };
  asm volatile("s_waitcnt vmcnt(12)\n\ts_barrier" ::: "memory");
  if (rfl(peek[0]) >= 2u) {
    part();
    asm volatile("s_waitcnt vmcnt(14)\n\ts_barrier" ::: "memory");
  } else {
    asm volatile("s_waitcnt vmcnt(6)\n\ts_barrier" ::: "memory");
    wait_ge(flags + (FL_P + blockIdx.x) * 32, 2u);
    part();
  }
  asm volatile("s_waitcnt vmcnt(8)\n\ts_barrier" ::: "memory");
  asm volatile("s_waitcnt vmcnt(0)\n\ts_barrier" ::: "memory");
}

DI void g2_compute(const u16* __restrict__ x, const u16* __restrict__ b2, char* __restrict__ ws, u16* __restrict__ out,
                   char* smem, const int w, const int l) {
  const int xcd = blockIdx.x & 7, idx = blockIdx.x >> 3;
  const int kh = xcd & 1, mt = ((xcd >> 1) & 1) * 8 + (idx & 7), nt = (xcd >> 2) * 4 + (idx >> 3);
  unsigned* flags = (unsigned*)(ws + WS_FLAG);
  STAMP(3, 0);
  const int h = l >> 5, wm = w & 1, wn = w >> 1;
  const bool own = (wn == kh);
  f32x16 accg[2][2] = {}, accs[2][2] = {};
  const int col0 = nt * 128 + kh * 64;
  const int row = mt * 128 + wm * 64 + wn * 32 + (l & 31);
  const float den = bf_hi(__hip_atomic_load((gu32*)(ws + WS_STAT) + row, RLX_AGENT));
  u32x4 xv[4];
  u32x2 bv[8];
#pragma unroll
  for (int k = 0; k < 4; ++k) xv[k] = *(const u32x4*)(x + (size_t)row * DM + col0 + 16 * k + 8 * h);
#pragma unroll
  for (int k = 0; k < 8; ++k) bv[k] = *(const u32x2*)(b2 + col0 + 8 * k + 4 * h);
  const LAS char* lbase = (const LAS char*)smem + l * 16;
  const int btr = (4 * h + ((l & 15) >> 2)) * 64 + ((l >> 4) & 1) * 32 + (l & 3) * 8 - l * 16;
#define LDB(p_) cat8(lds_tr(p_), lds_tr((p_) + 512))
#define NRD 3
  const int aF = wm * 8192, bF = G2_BOFF + wn * 2048 + btr;
  const int aW = wm * 8192 + wn * 2048, bW = G2_BOFF + wn * 8192 + btr;
  bf16x8 fa0[2], fb0[2], fa1[2], fb1[2];
#define G2_RD(FA, FB, base, ao, bo, ks) do { \
    FA[0] = lds_b128((base) + (ao) + (ks) * 1024); FB[0] = LDB((base) + (bo) + (ks) * 4096); \
    FA[1] = lds_b128((base) + (ao) + 4096 + (ks) * 1024); FB[1] = LDB((base) + (bo) + (ks) * 4096 + 1024); \
  } while (0)
#define G2_MM(ACC, FA, FB) do { \
    ACC[0][0] = MFMA32(FB[0], FA[0], ACC[0][0]); ACC[1][0] = MFMA32(FB[0], FA[1], ACC[1][0]); \
    ACC[0][1] = MFMA32(FB[1], FA[0], ACC[0][1]); ACC[1][1] = MFMA32(FB[1], FA[1], ACC[1][1]); \
  } while (0)
#define G2_SCHED() do { \
    __builtin_amdgcn_sched_group_barrier(0x008, 1, 0); __builtin_amdgcn_sched_group_barrier(0x100, NRD, 0); \
    __builtin_amdgcn_sched_group_barrier(0x008, 1, 0); __builtin_amdgcn_sched_group_barrier(0x100, NRD, 0); \
    __builtin_amdgcn_sched_group_barrier(0x008, 2, 0); SBAR(); \
  } while (0)
#define G2_BAR() do { asm volatile("s_waitcnt lgkmcnt(0)\n\ts_barrier" ::: "memory"); SBAR(); } while (0)

  G2_BAR();
  STAMP(3, 1);
  G2_RD(fa0, fb0, lbase, aF, bF, 0); SBAR();
  G2_RD(fa1, fb1, lbase, aF, bF, 1); G2_MM(accg, fa0, fb0); G2_SCHED();
  G2_RD(fa0, fb0, lbase, aF, bF, 2); G2_MM(accg, fa1, fb1); G2_SCHED();
  G2_RD(fa1, fb1, lbase, aF, bF, 3); G2_MM(accg, fa0, fb0); G2_SCHED();
#pragma unroll 1
  for (int i = 0; i < 27; ++i) {
    const LAS char* nb = lbase + ((i + 1) & 3) * G2_SLOT;
    G2_BAR();
    G2_RD(fa0, fb0, nb, aF, bF, 0); G2_MM(accg, fa1, fb1); G2_SCHED();
    G2_RD(fa1, fb1, nb, aF, bF, 1); G2_MM(accg, fa0, fb0); G2_SCHED();
    G2_RD(fa0, fb0, nb, aF, bF, 2); G2_MM(accg, fa1, fb1); G2_SCHED();
    G2_RD(fa1, fb1, nb, aF, bF, 3); G2_MM(accg, fa0, fb0); G2_SCHED();
    if (i == 3) { publish(flags + (FL_O + (blockIdx.x >> 4)) * 32, l); SBAR(); }
  }
  STAMP(3, 4);
  {
    G2_BAR();
    G2_RD(fa0, fb0, lbase, aW, bW, 0); G2_MM(accg, fa1, fb1); G2_SCHED();
    G2_RD(fa1, fb1, lbase, aW, bW, 1); G2_MM(accs, fa0, fb0); G2_SCHED();
  }
  char* pdst = ws + WS_PART + (size_t)blockIdx.x * 32768 + wm * 16384 + l * 16;
#define G2_SEND(m, p) do { if (!own) { asm volatile("s_nop 7" ::: "memory"); \
    _Pragma("unroll") for (int i_ = 0; i_ < 4; ++i_) \
      store16_dev(pdst + ((m) * 2 + (p)) * 4096 + i_ * 1024, (u32x4){__float_as_uint(accg[m][p][4 * i_]), __float_as_uint(accg[m][p][4 * i_ + 1]), \
                                                                   __float_as_uint(accg[m][p][4 * i_ + 2]), __float_as_uint(accg[m][p][4 * i_ + 3])}); } SBAR(); } while (0)
#define G2_WSTEP(i) do { const LAS char* nb = lbase + (((i) + 1) & 3) * G2_SLOT; G2_BAR(); \
    G2_RD(fa0, fb0, nb, aW, bW, 0); G2_MM(accs, fa1, fb1); G2_SCHED(); \
    G2_RD(fa1, fb1, nb, aW, bW, 1); G2_MM(accs, fa0, fb0); G2_SCHED(); } while (0)
  G2_SEND(0, 0);
  STAMP(3, 5);
  G2_WSTEP(28); G2_SEND(0, 1);
  G2_WSTEP(29); G2_SEND(1, 0);
  G2_WSTEP(30); G2_SEND(1, 1);
#pragma unroll 1
  for (int i = 31; i < 43; ++i) {
    if (i == 40) STAMP(3, 6);
    G2_WSTEP(i);
    if (i == 34 && !own) { publish(flags + (FL_P + (blockIdx.x ^ 1)) * 32, l); SBAR(); }
  }
#undef G2_SEND
#undef G2_WSTEP
  G2_MM(accs, fa1, fb1);
  STAMP(3, 2);

  {
    LAS char* xw = (LAS char*)smem + w * 16384 + l * 16;
#pragma unroll
    for (int p = 0; p < 2; ++p) {
      const f32x16 s = sel16(wn, accs[0][p], accs[1][p]);
#pragma unroll
      for (int i = 0; i < 4; ++i) *(LAS f32x4*)(xw + p * 4096 + i * 1024) = (f32x4){s[4 * i], s[4 * i + 1], s[4 * i + 2], s[4 * i + 3]};
    }
    if (own) {
#pragma unroll
      for (int p = 0; p < 2; ++p) {
        const f32x16 g = sel16(wn, accg[0][p], accg[1][p]);
#pragma unroll
        for (int i = 0; i < 4; ++i) *(LAS f32x4*)(xw + 8192 + p * 4096 + i * 1024) = (f32x4){g[4 * i], g[4 * i + 1], g[4 * i + 2], g[4 * i + 3]};
      }
    }
  }
  G2_BAR();
  STAMP(3, 7);
  {
    const LAS char* xr = (const LAS char*)smem + (w ^ 2) * 16384 + l * 16;
    const LAS char* xp = (const LAS char*)smem + G2_PART + wm * 16384 + wn * 8192 + l * 16;
    const float rd2 = 1.0f / (den * den);
    f32x16 gf[2];
    if (own) {
      gf[0] = sel16(wn, accg[1][0], accg[0][0]); gf[1] = sel16(wn, accg[1][1], accg[0][1]);
    } else {
#pragma unroll
      for (int p = 0; p < 2; ++p)
#pragma unroll
        for (int i = 0; i < 4; ++i) {
          const f32x4 t = *(const LAS f32x4*)(xr + 8192 + p * 4096 + i * 1024);
          gf[p][4 * i] = t[0]; gf[p][4 * i + 1] = t[1]; gf[p][4 * i + 2] = t[2]; gf[p][4 * i + 3] = t[3];
        }
    }
#pragma unroll
    for (int p = 0; p < 2; ++p) {
      const f32x16 s = sel16(wn, accs[1][p], accs[0][p]);
      unsigned pk[8];
#pragma unroll
      for (int k = 0; k < 2; ++k) {
        u32x4 xx = xv[p * 2 + k];
        { auto r0 = __builtin_amdgcn_permlane32_swap(xx[0], xx[2], false, false); xx[0] = r0[0]; xx[2] = r0[1];
          auto r1 = __builtin_amdgcn_permlane32_swap(xx[1], xx[3], false, false); xx[1] = r1[0]; xx[3] = r1[1]; }
#pragma unroll
        for (int ii = 0; ii < 2; ++ii) {
          const int i = 2 * k + ii;
          const f32x4 ts = *(const LAS f32x4*)(xr + p * 4096 + i * 1024);
          const f32x4 tp = *(const LAS f32x4*)(xp + p * 4096 + i * 1024);
          float r[4];
#pragma unroll
          for (int j = 0; j < 4; ++j) {
            const unsigned xw_ = xx[2 * ii + (j >> 1)];
            const float xf = (j & 1) ? bf_hi(xw_) : bf_lo(xw_);
            const unsigned bw_ = bv[p * 4 + i][j >> 1];
            const float z = (gf[p][4 * i + j] + tp[j]) * rd2 + ((j & 1) ? bf_hi(bw_) : bf_lo(bw_));
            const float sg = z * __builtin_amdgcn_rcpf(1.0f + __builtin_amdgcn_exp2f(-1.4426950408889634f * z));
            r[j] = xf + (s[4 * i + j] + ts[j]) * sg;
          }
          pk[4 * k + 2 * ii] = cvtpk(r[0], r[1]); pk[4 * k + 2 * ii + 1] = cvtpk(r[2], r[3]);
        }
        auto r0 = __builtin_amdgcn_permlane32_swap(pk[4 * k], pk[4 * k + 2], false, false);
        auto r1 = __builtin_amdgcn_permlane32_swap(pk[4 * k + 1], pk[4 * k + 3], false, false);
        *(u32x4*)(out + (size_t)row * DM + col0 + p * 32 + 16 * k + 8 * h) = (u32x4){r0[0], r1[0], r0[1], r1[1]};
      }
    }
  }
  STAMP(3, 3);
#undef G2_RD
#undef G2_MM
#undef G2_SCHED
#undef G2_BAR
}

__global__ __launch_bounds__(512, 2) void main_kernel(const u16* __restrict__ x, const u16* __restrict__ wq, const u16* __restrict__ wk,
                                                      const u16* __restrict__ wv, const u16* __restrict__ wo, const u16* __restrict__ w1,
                                                      const u16* __restrict__ b1, const u16* __restrict__ w2, const u16* __restrict__ b2,
                                                      char* __restrict__ ws, u16* __restrict__ out) {
  extern __shared__ __attribute__((aligned(16))) char smem[];
  TICK(t0_);
  const int l = threadIdx.x & 63, w = rfl(threadIdx.x >> 6);
  const unsigned lds0 = (unsigned)(size_t)(LAS char*)smem;
  const int xcd = blockIdx.x & 7, idx = blockIdx.x >> 3;
  const int mtile = (xcd & 1) * 4 + (idx & 3), cg = (idx >> 2) * 4 + (xcd >> 1);
  unsigned* flags = (unsigned*)(ws + WS_FLAG);
  if (w >= 4) {
    if (cg > 8) g1_load<G1_FFN>(ws, x, w1, nullptr, cg, mtile, smem, w - 4, l);
    else if (cg < 8) g1_load<G1_Q>(ws, x, wq, nullptr, cg, mtile, smem, w - 4, l);
    else g1_load<G1_KV>(ws, x, wk, wv, cg, mtile, smem, w - 4, l);
    TOUT(0, t0_);
    u32x4 hp[20];
    attn_phase<true>(ws, w2, wo, smem, w, l, nullptr, hp, 0, 0);
    g2_load(w2, wo, ws, lds0, w - 4, l);
  } else {
    unsigned* late = nullptr;
    u32x4 hp[20];
    if (cg > 8) { g1_compute<G1_FFN>(ws, x, b1, cg, mtile, smem, w, l, hp); late = flags + (FL_H + mtile) * 32; }
    else if (cg < 8) { g1_compute<G1_Q>(ws, x, b1, cg, mtile, smem, w, l, hp); publish(flags + (FL_Q + mtile * 8 + cg) * 32, l); }
    else { g1_compute<G1_KV>(ws, x, b1, cg, mtile, smem, w, l, hp); publish(flags + FL_KV * 32, l); }
    attn_phase<false>(ws, w2, wo, smem, w, l, late, hp, (cg - 9) * 5, mtile * 8 + w * 2);
    g2_compute(x, b2, ws, out, smem, w, l);
  }
}

extern "C" void kernel_launch(void* const* d_in, const int* in_sizes, int n_in, void* d_out, int out_size, void* d_ws,
                              size_t ws_size, hipStream_t stream) {
  const u16* x = (const u16*)d_in[0];
  const u16* wq = (const u16*)d_in[1];
  const u16* wk = (const u16*)d_in[2];
  const u16* wv = (const u16*)d_in[3];
  const u16* wo = (const u16*)d_in[4];
  const u16* w1 = (const u16*)d_in[6];
  const u16* b1 = (const u16*)d_in[7];
  const u16* w2 = (const u16*)d_in[8];
  const u16* b2 = (const u16*)d_in[9];
  char* ws = (char*)d_ws;
  (void)hipFuncSetAttribute((const void*)main_kernel, hipFuncAttributeMaxDynamicSharedMemorySize, MAIN_LDS);
  main_kernel<<<256, 512, MAIN_LDS, stream>>>(x, wq, wk, wv, wo, w1, b1, w2, b2, ws, (u16*)d_out);
}
```

```cpp
#include <hip/hip_runtime.h>
#include <hip/hip_bf16.h>

typedef short bf16x8 __attribute__((ext_vector_type(8)));
typedef short s16x4 __attribute__((ext_vector_type(4)));
typedef float f32x16 __attribute__((ext_vector_type(16)));
typedef float f32x4 __attribute__((ext_vector_type(4)));
typedef float f32x2 __attribute__((ext_vector_type(2)));
typedef __bf16 bf16x2_t __attribute__((ext_vector_type(2)));
typedef unsigned u32x4 __attribute__((ext_vector_type(4)));
typedef unsigned u32x2 __attribute__((ext_vector_type(2)));
typedef unsigned u32x8 __attribute__((ext_vector_type(8)));
typedef __bf16 bf16x8_t __attribute__((ext_vector_type(8)));
typedef __bf16 bf16x16_t __attribute__((ext_vector_type(16)));
typedef unsigned short u16;
#define LAS __attribute__((address_space(3)))
#define DI __device__ __forceinline__
#define SBAR() __builtin_amdgcn_sched_barrier(0)
#define PIN(x) asm volatile("" : "+v"(x))
#define MFMA32(a, b, c) __builtin_amdgcn_mfma_f32_32x32x16_bf16((a), (b), (c), 0, 0, 0)
#define MFMA16(a, b, c) __builtin_amdgcn_mfma_f32_16x16x32_bf16((a), (b), (c), 0, 0, 0)

constexpr int SEQ = 2048, DM = 1024, NH = 32, FF = 3584;
constexpr size_t KT = 262144;
constexpr size_t WS_STAT = 0;
constexpr size_t WS_Q = 4u << 20;
constexpr size_t WS_KV = 8u << 20;
constexpr size_t WS_O = 9u << 20;
constexpr size_t WS_H = 13u << 20;
constexpr size_t WS_PART = 28u << 20;
constexpr size_t WS_FLAG = 37u << 20;
constexpr int FL_P = 0, FL_O = 256, FL_H = 272, FL_Q = 280, FL_KV = 344, FL_S = 345, FL_N = 353;
constexpr size_t WS_HSA = WS_FLAG + 65536, WS_HSB = WS_HSA + 4096, WS_HSC = WS_HSB + 4096;
constexpr int COORD_BLOCK = 64;

#define STAMP(kid, idx) do {} while (0)
DI unsigned rfl(unsigned x) { return __builtin_amdgcn_readfirstlane(x); }
DI unsigned cvtpk(float lo, float hi) {
  f32x2 v = {lo, hi};
  bf16x2_t b = __builtin_convertvector(v, bf16x2_t);
  return __builtin_bit_cast(unsigned, b);
}
DI float fadd_s(float a, float b) { float r; asm("v_add_f32_e32 %0, %1, %2" : "=v"(r) : "v"(a), "v"(b)); return r; }
DI float bf_lo(unsigned u) { return __uint_as_float(u << 16); }
DI float bf_hi(unsigned u) { return __uint_as_float(u & 0xffff0000u); }
DI float bf_round(float x) { return bf_lo(cvtpk(x, x)); }

template <int IMM>
DI void glds16(unsigned voff, const void* sbase, unsigned ldsaddr) {
  asm volatile("s_mov_b32 m0, %2\n\ts_nop 0\n\tglobal_load_lds_dwordx4 %0, %1 offset:%3"
               :: "v"(voff), "s"(sbase), "s"(ldsaddr), "i"(IMM) : "memory");
}
template <int IMM>
DI void glds16_dev(unsigned voff, const void* sbase, unsigned ldsaddr) {
  asm volatile("s_mov_b32 m0, %2\n\ts_nop 0\n\tglobal_load_lds_dwordx4 %0, %1 offset:%3 sc1"
               :: "v"(voff), "s"(sbase), "s"(ldsaddr), "i"(IMM) : "memory");
}
DI void peek_dev(const void* word, unsigned ldsaddr) {
  asm volatile("s_mov_b32 m0, %2\n\ts_nop 0\n\tglobal_load_lds_dword %0, %1 sc1" :: "v"(0u), "s"(word), "s"(ldsaddr) : "memory");
}
DI void store16_dev(void* p, u32x4 v) {
  asm volatile("global_store_dwordx4 %0, %1, off sc1\n\ts_nop 1" :: "v"(p), "v"(v) : "memory");
}
DI void l2_touch(const void* p) { asm volatile("global_load_dword v255, %0, off" :: "v"(p) : "v255", "memory"); }
typedef __attribute__((address_space(1))) unsigned gu32;
#define RLX_AGENT __ATOMIC_RELAXED, __HIP_MEMORY_SCOPE_AGENT
DI void wait_ge(const unsigned* word, unsigned want) {
#pragma unroll 1
  for (unsigned spins = 0; spins < (1u << 22); ++spins) {
    if (rfl(__hip_atomic_load((gu32*)word, RLX_AGENT)) >= want) break;
    __builtin_amdgcn_s_sleep(2);
  }
  __builtin_amdgcn_fence(__ATOMIC_ACQUIRE, "wavefront");
}
DI void publish(unsigned* word, int l) {
  asm volatile("s_waitcnt vmcnt(0)" ::: "memory");
  if (l == 0) __hip_atomic_fetch_add((gu32*)word, 1u, RLX_AGENT);
}
DI bf16x8 load16_dev(const char* p) {
  typedef __attribute__((address_space(1))) unsigned long long gu64;
  const unsigned long long a = __hip_atomic_load((gu64*)p, RLX_AGENT), b = __hip_atomic_load((gu64*)p + 1, RLX_AGENT);
  return __builtin_bit_cast(bf16x8, (u32x4){(unsigned)a, (unsigned)(a >> 32), (unsigned)b, (unsigned)(b >> 32)});
}
DI float relu_s(float a) { float r; asm("v_max_f32_e32 %0, 0, %1" : "=v"(r) : "v"(a)); return r; }
DI bf16x8 lds_b128(const LAS char* p) { return *(const LAS bf16x8*)p; }
DI s16x4 lds_tr(const LAS char* p) {
  return __builtin_amdgcn_ds_read_tr16_b64_v4i16((LAS s16x4*)p);
}
DI f32x16 sel16(bool c, const f32x16& a, const f32x16& b) {
  f32x16 r;
#pragma unroll
  for (int i = 0; i < 16; ++i) r[i] = c ? a[i] : b[i];
  return r;
}
DI bf16x8 cat8(s16x4 lo, s16x4 hi) { return __builtin_shufflevector(lo, hi, 0, 1, 2, 3, 4, 5, 6, 7); }

DI float half_sum(float v) {
  v += __uint_as_float(__builtin_amdgcn_update_dpp(0u, __float_as_uint(v), 0xB1, 0xf, 0xf, true));
  v += __uint_as_float(__builtin_amdgcn_update_dpp(0u, __float_as_uint(v), 0x4E, 0xf, 0xf, true));
  v += __uint_as_float(__builtin_amdgcn_update_dpp(0u, __float_as_uint(v), 0x141, 0xf, 0xf, true));
  v += __uint_as_float(__builtin_amdgcn_update_dpp(0u, __float_as_uint(v), 0x140, 0xf, 0xf, true));
  auto r = __builtin_amdgcn_permlane16_swap(__float_as_uint(v), __float_as_uint(v), false, false);
  return __uint_as_float(r[0]) + __uint_as_float(r[1]);
}
DI unsigned row_stats(const u32x4 (&v)[4]) {
  f32x2 e[16], s2 = {0.f, 0.f};
#pragma unroll
  for (int i = 0; i < 16; ++i) { e[i] = (f32x2){bf_lo(v[i >> 2][i & 3]), bf_hi(v[i >> 2][i & 3])}; s2 += e[i]; }
  const float mean = half_sum(s2[0] + s2[1]) * (1.0f / DM);
  f32x2 q2 = {0.f, 0.f};
#pragma unroll
  for (int i = 0; i < 16; ++i) { const f32x2 d = e[i] - mean; q2 += d * d; }
  const float q = half_sum(q2[0] + q2[1]);
  const float den = __builtin_sqrtf(bf_round(bf_round(q * (1.0f / DM)) + 1e-5f));
  return cvtpk(-mean, den);
}

constexpr int G1_STAGE = 53248, G1_BOFF = 32768;
enum { G1_FFN = 5, G1_Q = 4, G1_KV = 2 };

constexpr int G1_ST = 87040;
constexpr int G1_HS = 159744;
constexpr int G1_CS = 81920;
#define PSTART 9
#define ESTART 3
#define WROT 0
#define KROT 0
DI float pair_sum(unsigned v) { return fadd_s(bf_lo(v), bf_hi(v)); }

template <int NP>
DI void g1_load(char* ws, const u16* x, const u16* wsrc, const u16* wv, int cg, int mtile, char* smem, int lw, int l) {
  constexpr int LD = (NP == G1_FFN) ? FF : (NP == G1_Q) ? DM : 32;
  const unsigned lds0 = (unsigned)(size_t)(LAS char*)smem;
  const char* abase = (const char*)(x + (size_t)(mtile * 256 + lw * 64) * DM);
  const unsigned ave = (unsigned)((l >> 3) * 2048 + (((l & 7) ^ (l >> 4)) << 4)), avo = ave ^ 64u;
  const unsigned bvoff = (unsigned)((lw * 16 + (l >> 2)) * LD * 2 + (l & 3) * 16);
  const int krot = (mtile & 3) * KROT;
  const char* bbase[NP];
#pragma unroll
  for (int p = 0; p < NP; ++p) {
    if (NP == G1_KV) bbase[p] = (const char*)(p ? wv : wsrc);
    else if (NP == G1_Q) bbase[p] = (const char*)(wsrc + (cg * 4 + p) * 32);
    else { const int cb = (cg - 9) * 5 + p; bbase[p] = (const char*)(wsrc + (cb > 111 ? 111 : cb) * 32); }
  }
  float cs[NP];
#pragma unroll
  for (int p = 0; p < NP; ++p) cs[p] = 0.f;
  const LAS char* cb0 = (const LAS char*)smem + G1_BOFF + lw * 1024 + (8 * (l >> 5) + ((l & 15) >> 2)) * 64 + ((l >> 4) & 1) * 32 + (l & 3) * 8;
  u32x2 t0[NP], t1[NP];
  auto cs_read = [&](int slot) {
#pragma unroll
    for (int p = 0; p < NP; ++p) {
      t0[p] = __builtin_bit_cast(u32x2, lds_tr(cb0 + slot * G1_STAGE + p * 4096));
      t1[p] = __builtin_bit_cast(u32x2, lds_tr(cb0 + slot * G1_STAGE + p * 4096 + 256));
    }
    SBAR();
  };
  auto cs_add = [&](int p) {
    cs[p] = fadd_s(cs[p], fadd_s(fadd_s(pair_sum(t0[p][0]), pair_sum(t0[p][1])), fadd_s(pair_sum(t1[p][0]), pair_sum(t1[p][1]))));
    PIN(cs[p]); SBAR();
  };
  auto issue = [&](int st, int slot, bool sum) {
    const int kt = (st + krot) & 15;
    const unsigned sa = lds0 + slot * G1_STAGE + lw * 8192;
    const char* a0 = abase + kt * 128;
    const unsigned sb = lds0 + slot * G1_STAGE + G1_BOFF + lw * 1024;
#pragma unroll
    for (int p = 0; p < NP; ++p) glds16<0>(bvoff, bbase[p] + (size_t)kt * (64 * LD * 2), sb + p * 4096);
#pragma unroll
    for (int i = 0; i < 8; ++i) {
      if (sum && i < NP) { SBAR(); cs_add(i); }
      glds16<0>((i & 1) ? avo : ave, a0 + i * 16384, sa + i * 1024);
    }
  };
  auto colsum = [&](int slot) {
    cs_read(slot);
#pragma unroll
    for (int p = 0; p < NP; ++p) cs_add(p);
  };
  unsigned* flags = (unsigned*)(ws + WS_FLAG);
  const bool coord = (NP == G1_KV) && blockIdx.x == COORD_BLOCK && lw == 1;
  if (coord) {
#pragma unroll
    for (int i = 0; i < 6; ++i) if (i * 64 + l < FL_N) __hip_atomic_store((gu32*)flags + (i * 64 + l) * 32, 0u, RLX_AGENT);
    store16_dev(ws + WS_HSC + l * 16, (u32x4){0u, 0u, 0u, 0u});
  }
  issue(0, 0, false); issue(1, 1, false); issue(2, 2, false);
  asm volatile("s_waitcnt vmcnt(%0)\n\ts_barrier" :: "n"(2 * (8 + NP)) : "memory");
  __builtin_amdgcn_s_setprio(2);
  int state = (lw == 0 || coord) ? 0 : 2;
  bool pend = false, first = true;
  u32x4 last = {0u, 0u, 0u, 0u};
  const unsigned found = rfl(*(const LAS unsigned*)((const LAS char*)smem + G1_HS));
  if (lw == 0 && l < 9) {
    const unsigned v = *(const LAS unsigned*)((const LAS char*)smem + G1_HS + 16 + (l & 7) * 4);
    __hip_atomic_store(l < 8 ? (gu32*)(ws + WS_STAT) + mtile * 256 + cg * 8 + l : (gu32*)(ws + WS_HSA) + blockIdx.x, l < 8 ? v : found + 1u, RLX_AGENT);
  }
  colsum(0);
  auto poll_issue = [&]() {
    if (NP == G1_KV && coord) {
      glds16_dev<0>(l * 16, ws + WS_HSA, lds0 + G1_HS + 1024); glds16_dev<0>(l * 16, ws + WS_HSC, lds0 + G1_HS + 2048);
    } else {
      const char* a_ = state == 0 ? ws + WS_HSB + blockIdx.x * 4 : (const char*)(flags + (FL_S + mtile) * 32);
      peek_dev(a_, lds0 + G1_HS + 256);
    }
  };
  auto poll_check = [&]() {
    if (NP == G1_KV && coord) {
      const u32x4 pa = *(const LAS u32x4*)((const LAS char*)smem + G1_HS + 1024 + l * 16), pc = *(const LAS u32x4*)((const LAS char*)smem + G1_HS + 2048 + l * 16);
      if (first || pa[0] != last[0] || pa[1] != last[1] || pa[2] != last[2] || pa[3] != last[3]) store16_dev(ws + WS_HSB + l * 16, pa);
      last = pa; first = false;
      if (__all((pc[0] & pc[1] & pc[2] & pc[3]) == 1u)) state = 2;
    } else {
      const unsigned v = rfl(*(const LAS unsigned*)((const LAS char*)smem + G1_HS + 256));
      if (state == 0) {
        if (v != found) {
          if (l == 0) {
            __hip_atomic_store((gu32*)(ws + WS_HSC) + blockIdx.x, 1u, RLX_AGENT);
            __hip_atomic_fetch_add((gu32*)flags + (FL_S + mtile) * 32, 1u, RLX_AGENT);
          }
          state = 1;
        }
      } else if (v >= 32u) state = 2;
    }
    state = rfl(state);
  };
  int slot = 0;
#pragma unroll 1
  for (int i = 0; i < 13; ++i) {
    asm volatile("s_waitcnt vmcnt(%0)\n\ts_barrier" :: "n"(8 + NP) : "memory");
    if (state < 2 && (i >= ESTART || coord)) {
      if (pend) poll_check();
      pend = state == 0 || (state == 1 && i >= PSTART);
      if (pend) poll_issue();
    }
    const int nslot = (slot == 2) ? 0 : slot + 1;
    cs_read(nslot);
    issue(i + 3, slot, true);
    slot = nslot;
  }
#pragma unroll 1
  for (unsigned spins = 0; state < 2 && spins < (1u << 22); ++spins) {
    if (!pend) poll_issue();
    asm volatile("s_waitcnt vmcnt(0)" ::: "memory");
    poll_check();
    pend = false;
    __builtin_amdgcn_s_sleep(1);
  }
  asm volatile("s_waitcnt vmcnt(%0)\n\ts_barrier" :: "n"(8 + NP) : "memory");
  if (lw == 0) glds16_dev<0>(l * 16, ws + WS_STAT + mtile * 1024, lds0 + G1_ST);
  colsum(2);
  asm volatile("s_waitcnt vmcnt(0)" ::: "memory");
  colsum(0);
#pragma unroll
  for (int p = 0; p < NP; ++p) *(LAS float*)((LAS char*)smem + G1_CS + p * 1024 + (l & 31) * 32 + (lw * 2 + (l >> 5)) * 4) = cs[p];
  asm volatile("s_waitcnt lgkmcnt(0)\n\ts_barrier" ::: "memory");
}

template <int NP>
DI void g1_compute(char* __restrict__ ws, const u16* __restrict__ x, const u16* __restrict__ b1, int cg, int mtile, char* smem, int w, int l) {
  STAMP(1, 0);
  const int h = l >> 5;
  unsigned st[2], bias[NP];
  {
    const int row = mtile * 256 + cg * 8 + w * 2 + h;
    u32x4 v[4];
#pragma unroll
    for (int i = 0; i < 4; ++i) v[i] = *(const u32x4*)(x + (size_t)row * DM + (i * 32 + (l & 31)) * 8);
    const unsigned found = __hip_atomic_load((gu32*)(ws + WS_HSB) + blockIdx.x, RLX_AGENT);
#pragma unroll
    for (int p = 0; p < NP; ++p) {
      int cb = (cg - 9) * 5 + p; cb = cb > 111 ? 111 : cb;
      bias[p] = (NP == G1_FFN) ? (unsigned)b1[cb * 32 + (l & 31)] : 0u;
    }
    SBAR();
    const unsigned rs = row_stats(v);
    if ((l & 31) == 0) *(LAS unsigned*)((LAS char*)smem + G1_HS + 16 + (w * 2 + h) * 4) = rs;
    if (w == 0 && l == 0) *(LAS unsigned*)((LAS char*)smem + G1_HS) = found;
  }
  f32x16 acc[2][NP];
#pragma unroll
  for (int p = 0; p < NP; ++p) { acc[0][p] = f32x16{}; acc[1][p] = f32x16{}; }
  const LAS char* lbase = (const LAS char*)smem;
  int aoff[4];
#pragma unroll
  for (int ks = 0; ks < 4; ++ks) aoff[ks] = (w * 64 + (l & 31)) * 128 + (((2 * ks + h) ^ ((l >> 1) & 7)) << 4);
  const int boff = G1_BOFF + (8 * h + ((l & 15) >> 2)) * 64 + ((l >> 4) & 1) * 32 + (l & 3) * 8;
  bf16x8 fa0[2], fb0[NP], fa1[2], fb1[NP];
#define G1_RD(FA, FB, base, ks) do { \
    FA[0] = lds_b128((base) + aoff[ks]); FA[1] = lds_b128((base) + aoff[ks] + 4096); \
    _Pragma("unroll") for (int p_ = 0; p_ < NP; ++p_) \
      FB[p_] = cat8(lds_tr((base) + boff + p_ * 4096 + (ks) * 1024), lds_tr((base) + boff + p_ * 4096 + (ks) * 1024 + 256)); \
  } while (0)
#define G1_MM(FA, FB) do { \
    _Pragma("unroll") for (int p_ = 0; p_ < NP; ++p_) { \
      if (NP == G1_KV && p_ == 1) { acc[0][p_] = MFMA32(FA[0], FB[p_], acc[0][p_]); acc[1][p_] = MFMA32(FA[1], FB[p_], acc[1][p_]); } \
      else { acc[0][p_] = MFMA32(FB[p_], FA[0], acc[0][p_]); acc[1][p_] = MFMA32(FB[p_], FA[1], acc[1][p_]); } } \
  } while (0)
#define G1_SCHED() do { \
    _Pragma("unroll") for (int q_ = 0; q_ < NP + 1; ++q_) { \
      __builtin_amdgcn_sched_group_barrier(0x008, 1, 0); __builtin_amdgcn_sched_group_barrier(0x100, 2, 0); } \
    __builtin_amdgcn_sched_group_barrier(0x008, NP - 1, 0); SBAR(); \
  } while (0)

  asm volatile("s_waitcnt lgkmcnt(0)\n\ts_barrier" ::: "memory");
  STAMP(1, 1);
  G1_RD(fa0, fb0, lbase, 0); SBAR();
  G1_RD(fa1, fb1, lbase, 1); G1_MM(fa0, fb0); G1_SCHED();
  G1_RD(fa0, fb0, lbase, 2); G1_MM(fa1, fb1); G1_SCHED();
  G1_RD(fa1, fb1, lbase, 3); G1_MM(fa0, fb0); G1_SCHED();
  int slot = 1;
#pragma unroll 1
  for (int i = 0; i < 15; ++i) {
    const LAS char* nb = lbase + slot * G1_STAGE;
    asm volatile("s_waitcnt lgkmcnt(0)\n\ts_barrier" ::: "memory"); SBAR();
    G1_RD(fa0, fb0, nb, 0); G1_MM(fa1, fb1); G1_SCHED();
    G1_RD(fa1, fb1, nb, 1); G1_MM(fa0, fb0); G1_SCHED();
    G1_RD(fa0, fb0, nb, 2); G1_MM(fa1, fb1); G1_SCHED();
    G1_RD(fa1, fb1, nb, 3); G1_MM(fa0, fb0); G1_SCHED();
    slot = (slot == 2) ? 0 : slot + 1;
  }
  STAMP(1, 4);
  {
    f32x4 t[NP][2];
#pragma unroll
    for (int m = 0; m < 2; ++m) st[m] = *(const LAS unsigned*)(lbase + G1_ST + (w * 64 + m * 32 + (l & 31)) * 4);
#pragma unroll
    for (int p = 0; p < NP; ++p) {
      t[p][0] = *(const LAS f32x4*)(lbase + G1_CS + p * 1024 + (l & 31) * 32);
      t[p][1] = *(const LAS f32x4*)(lbase + G1_CS + p * 1024 + (l & 31) * 32 + 16);
    }
    SBAR();
    G1_MM(fa1, fb1);
    const unsigned msk = h ? 0u : ~0u;
#pragma unroll
    for (int m = 0; m < 2; ++m) {
      const unsigned nm = st[m] & 0xffffu;
      fa0[m] = __builtin_bit_cast(bf16x8, (u32x4){(nm | (nm << 16)) & msk, st[m] & msk, 0u, 0u});
    }
#pragma unroll
    for (int p = 0; p < NP; ++p) {
      const f32x4 u = t[p][0] + t[p][1];
      const float c = (u[0] + u[1]) + (u[2] + u[3]);
      const unsigned c0 = cvtpk(c, c) & 0xffffu; const float r1 = c - bf_lo(c0);
      const unsigned c1 = cvtpk(r1, r1) & 0xffffu; const float r2 = r1 - bf_lo(c1);
      const unsigned c2 = cvtpk(r2, r2) & 0xffffu;
      fb0[p] = __builtin_bit_cast(bf16x8, (u32x4){c0 | (c1 << 16), c2 | (bias[p] << 16), 0u, 0u});
    }
  }
  G1_MM(fa0, fb0);
  float rden[2];
#pragma unroll
  for (int m = 0; m < 2; ++m) rden[m] = 1.0f / bf_hi(st[m]);
  asm volatile("s_nop 15\n\ts_nop 3" ::: "memory"); SBAR();
  STAMP(1, 2);

#pragma unroll
  for (int p = 0; p < NP; ++p) {
    const int cb = (NP == G1_FFN) ? (cg - 9) * 5 + p : cg * 4 + p;
    if (NP != G1_FFN || cb < 112) {
#pragma unroll
      for (int m = 0; m < 2; ++m) {
        const int tt = mtile * 8 + w * 2 + m;
        char* dst = (NP == G1_FFN) ? ws + WS_H + (size_t)(cb >> 1) * KT + (size_t)tt * 4096 + (cb & 1) * 2048 + l * 16
                  : (NP == G1_Q)   ? ws + WS_Q + (size_t)(tt * 32 + cb) * 2048 + l * 16
                                   : ws + WS_KV + (size_t)tt * 4096 + p * 2048 + l * 16;
        const float sc = (NP == G1_Q) ? rden[m] * (0.17677669529663687f * 1.4426950408889634f) : rden[m];
#pragma unroll
        for (int s = 0; s < 2; ++s) {
          u32x4 o;
#pragma unroll
          for (int j = 0; j < 4; ++j) {
            float a = acc[m][p][8 * s + 2 * j], b = acc[m][p][8 * s + 2 * j + 1];
            if (NP == G1_FFN) { a = relu_s(a); b = relu_s(b); a *= a; b *= b; }
            else if (NP == G1_KV && p == 1) {
              const int r = 8 * s + 2 * j, tk = (r & 3) + 8 * (r >> 2) + 4 * h;
              a *= __shfl(rden[m], tk); b *= __shfl(rden[m], tk + 1);
            } else { a *= sc; b *= sc; }
            o[j] = cvtpk(a, b);
          }
          store16_dev(dst + s * 1024, o);
        }
      }
    }
  }
  STAMP(1, 3);
#undef G1_RD
#undef G1_MM
#undef G1_SCHED
}

constexpr int AT_NCH = 8;
constexpr int AT_BUF = 32768;
constexpr int AT_BASE = 98304;
constexpr int AT_Q = 65536;
constexpr int MAIN_LDS = 163840;

DI float xhalf_max(float m) {
  auto rr = __builtin_amdgcn_permlane32_swap(__float_as_uint(m), __float_as_uint(m), false, false);
  return __builtin_fmaxf(__uint_as_float(rr[0]), __uint_as_float(rr[1]));
}
DI float xhalf_sum(float m) {
  auto rr = __builtin_amdgcn_permlane32_swap(__float_as_uint(m), __float_as_uint(m), false, false);
  return __uint_as_float(rr[0]) + __uint_as_float(rr[1]);
}
DI float max16(const f32x16& s) {
  float a = __builtin_fmaxf(s[0], s[1]);
#pragma unroll
  for (int r = 2; r < 16; ++r) a = __builtin_fmaxf(a, s[r]);
  return a;
}
DI bf16x8 pack8(const f32x16& p, int s) {
  u32x4 o;
#pragma unroll
  for (int j = 0; j < 4; ++j) o[j] = cvtpk(p[8 * s + 2 * j], p[8 * s + 2 * j + 1]);
  return __builtin_bit_cast(bf16x8, o);
}

DI void attn_fallback(const char* __restrict__ kv, bf16x8 q0, bf16x8 q1, int l, f32x16& o_out, float& l_out) {
  f32x16 o = {};
  float m = -INFINITY, lsum = 0.f;
  for (int t = 0; t < 64; ++t) {
    const char* p = kv + (size_t)t * 4096 + l * 16;
    const bf16x8 k0 = load16_dev(p), k1 = load16_dev(p + 1024), v0 = load16_dev(p + 2048), v1 = load16_dev(p + 3072);
    f32x16 s = {};
    s = MFMA32(k0, q0, s); s = MFMA32(k1, q1, s);
    const float mn = __builtin_fmaxf(m, xhalf_max(max16(s)));
    const float ms = (mn == -INFINITY) ? 0.f : mn;
    const float alpha = __builtin_amdgcn_exp2f(m - ms);
    m = mn;
    float ps = 0.f;
#pragma unroll
    for (int r = 0; r < 16; ++r) { s[r] = __builtin_amdgcn_exp2f(s[r] - ms); ps += s[r]; }
    lsum = lsum * alpha + ps;
#pragma unroll
    for (int r = 0; r < 16; ++r) o[r] *= alpha;
    o = MFMA32(v0, pack8(s, 0), o); o = MFMA32(v1, pack8(s, 1), o);
  }
  o_out = o;
  l_out = xhalf_sum(lsum);
}

constexpr int G2_SLOT = 32768, G2_BOFF = 16384, G2_PART = 131072;
struct G2Ld {
  const char* hb; const char* ob; const char* wb; const char* wb2; unsigned lds; unsigned voff, voffb; int rot;
  DI void init(const char* ws, const u16* w2, const u16* wo, unsigned lds0, int lw, int l) {
    const int xcd = blockIdx.x & 7, idx = blockIdx.x >> 3, kh = xcd & 1;
    const int mt = ((xcd >> 1) & 1) * 8 + (idx & 7), nt = (xcd >> 2) * 4 + (idx >> 3);
    hb = ws + WS_H + (size_t)(mt * 4 + lw) * 4096 + (size_t)kh * (28 * KT);
    ob = ws + WS_O + (size_t)(mt * 4 + lw) * 4096;
    wb = (const char*)(w2 + (size_t)(kh * 1792 + lw * 16) * DM + nt * 128);
    wb2 = (const char*)(wo + (size_t)(lw * 16) * DM + nt * 128 + kh * 64);
    voffb = (unsigned)((l >> 2) * DM * 2 + (l & 3) * 16);
    lds = lds0 + lw * 4096; voff = l * 16;
    rot = WROT * (nt & 3);
  }
  DI void ffn(int s) const {
    const unsigned d = lds + (s & 3) * G2_SLOT;
    const char* a = hb + (size_t)s * KT;
    glds16_dev<0>(voff, a, d); glds16_dev<1024>(voff, a, d); glds16_dev<2048>(voff, a, d); glds16_dev<3072>(voff, a, d);
    const char* b = wb + (size_t)s * 131072;
    glds16<0>(voffb, b, d + G2_BOFF); glds16<0>(voffb, b + 64, d + G2_BOFF + 1024); glds16<0>(voffb, b + 128, d + G2_BOFF + 2048); glds16<0>(voffb, b + 192, d + G2_BOFF + 3072);
  }
  template <int J> DI void ffn_piece(int s) const {
    const unsigned d = lds + (s & 3) * G2_SLOT;
    if (J < 4) glds16_dev<(J & 3) * 1024>(voff, hb + (size_t)s * KT, d);
    else glds16<0>(voffb, wb + (size_t)s * 131072 + (J & 3) * 64, d + G2_BOFF + (J & 3) * 1024);
  }
  DI void wo(int s) const {
    const unsigned d = lds + (s & 3) * G2_SLOT;
    const int k = (s - 28 + rot) & 15;
    const char* a = ob + (size_t)k * KT;
    glds16_dev<0>(voff, a, d); glds16_dev<1024>(voff, a, d); glds16_dev<2048>(voff, a, d); glds16_dev<3072>(voff, a, d);
    const char* b = wb2 + (size_t)k * 131072;
    glds16<0>(voffb, b, d + G2_BOFF); glds16<0>(voffb, b + 64, d + G2_BOFF + 1024);
  }
};

DI void attn_finish(char* __restrict__ ws, f32x16 o, f32x4 lacc, bf16x8 q0, bf16x8 q1, int l, char* dst) {
  const float l0 = __shfl(lacc[0] + lacc[1], l & 15), l1 = __shfl(lacc[2] + lacc[3], l & 15);
  float lsum = (l & 16) ? l1 : l0;
  bool ok = (lsum >= 9.5367431640625e-07f) && (lsum <= 1.8446744073709552e19f);
#pragma unroll
  for (int r = 0; r < 16; ++r) ok = ok && (__builtin_fabsf(o[r]) <= 3.0e38f);
  if (!__all(ok)) attn_fallback(ws + WS_KV, q0, q1, l, o, lsum);
  const float rl = 1.0f / lsum;
#pragma unroll
  for (int s = 0; s < 2; ++s) {
    u32x4 ov;
#pragma unroll
    for (int j = 0; j < 4; ++j) ov[j] = cvtpk(o[8 * s + 2 * j] * rl, o[8 * s + 2 * j + 1] * rl);
    store16_dev(dst + s * 1024, ov);
  }
}

template <bool LOADER>
DI void attn_phase(char* __restrict__ ws, const u16* w2, const u16* wo, char* smem, const int w, const int l, unsigned* late_pub) {
  const int tt = blockIdx.x >> 2, hg = blockIdx.x & 3;
  const int head = hg * 8 + w, lw = w & 3;
  const unsigned lds0 = (unsigned)(size_t)(LAS char*)smem;
  unsigned* flags = (unsigned*)(ws + WS_FLAG);
  const unsigned voff = l * 16;
  const char* ksrc = ws + WS_KV + (size_t)lw * 4096;
  const unsigned kdst = lds0 + AT_BASE + lw * 4096;
  auto ld = [&](int c, int half) {
    const char* s_ = ksrc + (size_t)c * AT_BUF + half * 16384;
    const unsigned d_ = kdst + (c & 1) * AT_BUF + half * 16384;
    glds16_dev<0>(voff, s_, d_); glds16_dev<1024>(voff, s_, d_); glds16_dev<2048>(voff, s_, d_); glds16_dev<3072>(voff, s_, d_);
  };
  G2Ld g2;
  const int xcd = blockIdx.x & 7, idx = blockIdx.x >> 3, gk = xcd & 1;
  const int mt = ((xcd >> 1) & 1) * 8 + (idx & 7), nt = (xcd >> 2) * 4 + (idx >> 3);
  STAMP(2, 0);
  if (LOADER) {
    const unsigned* word = flags + ((l & 1) ? FL_Q + (tt >> 3) * 8 + hg * 2 + (lw >> 1) : FL_KV) * 32;
    const unsigned need = (l & 1) ? 4u : 32u;
#pragma unroll 1
    for (unsigned spins = 0; spins < (1u << 22); ++spins) {
      if (__all(__hip_atomic_load((gu32*)word, RLX_AGENT) >= need)) break;
      __builtin_amdgcn_s_sleep(2);
    }
    __builtin_amdgcn_fence(__ATOMIC_ACQUIRE, "wavefront");
    const char* qs = ws + WS_Q + (size_t)(tt * 32 + hg * 8 + lw * 2) * 2048;
    const unsigned qd = lds0 + AT_Q + lw * 4096;
    glds16_dev<0>(voff, qs, qd); glds16_dev<1024>(voff, qs, qd); glds16_dev<2048>(voff, qs, qd); glds16_dev<3072>(voff, qs, qd);
    ld(0, 0); ld(0, 1); ld(1, 0); ld(1, 1);
    l2_touch(ksrc);
    g2.init(ws, w2, wo, lds0, lw, l);
  }
  const unsigned s_lo = (l & 14) == 0 ? 0x3f803f80u : 0u, s_hi = (l & 14) == 2 ? 0x3f803f80u : 0u;
  const bf16x8_t sel = __builtin_bit_cast(bf16x8_t, (u32x4){s_lo, s_lo, s_hi, s_hi});
  const int sidx = (l & 1) ? 0xeeee : 0x4444;
  f32x16 o = {}, sa, sb;
  f32x4 ls = {};
  u32x8 pa, pb = {};
  PIN(o);
  if (LOADER) asm volatile("s_waitcnt vmcnt(9)\n\ts_barrier" ::: "memory");
  else asm volatile("s_barrier" ::: "memory");
  STAMP(2, 1);

  const LAS char* qp = (const LAS char*)smem + AT_Q + w * 2048 + l * 16;
  const bf16x8 q0 = lds_b128(qp), q1 = lds_b128(qp + 1024);
  const LAS char* kvp = (const LAS char*)smem + AT_BASE + l * 16;
  bf16x8 kx0, kx1, ky0, ky1, va0, va1, vb0, vb1, vc0, vc1, vd0, vd1;
  kx0 = lds_b128(kvp); kx1 = lds_b128(kvp + 1024); ky0 = lds_b128(kvp + 4096); ky1 = lds_b128(kvp + 4096 + 1024);
  va0 = lds_b128(kvp + 2048); va1 = lds_b128(kvp + 3072); vb0 = lds_b128(kvp + 4096 + 2048); vb1 = lds_b128(kvp + 4096 + 3072);
  vd0 = va0; vd1 = va1;
  f32x16 nm;
  {
    sa = MFMA32(kx0, q0, o); sa = MFMA32(kx1, q1, sa);
    const float ma = -xhalf_max(max16(sa));
#pragma unroll
    for (int r = 0; r < 16; ++r) { nm[r] = ma; sa[r] += ma; }
    PIN(nm);
  }
#define EX(S, i) S[i] = __builtin_amdgcn_exp2f(S[i])
#define CV(P, j, S, i) P[j] = cvtpk(S[i], S[(i) + 1])
#define PLO(x) __builtin_bit_cast(bf16x8, __builtin_shufflevector(x, x, 0, 1, 2, 3))
#define PHI(x) __builtin_bit_cast(bf16x8, __builtin_shufflevector(x, x, 4, 5, 6, 7))
#define RSUM(x) ls = __builtin_amdgcn_smfmac_f32_16x16x64_bf16(sel, __builtin_bit_cast(bf16x16_t, x), ls, sidx, 0, 0)
#define STEP(S, P, SN, PP, KN0, KN1, VP0, VP1, KL0, KL1, VL0, VL1, NP) do { \
    SN = MFMA32(KN0, q0, nm); SBAR(); KL0 = lds_b128(NP); EX(S, 0); EX(S, 1); EX(S, 2); EX(S, 3); PIN(S); SBAR(); \
    SN = MFMA32(KN1, q1, SN); SBAR(); KL1 = lds_b128((NP) + 1024); CV(P, 0, S, 0); CV(P, 1, S, 2); EX(S, 4); EX(S, 5); EX(S, 6); EX(S, 7); PIN(S); PIN(P); SBAR(); \
    o = MFMA32(VP0, PLO(PP), o); SBAR(); VL0 = lds_b128((NP) + 2048); CV(P, 2, S, 4); EX(S, 8); CV(P, 3, S, 6); EX(S, 9); EX(S, 10); EX(S, 11); PIN(S); PIN(P); SBAR(); \
    o = MFMA32(VP1, PHI(PP), o); SBAR(); VL1 = lds_b128((NP) + 3072); CV(P, 4, S, 8); CV(P, 5, S, 10); EX(S, 12); EX(S, 13); EX(S, 14); PIN(S); PIN(P); SBAR(); \
    RSUM(PP); SBAR(); EX(S, 15); CV(P, 6, S, 12); CV(P, 7, S, 14); PIN(P); SBAR(); \
  } while (0)
#define XP(st, j) do { if (LOADER && (unsigned)((st) + 1) < 4u) g2.template ffn_piece<j>((st) + 1); SBAR(); } while (0)
#define PRIO(n) do { __builtin_amdgcn_s_setprio(n); SBAR(); } while (0)
#pragma unroll 1
  for (int c = 0; c < AT_NCH; ++c) {
    const LAS char* cb = kvp + (c & 1) * AT_BUF;
    const LAS char* nb = kvp + ((c + 1) & 1) * AT_BUF;
    PRIO(2);
    STEP(sa, pa, sb, pb, ky0, ky1, vd0, vd1, kx0, kx1, vc0, vc1, cb + 2 * 4096); XP(c - 5, 2);
    STEP(sb, pb, sa, pa, kx0, kx1, va0, va1, ky0, ky1, vd0, vd1, cb + 3 * 4096); XP(c - 5, 3);
    PRIO(1);
    STEP(sa, pa, sb, pb, ky0, ky1, vb0, vb1, kx0, kx1, va0, va1, cb + 4 * 4096); XP(c - 5, 4);
    STEP(sb, pb, sa, pa, kx0, kx1, vc0, vc1, ky0, ky1, vb0, vb1, cb + 5 * 4096); XP(c - 5, 5);
    STEP(sa, pa, sb, pb, ky0, ky1, vd0, vd1, kx0, kx1, vc0, vc1, cb + 6 * 4096); XP(c - 5, 6);
    PRIO(0);
    STEP(sb, pb, sa, pa, kx0, kx1, va0, va1, ky0, ky1, vd0, vd1, cb + 7 * 4096); XP(c - 5, 7);
    if (c < AT_NCH - 1) {
      if (LOADER) {
        if (c < 4) asm volatile("s_waitcnt vmcnt(1) lgkmcnt(0)\n\ts_barrier" ::: "memory");
        else asm volatile("s_waitcnt vmcnt(8) lgkmcnt(0)\n\ts_barrier" ::: "memory");
        if (c == 2)
          peek_dev(flags + (FL_H + (mt >> 1)) * 32, lds0 + G1_CS + lw * 256);
        if (c + 2 < AT_NCH) { ld(c + 2, 0); ld(c + 2, 1); }
        if (c < 2) {
          const int j = c * 4 + lw;
          l2_touch(j < 7 ? (const char*)(w2 + (size_t)(gk * 1792 + j * 256 + (mt & 7) * 32 + (l >> 1)) * DM + nt * 128 + (l & 1) * 64)
                         : (const char*)(wo + (size_t)((mt & 7) * 128 + l) * DM + nt * 128 + gk * 64));
        } else if (c == 2) {
          l2_touch(lw == 0 ? (const char*)(wo + (size_t)((mt & 7) * 128 + 64 + l) * DM + nt * 128 + gk * 64) : ksrc);
        } else if (c == 3) {
          if (rfl(*(const LAS unsigned*)((const LAS char*)smem + G1_CS + lw * 256)) < 92u) wait_ge(flags + (FL_H + (mt >> 1)) * 32, 92u);
        }
        else if (c == 6) l2_touch(ws + WS_H + (size_t)(gk * 28 + 4 + lw * 2 + (l >> 5)) * KT + (size_t)(mt * 4 + (nt & 3)) * 4096 + (l & 31) * 128);
      } else {
        asm volatile("s_waitcnt lgkmcnt(0)\n\ts_barrier" ::: "memory");
        if (c == 0 && late_pub) publish(late_pub, l);
        if (!(c & 1)) STAMP(2, 4 + (c >> 1));
      }
      PRIO(3);
    }
    SBAR();
    STEP(sa, pa, sb, pb, ky0, ky1, vb0, vb1, kx0, kx1, va0, va1, nb); XP(c - 4, 0);
    STEP(sb, pb, sa, pa, kx0, kx1, vc0, vc1, ky0, ky1, vb0, vb1, nb + 4096); XP(c - 4, 1);
  }
  o = MFMA32(vd0, PLO(pb), o); o = MFMA32(vd1, PHI(pb), o); RSUM(pb);
#undef EX
#undef CV
#undef PLO
#undef PHI
#undef RSUM
#undef STEP
#undef PRIO
#undef XP
  STAMP(2, 2);
  __builtin_amdgcn_s_setprio(0);
  attn_finish(ws, o, ls, q0, q1, l, ws + WS_O + (size_t)(head >> 1) * KT + (size_t)tt * 4096 + (head & 1) * 2048 + l * 16);
  STAMP(2, 3);
}

DI void g2_load(const u16* w2, const u16* wo, char* __restrict__ ws, unsigned lds0, const int lw, const int l) {
  const int xcd = blockIdx.x & 7, idx = blockIdx.x >> 3;
  const int mt = ((xcd >> 1) & 1) * 8 + (idx & 7);
  unsigned* flags = (unsigned*)(ws + WS_FLAG);
  G2Ld g; g.init(ws, w2, wo, lds0, lw, l);
  asm volatile("s_waitcnt vmcnt(10) lgkmcnt(0)\n\ts_barrier" ::: "memory");
  g.ffn(4); g.ffn(5);
  asm volatile("s_waitcnt vmcnt(16)\n\ts_barrier" ::: "memory");
  if (l == 0) __hip_atomic_fetch_add((gu32*)flags + (FL_O + (blockIdx.x >> 4)) * 32, 1u, RLX_AGENT);
  g.ffn(6);
  const LAS unsigned* peek = (const LAS unsigned*)(size_t)(lds0 + G2_PART);
#pragma unroll 1
  for (int i = 3; i < 24; ++i) {
    asm volatile("s_waitcnt vmcnt(16)\n\ts_barrier" ::: "memory");
    if (i == 20 && lw == 0) peek_dev(flags + (FL_O + mt) * 32, lds0 + G2_PART);
    g.ffn(i + 4);
  }
  asm volatile("s_waitcnt vmcnt(16)\n\ts_barrier" ::: "memory");
  if (rfl(peek[0]) < 128u) wait_ge(flags + (FL_O + mt) * 32, 128u);
  g.wo(28);
  peek = (const LAS unsigned*)(size_t)(lds0 + G2_BOFF + 2048);
#pragma unroll 1
  for (int i = 25; i < 37; ++i) {
    asm volatile("s_waitcnt vmcnt(12)\n\ts_barrier" ::: "memory");
    if (i == 35 && lw == 0) peek_dev(flags + (FL_P + blockIdx.x) * 32, lds0 + G2_BOFF + 2048);
    g.wo(i + 4);
  }
  auto part = [&]() {
    const char* src = ws + WS_PART + (size_t)(blockIdx.x ^ 1) * 32768 + lw * 8192;
    const unsigned d = lds0 + G2_PART + lw * 8192;
    glds16_dev<0>(g.voff, src, d); glds16_dev<1024>(g.voff, src, d); glds16_dev<2048>(g.voff, src, d); glds16_dev<3072>(g.voff, src, d);
    glds16_dev<0>(g.voff, src + 4096, d + 4096); glds16_dev<1024>(g.voff, src + 4096, d + 4096); glds16_dev<2048>(g.voff, src + 4096, d + 4096); glds16_dev<3072>(g.voff, src + 4096, d + 4096);
  };
  asm volatile("s_waitcnt vmcnt(12)\n\ts_barrier" ::: "memory");
  const bool early = rfl(peek[0]) >= 4u;
  g.wo(41);
  if (early) part();
#pragma unroll 1
  for (int i = 38; i < 41; ++i) {
    if (early) asm volatile("s_waitcnt vmcnt(20)\n\ts_barrier" ::: "memory");
    else asm volatile("s_waitcnt vmcnt(12)\n\ts_barrier" ::: "memory");
    if (i < 40) g.wo(i + 4);
  }
  asm volatile("s_waitcnt vmcnt(6)\n\ts_barrier" ::: "memory");
  if (!early) { wait_ge(flags + (FL_P + blockIdx.x) * 32, 4u); part(); }
  asm volatile("s_waitcnt vmcnt(0)\n\ts_barrier" ::: "memory");
}

DI void g2_compute(const u16* __restrict__ x, const u16* __restrict__ b2, char* __restrict__ ws, u16* __restrict__ out,
                   char* smem, const int w, const int l) {
  const int xcd = blockIdx.x & 7, idx = blockIdx.x >> 3;
  const int kh = xcd & 1, mt = ((xcd >> 1) & 1) * 8 + (idx & 7), nt = (xcd >> 2) * 4 + (idx >> 3);
  unsigned* flags = (unsigned*)(ws + WS_FLAG);
  STAMP(3, 0);
  const int h = l >> 5, wm = w & 1, wn = w >> 1;
  f32x16 accg[2] = {}, accp[2] = {}, accs[2] = {};
  const int col0 = nt * 128 + kh * 64 + wn * 32;
  const int row0 = mt * 128 + wm * 64 + (l & 31);
  unsigned stw[2];
  u32x4 xv[4];
  u32x2 bv[4];
#pragma unroll
  for (int m = 0; m < 2; ++m) stw[m] = __hip_atomic_load((gu32*)(ws + WS_STAT) + row0 + m * 32, RLX_AGENT);
#pragma unroll
  for (int k = 0; k < 4; ++k) xv[k] = *(const u32x4*)(x + (size_t)(row0 + (k >> 1) * 32) * DM + col0 + 16 * (k & 1) + 8 * h);
#pragma unroll
  for (int k = 0; k < 4; ++k) bv[k] = *(const u32x2*)(b2 + col0 + 8 * k + 4 * h);
  const LAS char* lbase = (const LAS char*)smem + l * 16;
  const int btr = (4 * h + ((l & 15) >> 2)) * 64 + ((l >> 4) & 1) * 32 + (l & 3) * 8 - l * 16;
#define LDB(p_) cat8(lds_tr(p_), lds_tr((p_) + 512))
  const int ao = wm * 8192;
  const int bG = G2_BOFF + (kh * 2 + wn) * 1024 + btr, bP = G2_BOFF + ((kh ^ 1) * 2 + wn) * 1024 + btr;
  const int bW = G2_BOFF + wn * 1024 + btr;
  bf16x8 fa0[2], fb0[2], fa1[2], fb1[2];
#define G2_RD(FA, FB, base, ks) do { \
    FA[0] = lds_b128((base) + ao + (ks) * 1024); FB[0] = LDB((base) + bG + (ks) * 4096); \
    FA[1] = lds_b128((base) + ao + 4096 + (ks) * 1024); FB[1] = LDB((base) + bP + (ks) * 4096); \
  } while (0)
#define G2_MM(FA, FB) do { \
    accg[0] = MFMA32(FB[0], FA[0], accg[0]); accg[1] = MFMA32(FB[0], FA[1], accg[1]); \
    accp[0] = MFMA32(FB[1], FA[0], accp[0]); accp[1] = MFMA32(FB[1], FA[1], accp[1]); \
  } while (0)
#define G2_SCHED(NR) do { \
    __builtin_amdgcn_sched_group_barrier(0x008, 1, 0); __builtin_amdgcn_sched_group_barrier(0x100, NR, 0); \
    __builtin_amdgcn_sched_group_barrier(0x008, 1, 0); __builtin_amdgcn_sched_group_barrier(0x100, NR, 0); \
    __builtin_amdgcn_sched_group_barrier(0x008, 2, 0); SBAR(); \
  } while (0)
#define G2_BAR() do { asm volatile("s_waitcnt lgkmcnt(0)\n\ts_barrier" ::: "memory"); SBAR(); } while (0)

  SBAR();
  STAMP(3, 1);
  G2_RD(fa0, fb0, lbase, 0); SBAR();
  G2_RD(fa1, fb1, lbase, 1); G2_MM(fa0, fb0); G2_SCHED(3);
  G2_RD(fa0, fb0, lbase, 2); G2_MM(fa1, fb1); G2_SCHED(3);
  G2_RD(fa1, fb1, lbase, 3); G2_MM(fa0, fb0); G2_SCHED(3);
#pragma unroll 1
  for (int i = 0; i < 27; ++i) {
    const LAS char* nb = lbase + ((i + 1) & 3) * G2_SLOT;
    if (i > 0) G2_BAR();
    G2_RD(fa0, fb0, nb, 0); G2_MM(fa1, fb1); G2_SCHED(3);
    G2_RD(fa1, fb1, nb, 1); G2_MM(fa0, fb0); G2_SCHED(3);
    G2_RD(fa0, fb0, nb, 2); G2_MM(fa1, fb1); G2_SCHED(3);
    G2_RD(fa1, fb1, nb, 3); G2_MM(fa0, fb0); G2_SCHED(3);
    if (i == 3) { publish(flags + (FL_O + (blockIdx.x >> 4)) * 32, l); SBAR(); }
  }
  STAMP(3, 4);
  bf16x8 ga0[4], gb0[2], ga1[4], gb1[2];
#define W_RD(GA, GB, base, ks) do { \
    GA[0] = lds_b128((base) + ao + (ks) * 1024); GB[0] = LDB((base) + bW + (ks) * 4096); GA[1] = lds_b128((base) + ao + 4096 + (ks) * 1024); \
    GA[2] = lds_b128((base) + ao + (ks) * 1024 + 1024); GB[1] = LDB((base) + bW + (ks) * 4096 + 4096); GA[3] = lds_b128((base) + ao + 4096 + (ks) * 1024 + 1024); \
  } while (0)
#define W_MM(GA, GB) do { \
    accs[0] = MFMA32(GB[0], GA[0], accs[0]); accs[1] = MFMA32(GB[0], GA[1], accs[1]); \
    accs[0] = MFMA32(GB[1], GA[2], accs[0]); accs[1] = MFMA32(GB[1], GA[3], accs[1]); \
  } while (0)
#define W_STEP(i) do { const LAS char* nb_ = lbase + (((i) + 1) & 3) * G2_SLOT; G2_BAR(); \
    W_RD(ga0, gb0, nb_, 0); W_MM(ga1, gb1); G2_SCHED(4); \
    W_RD(ga1, gb1, nb_, 2); W_MM(ga0, gb0); G2_SCHED(4); } while (0)
  char* pdst = ws + WS_PART + (size_t)blockIdx.x * 32768 + w * 8192 + l * 16;
#define G2_SEND(q) do { asm volatile("s_nop 7" ::: "memory"); \
    _Pragma("unroll") for (int i_ = 2 * ((q) & 1); i_ < 2 * ((q) & 1) + 2; ++i_) \
      store16_dev(pdst + ((q) >> 1) * 4096 + i_ * 1024, (u32x4){__float_as_uint(accp[(q) >> 1][4 * i_]), __float_as_uint(accp[(q) >> 1][4 * i_ + 1]), \
                                                               __float_as_uint(accp[(q) >> 1][4 * i_ + 2]), __float_as_uint(accp[(q) >> 1][4 * i_ + 3])}); SBAR(); } while (0)
  G2_BAR();
  W_RD(ga0, gb0, lbase, 0); G2_MM(fa1, fb1); G2_SCHED(4);
  W_RD(ga1, gb1, lbase, 2); W_MM(ga0, gb0); G2_SCHED(4);
  G2_SEND(0);
  STAMP(3, 5);
  W_STEP(28); G2_SEND(1);
  W_STEP(29); G2_SEND(2);
  W_STEP(30); G2_SEND(3);
#pragma unroll 1
  for (int i = 31; i < 42; ++i) {
    if (i == 40) STAMP(3, 6);
    W_STEP(i);
    if (i == 33) { publish(flags + (FL_P + (blockIdx.x ^ 1)) * 32, l); SBAR(); }
  }
  W_STEP(42);
  W_MM(ga1, gb1);
  STAMP(3, 2);
  {
    const LAS char* xp = (const LAS char*)smem + G2_PART + w * 8192 + l * 16;
#pragma unroll
    for (int m = 0; m < 2; ++m) {
      const float den = bf_hi(stw[m]), rd2 = 1.0f / (den * den);
#pragma unroll
      for (int k = 0; k < 2; ++k) {
        u32x4 xx = xv[m * 2 + k];
        { auto r0 = __builtin_amdgcn_permlane32_swap(xx[0], xx[2], false, false); xx[0] = r0[0]; xx[2] = r0[1];
          auto r1 = __builtin_amdgcn_permlane32_swap(xx[1], xx[3], false, false); xx[1] = r1[0]; xx[3] = r1[1]; }
        unsigned pk[4];
#pragma unroll
        for (int ii = 0; ii < 2; ++ii) {
          const int i = 2 * k + ii;
          const f32x4 tp = *(const LAS f32x4*)(xp + m * 4096 + i * 1024);
          float r[4];
#pragma unroll
          for (int j = 0; j < 4; ++j) {
            const unsigned xw_ = xx[2 * ii + (j >> 1)], bw_ = bv[i][j >> 1];
            const float z = (accg[m][4 * i + j] + tp[j]) * rd2 + ((j & 1) ? bf_hi(bw_) : bf_lo(bw_));
            const float sg = z * __builtin_amdgcn_rcpf(1.0f + __builtin_amdgcn_exp2f(-1.4426950408889634f * z));
            r[j] = ((j & 1) ? bf_hi(xw_) : bf_lo(xw_)) + accs[m][4 * i + j] * sg;
          }
          pk[2 * ii] = cvtpk(r[0], r[1]); pk[2 * ii + 1] = cvtpk(r[2], r[3]);
        }
        auto r0 = __builtin_amdgcn_permlane32_swap(pk[0], pk[2], false, false);
        auto r1 = __builtin_amdgcn_permlane32_swap(pk[1], pk[3], false, false);
        *(u32x4*)(out + (size_t)(row0 + m * 32) * DM + col0 + 16 * k + 8 * h) = (u32x4){r0[0], r1[0], r0[1], r1[1]};
      }
    }
  }
  STAMP(3, 3);
#undef G2_RD
#undef G2_MM
#undef G2_SCHED
#undef G2_BAR
#undef G2_SEND
#undef W_RD
#undef W_MM
#undef W_STEP
#undef LDB
}

__global__ __launch_bounds__(512, 2) void main_kernel(const u16* __restrict__ x, const u16* __restrict__ wq, const u16* __restrict__ wk,
                                                      const u16* __restrict__ wv, const u16* __restrict__ wo, const u16* __restrict__ w1,
                                                      const u16* __restrict__ b1, const u16* __restrict__ w2, const u16* __restrict__ b2,
                                                      char* __restrict__ ws, u16* __restrict__ out) {
  extern __shared__ __attribute__((aligned(16))) char smem[];
  const int l = threadIdx.x & 63, w = rfl(threadIdx.x >> 6);
  const unsigned lds0 = (unsigned)(size_t)(LAS char*)smem;
  const int xcd = blockIdx.x & 7, idx = blockIdx.x >> 3;
  const int mtile = (xcd & 1) * 4 + (idx & 3), cg = (idx >> 2) * 4 + (xcd >> 1);
  unsigned* flags = (unsigned*)(ws + WS_FLAG);
  if (w >= 4) {
    if (cg > 8) g1_load<G1_FFN>(ws, x, w1, nullptr, cg, mtile, smem, w - 4, l);
    else if (cg < 8) g1_load<G1_Q>(ws, x, wq, nullptr, cg, mtile, smem, w - 4, l);
    else g1_load<G1_KV>(ws, x, wk, wv, cg, mtile, smem, w - 4, l);
    attn_phase<true>(ws, w2, wo, smem, w, l, nullptr);
    g2_load(w2, wo, ws, lds0, w - 4, l);
  } else {
    unsigned* late = nullptr;
    if (cg > 8) { g1_compute<G1_FFN>(ws, x, b1, cg, mtile, smem, w, l); late = flags + (FL_H + mtile) * 32; }
    else if (cg < 8) { g1_compute<G1_Q>(ws, x, b1, cg, mtile, smem, w, l); publish(flags + (FL_Q + mtile * 8 + cg) * 32, l); }
    else { g1_compute<G1_KV>(ws, x, b1, cg, mtile, smem, w, l); publish(flags + FL_KV * 32, l); }
    attn_phase<false>(ws, w2, wo, smem, w, l, late);
    g2_compute(x, b2, ws, out, smem, w, l);
  }
}

extern "C" void kernel_launch(void* const* d_in, const int* in_sizes, int n_in, void* d_out, int out_size, void* d_ws,
                              size_t ws_size, hipStream_t stream) {
  const u16* x = (const u16*)d_in[0];
  const u16* wq = (const u16*)d_in[1];
  const u16* wk = (const u16*)d_in[2];
  const u16* wv = (const u16*)d_in[3];
  const u16* wo = (const u16*)d_in[4];
  const u16* w1 = (const u16*)d_in[6];
  const u16* b1 = (const u16*)d_in[7];
  const u16* w2 = (const u16*)d_in[8];
  const u16* b2 = (const u16*)d_in[9];
  char* ws = (char*)d_ws;
  (void)hipFuncSetAttribute((const void*)main_kernel, hipFuncAttributeMaxDynamicSharedMemorySize, MAIN_LDS);
  main_kernel<<<256, 512, MAIN_LDS, stream>>>(x, wq, wk, wv, wo, w1, b1, w2, b2, ws, (u16*)d_out);
}
```

```cpp
#define CPF 1
#include <hip/hip_runtime.h>
#include <hip/hip_bf16.h>

typedef short bf16x8 __attribute__((ext_vector_type(8)));
typedef short s16x4 __attribute__((ext_vector_type(4)));
typedef float f32x16 __attribute__((ext_vector_type(16)));
typedef float f32x4 __attribute__((ext_vector_type(4)));
typedef float f32x2 __attribute__((ext_vector_type(2)));
typedef __bf16 bf16x2_t __attribute__((ext_vector_type(2)));
typedef unsigned u32x4 __attribute__((ext_vector_type(4)));
typedef unsigned u32x2 __attribute__((ext_vector_type(2)));
typedef unsigned u32x8 __attribute__((ext_vector_type(8)));
typedef __bf16 bf16x8_t __attribute__((ext_vector_type(8)));
typedef __bf16 bf16x16_t __attribute__((ext_vector_type(16)));
typedef unsigned short u16;
#define LAS __attribute__((address_space(3)))
#define DI __device__ __forceinline__
#define SBAR() __builtin_amdgcn_sched_barrier(0)
#define PIN(x) asm volatile("" : "+v"(x))
#define MFMA32(a, b, c) __builtin_amdgcn_mfma_f32_32x32x16_bf16((a), (b), (c), 0, 0, 0)
#define MFMA16(a, b, c) __builtin_amdgcn_mfma_f32_16x16x32_bf16((a), (b), (c), 0, 0, 0)

constexpr int SEQ = 2048, DM = 1024, NH = 32, FF = 3584;
constexpr size_t KT = 262144;
constexpr size_t WS_STAT = 0;
constexpr size_t WS_Q = 4u << 20;
constexpr size_t WS_KV = 8u << 20;
constexpr size_t WS_O = 9u << 20;
constexpr size_t WS_H = 13u << 20;
constexpr size_t WS_PART = 28u << 20;
constexpr size_t WS_FLAG = 37u << 20;
constexpr int FL_P = 0, FL_O = 256, FL_H = 272, FL_Q = 280, FL_KV = 344, FL_S = 345, FL_N = 353;
constexpr size_t WS_HSA = WS_FLAG + 65536, WS_HSB = WS_HSA + 4096, WS_HSC = WS_HSB + 4096;
constexpr int COORD_BLOCK = 64;

#define STAMP(kid, idx) do {} while (0)
DI unsigned rfl(unsigned x) { return __builtin_amdgcn_readfirstlane(x); }
DI unsigned cvtpk(float lo, float hi) {
  f32x2 v = {lo, hi};
  bf16x2_t b = __builtin_convertvector(v, bf16x2_t);
  return __builtin_bit_cast(unsigned, b);
}
DI float fadd_s(float a, float b) { float r; asm("v_add_f32_e32 %0, %1, %2" : "=v"(r) : "v"(a), "v"(b)); return r; }
DI float bf_lo(unsigned u) { return __uint_as_float(u << 16); }
DI float bf_hi(unsigned u) { return __uint_as_float(u & 0xffff0000u); }
DI float bf_round(float x) { return bf_lo(cvtpk(x, x)); }

template <int IMM>
DI void glds16(unsigned voff, const void* sbase, unsigned ldsaddr) {
  asm volatile("s_mov_b32 m0, %2\n\ts_nop 0\n\tglobal_load_lds_dwordx4 %0, %1 offset:%3"
               :: "v"(voff), "s"(sbase), "s"(ldsaddr), "i"(IMM) : "memory");
}
template <int IMM>
DI void glds16_dev(unsigned voff, const void* sbase, unsigned ldsaddr) {
  asm volatile("s_mov_b32 m0, %2\n\ts_nop 0\n\tglobal_load_lds_dwordx4 %0, %1 offset:%3 sc1"
               :: "v"(voff), "s"(sbase), "s"(ldsaddr), "i"(IMM) : "memory");
}
DI void peek_dev(const void* word, unsigned ldsaddr) {
  asm volatile("s_mov_b32 m0, %2\n\ts_nop 0\n\tglobal_load_lds_dword %0, %1 sc1" :: "v"(0u), "s"(word), "s"(ldsaddr) : "memory");
}
DI void store16_dev(void* p, u32x4 v) {
  asm volatile("global_store_dwordx4 %0, %1, off sc1\n\ts_nop 1" :: "v"(p), "v"(v) : "memory");
}
DI void l2_touch(const void* p, unsigned ldsaddr) {
  asm volatile("s_mov_b32 m0, %1\n\ts_nop 0\n\tglobal_load_lds_dword %0, off" :: "v"(p), "s"(ldsaddr) : "memory");
}
typedef __attribute__((address_space(1))) unsigned gu32;
#define RLX_AGENT __ATOMIC_RELAXED, __HIP_MEMORY_SCOPE_AGENT
DI void wait_ge(const unsigned* word, unsigned want) {
#pragma unroll 1
  for (unsigned spins = 0; spins < (1u << 22); ++spins) {
    if (rfl(__hip_atomic_load((gu32*)word, RLX_AGENT)) >= want) break;
    __builtin_amdgcn_s_sleep(2);
  }
  __builtin_amdgcn_fence(__ATOMIC_ACQUIRE, "wavefront");
}
DI void publish(unsigned* word, int l) {
  asm volatile("s_waitcnt vmcnt(0)" ::: "memory");
  if (l == 0) __hip_atomic_fetch_add((gu32*)word, 1u, RLX_AGENT);
}
DI bf16x8 load16_dev(const char* p) {
  typedef __attribute__((address_space(1))) unsigned long long gu64;
  const unsigned long long a = __hip_atomic_load((gu64*)p, RLX_AGENT), b = __hip_atomic_load((gu64*)p + 1, RLX_AGENT);
  return __builtin_bit_cast(bf16x8, (u32x4){(unsigned)a, (unsigned)(a >> 32), (unsigned)b, (unsigned)(b >> 32)});
}
DI float relu_s(float a) { float r; asm("v_max_f32_e32 %0, 0, %1" : "=v"(r) : "v"(a)); return r; }
DI bf16x8 lds_b128(const LAS char* p) { return *(const LAS bf16x8*)p; }
DI s16x4 lds_tr(const LAS char* p) {
  return __builtin_amdgcn_ds_read_tr16_b64_v4i16((LAS s16x4*)p);
}
DI f32x16 sel16(bool c, const f32x16& a, const f32x16& b) {
  f32x16 r;
#pragma unroll
  for (int i = 0; i < 16; ++i) r[i] = c ? a[i] : b[i];
  return r;
}
DI bf16x8 cat8(s16x4 lo, s16x4 hi) { return __builtin_shufflevector(lo, hi, 0, 1, 2, 3, 4, 5, 6, 7); }

DI float half_sum(float v) {
  v += __uint_as_float(__builtin_amdgcn_update_dpp(0u, __float_as_uint(v), 0xB1, 0xf, 0xf, true));
  v += __uint_as_float(__builtin_amdgcn_update_dpp(0u, __float_as_uint(v), 0x4E, 0xf, 0xf, true));
  v += __uint_as_float(__builtin_amdgcn_update_dpp(0u, __float_as_uint(v), 0x141, 0xf, 0xf, true));
  v += __uint_as_float(__builtin_amdgcn_update_dpp(0u, __float_as_uint(v), 0x140, 0xf, 0xf, true));
  auto r = __builtin_amdgcn_permlane16_swap(__float_as_uint(v), __float_as_uint(v), false, false);
  return __uint_as_float(r[0]) + __uint_as_float(r[1]);
}
DI unsigned row_stats(const u32x4 (&v)[4]) {
  f32x2 e[16], s2 = {0.f, 0.f};
#pragma unroll
  for (int i = 0; i < 16; ++i) { e[i] = (f32x2){bf_lo(v[i >> 2][i & 3]), bf_hi(v[i >> 2][i & 3])}; s2 += e[i]; }
  const float mean = half_sum(s2[0] + s2[1]) * (1.0f / DM);
  f32x2 q2 = {0.f, 0.f};
#pragma unroll
  for (int i = 0; i < 16; ++i) { const f32x2 d = e[i] - mean; q2 += d * d; }
  const float q = half_sum(q2[0] + q2[1]);
  const float den = __builtin_sqrtf(bf_round(bf_round(q * (1.0f / DM)) + 1e-5f));
  return cvtpk(-mean, den);
}

constexpr int G1_STAGE = 53248, G1_BOFF = 32768;
enum { G1_FFN = 5, G1_Q = 4, G1_KV = 2 };

constexpr int G1_ST = 87040;
constexpr int G1_HS = 159744;
constexpr int G1_CS = 81920;
#define PSTART 9
#define ESTART 3
#define FROT 3
#define WROT (FROT ? 2 : 0)
#define KROT 4
DI float pair_sum(unsigned v) { return fadd_s(bf_lo(v), bf_hi(v)); }

template <int NP>
DI void g1_load(char* ws, const u16* x, const u16* wsrc, const u16* wv, int cg, int mtile, char* smem, int lw, int l) {
  constexpr int LD = (NP == G1_FFN) ? FF : (NP == G1_Q) ? DM : 32;
  const unsigned lds0 = (unsigned)(size_t)(LAS char*)smem;
  const char* abase = (const char*)(x + (size_t)(mtile * 256 + lw * 64) * DM);
  const unsigned ave = (unsigned)((l >> 3) * 2048 + (((l & 7) ^ (l >> 4)) << 4)), avo = ave ^ 64u;
  const unsigned bvoff = (unsigned)((lw * 16 + (l >> 2)) * LD * 2 + (l & 3) * 16);
  const int krot = NP == G1_Q ? (mtile & 3) * KROT : 0;
  const char* bbase[NP];
#pragma unroll
  for (int p = 0; p < NP; ++p) {
    if (NP == G1_KV) bbase[p] = (const char*)(p ? wv : wsrc);
    else if (NP == G1_Q) bbase[p] = (const char*)(wsrc + (cg * 4 + p) * 32);
    else { const int cb = (cg - 9) * 5 + p; bbase[p] = (const char*)(wsrc + (cb > 111 ? 111 : cb) * 32); }
  }
  float cs[NP];
#pragma unroll
  for (int p = 0; p < NP; ++p) cs[p] = 0.f;
  const LAS char* cb0 = (const LAS char*)smem + G1_BOFF + lw * 1024 + (8 * (l >> 5) + ((l & 15) >> 2)) * 64 + ((l >> 4) & 1) * 32 + (l & 3) * 8;
  u32x2 t0[NP], t1[NP];
  auto cs_read = [&](int slot) {
#pragma unroll
    for (int p = 0; p < NP; ++p) {
      t0[p] = __builtin_bit_cast(u32x2, lds_tr(cb0 + slot * G1_STAGE + p * 4096));
      t1[p] = __builtin_bit_cast(u32x2, lds_tr(cb0 + slot * G1_STAGE + p * 4096 + 256));
    }
    SBAR();
  };
  auto cs_add = [&](int p) {
    cs[p] = fadd_s(cs[p], fadd_s(fadd_s(pair_sum(t0[p][0]), pair_sum(t0[p][1])), fadd_s(pair_sum(t1[p][0]), pair_sum(t1[p][1]))));
    PIN(cs[p]); SBAR();
  };
  auto issue = [&](int st, int slot, bool sum) {
    const int kt = (st + krot) & 15;
    const unsigned sa = lds0 + slot * G1_STAGE + lw * 8192;
    const char* a0 = abase + kt * 128;
    const unsigned sb = lds0 + slot * G1_STAGE + G1_BOFF + lw * 1024;
#pragma unroll
    for (int p = 0; p < NP; ++p) glds16<0>(bvoff, bbase[p] + (size_t)kt * (64 * LD * 2), sb + p * 4096);
#pragma unroll
    for (int i = 0; i < 8; ++i) {
      if (sum && i < NP) { SBAR(); cs_add(i); }
      glds16<0>((i & 1) ? avo : ave, a0 + i * 16384, sa + i * 1024);
    }
  };
  auto colsum = [&](int slot) {
    cs_read(slot);
#pragma unroll
    for (int p = 0; p < NP; ++p) cs_add(p);
  };
  unsigned* flags = (unsigned*)(ws + WS_FLAG);
  const bool coord = (NP == G1_KV) && blockIdx.x == COORD_BLOCK && lw == 1;
  if (coord) {
#pragma unroll
    for (int i = 0; i < 6; ++i) if (i * 64 + l < FL_N) __hip_atomic_store((gu32*)flags + (i * 64 + l) * 32, 0u, RLX_AGENT);
    store16_dev(ws + WS_HSC + l * 16, (u32x4){0u, 0u, 0u, 0u});
  }
  issue(0, 0, false); issue(1, 1, false); issue(2, 2, false);
  asm volatile("s_waitcnt vmcnt(%0)\n\ts_barrier" :: "n"(2 * (8 + NP)) : "memory");
  __builtin_amdgcn_s_setprio(2);
  int state = (lw == 0 || coord) ? 0 : 2;
  bool pend = false, first = true;
  u32x4 last = {0u, 0u, 0u, 0u};
  const unsigned found = rfl(*(const LAS unsigned*)((const LAS char*)smem + G1_HS));
  if (lw == 0 && l < 9) {
    const unsigned v = *(const LAS unsigned*)((const LAS char*)smem + G1_HS + 16 + (l & 7) * 4);
    __hip_atomic_store(l < 8 ? (gu32*)(ws + WS_STAT) + mtile * 256 + cg * 8 + l : (gu32*)(ws + WS_HSA) + blockIdx.x, l < 8 ? v : found + 1u, RLX_AGENT);
  }
  colsum(0);
  auto poll_issue = [&]() {
    if (NP == G1_KV && coord) {
      glds16_dev<0>(l * 16, ws + WS_HSA, lds0 + G1_HS + 1024); glds16_dev<0>(l * 16, ws + WS_HSC, lds0 + G1_HS + 2048);
    } else {
      const char* a_ = state == 0 ? ws + WS_HSB + blockIdx.x * 4 : (const char*)(flags + (FL_S + mtile) * 32);
      peek_dev(a_, lds0 + G1_HS + 256);
    }
  };
  auto poll_check = [&]() {
    if (NP == G1_KV && coord) {
      const u32x4 pa = *(const LAS u32x4*)((const LAS char*)smem + G1_HS + 1024 + l * 16), pc = *(const LAS u32x4*)((const LAS char*)smem + G1_HS + 2048 + l * 16);
      if (first || pa[0] != last[0] || pa[1] != last[1] || pa[2] != last[2] || pa[3] != last[3]) store16_dev(ws + WS_HSB + l * 16, pa);
      last = pa; first = false;
      if (__all((pc[0] & pc[1] & pc[2] & pc[3]) == 1u)) state = 2;
    } else {
      const unsigned v = rfl(*(const LAS unsigned*)((const LAS char*)smem + G1_HS + 256));
      if (state == 0) {
        if (v != found) {
          if (l == 0) {
            __hip_atomic_store((gu32*)(ws + WS_HSC) + blockIdx.x, 1u, RLX_AGENT);
            __hip_atomic_fetch_add((gu32*)flags + (FL_S + mtile) * 32, 1u, RLX_AGENT);
          }
          state = 1;
        }
      } else if (v >= 32u) state = 2;
    }
    state = rfl(state);
  };
  int slot = 0;
#pragma unroll 1
  for (int i = 0; i < 13; ++i) {
    asm volatile("s_waitcnt vmcnt(%0)\n\ts_barrier" :: "n"(8 + NP) : "memory");
    if (state < 2 && (i >= ESTART || coord)) {
      if (pend) poll_check();
      pend = state == 0 || (state == 1 && i >= PSTART);
      if (pend) poll_issue();
    }
    const int nslot = (slot == 2) ? 0 : slot + 1;
    cs_read(nslot);
    issue(i + 3, slot, true);
    slot = nslot;
  }
#pragma unroll 1
  for (unsigned spins = 0; state < 2 && spins < (1u << 22); ++spins) {
    if (!pend) poll_issue();
    asm volatile("s_waitcnt vmcnt(0)" ::: "memory");
    poll_check();
    pend = false;
    __builtin_amdgcn_s_sleep(1);
  }
  asm volatile("s_waitcnt vmcnt(%0)\n\ts_barrier" :: "n"(8 + NP) : "memory");
  if (lw == 0) glds16_dev<0>(l * 16, ws + WS_STAT + mtile * 1024, lds0 + G1_ST);
  colsum(2);
  asm volatile("s_waitcnt vmcnt(0)" ::: "memory");
  colsum(0);
#pragma unroll
  for (int p = 0; p < NP; ++p) *(LAS float*)((LAS char*)smem + G1_CS + p * 1024 + (l & 31) * 32 + (lw * 2 + (l >> 5)) * 4) = cs[p];
  asm volatile("s_waitcnt lgkmcnt(0)\n\ts_barrier" ::: "memory");
}

template <int NP>
DI void g1_compute(char* __restrict__ ws, const u16* __restrict__ x, const u16* __restrict__ b1, int cg, int mtile, char* smem, int w, int l,
                   u32x4 (&hp)[20]) {
  STAMP(1, 0);
  const int h = l >> 5;
  unsigned st[2], bias[NP];
  {
    const int row = mtile * 256 + cg * 8 + w * 2 + h;
    u32x4 v[4];
#pragma unroll
    for (int i = 0; i < 4; ++i) v[i] = *(const u32x4*)(x + (size_t)row * DM + (i * 32 + (l & 31)) * 8);
    const unsigned found = __hip_atomic_load((gu32*)(ws + WS_HSB) + blockIdx.x, RLX_AGENT);
#pragma unroll
    for (int p = 0; p < NP; ++p) {
      int cb = (cg - 9) * 5 + p; cb = cb > 111 ? 111 : cb;
      bias[p] = (NP == G1_FFN) ? (unsigned)b1[cb * 32 + (l & 31)] : 0u;
    }
    SBAR();
    const unsigned rs = row_stats(v);
    if ((l & 31) == 0) *(LAS unsigned*)((LAS char*)smem + G1_HS + 16 + (w * 2 + h) * 4) = rs;
    if (w == 0 && l == 0) *(LAS unsigned*)((LAS char*)smem + G1_HS) = found;
  }
  f32x16 acc[2][NP];
#pragma unroll
  for (int p = 0; p < NP; ++p) { acc[0][p] = f32x16{}; acc[1][p] = f32x16{}; }
  const LAS char* lbase = (const LAS char*)smem;
  int aoff[4];
#pragma unroll
  for (int ks = 0; ks < 4; ++ks) aoff[ks] = (w * 64 + (l & 31)) * 128 + (((2 * ks + h) ^ ((l >> 1) & 7)) << 4);
  const int boff = G1_BOFF + (8 * h + ((l & 15) >> 2)) * 64 + ((l >> 4) & 1) * 32 + (l & 3) * 8;
  bf16x8 fa0[2], fb0[NP], fa1[2], fb1[NP];
#define G1_RD(FA, FB, base, ks) do { \
    FA[0] = lds_b128((base) + aoff[ks]); FA[1] = lds_b128((base) + aoff[ks] + 4096); \
    _Pragma("unroll") for (int p_ = 0; p_ < NP; ++p_) \
      FB[p_] = cat8(lds_tr((base) + boff + p_ * 4096 + (ks) * 1024), lds_tr((base) + boff + p_ * 4096 + (ks) * 1024 + 256)); \
  } while (0)
#define G1_MM(FA, FB) do { \
    _Pragma("unroll") for (int p_ = 0; p_ < NP; ++p_) { \
      if (NP == G1_KV && p_ == 1) { acc[0][p_] = MFMA32(FA[0], FB[p_], acc[0][p_]); acc[1][p_] = MFMA32(FA[1], FB[p_], acc[1][p_]); } \
      else { acc[0][p_] = MFMA32(FB[p_], FA[0], acc[0][p_]); acc[1][p_] = MFMA32(FB[p_], FA[1], acc[1][p_]); } } \
  } while (0)
#define G1_SCHED() do { \
    _Pragma("unroll") for (int q_ = 0; q_ < NP + 1; ++q_) { \
      __builtin_amdgcn_sched_group_barrier(0x008, 1, 0); __builtin_amdgcn_sched_group_barrier(0x100, 2, 0); } \
    __builtin_amdgcn_sched_group_barrier(0x008, NP - 1, 0); SBAR(); \
  } while (0)

  asm volatile("s_waitcnt lgkmcnt(0)\n\ts_barrier" ::: "memory");
  STAMP(1, 1);
  G1_RD(fa0, fb0, lbase, 0); SBAR();
  G1_RD(fa1, fb1, lbase, 1); G1_MM(fa0, fb0); G1_SCHED();
  G1_RD(fa0, fb0, lbase, 2); G1_MM(fa1, fb1); G1_SCHED();
  G1_RD(fa1, fb1, lbase, 3); G1_MM(fa0, fb0); G1_SCHED();
  int slot = 1;
#pragma unroll 1
  for (int i = 0; i < 15; ++i) {
    const LAS char* nb = lbase + slot * G1_STAGE;
    asm volatile("s_waitcnt lgkmcnt(0)\n\ts_barrier" ::: "memory"); SBAR();
    G1_RD(fa0, fb0, nb, 0); G1_MM(fa1, fb1); G1_SCHED();
    G1_RD(fa1, fb1, nb, 1); G1_MM(fa0, fb0); G1_SCHED();
    G1_RD(fa0, fb0, nb, 2); G1_MM(fa1, fb1); G1_SCHED();
    G1_RD(fa1, fb1, nb, 3); G1_MM(fa0, fb0); G1_SCHED();
    slot = (slot == 2) ? 0 : slot + 1;
  }
  STAMP(1, 4);
  {
    f32x4 t[NP][2];
#pragma unroll
    for (int m = 0; m < 2; ++m) st[m] = *(const LAS unsigned*)(lbase + G1_ST + (w * 64 + m * 32 + (l & 31)) * 4);
#pragma unroll
    for (int p = 0; p < NP; ++p) {
      t[p][0] = *(const LAS f32x4*)(lbase + G1_CS + p * 1024 + (l & 31) * 32);
      t[p][1] = *(const LAS f32x4*)(lbase + G1_CS + p * 1024 + (l & 31) * 32 + 16);
    }
    SBAR();
    G1_MM(fa1, fb1);
    const unsigned msk = h ? 0u : ~0u;
#pragma unroll
    for (int m = 0; m < 2; ++m) {
      const unsigned nm = st[m] & 0xffffu;
      fa0[m] = __builtin_bit_cast(bf16x8, (u32x4){(nm | (nm << 16)) & msk, st[m] & msk, 0u, 0u});
    }
#pragma unroll
    for (int p = 0; p < NP; ++p) {
      const f32x4 u = t[p][0] + t[p][1];
      const float c = (u[0] + u[1]) + (u[2] + u[3]);
      const unsigned c0 = cvtpk(c, c) & 0xffffu; const float r1 = c - bf_lo(c0);
      const unsigned c1 = cvtpk(r1, r1) & 0xffffu; const float r2 = r1 - bf_lo(c1);
      const unsigned c2 = cvtpk(r2, r2) & 0xffffu;
      fb0[p] = __builtin_bit_cast(bf16x8, (u32x4){c0 | (c1 << 16), c2 | (bias[p] << 16), 0u, 0u});
    }
  }
  G1_MM(fa0, fb0);
  float rden[2];
#pragma unroll
  for (int m = 0; m < 2; ++m) rden[m] = 1.0f / bf_hi(st[m]);
  asm volatile("s_nop 15\n\ts_nop 3" ::: "memory"); SBAR();
  STAMP(1, 2);

#pragma unroll
  for (int p = 0; p < NP; ++p) {
    const int cb = (NP == G1_FFN) ? (cg - 9) * 5 + p : cg * 4 + p;
    {
#pragma unroll
      for (int m = 0; m < 2; ++m) {
        const int tt = mtile * 8 + w * 2 + m;
        char* dst = (NP == G1_FFN) ? ws + WS_H + (size_t)(cb >> 1) * KT + (size_t)tt * 4096 + (cb & 1) * 2048 + l * 16
                  : (NP == G1_Q)   ? ws + WS_Q + (size_t)(tt * 32 + cb) * 2048 + l * 16
                                   : ws + WS_KV + (size_t)tt * 4096 + p * 2048 + l * 16;
        const float sc = (NP == G1_Q) ? rden[m] * (0.17677669529663687f * 1.4426950408889634f) : rden[m];
#pragma unroll
        for (int s = 0; s < 2; ++s) {
          u32x4 o;
#pragma unroll
          for (int j = 0; j < 4; ++j) {
            float a = acc[m][p][8 * s + 2 * j], b = acc[m][p][8 * s + 2 * j + 1];
            if (NP == G1_FFN) { a = relu_s(a); b = relu_s(b); a *= a; b *= b; }
            else if (NP == G1_KV && p == 1) {
              const int r = 8 * s + 2 * j, tk = (r & 3) + 8 * (r >> 2) + 4 * h;
              a *= __shfl(rden[m], tk); b *= __shfl(rden[m], tk + 1);
            } else { a *= sc; b *= sc; }
            o[j] = cvtpk(a, b);
          }
          if (NP == G1_FFN) { hp[p * 4 + m * 2 + s] = o; PIN(hp[p * 4 + m * 2 + s]); }
          else store16_dev(dst + s * 1024, o);
        }
      }
    }
  }
  if (NP != G1_FFN) {
#pragma unroll
    for (int i = 0; i < 20; ++i) hp[i] = (u32x4){0u, 0u, 0u, 0u};
  }
  STAMP(1, 3);
#undef G1_RD
#undef G1_MM
#undef G1_SCHED
}

constexpr int AT_NCH = 8;
constexpr int AT_BUF = 32768;
constexpr int AT_BASE = 98304;
constexpr int AT_Q = 65536;
constexpr int AT_DUMP = 90112;
constexpr int MAIN_LDS = 163840;

DI float xhalf_max(float m) {
  auto rr = __builtin_amdgcn_permlane32_swap(__float_as_uint(m), __float_as_uint(m), false, false);
  return __builtin_fmaxf(__uint_as_float(rr[0]), __uint_as_float(rr[1]));
}
DI float xhalf_sum(float m) {
  auto rr = __builtin_amdgcn_permlane32_swap(__float_as_uint(m), __float_as_uint(m), false, false);
  return __uint_as_float(rr[0]) + __uint_as_float(rr[1]);
}
DI float max16(const f32x16& s) {
  float a = __builtin_fmaxf(s[0], s[1]);
#pragma unroll
  for (int r = 2; r < 16; ++r) a = __builtin_fmaxf(a, s[r]);
  return a;
}
DI bf16x8 pack8(const f32x16& p, int s) {
  u32x4 o;
#pragma unroll
  for (int j = 0; j < 4; ++j) o[j] = cvtpk(p[8 * s + 2 * j], p[8 * s + 2 * j + 1]);
  return __builtin_bit_cast(bf16x8, o);
}

DI void attn_fallback(const char* __restrict__ kv, bf16x8 q0, bf16x8 q1, int l, f32x16& o_out, float& l_out) {
  f32x16 o = {};
  float m = -INFINITY, lsum = 0.f;
  for (int t = 0; t < 64; ++t) {
    const char* p = kv + (size_t)t * 4096 + l * 16;
    const bf16x8 k0 = load16_dev(p), k1 = load16_dev(p + 1024), v0 = load16_dev(p + 2048), v1 = load16_dev(p + 3072);
    f32x16 s = {};
    s = MFMA32(k0, q0, s); s = MFMA32(k1, q1, s);
    const float mn = __builtin_fmaxf(m, xhalf_max(max16(s)));
    const float ms = (mn == -INFINITY) ? 0.f : mn;
    const float alpha = __builtin_amdgcn_exp2f(m - ms);
    m = mn;
    float ps = 0.f;
#pragma unroll
    for (int r = 0; r < 16; ++r) { s[r] = __builtin_amdgcn_exp2f(s[r] - ms); ps += s[r]; }
    lsum = lsum * alpha + ps;
#pragma unroll
    for (int r = 0; r < 16; ++r) o[r] *= alpha;
    o = MFMA32(v0, pack8(s, 0), o); o = MFMA32(v1, pack8(s, 1), o);
  }
  o_out = o;
  l_out = xhalf_sum(lsum);
}

constexpr int G2_SLOT = 32768, G2_BOFF = 16384, G2_PART = 131072;
struct G2Ld {
  const char* hb; const char* ob; const char* wb; const char* wb2; unsigned lds; unsigned voff, voffb; int rot, frot;
  DI void init(const char* ws, const u16* w2, const u16* wo, unsigned lds0, int lw, int l) {
    const int xcd = blockIdx.x & 7, idx = blockIdx.x >> 3, kh = xcd & 1;
    const int mt = ((xcd >> 1) & 1) * 8 + (idx & 7), nt = (xcd >> 2) * 4 + (idx >> 3);
    hb = ws + WS_H + (size_t)(mt * 4 + lw) * 4096 + (size_t)kh * (28 * KT);
    ob = ws + WS_O + (size_t)(mt * 4 + lw) * 4096;
    wb = (const char*)(w2 + (size_t)(kh * 1792 + lw * 16) * DM + nt * 128);
    wb2 = (const char*)(wo + (size_t)(lw * 16) * DM + nt * 128 + kh * 64);
    voffb = (unsigned)((l >> 2) * DM * 2 + (l & 3) * 16);
    lds = lds0 + lw * 4096; voff = l * 16;
    frot = FROT * (mt & 7);
    rot = WROT * (mt & 7);
  }
  DI void ffn(int s) const {
    const unsigned d = lds + (s & 3) * G2_SLOT;
    s += frot; s = s >= 28 ? s - 28 : s;
    const char* a = hb + (size_t)s * KT;
    glds16_dev<0>(voff, a, d); glds16_dev<1024>(voff, a, d); glds16_dev<2048>(voff, a, d); glds16_dev<3072>(voff, a, d);
    const char* b = wb + (size_t)s * 131072;
    glds16<0>(voffb, b, d + G2_BOFF); glds16<0>(voffb, b + 64, d + G2_BOFF + 1024); glds16<0>(voffb, b + 128, d + G2_BOFF + 2048); glds16<0>(voffb, b + 192, d + G2_BOFF + 3072);
  }
  template <int J> DI void ffn_piece(int s) const {
    const unsigned d = lds + (s & 3) * G2_SLOT;
    s += frot; s = s >= 28 ? s - 28 : s;
    if (J < 4) glds16_dev<(J & 3) * 1024>(voff, hb + (size_t)s * KT, d);
    else glds16<0>(voffb, wb + (size_t)s * 131072 + (J & 3) * 64, d + G2_BOFF + (J & 3) * 1024);
  }
  DI void wo(int s) const {
    const unsigned d = lds + (s & 3) * G2_SLOT;
    const int k = (s - 28 + rot) & 15;
    const char* a = ob + (size_t)k * KT;
    glds16_dev<0>(voff, a, d); glds16_dev<1024>(voff, a, d); glds16_dev<2048>(voff, a, d); glds16_dev<3072>(voff, a, d);
    const char* b = wb2 + (size_t)k * 131072;
    glds16<0>(voffb, b, d + G2_BOFF); glds16<0>(voffb, b + 64, d + G2_BOFF + 1024);
  }
};

DI void attn_finish(char* __restrict__ ws, f32x16 o, f32x4 lacc, bf16x8 q0, bf16x8 q1, int l, char* dst) {
  const float l0 = __shfl(lacc[0] + lacc[1], l & 15), l1 = __shfl(lacc[2] + lacc[3], l & 15);
  float lsum = (l & 16) ? l1 : l0;
  bool ok = (lsum >= 9.5367431640625e-07f) && (lsum <= 1.8446744073709552e19f);
#pragma unroll
  for (int r = 0; r < 16; ++r) ok = ok && (__builtin_fabsf(o[r]) <= 3.0e38f);
  if (!__all(ok)) attn_fallback(ws + WS_KV, q0, q1, l, o, lsum);
  const float rl = 1.0f / lsum;
#pragma unroll
  for (int s = 0; s < 2; ++s) {
    u32x4 ov;
#pragma unroll
    for (int j = 0; j < 4; ++j) ov[j] = cvtpk(o[8 * s + 2 * j] * rl, o[8 * s + 2 * j + 1] * rl);
    store16_dev(dst + s * 1024, ov);
  }
}

template <bool LOADER>
DI void attn_phase(char* __restrict__ ws, const u16* w2, const u16* wo, char* smem, const int w, const int l, unsigned* late_pub,
                   const u32x4 (&hp)[20], int hcb, int htt) {
  const int tt = blockIdx.x >> 2, hg = blockIdx.x & 3;
  const int head = hg * 8 + w, lw = w & 3;
  const unsigned lds0 = (unsigned)(size_t)(LAS char*)smem;
  unsigned* flags = (unsigned*)(ws + WS_FLAG);
  const unsigned voff = l * 16;
  const char* ksrc = ws + WS_KV + (size_t)lw * 4096;
  const unsigned kdst = lds0 + AT_BASE + lw * 4096;
  auto ld = [&](int c, int half) {
    const char* s_ = ksrc + (size_t)c * AT_BUF + half * 16384;
    const unsigned d_ = kdst + (c & 1) * AT_BUF + half * 16384;
    glds16_dev<0>(voff, s_, d_); glds16_dev<1024>(voff, s_, d_); glds16_dev<2048>(voff, s_, d_); glds16_dev<3072>(voff, s_, d_);
  };
  G2Ld g2;
  const unsigned tdump = lds0 + AT_DUMP + lw * 256;
  const int xcd = blockIdx.x & 7, idx = blockIdx.x >> 3, gk = xcd & 1;
  const int mt = ((xcd >> 1) & 1) * 8 + (idx & 7), nt = (xcd >> 2) * 4 + (idx >> 3);
  STAMP(2, 0);
  if (LOADER) {
    const unsigned* word = flags + ((l & 1) ? FL_Q + (tt >> 3) * 8 + hg * 2 + (lw >> 1) : FL_KV) * 32;
    const unsigned need = (l & 1) ? 4u : 32u;
#pragma unroll 1
    for (unsigned spins = 0; spins < (1u << 22); ++spins) {
      if (__all(__hip_atomic_load((gu32*)word, RLX_AGENT) >= need)) break;
      __builtin_amdgcn_s_sleep(2);
    }
    __builtin_amdgcn_fence(__ATOMIC_ACQUIRE, "wavefront");
    const char* qs = ws + WS_Q + (size_t)(tt * 32 + hg * 8 + lw * 2) * 2048;
    const unsigned qd = lds0 + AT_Q + lw * 4096;
    glds16_dev<0>(voff, qs, qd); glds16_dev<1024>(voff, qs, qd); glds16_dev<2048>(voff, qs, qd); glds16_dev<3072>(voff, qs, qd);
    ld(0, 0); ld(0, 1); ld(1, 0); ld(1, 1);
    l2_touch(wo, tdump);
    g2.init(ws, w2, wo, lds0, lw, l);
  }
  const unsigned s_lo = (l & 14) == 0 ? 0x3f803f80u : 0u, s_hi = (l & 14) == 2 ? 0x3f803f80u : 0u;
  const bf16x8_t sel = __builtin_bit_cast(bf16x8_t, (u32x4){s_lo, s_lo, s_hi, s_hi});
  const int sidx = (l & 1) ? 0xeeee : 0x4444;
  f32x16 o = {}, sa, sb;
  f32x4 ls = {};
  u32x8 pa, pb = {};
  PIN(o);
  if (LOADER) asm volatile("s_waitcnt vmcnt(9)\n\ts_barrier" ::: "memory");
  else asm volatile("s_barrier" ::: "memory");
  STAMP(2, 1);

  const LAS char* qp = (const LAS char*)smem + AT_Q + w * 2048 + l * 16;
  const bf16x8 q0 = lds_b128(qp), q1 = lds_b128(qp + 1024);
  const LAS char* kvp = (const LAS char*)smem + AT_BASE + l * 16;
  bf16x8 kx0, kx1, ky0, ky1, va0, va1, vb0, vb1, vc0, vc1, vd0, vd1;
  kx0 = lds_b128(kvp); kx1 = lds_b128(kvp + 1024); ky0 = lds_b128(kvp + 4096); ky1 = lds_b128(kvp + 4096 + 1024);
  va0 = lds_b128(kvp + 2048); va1 = lds_b128(kvp + 3072); vb0 = lds_b128(kvp + 4096 + 2048); vb1 = lds_b128(kvp + 4096 + 3072);
  vd0 = va0; vd1 = va1;
  f32x16 nm;
  {
    sa = MFMA32(kx0, q0, o); sa = MFMA32(kx1, q1, sa);
    const float ma = -xhalf_max(max16(sa));
#pragma unroll
    for (int r = 0; r < 16; ++r) { nm[r] = ma; sa[r] += ma; }
    PIN(nm);
  }
#define EX(S, i) S[i] = __builtin_amdgcn_exp2f(S[i])
#define CV(P, j, S, i) P[j] = cvtpk(S[i], S[(i) + 1])
#define PLO(x) __builtin_bit_cast(bf16x8, __builtin_shufflevector(x, x, 0, 1, 2, 3))
#define PHI(x) __builtin_bit_cast(bf16x8, __builtin_shufflevector(x, x, 4, 5, 6, 7))
#define RSUM(x) ls = __builtin_amdgcn_smfmac_f32_16x16x64_bf16(sel, __builtin_bit_cast(bf16x16_t, x), ls, sidx, 0, 0)
#define STEP(S, P, SN, PP, KN0, KN1, VP0, VP1, KL0, KL1, VL0, VL1, NP) do { \
    SN = MFMA32(KN0, q0, nm); SBAR(); KL0 = lds_b128(NP); EX(S, 0); EX(S, 1); EX(S, 2); EX(S, 3); PIN(S); SBAR(); \
    SN = MFMA32(KN1, q1, SN); SBAR(); KL1 = lds_b128((NP) + 1024); CV(P, 0, S, 0); CV(P, 1, S, 2); EX(S, 4); EX(S, 5); EX(S, 6); EX(S, 7); PIN(S); PIN(P); SBAR(); \
    o = MFMA32(VP0, PLO(PP), o); SBAR(); VL0 = lds_b128((NP) + 2048); CV(P, 2, S, 4); EX(S, 8); CV(P, 3, S, 6); EX(S, 9); EX(S, 10); EX(S, 11); PIN(S); PIN(P); SBAR(); \
    o = MFMA32(VP1, PHI(PP), o); SBAR(); VL1 = lds_b128((NP) + 3072); CV(P, 4, S, 8); CV(P, 5, S, 10); EX(S, 12); EX(S, 13); EX(S, 14); PIN(S); PIN(P); SBAR(); \
    RSUM(PP); SBAR(); EX(S, 15); CV(P, 6, S, 12); CV(P, 7, S, 14); PIN(P); SBAR(); \
  } while (0)
#define XP(st, j) do { if (LOADER && (unsigned)(st) < 3u) { if ((st) == 0) g2.template ffn_piece<j>(0); g2.template ffn_piece<j>((st) + 1); } SBAR(); } while (0)
#define PRIO(n) do { __builtin_amdgcn_s_setprio(n); SBAR(); } while (0)
#define HST(i) do { if (late_pub && hcb + ((i) >> 2) < 112) { const int cb_ = hcb + ((i) >> 2); \
      const char* b_ = ws + WS_H + (size_t)(cb_ >> 1) * KT + (size_t)(htt + 1) * 4096 + (cb_ & 1) * 2048; \
      asm volatile("global_store_dwordx4 %0, %1, %2 offset:%3 sc1\n\ts_nop 1" :: "v"(voff), "v"(hp[i]), "s"(b_), "i"((((i) >> 1) & 1) * 4096 - 4096 + ((i) & 1) * 1024) : "memory"); } SBAR(); } while (0)
#define MID(c) do { \
    if (LOADER) { \
      if ((c) < 5) asm volatile("s_waitcnt vmcnt(1) lgkmcnt(0)\n\ts_barrier" ::: "memory"); \
      else if ((c) == 5) asm volatile("s_waitcnt vmcnt(16) lgkmcnt(0)\n\ts_barrier" ::: "memory"); \
      else asm volatile("s_waitcnt vmcnt(8) lgkmcnt(0)\n\ts_barrier" ::: "memory"); \
      if ((c) == 3) peek_dev(flags + (FL_H + (mt >> 1)) * 32, lds0 + G1_CS + lw * 256); \
      if ((c) + 2 < AT_NCH) { ld((c) + 2, 0); ld((c) + 2, 1); } \
      if ((c) < 2) { \
        const int j_ = (c) * 4 + lw; \
        l2_touch(j_ < 7 ? (const char*)(w2 + (size_t)(gk * 1792 + j_ * 256 + (mt & 7) * 32 + (l >> 1)) * DM + nt * 128 + (l & 1) * 64) \
                        : (const char*)(wo + (size_t)((mt & 7) * 128 + l) * DM + nt * 128 + gk * 64), tdump); \
      } else if ((c) < 4) { \
        l2_touch(((c) == 2 && lw == 0) ? (const char*)(wo + (size_t)((mt & 7) * 128 + 64 + l) * DM + nt * 128 + gk * 64) : (const char*)wo, tdump); \
      } else if ((c) == 4) { \
        if (rfl(*(const LAS unsigned*)((const LAS char*)smem + G1_CS + lw * 256)) < 92u) wait_ge(flags + (FL_H + (mt >> 1)) * 32, 92u); \
      } \
    } else { \
      asm volatile("s_waitcnt lgkmcnt(0)\n\ts_barrier" ::: "memory"); \
      if ((c) == HPUB && late_pub) publish(late_pub, l); \
      if (!((c) & 1)) STAMP(2, 4 + ((c) >> 1)); \
    } \
    PRIO(3); \
  } while (0)
#define CHUNK(c, X0, X1, X2, X3, X4, X5, X6, X7) do { \
    const LAS char* cb = kvp + ((c) & 1) * AT_BUF; \
    const LAS char* nb = kvp + (((c) + 1) & 1) * AT_BUF; \
    PRIO(2); \
    STEP(sa, pa, sb, pb, ky0, ky1, vd0, vd1, kx0, kx1, vc0, vc1, cb + 2 * 4096); X0; \
    STEP(sb, pb, sa, pa, kx0, kx1, va0, va1, ky0, ky1, vd0, vd1, cb + 3 * 4096); X1; \
    PRIO(1); \
    STEP(sa, pa, sb, pb, ky0, ky1, vb0, vb1, kx0, kx1, va0, va1, cb + 4 * 4096); X2; \
    STEP(sb, pb, sa, pa, kx0, kx1, vc0, vc1, ky0, ky1, vb0, vb1, cb + 5 * 4096); X3; \
    STEP(sa, pa, sb, pb, ky0, ky1, vd0, vd1, kx0, kx1, vc0, vc1, cb + 6 * 4096); X4; \
    PRIO(0); \
    STEP(sb, pb, sa, pa, kx0, kx1, va0, va1, ky0, ky1, vd0, vd1, cb + 7 * 4096); X5; \
    if ((c) < AT_NCH - 1) MID(c); \
    SBAR(); \
    STEP(sa, pa, sb, pb, ky0, ky1, vb0, vb1, kx0, kx1, va0, va1, nb); X6; \
    STEP(sb, pb, sa, pa, kx0, kx1, vc0, vc1, ky0, ky1, vb0, vb1, nb + 4096); X7; \
  } while (0)
#define HMODE 0
#define HPUB (HMODE == 0 ? 2 : 1)
  if (!LOADER) {
    CHUNK(0, HST(0); HST(1), HST(2), HST(3), HST(4); HST(5), HST(6), HST(7), HST(8), HST(9));
    CHUNK(1, HST(10); HST(11), HST(12), HST(13), HST(14); HST(15), HST(16), HST(17), HST(18), HST(19));
#pragma unroll 1
    for (int c = HMODE == 0 ? 2 : 1; c < AT_NCH; ++c) CHUNK(c, (void)0, (void)0, (void)0, (void)0, (void)0, (void)0, (void)0, (void)0);
  } else {
#pragma unroll 1
    for (int c = 0; c < AT_NCH; ++c) CHUNK(c, XP(c - 5, 2), XP(c - 5, 3), XP(c - 5, 4), XP(c - 5, 5), XP(c - 5, 6), XP(c - 5, 7), XP(c - 4, 0), XP(c - 4, 1));
  }
#undef HST
#undef MID
#undef CHUNK
  o = MFMA32(vd0, PLO(pb), o); o = MFMA32(vd1, PHI(pb), o); RSUM(pb);
#undef EX
#undef CV
#undef PLO
#undef PHI
#undef RSUM
#undef STEP
#undef PRIO
#undef XP
  STAMP(2, 2);
  __builtin_amdgcn_s_setprio(0);
  attn_finish(ws, o, ls, q0, q1, l, ws + WS_O + (size_t)(head >> 1) * KT + (size_t)tt * 4096 + (head & 1) * 2048 + l * 16);
  STAMP(2, 3);
}

DI void g2_load(const u16* w2, const u16* wo, char* __restrict__ ws, unsigned lds0, const int lw, const int l) {
  const int xcd = blockIdx.x & 7, idx = blockIdx.x >> 3;
  const int mt = ((xcd >> 1) & 1) * 8 + (idx & 7);
  unsigned* flags = (unsigned*)(ws + WS_FLAG);
  G2Ld g; g.init(ws, w2, wo, lds0, lw, l);
  asm volatile("s_waitcnt vmcnt(10) lgkmcnt(0)\n\ts_barrier" ::: "memory");
  g.ffn(4); g.ffn(5);
  asm volatile("s_waitcnt vmcnt(16)\n\ts_barrier" ::: "memory");
  if (l == 0) __hip_atomic_fetch_add((gu32*)flags + (FL_O + (blockIdx.x >> 4)) * 32, 1u, RLX_AGENT);
  g.ffn(6);
  const LAS unsigned* peek = (const LAS unsigned*)(size_t)(lds0 + G2_PART);
#pragma unroll 1
  for (int i = 3; i < 24; ++i) {
    asm volatile("s_waitcnt vmcnt(16)\n\ts_barrier" ::: "memory");
    if (i == 20 && lw == 0) peek_dev(flags + (FL_O + mt) * 32, lds0 + G2_PART);
    g.ffn(i + 4);
  }
  asm volatile("s_waitcnt vmcnt(16)\n\ts_barrier" ::: "memory");
  if (rfl(peek[0]) < 128u) wait_ge(flags + (FL_O + mt) * 32, 128u);
  g.wo(28);
  peek = (const LAS unsigned*)(size_t)(lds0 + G2_BOFF + 2048);
#pragma unroll 1
  for (int i = 25; i < 37; ++i) {
    asm volatile("s_waitcnt vmcnt(12)\n\ts_barrier" ::: "memory");
    if (i == 35 && lw == 0) peek_dev(flags + (FL_P + blockIdx.x) * 32, lds0 + G2_BOFF + 2048);
    g.wo(i + 4);
  }
  auto part = [&]() {
    const char* src = ws + WS_PART + (size_t)(blockIdx.x ^ 1) * 32768 + lw * 8192;
    const unsigned d = lds0 + G2_PART + lw * 8192;
    glds16_dev<0>(g.voff, src, d); glds16_dev<1024>(g.voff, src, d); glds16_dev<2048>(g.voff, src, d); glds16_dev<3072>(g.voff, src, d);
    glds16_dev<0>(g.voff, src + 4096, d + 4096); glds16_dev<1024>(g.voff, src + 4096, d + 4096); glds16_dev<2048>(g.voff, src + 4096, d + 4096); glds16_dev<3072>(g.voff, src + 4096, d + 4096);
  };
  asm volatile("s_waitcnt vmcnt(12)\n\ts_barrier" ::: "memory");
  const bool early = rfl(peek[0]) >= 4u;
  g.wo(41);
  if (early) part();
#pragma unroll 1
  for (int i = 38; i < 41; ++i) {
    if (early) asm volatile("s_waitcnt vmcnt(20)\n\ts_barrier" ::: "memory");
    else asm volatile("s_waitcnt vmcnt(12)\n\ts_barrier" ::: "memory");
    if (i < 40) g.wo(i + 4);
  }
  asm volatile("s_waitcnt vmcnt(6)\n\ts_barrier" ::: "memory");
  if (!early) { wait_ge(flags + (FL_P + blockIdx.x) * 32, 4u); part(); }
  asm volatile("s_waitcnt vmcnt(0)\n\ts_barrier" ::: "memory");
}

DI void g2_compute(const u16* __restrict__ x, const u16* __restrict__ b2, char* __restrict__ ws, u16* __restrict__ out,
                   char* smem, const int w, const int l) {
  const int xcd = blockIdx.x & 7, idx = blockIdx.x >> 3;
  const int kh = xcd & 1, mt = ((xcd >> 1) & 1) * 8 + (idx & 7), nt = (xcd >> 2) * 4 + (idx >> 3);
  unsigned* flags = (unsigned*)(ws + WS_FLAG);
  STAMP(3, 0);
  const int h = l >> 5, wm = w & 1, wn = w >> 1;
  f32x16 accg[2] = {}, accp[2] = {}, accs[2] = {};
  const int col0 = nt * 128 + kh * 64 + wn * 32;
  const int row0 = mt * 128 + wm * 64 + (l & 31);
  unsigned stw[2];
  u32x4 xv[4];
  u32x2 bv[4];
#pragma unroll
  for (int m = 0; m < 2; ++m) stw[m] = __hip_atomic_load((gu32*)(ws + WS_STAT) + row0 + m * 32, RLX_AGENT);
#pragma unroll
  for (int k = 0; k < 4; ++k) xv[k] = *(const u32x4*)(x + (size_t)(row0 + (k >> 1) * 32) * DM + col0 + 16 * (k & 1) + 8 * h);
#pragma unroll
  for (int k = 0; k < 4; ++k) bv[k] = *(const u32x2*)(b2 + col0 + 8 * k + 4 * h);
  const LAS char* lbase = (const LAS char*)smem + l * 16;
  const int btr = (4 * h + ((l & 15) >> 2)) * 64 + ((l >> 4) & 1) * 32 + (l & 3) * 8 - l * 16;
#define LDB(p_) cat8(lds_tr(p_), lds_tr((p_) + 512))
  const int ao = wm * 8192;
  const int bG = G2_BOFF + (kh * 2 + wn) * 1024 + btr, bP = G2_BOFF + ((kh ^ 1) * 2 + wn) * 1024 + btr;
  const int bW = G2_BOFF + wn * 1024 + btr;
  bf16x8 fa0[2], fb0[2], fa1[2], fb1[2];
#define G2_RD(FA, FB, base, ks) do { \
    FA[0] = lds_b128((base) + ao + (ks) * 1024); FB[0] = LDB((base) + bG + (ks) * 4096); \
    FA[1] = lds_b128((base) + ao + 4096 + (ks) * 1024); FB[1] = LDB((base) + bP + (ks) * 4096); \
  } while (0)
#define G2_MM(FA, FB) do { \
    accg[0] = MFMA32(FB[0], FA[0], accg[0]); accg[1] = MFMA32(FB[0], FA[1], accg[1]); \
    accp[0] = MFMA32(FB[1], FA[0], accp[0]); accp[1] = MFMA32(FB[1], FA[1], accp[1]); \
  } while (0)
#define G2_SCHED(NR) do { \
    __builtin_amdgcn_sched_group_barrier(0x008, 1, 0); __builtin_amdgcn_sched_group_barrier(0x100, NR, 0); \
    __builtin_amdgcn_sched_group_barrier(0x008, 1, 0); __builtin_amdgcn_sched_group_barrier(0x100, NR, 0); \
    __builtin_amdgcn_sched_group_barrier(0x008, 2, 0); SBAR(); \
  } while (0)
#define G2_BAR() do { asm volatile("s_waitcnt lgkmcnt(0)\n\ts_barrier" ::: "memory"); SBAR(); } while (0)

  SBAR();
  STAMP(3, 1);
  G2_RD(fa0, fb0, lbase, 0); SBAR();
  G2_RD(fa1, fb1, lbase, 1); G2_MM(fa0, fb0); G2_SCHED(3);
  G2_RD(fa0, fb0, lbase, 2); G2_MM(fa1, fb1); G2_SCHED(3);
  G2_RD(fa1, fb1, lbase, 3); G2_MM(fa0, fb0); G2_SCHED(3);
#pragma unroll 1
  for (int i = 0; i < 27; ++i) {
    const LAS char* nb = lbase + ((i + 1) & 3) * G2_SLOT;
    if (i > 0) G2_BAR();
    G2_RD(fa0, fb0, nb, 0); G2_MM(fa1, fb1); G2_SCHED(3);
    G2_RD(fa1, fb1, nb, 1); G2_MM(fa0, fb0); G2_SCHED(3);
    G2_RD(fa0, fb0, nb, 2); G2_MM(fa1, fb1); G2_SCHED(3);
    G2_RD(fa1, fb1, nb, 3); G2_MM(fa0, fb0); G2_SCHED(3);
    if (i == 3) { publish(flags + (FL_O + (blockIdx.x >> 4)) * 32, l); SBAR(); }
  }
  STAMP(3, 4);
  bf16x8 ga0[4], gb0[2], ga1[4], gb1[2];
#define W_RD(GA, GB, base, ks) do { \
    GA[0] = lds_b128((base) + ao + (ks) * 1024); GB[0] = LDB((base) + bW + (ks) * 4096); GA[1] = lds_b128((base) + ao + 4096 + (ks) * 1024); \
    GA[2] = lds_b128((base) + ao + (ks) * 1024 + 1024); GB[1] = LDB((base) + bW + (ks) * 4096 + 4096); GA[3] = lds_b128((base) + ao + 4096 + (ks) * 1024 + 1024); \
  } while (0)
#define W_MM(GA, GB) do { \
    accs[0] = MFMA32(GB[0], GA[0], accs[0]); accs[1] = MFMA32(GB[0], GA[1], accs[1]); \
    accs[0] = MFMA32(GB[1], GA[2], accs[0]); accs[1] = MFMA32(GB[1], GA[3], accs[1]); \
  } while (0)
#define W_STEP(i) do { const LAS char* nb_ = lbase + (((i) + 1) & 3) * G2_SLOT; G2_BAR(); \
    W_RD(ga0, gb0, nb_, 0); W_MM(ga1, gb1); G2_SCHED(4); \
    W_RD(ga1, gb1, nb_, 2); W_MM(ga0, gb0); G2_SCHED(4); } while (0)
  char* pdst = ws + WS_PART + (size_t)blockIdx.x * 32768 + w * 8192 + l * 16;
#define G2_SEND(q) do { asm volatile("s_nop 7" ::: "memory"); \
    _Pragma("unroll") for (int i_ = 2 * ((q) & 1); i_ < 2 * ((q) & 1) + 2; ++i_) \
      store16_dev(pdst + ((q) >> 1) * 4096 + i_ * 1024, (u32x4){__float_as_uint(accp[(q) >> 1][4 * i_]), __float_as_uint(accp[(q) >> 1][4 * i_ + 1]), \
                                                               __float_as_uint(accp[(q) >> 1][4 * i_ + 2]), __float_as_uint(accp[(q) >> 1][4 * i_ + 3])}); SBAR(); } while (0)
  G2_BAR();
  W_RD(ga0, gb0, lbase, 0); G2_MM(fa1, fb1); G2_SCHED(4);
  W_RD(ga1, gb1, lbase, 2); W_MM(ga0, gb0); G2_SCHED(4);
  G2_SEND(0);
  STAMP(3, 5);
  W_STEP(28); G2_SEND(1);
  W_STEP(29); G2_SEND(2);
  W_STEP(30); G2_SEND(3);
#pragma unroll 1
  for (int i = 31; i < 42; ++i) {
    if (i == 40) STAMP(3, 6);
    W_STEP(i);
    if (i == 33) { publish(flags + (FL_P + (blockIdx.x ^ 1)) * 32, l); SBAR(); }
  }
  W_STEP(42);
  W_MM(ga1, gb1);
  STAMP(3, 2);
  {
    const LAS char* xp = (const LAS char*)smem + G2_PART + w * 8192 + l * 16;
#pragma unroll
    for (int m = 0; m < 2; ++m) {
      const float den = bf_hi(stw[m]), rd2 = 1.0f / (den * den);
#pragma unroll
      for (int k = 0; k < 2; ++k) {
        u32x4 xx = xv[m * 2 + k];
        { auto r0 = __builtin_amdgcn_permlane32_swap(xx[0], xx[2], false, false); xx[0] = r0[0]; xx[2] = r0[1];
          auto r1 = __builtin_amdgcn_permlane32_swap(xx[1], xx[3], false, false); xx[1] = r1[0]; xx[3] = r1[1]; }
        unsigned pk[4];
#pragma unroll
        for (int ii = 0; ii < 2; ++ii) {
          const int i = 2 * k + ii;
          const f32x4 tp = *(const LAS f32x4*)(xp + m * 4096 + i * 1024);
          float r[4];
#pragma unroll
          for (int j = 0; j < 4; ++j) {
            const unsigned xw_ = xx[2 * ii + (j >> 1)], bw_ = bv[i][j >> 1];
            const float z = (accg[m][4 * i + j] + tp[j]) * rd2 + ((j & 1) ? bf_hi(bw_) : bf_lo(bw_));
            const float sg = z * __builtin_amdgcn_rcpf(1.0f + __builtin_amdgcn_exp2f(-1.4426950408889634f * z));
            r[j] = ((j & 1) ? bf_hi(xw_) : bf_lo(xw_)) + accs[m][4 * i + j] * sg;
          }
          pk[2 * ii] = cvtpk(r[0], r[1]); pk[2 * ii + 1] = cvtpk(r[2], r[3]);
        }
        auto r0 = __builtin_amdgcn_permlane32_swap(pk[0], pk[2], false, false);
        auto r1 = __builtin_amdgcn_permlane32_swap(pk[1], pk[3], false, false);
        *(u32x4*)(out + (size_t)(row0 + m * 32) * DM + col0 + 16 * k + 8 * h) = (u32x4){r0[0], r1[0], r0[1], r1[1]};
      }
    }
  }
  STAMP(3, 3);
#undef G2_RD
#undef G2_MM
#undef G2_SCHED
#undef G2_BAR
#undef G2_SEND
#undef W_RD
#undef W_MM
#undef W_STEP
#undef LDB
}

__global__ __launch_bounds__(512, 2) void main_kernel(const u16* __restrict__ x, const u16* __restrict__ wq, const u16* __restrict__ wk,
                                                      const u16* __restrict__ wv, const u16* __restrict__ wo, const u16* __restrict__ w1,
                                                      const u16* __restrict__ b1, const u16* __restrict__ w2, const u16* __restrict__ b2,
                                                      char* __restrict__ ws, u16* __restrict__ out) {
  extern __shared__ __attribute__((aligned(16))) char smem[];
  const int l = threadIdx.x & 63, w = rfl(threadIdx.x >> 6);
  const unsigned lds0 = (unsigned)(size_t)(LAS char*)smem;
  const int xcd = blockIdx.x & 7, idx = blockIdx.x >> 3;
  const int mtile = (xcd & 1) * 4 + (idx & 3), cg = (idx >> 2) * 4 + (xcd >> 1);
  unsigned* flags = (unsigned*)(ws + WS_FLAG);
  if (w == 3) {
    unsigned long long pc; unsigned len;
    asm volatile("s_getpc_b64 %0\n.Lmk_code_begin:\n\ts_mov_b32 %1, .Lmk_code_end-.Lmk_code_begin" : "=s"(pc), "=s"(len));
    unsigned off = (unsigned)(idx * 16 + (l & 15)) * 128u;
    off = off < len ? off : 0u;
    l2_touch((const char*)pc + off, lds0 + G1_HS + 3072);
  }
  if (w >= 4) {
    if (cg > 8) g1_load<G1_FFN>(ws, x, w1, nullptr, cg, mtile, smem, w - 4, l);
    else if (cg < 8) g1_load<G1_Q>(ws, x, wq, nullptr, cg, mtile, smem, w - 4, l);
    else g1_load<G1_KV>(ws, x, wk, wv, cg, mtile, smem, w - 4, l);
    u32x4 hp[20];
    attn_phase<true>(ws, w2, wo, smem, w, l, nullptr, hp, 0, 0);
    g2_load(w2, wo, ws, lds0, w - 4, l);
    asm volatile(".Lmk_code_end:");
  } else {
    unsigned* late = nullptr;
    u32x4 hp[20];
    if (cg > 8) { g1_compute<G1_FFN>(ws, x, b1, cg, mtile, smem, w, l, hp); late = flags + (FL_H + mtile) * 32; }
    else if (cg < 8) { g1_compute<G1_Q>(ws, x, b1, cg, mtile, smem, w, l, hp); publish(flags + (FL_Q + mtile * 8 + cg) * 32, l); }
    else { g1_compute<G1_KV>(ws, x, b1, cg, mtile, smem, w, l, hp); publish(flags + FL_KV * 32, l); }
    attn_phase<false>(ws, w2, wo, smem, w, l, late, hp, (cg - 9) * 5, mtile * 8 + w * 2);
    g2_compute(x, b2, ws, out, smem, w, l);
  }
}

extern "C" void kernel_launch(void* const* d_in, const int* in_sizes, int n_in, void* d_out, int out_size, void* d_ws,
                              size_t ws_size, hipStream_t stream) {
  const u16* x = (const u16*)d_in[0];
  const u16* wq = (const u16*)d_in[1];
  const u16* wk = (const u16*)d_in[2];
  const u16* wv = (const u16*)d_in[3];
  const u16* wo = (const u16*)d_in[4];
  const u16* w1 = (const u16*)d_in[6];
  const u16* b1 = (const u16*)d_in[7];
  const u16* w2 = (const u16*)d_in[8];
  const u16* b2 = (const u16*)d_in[9];
  char* ws = (char*)d_ws;
  (void)hipFuncSetAttribute((const void*)main_kernel, hipFuncAttributeMaxDynamicSharedMemorySize, MAIN_LDS);
  main_kernel<<<256, 512, MAIN_LDS, stream>>>(x, wq, wk, wv, wo, w1, b1, w2, b2, ws, (u16*)d_out);
}
```

```cpp
#include <hip/hip_runtime.h>
#include <hip/hip_bf16.h>

typedef short bf16x8 __attribute__((ext_vector_type(8)));
typedef short s16x4 __attribute__((ext_vector_type(4)));
typedef float f32x16 __attribute__((ext_vector_type(16)));
typedef float f32x4 __attribute__((ext_vector_type(4)));
typedef float f32x2 __attribute__((ext_vector_type(2)));
typedef __bf16 bf16x2_t __attribute__((ext_vector_type(2)));
typedef unsigned u32x4 __attribute__((ext_vector_type(4)));
typedef unsigned u32x2 __attribute__((ext_vector_type(2)));
typedef unsigned u32x8 __attribute__((ext_vector_type(8)));
typedef __bf16 bf16x8_t __attribute__((ext_vector_type(8)));
typedef __bf16 bf16x16_t __attribute__((ext_vector_type(16)));
typedef unsigned short u16;
#define LAS __attribute__((address_space(3)))
#define DI __device__ __forceinline__
#define SBAR() __builtin_amdgcn_sched_barrier(0)
#define PIN(x) asm volatile("" : "+v"(x))
#define MFMA32(a, b, c) __builtin_amdgcn_mfma_f32_32x32x16_bf16((a), (b), (c), 0, 0, 0)

constexpr int DM = 1024, FF = 3584;
constexpr size_t KT = 262144;
constexpr size_t WS_STAT = 0;
constexpr size_t WS_Q = 4u << 20;
constexpr size_t WS_KV = 8u << 20;
constexpr size_t WS_O = 9u << 20;
constexpr size_t WS_H = 13u << 20;
constexpr size_t WS_PART = 28u << 20;
constexpr size_t WS_FLAG = 37u << 20;
constexpr int FL_P = 0, FL_O = 256, FL_H = 272, FL_Q = 280, FL_KV = 344, FL_S = 345, FL_N = 353;
constexpr size_t WS_HSA = WS_FLAG + 65536, WS_HSB = WS_HSA + 4096, WS_HSC = WS_HSB + 4096;
constexpr int COORD_BLOCK = 64;

#define STAMP(kid, idx) do {} while (0)
DI unsigned rfl(unsigned x) { return __builtin_amdgcn_readfirstlane(x); }
DI unsigned cvtpk(float lo, float hi) {
  f32x2 v = {lo, hi};
  bf16x2_t b = __builtin_convertvector(v, bf16x2_t);
  return __builtin_bit_cast(unsigned, b);
}
DI float fadd_s(float a, float b) { float r; asm("v_add_f32_e32 %0, %1, %2" : "=v"(r) : "v"(a), "v"(b)); return r; }
DI float bf_lo(unsigned u) { return __uint_as_float(u << 16); }
DI float bf_hi(unsigned u) { return __uint_as_float(u & 0xffff0000u); }
DI float bf_round(float x) { return bf_lo(cvtpk(x, x)); }

template <int IMM>
DI void glds16(unsigned voff, const void* sbase, unsigned ldsaddr) {
  asm volatile("s_mov_b32 m0, %2\n\ts_nop 0\n\tglobal_load_lds_dwordx4 %0, %1 offset:%3"
               :: "v"(voff), "s"(sbase), "s"(ldsaddr), "i"(IMM) : "memory");
}
template <int IMM>
DI void glds16_dev(unsigned voff, const void* sbase, unsigned ldsaddr) {
  asm volatile("s_mov_b32 m0, %2\n\ts_nop 0\n\tglobal_load_lds_dwordx4 %0, %1 offset:%3 sc1"
               :: "v"(voff), "s"(sbase), "s"(ldsaddr), "i"(IMM) : "memory");
}
DI void peek_dev(const void* word, unsigned ldsaddr) {
  asm volatile("s_mov_b32 m0, %2\n\ts_nop 0\n\tglobal_load_lds_dword %0, %1 sc1" :: "v"(0u), "s"(word), "s"(ldsaddr) : "memory");
}
DI void store16_dev(void* p, u32x4 v) {
  asm volatile("global_store_dwordx4 %0, %1, off sc1\n\ts_nop 1" :: "v"(p), "v"(v) : "memory");
}
DI void l2_touch(const void* p, unsigned ldsaddr) {
  asm volatile("s_mov_b32 m0, %1\n\ts_nop 0\n\tglobal_load_lds_dword %0, off" :: "v"(p), "s"(ldsaddr) : "memory");
}
constexpr unsigned SPIN_CAP = 1u << 20;
typedef __attribute__((address_space(1))) unsigned gu32;
#define RLX_AGENT __ATOMIC_RELAXED, __HIP_MEMORY_SCOPE_AGENT
DI void wait_ge(const unsigned* word, unsigned want) {
#pragma unroll 1
  for (unsigned spins = 0; spins < SPIN_CAP; ++spins) {
    if (rfl(__hip_atomic_load((gu32*)word, RLX_AGENT)) >= want) break;
    __builtin_amdgcn_s_sleep(2);
  }
  __builtin_amdgcn_fence(__ATOMIC_ACQUIRE, "wavefront");
}
DI void publish(unsigned* word, int l) {
  asm volatile("s_waitcnt vmcnt(0)" ::: "memory");
  if (l == 0) __hip_atomic_fetch_add((gu32*)word, 1u, RLX_AGENT);
}
DI bf16x8 load16_dev(const char* p) {
  typedef __attribute__((address_space(1))) unsigned long long gu64;
  const unsigned long long a = __hip_atomic_load((gu64*)p, RLX_AGENT), b = __hip_atomic_load((gu64*)p + 1, RLX_AGENT);
  return __builtin_bit_cast(bf16x8, (u32x4){(unsigned)a, (unsigned)(a >> 32), (unsigned)b, (unsigned)(b >> 32)});
}
DI float relu_s(float a) { return __builtin_amdgcn_fmed3f(a, 0.0f, 3.4028234663852886e38f); }
DI bf16x8 lds_b128(const LAS char* p) { return *(const LAS bf16x8*)p; }
DI s16x4 lds_tr(const LAS char* p) {
  return __builtin_amdgcn_ds_read_tr16_b64_v4i16((LAS s16x4*)p);
}
DI bf16x8 cat8(s16x4 lo, s16x4 hi) { return __builtin_shufflevector(lo, hi, 0, 1, 2, 3, 4, 5, 6, 7); }

DI float half_sum(float v) {
  v += __uint_as_float(__builtin_amdgcn_update_dpp(0u, __float_as_uint(v), 0xB1, 0xf, 0xf, true));
  v += __uint_as_float(__builtin_amdgcn_update_dpp(0u, __float_as_uint(v), 0x4E, 0xf, 0xf, true));
  v += __uint_as_float(__builtin_amdgcn_update_dpp(0u, __float_as_uint(v), 0x141, 0xf, 0xf, true));
  v += __uint_as_float(__builtin_amdgcn_update_dpp(0u, __float_as_uint(v), 0x140, 0xf, 0xf, true));
  auto r = __builtin_amdgcn_permlane16_swap(__float_as_uint(v), __float_as_uint(v), false, false);
  return __uint_as_float(r[0]) + __uint_as_float(r[1]);
}
DI unsigned row_stats(const u32x4 (&v)[4]) {
  f32x2 e[16], s2 = {0.f, 0.f};
#pragma unroll
  for (int i = 0; i < 16; ++i) { e[i] = (f32x2){bf_lo(v[i >> 2][i & 3]), bf_hi(v[i >> 2][i & 3])}; s2 += e[i]; }
  const float mean = half_sum(s2[0] + s2[1]) * (1.0f / DM);
  f32x2 q2 = {0.f, 0.f};
#pragma unroll
  for (int i = 0; i < 16; ++i) { const f32x2 d = e[i] - mean; q2 += d * d; }
  const float q = half_sum(q2[0] + q2[1]);
  const float den = __builtin_sqrtf(bf_round(bf_round(q * (1.0f / DM)) + 1e-5f));
  return cvtpk(-mean, den);
}

constexpr int G1_STAGE = 53248, G1_BOFF = 32768;
enum { G1_FFN = 5, G1_Q = 4, G1_KV = 2 };

constexpr int G1_ST = 87040;
constexpr int G1_HS = 159744;
constexpr int G1_CS = 81920;
constexpr int ESTART = 3, PSTART = 9;
DI float pair_sum(unsigned v) { return fadd_s(bf_lo(v), bf_hi(v)); }

template <int NP>
DI void g1_load(char* ws, const u16* x, const u16* wsrc, const u16* wv, int cg, int mtile, char* smem, int lw, int l) {
  constexpr int LD = (NP == G1_FFN) ? FF : (NP == G1_Q) ? DM : 32;
  const unsigned lds0 = (unsigned)(size_t)(LAS char*)smem;
  const char* abase = (const char*)(x + (size_t)(mtile * 256 + lw * 64) * DM);
  const unsigned ave = (unsigned)((l >> 3) * 2048 + (((l & 7) ^ (l >> 4)) << 4)), avo = ave ^ 64u;
  const unsigned bvoff = (unsigned)((lw * 16 + (l >> 2)) * LD * 2 + (l & 3) * 16);
  const int krot = NP == G1_Q ? (mtile & 3) * 4 : 0;
  const char* bbase[NP];
#pragma unroll
  for (int p = 0; p < NP; ++p) {
    if (NP == G1_KV) bbase[p] = (const char*)(p ? wv : wsrc);
    else if (NP == G1_Q) bbase[p] = (const char*)(wsrc + (cg * 4 + p) * 32);
    else { const int cb = (cg - 9) * 5 + p; bbase[p] = (const char*)(wsrc + (cb > 111 ? 111 : cb) * 32); }
  }
  float cs[NP];
#pragma unroll
  for (int p = 0; p < NP; ++p) cs[p] = 0.f;
  const LAS char* cb0 = (const LAS char*)smem + G1_BOFF + lw * 1024 + (8 * (l >> 5) + ((l & 15) >> 2)) * 64 + ((l >> 4) & 1) * 32 + (l & 3) * 8;
  u32x2 t0[NP], t1[NP];
  auto cs_read = [&](int slot) {
#pragma unroll
    for (int p = 0; p < NP; ++p) {
      t0[p] = __builtin_bit_cast(u32x2, lds_tr(cb0 + slot * G1_STAGE + p * 4096));
      t1[p] = __builtin_bit_cast(u32x2, lds_tr(cb0 + slot * G1_STAGE + p * 4096 + 256));
    }
    SBAR();
  };
  auto cs_add = [&](int p) {
    cs[p] = fadd_s(cs[p], fadd_s(fadd_s(pair_sum(t0[p][0]), pair_sum(t0[p][1])), fadd_s(pair_sum(t1[p][0]), pair_sum(t1[p][1]))));
    PIN(cs[p]); SBAR();
  };
  auto issue = [&](int st, int slot, bool sum) {
    const int kt = (st + krot) & 15;
    const unsigned sa = lds0 + slot * G1_STAGE + lw * 8192;
    const char* a0 = abase + kt * 128;
    const unsigned sb = lds0 + slot * G1_STAGE + G1_BOFF + lw * 1024;
#pragma unroll
    for (int p = 0; p < NP; ++p) glds16<0>(bvoff, bbase[p] + (size_t)kt * (64 * LD * 2), sb + p * 4096);
#pragma unroll
    for (int i = 0; i < 8; ++i) {
      if (sum && i < NP) { SBAR(); cs_add(i); }
      glds16<0>((i & 1) ? avo : ave, a0 + i * 16384, sa + i * 1024);
    }
  };
  auto colsum = [&](int slot) {
    cs_read(slot);
#pragma unroll
    for (int p = 0; p < NP; ++p) cs_add(p);
  };
  unsigned* flags = (unsigned*)(ws + WS_FLAG);
  const bool coord = (NP == G1_KV) && blockIdx.x == COORD_BLOCK && lw == 1;
  if (coord) {
#pragma unroll
    for (int i = 0; i < 6; ++i) if (i * 64 + l < FL_N) __hip_atomic_store((gu32*)flags + (i * 64 + l) * 32, 0u, RLX_AGENT);
    store16_dev(ws + WS_HSC + l * 16, (u32x4){0u, 0u, 0u, 0u});
    asm volatile("s_waitcnt vmcnt(0)" ::: "memory");
  }
  issue(0, 0, false); issue(1, 1, false); issue(2, 2, false);
  asm volatile("s_waitcnt vmcnt(%0)\n\ts_barrier" :: "n"(2 * (8 + NP)) : "memory");
  __builtin_amdgcn_s_setprio(2);
  int state = (lw == 0 || coord) ? 0 : 2;
  bool pend = false, first = true;
  u32x4 last = {0u, 0u, 0u, 0u};
  const unsigned found = rfl(*(const LAS unsigned*)((const LAS char*)smem + G1_HS));
  if (lw == 0 && l < 9) {
    const unsigned v = *(const LAS unsigned*)((const LAS char*)smem + G1_HS + 16 + (l & 7) * 4);
    __hip_atomic_store(l < 8 ? (gu32*)(ws + WS_STAT) + mtile * 256 + cg * 8 + l : (gu32*)(ws + WS_HSA) + blockIdx.x, l < 8 ? v : found + 1u, RLX_AGENT);
  }
  colsum(0);
  auto poll_issue = [&]() {
    if (NP == G1_KV && coord) {
      glds16_dev<0>(l * 16, ws + WS_HSA, lds0 + G1_HS + 1024); glds16_dev<0>(l * 16, ws + WS_HSC, lds0 + G1_HS + 2048);
    } else {
      const char* a_ = state == 0 ? ws + WS_HSB + blockIdx.x * 4 : (const char*)(flags + (FL_S + mtile) * 32);
      peek_dev(a_, lds0 + G1_HS + 256);
    }
  };
  auto poll_check = [&]() {
    if (NP == G1_KV && coord) {
      const u32x4 pa = *(const LAS u32x4*)((const LAS char*)smem + G1_HS + 1024 + l * 16), pc = *(const LAS u32x4*)((const LAS char*)smem + G1_HS + 2048 + l * 16);
      if (first || pa[0] != last[0] || pa[1] != last[1] || pa[2] != last[2] || pa[3] != last[3]) store16_dev(ws + WS_HSB + l * 16, pa);
      last = pa; first = false;
      if (__all((pc[0] & pc[1] & pc[2] & pc[3]) == 1u)) state = 2;
    } else {
      const unsigned v = rfl(*(const LAS unsigned*)((const LAS char*)smem + G1_HS + 256));
      if (state == 0) {
        if (v != found) {
          if (l == 0) {
            __hip_atomic_store((gu32*)(ws + WS_HSC) + blockIdx.x, 1u, RLX_AGENT);
            __hip_atomic_fetch_add((gu32*)flags + (FL_S + mtile) * 32, 1u, RLX_AGENT);
          }
          state = 1;
        }
      } else if (v >= 32u) state = 2;
    }
    state = rfl(state);
  };
  int slot = 0;
#pragma unroll 1
  for (int i = 0; i < 13; ++i) {
    asm volatile("s_waitcnt vmcnt(%0)\n\ts_barrier" :: "n"(8 + NP) : "memory");
    if (state < 2 && (i >= ESTART || coord)) {
      if (pend) poll_check();
      pend = state == 0 || (state == 1 && i >= PSTART);
      if (pend) poll_issue();
    }
    const int nslot = (slot == 2) ? 0 : slot + 1;
    cs_read(nslot);
    issue(i + 3, slot, true);
    slot = nslot;
  }
#pragma unroll 1
  for (unsigned spins = 0; state < 2 && spins < SPIN_CAP; ++spins) {
    if (!pend) poll_issue();
    asm volatile("s_waitcnt vmcnt(0)" ::: "memory");
    poll_check();
    pend = false;
    __builtin_amdgcn_s_sleep(1);
  }
  asm volatile("s_waitcnt vmcnt(%0)\n\ts_barrier" :: "n"(8 + NP) : "memory");
  if (lw == 0) glds16_dev<0>(l * 16, ws + WS_STAT + mtile * 1024, lds0 + G1_ST);
  colsum(2);
  asm volatile("s_waitcnt vmcnt(0)" ::: "memory");
  colsum(0);
#pragma unroll
  for (int p = 0; p < NP; ++p) *(LAS float*)((LAS char*)smem + G1_CS + p * 1024 + (l & 31) * 32 + (lw * 2 + (l >> 5)) * 4) = cs[p];
  asm volatile("s_waitcnt lgkmcnt(0)\n\ts_barrier" ::: "memory");
}

template <int NP>
DI void g1_compute(char* __restrict__ ws, const u16* __restrict__ x, const u16* __restrict__ b1, int cg, int mtile, char* smem, int w, int l) {
  STAMP(1, 0);
  const int h = l >> 5;
  unsigned st[2], bias[NP];
  {
    const int row = mtile * 256 + cg * 8 + w * 2 + h;
    u32x4 v[4];
#pragma unroll
    for (int i = 0; i < 4; ++i) v[i] = *(const u32x4*)(x + (size_t)row * DM + (i * 32 + (l & 31)) * 8);
    const unsigned found = __hip_atomic_load((gu32*)(ws + WS_HSB) + blockIdx.x, RLX_AGENT);
#pragma unroll
    for (int p = 0; p < NP; ++p) {
      int cb = (cg - 9) * 5 + p; cb = cb > 111 ? 111 : cb;
      bias[p] = (NP == G1_FFN) ? (unsigned)b1[cb * 32 + (l & 31)] : 0u;
    }
    SBAR();
    const unsigned rs = row_stats(v);
    if ((l & 31) == 0) *(LAS unsigned*)((LAS char*)smem + G1_HS + 16 + (w * 2 + h) * 4) = rs;
    if (w == 0 && l == 0) *(LAS unsigned*)((LAS char*)smem + G1_HS) = found;
  }
  f32x16 acc[2][NP];
#pragma unroll
  for (int p = 0; p < NP; ++p) { acc[0][p] = f32x16{}; acc[1][p] = f32x16{}; }
  const LAS char* lbase = (const LAS char*)smem;
  int aoff[4];
#pragma unroll
  for (int ks = 0; ks < 4; ++ks) aoff[ks] = (w * 64 + (l & 31)) * 128 + (((2 * ks + h) ^ ((l >> 1) & 7)) << 4);
  const int boff = G1_BOFF + (8 * h + ((l & 15) >> 2)) * 64 + ((l >> 4) & 1) * 32 + (l & 3) * 8;
  bf16x8 fa0[2], fb0[NP], fa1[2], fb1[NP];
#define G1_RD(FA, FB, base, ks) do { \
    FA[0] = lds_b128((base) + aoff[ks]); FA[1] = lds_b128((base) + aoff[ks] + 4096); \
    _Pragma("unroll") for (int p_ = 0; p_ < NP; ++p_) \
      FB[p_] = cat8(lds_tr((base) + boff + p_ * 4096 + (ks) * 1024), lds_tr((base) + boff + p_ * 4096 + (ks) * 1024 + 256)); \
  } while (0)
#define G1_MM(FA, FB) do { \
    _Pragma("unroll") for (int p_ = 0; p_ < NP; ++p_) { \
      if (NP == G1_KV && p_ == 1) { acc[0][p_] = MFMA32(FA[0], FB[p_], acc[0][p_]); acc[1][p_] = MFMA32(FA[1], FB[p_], acc[1][p_]); } \
      else { acc[0][p_] = MFMA32(FB[p_], FA[0], acc[0][p_]); acc[1][p_] = MFMA32(FB[p_], FA[1], acc[1][p_]); } } \
  } while (0)
#define G1_SCHED() do { \
    _Pragma("unroll") for (int q_ = 0; q_ < NP + 1; ++q_) { \
      __builtin_amdgcn_sched_group_barrier(0x008, 1, 0); __builtin_amdgcn_sched_group_barrier(0x100, 2, 0); } \
    __builtin_amdgcn_sched_group_barrier(0x008, NP - 1, 0); SBAR(); \
  } while (0)

  asm volatile("s_waitcnt lgkmcnt(0)\n\ts_barrier" ::: "memory");
  STAMP(1, 1);
  G1_RD(fa0, fb0, lbase, 0); SBAR();
  G1_RD(fa1, fb1, lbase, 1); G1_MM(fa0, fb0); G1_SCHED();
  G1_RD(fa0, fb0, lbase, 2); G1_MM(fa1, fb1); G1_SCHED();
  G1_RD(fa1, fb1, lbase, 3); G1_MM(fa0, fb0); G1_SCHED();
  int slot = 1;
#pragma unroll 1
  for (int i = 0; i < 15; ++i) {
    const LAS char* nb = lbase + slot * G1_STAGE;
    asm volatile("s_waitcnt lgkmcnt(0)\n\ts_barrier" ::: "memory"); SBAR();
    G1_RD(fa0, fb0, nb, 0); G1_MM(fa1, fb1); G1_SCHED();
    G1_RD(fa1, fb1, nb, 1); G1_MM(fa0, fb0); G1_SCHED();
    G1_RD(fa0, fb0, nb, 2); G1_MM(fa1, fb1); G1_SCHED();
    G1_RD(fa1, fb1, nb, 3); G1_MM(fa0, fb0); G1_SCHED();
    slot = (slot == 2) ? 0 : slot + 1;
  }
  STAMP(1, 4);
  {
    f32x4 t[NP][2];
#pragma unroll
    for (int m = 0; m < 2; ++m) st[m] = *(const LAS unsigned*)(lbase + G1_ST + (w * 64 + m * 32 + (l & 31)) * 4);
#pragma unroll
    for (int p = 0; p < NP; ++p) {
      t[p][0] = *(const LAS f32x4*)(lbase + G1_CS + p * 1024 + (l & 31) * 32);
      t[p][1] = *(const LAS f32x4*)(lbase + G1_CS + p * 1024 + (l & 31) * 32 + 16);
    }
    SBAR();
    G1_MM(fa1, fb1);
    const unsigned msk = h ? 0u : ~0u;
#pragma unroll
    for (int m = 0; m < 2; ++m) {
      const unsigned nm = st[m] & 0xffffu;
      fa0[m] = __builtin_bit_cast(bf16x8, (u32x4){(nm | (nm << 16)) & msk, st[m] & msk, 0u, 0u});
    }
#pragma unroll
    for (int p = 0; p < NP; ++p) {
      const f32x4 u = t[p][0] + t[p][1];
      const float c = (u[0] + u[1]) + (u[2] + u[3]);
      const unsigned c0 = cvtpk(c, c) & 0xffffu; const float r1 = c - bf_lo(c0);
      const unsigned c1 = cvtpk(r1, r1) & 0xffffu; const float r2 = r1 - bf_lo(c1);
      const unsigned c2 = cvtpk(r2, r2) & 0xffffu;
      fb0[p] = __builtin_bit_cast(bf16x8, (u32x4){c0 | (c1 << 16), c2 | (bias[p] << 16), 0u, 0u});
    }
  }
  G1_MM(fa0, fb0);
  float rden[2];
#pragma unroll
  for (int m = 0; m < 2; ++m) rden[m] = 1.0f / bf_hi(st[m]);
  SBAR();
  STAMP(1, 2);

#pragma unroll
  for (int p = 0; p < NP; ++p) {
    const int cb = (NP == G1_FFN) ? (cg - 9) * 5 + p : cg * 4 + p;
    if (NP != G1_FFN || cb < 112) {
#pragma unroll
      for (int m = 0; m < 2; ++m) {
        const int tt = mtile * 8 + w * 2 + m;
        char* dst = (NP == G1_FFN) ? ws + WS_H + (size_t)(cb >> 1) * KT + (size_t)tt * 4096 + (cb & 1) * 2048 + l * 16
                  : (NP == G1_Q)   ? ws + WS_Q + (size_t)(tt * 32 + cb) * 2048 + l * 16
                                   : ws + WS_KV + (size_t)tt * 4096 + p * 2048 + l * 16;
        const float sc = (NP == G1_Q) ? rden[m] * (0.17677669529663687f * 1.4426950408889634f) : rden[m];
#pragma unroll
        for (int s = 0; s < 2; ++s) {
          u32x4 o;
#pragma unroll
          for (int j = 0; j < 4; ++j) {
            float a = acc[m][p][8 * s + 2 * j], b = acc[m][p][8 * s + 2 * j + 1];
            if (NP == G1_FFN) { a = relu_s(a); b = relu_s(b); a *= a; b *= b; }
            else if (NP == G1_KV && p == 1) {
              const int r = 8 * s + 2 * j, tk = (r & 3) + 8 * (r >> 2) + 4 * h;
              a *= __shfl(rden[m], tk); b *= __shfl(rden[m], tk + 1);
            } else { a *= sc; b *= sc; }
            o[j] = cvtpk(a, b);
          }
          store16_dev(dst + s * 1024, o);
        }
      }
    }
  }
  STAMP(1, 3);
#undef G1_RD
#undef G1_MM
#undef G1_SCHED
}

constexpr int AT_NCH = 8;
constexpr int AT_BUF = 32768;
constexpr int AT_BASE = 98304;
constexpr int AT_Q = 65536;
constexpr int AT_DUMP = 90112;
constexpr int MAIN_LDS = 163840;

DI float xhalf_max(float m) {
  auto rr = __builtin_amdgcn_permlane32_swap(__float_as_uint(m), __float_as_uint(m), false, false);
  return __builtin_fmaxf(__uint_as_float(rr[0]), __uint_as_float(rr[1]));
}
DI float xhalf_sum(float m) {
  auto rr = __builtin_amdgcn_permlane32_swap(__float_as_uint(m), __float_as_uint(m), false, false);
  return __uint_as_float(rr[0]) + __uint_as_float(rr[1]);
}
DI float max16(const f32x16& s) {
  float a = __builtin_fmaxf(s[0], s[1]);
#pragma unroll
  for (int r = 2; r < 16; ++r) a = __builtin_fmaxf(a, s[r]);
  return a;
}
DI bf16x8 pack8(const f32x16& p, int s) {
  u32x4 o;
#pragma unroll
  for (int j = 0; j < 4; ++j) o[j] = cvtpk(p[8 * s + 2 * j], p[8 * s + 2 * j + 1]);
  return __builtin_bit_cast(bf16x8, o);
}

DI void attn_fallback(const char* __restrict__ kv, bf16x8 q0, bf16x8 q1, int l, f32x16& o_out, float& l_out) {
  f32x16 o = {};
  float m = -INFINITY, lsum = 0.f;
  for (int t = 0; t < 64; ++t) {
    const char* p = kv + (size_t)t * 4096 + l * 16;
    const bf16x8 k0 = load16_dev(p), k1 = load16_dev(p + 1024), v0 = load16_dev(p + 2048), v1 = load16_dev(p + 3072);
    f32x16 s = {};
    s = MFMA32(k0, q0, s); s = MFMA32(k1, q1, s);
    const float mn = __builtin_fmaxf(m, xhalf_max(max16(s)));
    const float ms = (mn == -INFINITY) ? 0.f : mn;
    const float alpha = __builtin_amdgcn_exp2f(m - ms);
    m = mn;
    float ps = 0.f;
#pragma unroll
    for (int r = 0; r < 16; ++r) { s[r] = __builtin_amdgcn_exp2f(s[r] - ms); ps += s[r]; }
    lsum = lsum * alpha + ps;
#pragma unroll
    for (int r = 0; r < 16; ++r) o[r] *= alpha;
    o = MFMA32(v0, pack8(s, 0), o); o = MFMA32(v1, pack8(s, 1), o);
  }
  o_out = o;
  l_out = xhalf_sum(lsum);
}

constexpr int G2_SLOT = 32768, G2_BOFF = 16384, G2_PART = 131072;
struct G2Ld {
  const char* hb; const char* ob; const char* wb; const char* wb2; unsigned lds; unsigned voff, voffb; int rot, frot;
  DI void init(const char* ws, const u16* w2, const u16* wo, unsigned lds0, int lw, int l) {
    const int xcd = blockIdx.x & 7, idx = blockIdx.x >> 3, kh = xcd & 1;
    const int mt = ((xcd >> 1) & 1) * 8 + (idx & 7), nt = (xcd >> 2) * 4 + (idx >> 3);
    hb = ws + WS_H + (size_t)(mt * 4 + lw) * 4096 + (size_t)kh * (28 * KT);
    ob = ws + WS_O + (size_t)(mt * 4 + lw) * 4096;
    wb = (const char*)(w2 + (size_t)(kh * 1792 + lw * 16) * DM + nt * 128);
    wb2 = (const char*)(wo + (size_t)(lw * 16) * DM + nt * 128 + kh * 64);
    voffb = (unsigned)((l >> 2) * DM * 2 + (l & 3) * 16);
    lds = lds0 + lw * 4096; voff = l * 16;
    frot = 3 * (mt & 7);
    rot = 2 * (mt & 7);
  }
  DI void ffn(int s) const {
    const unsigned d = lds + (s & 3) * G2_SLOT;
    s += frot; s = s >= 28 ? s - 28 : s;
    const char* a = hb + (size_t)s * KT;
    glds16_dev<0>(voff, a, d); glds16_dev<1024>(voff, a, d); glds16_dev<2048>(voff, a, d); glds16_dev<3072>(voff, a, d);
    const char* b = wb + (size_t)s * 131072;
    glds16<0>(voffb, b, d + G2_BOFF); glds16<0>(voffb, b + 64, d + G2_BOFF + 1024); glds16<0>(voffb, b + 128, d + G2_BOFF + 2048); glds16<0>(voffb, b + 192, d + G2_BOFF + 3072);
  }
  template <int J> DI void ffn_piece(int s) const {
    const unsigned d = lds + (s & 3) * G2_SLOT;
    s += frot; s = s >= 28 ? s - 28 : s;
    if (J < 4) glds16_dev<(J & 3) * 1024>(voff, hb + (size_t)s * KT, d);
    else glds16<0>(voffb, wb + (size_t)s * 131072 + (J & 3) * 64, d + G2_BOFF + (J & 3) * 1024);
  }
  DI void wo(int s) const {
    const unsigned d = lds + (s & 3) * G2_SLOT;
    const int k = (s - 28 + rot) & 15;
    const char* a = ob + (size_t)k * KT;
    glds16_dev<0>(voff, a, d); glds16_dev<1024>(voff, a, d); glds16_dev<2048>(voff, a, d); glds16_dev<3072>(voff, a, d);
    const char* b = wb2 + (size_t)k * 131072;
    glds16<0>(voffb, b, d + G2_BOFF); glds16<0>(voffb, b + 64, d + G2_BOFF + 1024);
  }
};

DI void attn_finish(char* __restrict__ ws, f32x16 o, f32x4 lacc, bf16x8 q0, bf16x8 q1, int l, char* dst) {
  const float l0 = __shfl(lacc[0] + lacc[1], l & 15), l1 = __shfl(lacc[2] + lacc[3], l & 15);
  float lsum = (l & 16) ? l1 : l0;
  bool ok = (lsum >= 9.5367431640625e-07f) && (lsum <= 1.8446744073709552e19f);
#pragma unroll
  for (int r = 0; r < 16; ++r) ok = ok && (__builtin_fabsf(o[r]) <= 3.0e38f);
  if (!__all(ok)) attn_fallback(ws + WS_KV, q0, q1, l, o, lsum);
  const float rl = 1.0f / lsum;
#pragma unroll
  for (int s = 0; s < 2; ++s) {
    u32x4 ov;
#pragma unroll
    for (int j = 0; j < 4; ++j) ov[j] = cvtpk(o[8 * s + 2 * j] * rl, o[8 * s + 2 * j + 1] * rl);
    store16_dev(dst + s * 1024, ov);
  }
}

template <bool LOADER>
DI void attn_phase(char* __restrict__ ws, const u16* w2, const u16* wo, char* smem, const int w, const int l, unsigned* late_pub) {
  const int tt = blockIdx.x >> 2, hg = blockIdx.x & 3;
  const int head = hg * 8 + w, lw = w & 3;
  const unsigned lds0 = (unsigned)(size_t)(LAS char*)smem;
  unsigned* flags = (unsigned*)(ws + WS_FLAG);
  const unsigned voff = l * 16;
  const char* ksrc = ws + WS_KV + (size_t)lw * 4096;
  const unsigned kdst = lds0 + AT_BASE + lw * 4096;
  auto ld = [&](int c, int half) {
    const char* s_ = ksrc + (size_t)c * AT_BUF + half * 16384;
    const unsigned d_ = kdst + (c & 1) * AT_BUF + half * 16384;
    glds16_dev<0>(voff, s_, d_); glds16_dev<1024>(voff, s_, d_); glds16_dev<2048>(voff, s_, d_); glds16_dev<3072>(voff, s_, d_);
  };
  G2Ld g2;
  const unsigned tdump = lds0 + AT_DUMP + lw * 256;
  const int xcd = blockIdx.x & 7, idx = blockIdx.x >> 3, gk = xcd & 1;
  const int mt = ((xcd >> 1) & 1) * 8 + (idx & 7), nt = (xcd >> 2) * 4 + (idx >> 3);
  STAMP(2, 0);
  if (LOADER) {
    const unsigned* word = flags + ((l & 1) ? FL_Q + (tt >> 3) * 8 + hg * 2 + (lw >> 1) : FL_KV) * 32;
    const unsigned need = (l & 1) ? 4u : 32u;
#pragma unroll 1
    for (unsigned spins = 0; spins < SPIN_CAP; ++spins) {
      if (__all(__hip_atomic_load((gu32*)word, RLX_AGENT) >= need)) break;
      __builtin_amdgcn_s_sleep(2);
    }
    __builtin_amdgcn_fence(__ATOMIC_ACQUIRE, "wavefront");
    const char* qs = ws + WS_Q + (size_t)(tt * 32 + hg * 8 + lw * 2) * 2048;
    const unsigned qd = lds0 + AT_Q + lw * 4096;
    glds16_dev<0>(voff, qs, qd); glds16_dev<1024>(voff, qs, qd); glds16_dev<2048>(voff, qs, qd); glds16_dev<3072>(voff, qs, qd);
    ld(0, 0); ld(0, 1); ld(1, 0); ld(1, 1);
    l2_touch(wo, tdump);
    g2.init(ws, w2, wo, lds0, lw, l);
  }
  const unsigned s_lo = (l & 14) == 0 ? 0x3f803f80u : 0u, s_hi = (l & 14) == 2 ? 0x3f803f80u : 0u;
  const bf16x8_t sel = __builtin_bit_cast(bf16x8_t, (u32x4){s_lo, s_lo, s_hi, s_hi});
  const int sidx = (l & 1) ? 0xeeee : 0x4444;
  f32x16 o = {}, sa, sb;
  f32x4 ls = {};
  u32x8 pa, pb = {};
  PIN(o);
  if (LOADER) asm volatile("s_waitcnt vmcnt(9)\n\ts_barrier" ::: "memory");
  else asm volatile("s_barrier" ::: "memory");
  STAMP(2, 1);

  const LAS char* qp = (const LAS char*)smem + AT_Q + w * 2048 + l * 16;
  const bf16x8 q0 = lds_b128(qp), q1 = lds_b128(qp + 1024);
  const LAS char* kvp = (const LAS char*)smem + AT_BASE + l * 16;
  bf16x8 kx0, kx1, ky0, ky1, va0, va1, vb0, vb1, vc0, vc1, vd0, vd1;
  kx0 = lds_b128(kvp); kx1 = lds_b128(kvp + 1024); ky0 = lds_b128(kvp + 4096); ky1 = lds_b128(kvp + 4096 + 1024);
  va0 = lds_b128(kvp + 2048); va1 = lds_b128(kvp + 3072); vb0 = lds_b128(kvp + 4096 + 2048); vb1 = lds_b128(kvp + 4096 + 3072);
  vd0 = va0; vd1 = va1;
  f32x16 nm;
  {
    sa = MFMA32(kx0, q0, o); sa = MFMA32(kx1, q1, sa);
    const float ma = -xhalf_max(max16(sa));
#pragma unroll
    for (int r = 0; r < 16; ++r) { nm[r] = ma; sa[r] += ma; }
    PIN(nm);
  }
#define EX(S, i) S[i] = __builtin_amdgcn_exp2f(S[i])
#define CV(P, j, S, i) P[j] = cvtpk(S[i], S[(i) + 1])
#define PLO(x) __builtin_bit_cast(bf16x8, __builtin_shufflevector(x, x, 0, 1, 2, 3))
#define PHI(x) __builtin_bit_cast(bf16x8, __builtin_shufflevector(x, x, 4, 5, 6, 7))
#define RSUM(x) ls = __builtin_amdgcn_smfmac_f32_16x16x64_bf16(sel, __builtin_bit_cast(bf16x16_t, x), ls, sidx, 0, 0)
#define STEP(S, P, SN, PP, KN0, KN1, VP0, VP1, KL0, KL1, VL0, VL1, NP) do { \
    SN = MFMA32(KN0, q0, nm); SBAR(); KL0 = lds_b128(NP); EX(S, 0); EX(S, 1); EX(S, 2); EX(S, 3); PIN(S); SBAR(); \
    SN = MFMA32(KN1, q1, SN); SBAR(); KL1 = lds_b128((NP) + 1024); CV(P, 0, S, 0); CV(P, 1, S, 2); EX(S, 4); EX(S, 5); EX(S, 6); EX(S, 7); PIN(S); PIN(P); SBAR(); \
    o = MFMA32(VP0, PLO(PP), o); SBAR(); VL0 = lds_b128((NP) + 2048); CV(P, 2, S, 4); EX(S, 8); CV(P, 3, S, 6); EX(S, 9); EX(S, 10); EX(S, 11); PIN(S); PIN(P); SBAR(); \
    o = MFMA32(VP1, PHI(PP), o); SBAR(); VL1 = lds_b128((NP) + 3072); CV(P, 4, S, 8); CV(P, 5, S, 10); EX(S, 12); EX(S, 13); EX(S, 14); PIN(S); PIN(P); SBAR(); \
    RSUM(PP); SBAR(); EX(S, 15); CV(P, 6, S, 12); CV(P, 7, S, 14); PIN(P); SBAR(); \
  } while (0)
#define XP(st, j) do { if (LOADER && (unsigned)((st) + 1) < 4u) g2.template ffn_piece<j>((st) + 1); SBAR(); } while (0)
#define PRIO(n) do { __builtin_amdgcn_s_setprio(n); SBAR(); } while (0)
#pragma unroll 1
  for (int c = 0; c < AT_NCH; ++c) {
    const LAS char* cb = kvp + (c & 1) * AT_BUF;
    const LAS char* nb = kvp + ((c + 1) & 1) * AT_BUF;
    PRIO(2);
    STEP(sa, pa, sb, pb, ky0, ky1, vd0, vd1, kx0, kx1, vc0, vc1, cb + 2 * 4096); XP(c - 5, 2);
    STEP(sb, pb, sa, pa, kx0, kx1, va0, va1, ky0, ky1, vd0, vd1, cb + 3 * 4096); XP(c - 5, 3);
    PRIO(1);
    STEP(sa, pa, sb, pb, ky0, ky1, vb0, vb1, kx0, kx1, va0, va1, cb + 4 * 4096); XP(c - 5, 4);
    STEP(sb, pb, sa, pa, kx0, kx1, vc0, vc1, ky0, ky1, vb0, vb1, cb + 5 * 4096); XP(c - 5, 5);
    STEP(sa, pa, sb, pb, ky0, ky1, vd0, vd1, kx0, kx1, vc0, vc1, cb + 6 * 4096); XP(c - 5, 6);
    PRIO(0);
    STEP(sb, pb, sa, pa, kx0, kx1, va0, va1, ky0, ky1, vd0, vd1, cb + 7 * 4096); XP(c - 5, 7);
    if (c < AT_NCH - 1) {
      if (LOADER) {
        if (c < 4) asm volatile("s_waitcnt vmcnt(1) lgkmcnt(0)\n\ts_barrier" ::: "memory");
        else asm volatile("s_waitcnt vmcnt(8) lgkmcnt(0)\n\ts_barrier" ::: "memory");
        if (c == 2)
          peek_dev(flags + (FL_H + (mt >> 1)) * 32, lds0 + G1_CS + lw * 256);
        if (c + 2 < AT_NCH) { ld(c + 2, 0); ld(c + 2, 1); }
        if (c < 2) {
          const int j = c * 4 + lw;
          l2_touch(j < 7 ? (const char*)(w2 + (size_t)(gk * 1792 + j * 256 + (mt & 7) * 32 + (l >> 1)) * DM + nt * 128 + (l & 1) * 64)
                         : (const char*)(wo + (size_t)((mt & 7) * 128 + l) * DM + nt * 128 + gk * 64), tdump);
        } else if (c == 2) {
          l2_touch(lw == 0 ? (const char*)(wo + (size_t)((mt & 7) * 128 + 64 + l) * DM + nt * 128 + gk * 64) : (const char*)wo, tdump);
        } else if (c == 3) {
          if (rfl(*(const LAS unsigned*)((const LAS char*)smem + G1_CS + lw * 256)) < 92u) wait_ge(flags + (FL_H + (mt >> 1)) * 32, 92u);
        }
      } else {
        asm volatile("s_waitcnt lgkmcnt(0)\n\ts_barrier" ::: "memory");
        if (c == 0 && late_pub) publish(late_pub, l);
        if (!(c & 1)) STAMP(2, 4 + (c >> 1));
      }
      PRIO(3);
    }
    SBAR();
    STEP(sa, pa, sb, pb, ky0, ky1, vb0, vb1, kx0, kx1, va0, va1, nb); XP(c - 4, 0);
    STEP(sb, pb, sa, pa, kx0, kx1, vc0, vc1, ky0, ky1, vb0, vb1, nb + 4096); XP(c - 4, 1);
  }
  o = MFMA32(vd0, PLO(pb), o); o = MFMA32(vd1, PHI(pb), o); RSUM(pb);
#undef EX
#undef CV
#undef PLO
#undef PHI
#undef RSUM
#undef STEP
#undef PRIO
#undef XP
  STAMP(2, 2);
  __builtin_amdgcn_s_setprio(0);
  attn_finish(ws, o, ls, q0, q1, l, ws + WS_O + (size_t)(head >> 1) * KT + (size_t)tt * 4096 + (head & 1) * 2048 + l * 16);
  STAMP(2, 3);
}

DI void g2_load(const u16* w2, const u16* wo, char* __restrict__ ws, unsigned lds0, const int lw, const int l) {
  const int xcd = blockIdx.x & 7, idx = blockIdx.x >> 3;
  const int mt = ((xcd >> 1) & 1) * 8 + (idx & 7);
  unsigned* flags = (unsigned*)(ws + WS_FLAG);
  G2Ld g; g.init(ws, w2, wo, lds0, lw, l);
  asm volatile("s_waitcnt vmcnt(10) lgkmcnt(0)\n\ts_barrier" ::: "memory");
  g.ffn(4); g.ffn(5);
  const LAS unsigned* peek = (const LAS unsigned*)(size_t)(lds0 + G2_PART);
#pragma unroll 1
  for (int i = 2; i < 24; ++i) {
    asm volatile("s_waitcnt vmcnt(16)\n\ts_barrier" ::: "memory");
    if (i == 6 && l == 0) __hip_atomic_fetch_add((gu32*)flags + (FL_O + (blockIdx.x >> 4)) * 32, 1u, RLX_AGENT);
    if (i == 20 && lw == 0) peek_dev(flags + (FL_O + mt) * 32, lds0 + G2_PART);
    g.ffn(i + 4);
  }
  asm volatile("s_waitcnt vmcnt(16)\n\ts_barrier" ::: "memory");
  if (rfl(peek[0]) < 128u) wait_ge(flags + (FL_O + mt) * 32, 128u);
  g.wo(28);
  peek = (const LAS unsigned*)(size_t)(lds0 + G2_BOFF + 2048);
#pragma unroll 1
  for (int i = 25; i < 37; ++i) {
    asm volatile("s_waitcnt vmcnt(12)\n\ts_barrier" ::: "memory");
    if (i == 35 && lw == 0) peek_dev(flags + (FL_P + blockIdx.x) * 32, lds0 + G2_BOFF + 2048);
    g.wo(i + 4);
  }
  auto part = [&]() {
    const char* src = ws + WS_PART + (size_t)(blockIdx.x ^ 1) * 32768 + lw * 8192;
    const unsigned d = lds0 + G2_PART + lw * 8192;
    glds16_dev<0>(g.voff, src, d); glds16_dev<1024>(g.voff, src, d); glds16_dev<2048>(g.voff, src, d); glds16_dev<3072>(g.voff, src, d);
    glds16_dev<0>(g.voff, src + 4096, d + 4096); glds16_dev<1024>(g.voff, src + 4096, d + 4096); glds16_dev<2048>(g.voff, src + 4096, d + 4096); glds16_dev<3072>(g.voff, src + 4096, d + 4096);
  };
  asm volatile("s_waitcnt vmcnt(12)\n\ts_barrier" ::: "memory");
  const bool early = rfl(peek[0]) >= 4u;
  g.wo(41);
  if (early) part();
#pragma unroll 1
  for (int i = 38; i < 41; ++i) {
    if (early) asm volatile("s_waitcnt vmcnt(20)\n\ts_barrier" ::: "memory");
    else asm volatile("s_waitcnt vmcnt(12)\n\ts_barrier" ::: "memory");
    if (i < 40) g.wo(i + 4);
  }
  asm volatile("s_waitcnt vmcnt(6)\n\ts_barrier" ::: "memory");
  if (!early) { wait_ge(flags + (FL_P + blockIdx.x) * 32, 4u); part(); }
  asm volatile("s_waitcnt vmcnt(0)\n\ts_barrier" ::: "memory");
}

DI void g2_compute(const u16* __restrict__ x, const u16* __restrict__ b2, char* __restrict__ ws, u16* __restrict__ out,
                   char* smem, const int w, const int l) {
  const int xcd = blockIdx.x & 7, idx = blockIdx.x >> 3;
  const int kh = xcd & 1, mt = ((xcd >> 1) & 1) * 8 + (idx & 7), nt = (xcd >> 2) * 4 + (idx >> 3);
  unsigned* flags = (unsigned*)(ws + WS_FLAG);
  STAMP(3, 0);
  const int h = l >> 5, wm = w & 1, wn = w >> 1;
  f32x16 accg[2] = {}, accp[2] = {}, accs[2] = {};
  const int col0 = nt * 128 + kh * 64 + wn * 32;
  const int row0 = mt * 128 + wm * 64 + (l & 31);
  unsigned stw[2];
  u32x4 xv[4];
  u32x2 bv[4];
#pragma unroll
  for (int m = 0; m < 2; ++m) stw[m] = __hip_atomic_load((gu32*)(ws + WS_STAT) + row0 + m * 32, RLX_AGENT);
#pragma unroll
  for (int k = 0; k < 4; ++k) xv[k] = *(const u32x4*)(x + (size_t)(row0 + (k >> 1) * 32) * DM + col0 + 16 * (k & 1) + 8 * h);
#pragma unroll
  for (int k = 0; k < 4; ++k) bv[k] = *(const u32x2*)(b2 + col0 + 8 * k + 4 * h);
  const LAS char* lbase = (const LAS char*)smem + l * 16;
  const int btr = (4 * h + ((l & 15) >> 2)) * 64 + ((l >> 4) & 1) * 32 + (l & 3) * 8 - l * 16;
#define LDB(p_) cat8(lds_tr(p_), lds_tr((p_) + 512))
  const int ao = wm * 8192;
  const int bG = G2_BOFF + (kh * 2 + wn) * 1024 + btr, bP = G2_BOFF + ((kh ^ 1) * 2 + wn) * 1024 + btr;
  const int bW = G2_BOFF + wn * 1024 + btr;
  bf16x8 fa0[2], fb0[2], fa1[2], fb1[2];
#define G2_RD(FA, FB, base, ks) do { \
    FA[0] = lds_b128((base) + ao + (ks) * 1024); FB[0] = LDB((base) + bG + (ks) * 4096); \
    FA[1] = lds_b128((base) + ao + 4096 + (ks) * 1024); FB[1] = LDB((base) + bP + (ks) * 4096); \
  } while (0)
#define G2_MM(FA, FB) do { \
    accg[0] = MFMA32(FB[0], FA[0], accg[0]); accg[1] = MFMA32(FB[0], FA[1], accg[1]); \
    accp[0] = MFMA32(FB[1], FA[0], accp[0]); accp[1] = MFMA32(FB[1], FA[1], accp[1]); \
  } while (0)
#define G2_SCHED(NR) do { \
    __builtin_amdgcn_sched_group_barrier(0x008, 1, 0); __builtin_amdgcn_sched_group_barrier(0x100, NR, 0); \
    __builtin_amdgcn_sched_group_barrier(0x008, 1, 0); __builtin_amdgcn_sched_group_barrier(0x100, NR, 0); \
    __builtin_amdgcn_sched_group_barrier(0x008, 2, 0); SBAR(); \
  } while (0)
#define G2_BAR() do { asm volatile("s_waitcnt lgkmcnt(0)\n\ts_barrier" ::: "memory"); SBAR(); } while (0)

  SBAR();
  STAMP(3, 1);
  G2_RD(fa0, fb0, lbase, 0); SBAR();
  G2_RD(fa1, fb1, lbase, 1); G2_MM(fa0, fb0); G2_SCHED(3);
  G2_RD(fa0, fb0, lbase, 2); G2_MM(fa1, fb1); G2_SCHED(3);
  G2_RD(fa1, fb1, lbase, 3); G2_MM(fa0, fb0); G2_SCHED(3);
#pragma unroll 1
  for (int i = 0; i < 27; ++i) {
    const LAS char* nb = lbase + ((i + 1) & 3) * G2_SLOT;
    if (i > 0) G2_BAR();
    G2_RD(fa0, fb0, nb, 0); G2_MM(fa1, fb1); G2_SCHED(3);
    G2_RD(fa1, fb1, nb, 1); G2_MM(fa0, fb0); G2_SCHED(3);
    G2_RD(fa0, fb0, nb, 2); G2_MM(fa1, fb1); G2_SCHED(3);
    G2_RD(fa1, fb1, nb, 3); G2_MM(fa0, fb0); G2_SCHED(3);
    if (i == 3) { publish(flags + (FL_O + (blockIdx.x >> 4)) * 32, l); SBAR(); }
  }
  STAMP(3, 4);
  bf16x8 ga0[4], gb0[2], ga1[4], gb1[2];
#define W_RD(GA, GB, base, ks) do { \
    GA[0] = lds_b128((base) + ao + (ks) * 1024); GB[0] = LDB((base) + bW + (ks) * 4096); GA[1] = lds_b128((base) + ao + 4096 + (ks) * 1024); \
    GA[2] = lds_b128((base) + ao + (ks) * 1024 + 1024); GB[1] = LDB((base) + bW + (ks) * 4096 + 4096); GA[3] = lds_b128((base) + ao + 4096 + (ks) * 1024 + 1024); \
  } while (0)
#define W_MM(GA, GB) do { \
    accs[0] = MFMA32(GB[0], GA[0], accs[0]); accs[1] = MFMA32(GB[0], GA[1], accs[1]); \
    accs[0] = MFMA32(GB[1], GA[2], accs[0]); accs[1] = MFMA32(GB[1], GA[3], accs[1]); \
  } while (0)
#define W_STEP(i) do { const LAS char* nb_ = lbase + (((i) + 1) & 3) * G2_SLOT; G2_BAR(); \
    W_RD(ga0, gb0, nb_, 0); W_MM(ga1, gb1); G2_SCHED(4); \
    W_RD(ga1, gb1, nb_, 2); W_MM(ga0, gb0); G2_SCHED(4); } while (0)
  char* pdst = ws + WS_PART + (size_t)blockIdx.x * 32768 + w * 8192 + l * 16;
#define G2_SEND(q) do { asm volatile("s_nop 7" ::: "memory"); \
    _Pragma("unroll") for (int i_ = 2 * ((q) & 1); i_ < 2 * ((q) & 1) + 2; ++i_) \
      store16_dev(pdst + ((q) >> 1) * 4096 + i_ * 1024, (u32x4){__float_as_uint(accp[(q) >> 1][4 * i_]), __float_as_uint(accp[(q) >> 1][4 * i_ + 1]), \
                                                               __float_as_uint(accp[(q) >> 1][4 * i_ + 2]), __float_as_uint(accp[(q) >> 1][4 * i_ + 3])}); SBAR(); } while (0)
  G2_BAR();
  W_RD(ga0, gb0, lbase, 0); G2_MM(fa1, fb1); G2_SCHED(4);
  W_RD(ga1, gb1, lbase, 2); W_MM(ga0, gb0); G2_SCHED(4);
  G2_SEND(0);
  STAMP(3, 5);
  W_STEP(28); G2_SEND(1);
  W_STEP(29); G2_SEND(2);
  W_STEP(30); G2_SEND(3);
#pragma unroll 1
  for (int i = 31; i < 42; ++i) {
    if (i == 40) STAMP(3, 6);
    W_STEP(i);
    if (i == 33) { publish(flags + (FL_P + (blockIdx.x ^ 1)) * 32, l); SBAR(); }
  }
  W_STEP(42);
  W_MM(ga1, gb1);
  STAMP(3, 2);
  {
    const LAS char* xp = (const LAS char*)smem + G2_PART + w * 8192 + l * 16;
#pragma unroll
    for (int m = 0; m < 2; ++m) {
      const float den = bf_hi(stw[m]), rd2 = 1.0f / (den * den);
#pragma unroll
      for (int k = 0; k < 2; ++k) {
        u32x4 xx = xv[m * 2 + k];
        { auto r0 = __builtin_amdgcn_permlane32_swap(xx[0], xx[2], false, false); xx[0] = r0[0]; xx[2] = r0[1];
          auto r1 = __builtin_amdgcn_permlane32_swap(xx[1], xx[3], false, false); xx[1] = r1[0]; xx[3] = r1[1]; }
        unsigned pk[4];
#pragma unroll
        for (int ii = 0; ii < 2; ++ii) {
          const int i = 2 * k + ii;
          const f32x4 tp = *(const LAS f32x4*)(xp + m * 4096 + i * 1024);
          float r[4];
#pragma unroll
          for (int j = 0; j < 4; ++j) {
            const unsigned xw_ = xx[2 * ii + (j >> 1)], bw_ = bv[i][j >> 1];
            const float z = (accg[m][4 * i + j] + tp[j]) * rd2 + ((j & 1) ? bf_hi(bw_) : bf_lo(bw_));
            const float sg = z * __builtin_amdgcn_rcpf(1.0f + __builtin_amdgcn_exp2f(-1.4426950408889634f * z));
            r[j] = ((j & 1) ? bf_hi(xw_) : bf_lo(xw_)) + accs[m][4 * i + j] * sg;
          }
          pk[2 * ii] = cvtpk(r[0], r[1]); pk[2 * ii + 1] = cvtpk(r[2], r[3]);
        }
        auto r0 = __builtin_amdgcn_permlane32_swap(pk[0], pk[2], false, false);
        auto r1 = __builtin_amdgcn_permlane32_swap(pk[1], pk[3], false, false);
        *(u32x4*)(out + (size_t)(row0 + m * 32) * DM + col0 + 16 * k + 8 * h) = (u32x4){r0[0], r1[0], r0[1], r1[1]};
      }
    }
  }
  STAMP(3, 3);
#undef G2_RD
#undef G2_MM
#undef G2_SCHED
#undef G2_BAR
#undef G2_SEND
#undef W_RD
#undef W_MM
#undef W_STEP
#undef LDB
}

__global__ __launch_bounds__(512, 2) void main_kernel(const u16* __restrict__ x, const u16* __restrict__ wq, const u16* __restrict__ wk,
                                                      const u16* __restrict__ wv, const u16* __restrict__ wo, const u16* __restrict__ w1,
                                                      const u16* __restrict__ b1, const u16* __restrict__ w2, const u16* __restrict__ b2,
                                                      char* __restrict__ ws, u16* __restrict__ out) {
  extern __shared__ __attribute__((aligned(16))) char smem[];
  const int l = threadIdx.x & 63, w = rfl(threadIdx.x >> 6);
  const unsigned lds0 = (unsigned)(size_t)(LAS char*)smem;
  const int xcd = blockIdx.x & 7, idx = blockIdx.x >> 3;
  const int mtile = (xcd & 1) * 4 + (idx & 3), cg = (idx >> 2) * 4 + (xcd >> 1);
  unsigned* flags = (unsigned*)(ws + WS_FLAG);
  if (w == 3) {
    unsigned long long pc; unsigned len;
    asm volatile("s_getpc_b64 %0\n.Lmk_code_begin:\n\ts_mov_b32 %1, .Lmk_code_end-.Lmk_code_begin" : "=s"(pc), "=s"(len));
    unsigned off = (unsigned)(idx * 16 + (l & 15)) * 128u;
    off = off < len ? off : 0u;
    l2_touch((const char*)pc + off, lds0 + G1_HS + 3072);
  }
  if (w >= 4) {
    if (cg > 8) g1_load<G1_FFN>(ws, x, w1, nullptr, cg, mtile, smem, w - 4, l);
    else if (cg < 8) g1_load<G1_Q>(ws, x, wq, nullptr, cg, mtile, smem, w - 4, l);
    else g1_load<G1_KV>(ws, x, wk, wv, cg, mtile, smem, w - 4, l);
    attn_phase<true>(ws, w2, wo, smem, w, l, nullptr);
    g2_load(w2, wo, ws, lds0, w - 4, l);
    asm volatile(".Lmk_code_end:");
  } else {
    unsigned* late = nullptr;
    if (cg > 8) { g1_compute<G1_FFN>(ws, x, b1, cg, mtile, smem, w, l); late = flags + (FL_H + mtile) * 32; }
    else if (cg < 8) { g1_compute<G1_Q>(ws, x, b1, cg, mtile, smem, w, l); publish(flags + (FL_Q + mtile * 8 + cg) * 32, l); }
    else { g1_compute<G1_KV>(ws, x, b1, cg, mtile, smem, w, l); publish(flags + FL_KV * 32, l); }
    attn_phase<false>(ws, w2, wo, smem, w, l, late);
    g2_compute(x, b2, ws, out, smem, w, l);
  }
}

extern "C" void kernel_launch(void* const* d_in, const int* in_sizes, int n_in, void* d_out, int out_size, void* d_ws,
                              size_t ws_size, hipStream_t stream) {
  const u16* x = (const u16*)d_in[0];
  const u16* wq = (const u16*)d_in[1];
  const u16* wk = (const u16*)d_in[2];
  const u16* wv = (const u16*)d_in[3];
  const u16* wo = (const u16*)d_in[4];
  const u16* w1 = (const u16*)d_in[6];
  const u16* b1 = (const u16*)d_in[7];
  const u16* w2 = (const u16*)d_in[8];
  const u16* b2 = (const u16*)d_in[9];
  char* ws = (char*)d_ws;
  (void)hipFuncSetAttribute((const void*)main_kernel, hipFuncAttributeMaxDynamicSharedMemorySize, MAIN_LDS);
  main_kernel<<<256, 512, MAIN_LDS, stream>>>(x, wq, wk, wv, wo, w1, b1, w2, b2, ws, (u16*)d_out);
}
```

```cpp
#include <hip/hip_runtime.h>
#include <hip/hip_bf16.h>

typedef short bf16x8 __attribute__((ext_vector_type(8)));
typedef short s16x4 __attribute__((ext_vector_type(4)));
typedef float f32x16 __attribute__((ext_vector_type(16)));
typedef float f32x4 __attribute__((ext_vector_type(4)));
typedef float f32x2 __attribute__((ext_vector_type(2)));
typedef __bf16 bf16x2_t __attribute__((ext_vector_type(2)));
typedef unsigned u32x4 __attribute__((ext_vector_type(4)));
typedef unsigned u32x2 __attribute__((ext_vector_type(2)));
typedef unsigned u32x8 __attribute__((ext_vector_type(8)));
typedef __bf16 bf16x8_t __attribute__((ext_vector_type(8)));
typedef __bf16 bf16x16_t __attribute__((ext_vector_type(16)));
typedef unsigned short u16;
#define LAS __attribute__((address_space(3)))
#define DI __device__ __forceinline__
#define SBAR() __builtin_amdgcn_sched_barrier(0)
#define PIN(x) asm volatile("" : "+v"(x))
#define MFMA32(a, b, c) __builtin_amdgcn_mfma_f32_32x32x16_bf16((a), (b), (c), 0, 0, 0)

constexpr int DM = 1024, FF = 3584;
constexpr size_t KT = 262144;
constexpr size_t WS_STAT = 0;
constexpr size_t WS_Q = 4u << 20;
constexpr size_t WS_KV = 8u << 20;
constexpr size_t WS_O = 9u << 20;
constexpr size_t WS_H = 13u << 20;
constexpr size_t WS_PART = 28u << 20;
constexpr size_t WS_FLAG = 37u << 20;
constexpr int FL_P = 0, FL_O = 256, FL_H = 272, FL_Q = 280, FL_KV = 344, FL_S = 345, FL_N = 353;
constexpr size_t WS_HSA = WS_FLAG + 65536, WS_HSB = WS_HSA + 4096, WS_HSC = WS_HSB + 4096;
constexpr int COORD_BLOCK = 64;

DI unsigned rfl(unsigned x) { return __builtin_amdgcn_readfirstlane(x); }
DI unsigned cvtpk(float lo, float hi) {
  f32x2 v = {lo, hi};
  bf16x2_t b = __builtin_convertvector(v, bf16x2_t);
  return __builtin_bit_cast(unsigned, b);
}
DI float fadd_s(float a, float b) { float r; asm("v_add_f32_e32 %0, %1, %2" : "=v"(r) : "v"(a), "v"(b)); return r; }
DI float bf_lo(unsigned u) { return __uint_as_float(u << 16); }
DI float bf_hi(unsigned u) { return __uint_as_float(u & 0xffff0000u); }
DI float bf_round(float x) { return bf_lo(cvtpk(x, x)); }

template <int IMM>
DI void glds16(unsigned voff, const void* sbase, unsigned ldsaddr) {
  asm volatile("s_mov_b32 m0, %2\n\ts_nop 0\n\tglobal_load_lds_dwordx4 %0, %1 offset:%3"
               :: "v"(voff), "s"(sbase), "s"(ldsaddr), "i"(IMM) : "memory");
}
template <int IMM>
DI void glds16_dev(unsigned voff, const void* sbase, unsigned ldsaddr) {
  asm volatile("s_mov_b32 m0, %2\n\ts_nop 0\n\tglobal_load_lds_dwordx4 %0, %1 offset:%3 sc1"
               :: "v"(voff), "s"(sbase), "s"(ldsaddr), "i"(IMM) : "memory");
}
DI void peek_dev(const void* word, unsigned ldsaddr) {
  asm volatile("s_mov_b32 m0, %2\n\ts_nop 0\n\tglobal_load_lds_dword %0, %1 sc1" :: "v"(0u), "s"(word), "s"(ldsaddr) : "memory");
}
DI void store16_dev(void* p, u32x4 v) {
  asm volatile("global_store_dwordx4 %0, %1, off sc1\n\ts_nop 1" :: "v"(p), "v"(v) : "memory");
}
DI void l2_touch(const void* p, unsigned ldsaddr) {
  asm volatile("s_mov_b32 m0, %1\n\ts_nop 0\n\tglobal_load_lds_dword %0, off" :: "v"(p), "s"(ldsaddr) : "memory");
}
constexpr unsigned SPIN_CAP = 1u << 20;
typedef __attribute__((address_space(1))) unsigned gu32;
#define RLX_AGENT __ATOMIC_RELAXED, __HIP_MEMORY_SCOPE_AGENT
DI void wait_ge(const unsigned* word, unsigned want) {
#pragma unroll 1
  for (unsigned spins = 0; spins < SPIN_CAP; ++spins) {
    if (rfl(__hip_atomic_load((gu32*)word, RLX_AGENT)) >= want) break;
    __builtin_amdgcn_s_sleep(2);
  }
  __builtin_amdgcn_fence(__ATOMIC_ACQUIRE, "wavefront");
}
DI void publish(unsigned* word, int l) {
  asm volatile("s_waitcnt vmcnt(0)" ::: "memory");
  if (l == 0) __hip_atomic_fetch_add((gu32*)word, 1u, RLX_AGENT);
}
DI bf16x8 load16_dev(const char* p) {
  typedef __attribute__((address_space(1))) unsigned long long gu64;
  const unsigned long long a = __hip_atomic_load((gu64*)p, RLX_AGENT), b = __hip_atomic_load((gu64*)p + 1, RLX_AGENT);
  return __builtin_bit_cast(bf16x8, (u32x4){(unsigned)a, (unsigned)(a >> 32), (unsigned)b, (unsigned)(b >> 32)});
}
DI float relu_s(float a) { return __builtin_amdgcn_fmed3f(a, 0.0f, 3.4028234663852886e38f); }
DI bf16x8 lds_b128(const LAS char* p) { return *(const LAS bf16x8*)p; }
DI s16x4 lds_tr(const LAS char* p) {
  return __builtin_amdgcn_ds_read_tr16_b64_v4i16((LAS s16x4*)p);
}
DI bf16x8 cat8(s16x4 lo, s16x4 hi) { return __builtin_shufflevector(lo, hi, 0, 1, 2, 3, 4, 5, 6, 7); }

DI float half_sum(float v) {
  v += __uint_as_float(__builtin_amdgcn_update_dpp(0u, __float_as_uint(v), 0xB1, 0xf, 0xf, true));
  v += __uint_as_float(__builtin_amdgcn_update_dpp(0u, __float_as_uint(v), 0x4E, 0xf, 0xf, true));
  v += __uint_as_float(__builtin_amdgcn_update_dpp(0u, __float_as_uint(v), 0x141, 0xf, 0xf, true));
  v += __uint_as_float(__builtin_amdgcn_update_dpp(0u, __float_as_uint(v), 0x140, 0xf, 0xf, true));
  auto r = __builtin_amdgcn_permlane16_swap(__float_as_uint(v), __float_as_uint(v), false, false);
  return __uint_as_float(r[0]) + __uint_as_float(r[1]);
}
DI unsigned row_stats(const u32x4 (&v)[4]) {
  f32x2 e[16], s2 = {0.f, 0.f};
#pragma unroll
  for (int i = 0; i < 16; ++i) { e[i] = (f32x2){bf_lo(v[i >> 2][i & 3]), bf_hi(v[i >> 2][i & 3])}; s2 += e[i]; }
  const float mean = half_sum(s2[0] + s2[1]) * (1.0f / DM);
  f32x2 q2 = {0.f, 0.f};
#pragma unroll
  for (int i = 0; i < 16; ++i) { const f32x2 d = e[i] - mean; q2 += d * d; }
  const float q = half_sum(q2[0] + q2[1]);
  const float den = __builtin_sqrtf(bf_round(bf_round(q * (1.0f / DM)) + 1e-5f));
  return cvtpk(-mean, den);
}

constexpr int G1_STAGE = 53248, G1_BOFF = 32768;
enum { G1_FFN = 5, G1_Q = 4, G1_KV = 2 };

constexpr int G1_ST = 87040;
constexpr int AT_RDY = 89088;
constexpr int G1_HS = 159744;
constexpr int G1_CS = 81920;
constexpr int ESTART = 3, PSTART = 9;
DI float pair_sum(unsigned v) { return fadd_s(bf_lo(v), bf_hi(v)); }

template <int NP>
DI void g1_load(char* ws, const u16* x, const u16* wsrc, const u16* wv, int cg, int mtile, char* smem, int lw, int l) {
  constexpr int LD = (NP == G1_FFN) ? FF : (NP == G1_Q) ? DM : 32;
  const unsigned lds0 = (unsigned)(size_t)(LAS char*)smem;
  const char* abase = (const char*)(x + (size_t)(mtile * 256 + lw * 64) * DM);
  const unsigned ave = (unsigned)((l >> 3) * 2048 + (((l & 7) ^ (l >> 4)) << 4)), avo = ave ^ 64u;
  const unsigned bvoff = (unsigned)((lw * 16 + (l >> 2)) * LD * 2 + (l & 3) * 16);
  const int krot = NP == G1_Q ? (mtile & 3) * 4 : 0;
  const char* bbase[NP];
#pragma unroll
  for (int p = 0; p < NP; ++p) {
    if (NP == G1_KV) bbase[p] = (const char*)(p ? wv : wsrc);
    else if (NP == G1_Q) bbase[p] = (const char*)(wsrc + (cg * 4 + p) * 32);
    else { const int cb = (cg - 9) * 5 + p; bbase[p] = (const char*)(wsrc + (cb > 111 ? 111 : cb) * 32); }
  }
  float cs[NP];
#pragma unroll
  for (int p = 0; p < NP; ++p) cs[p] = 0.f;
  const LAS char* cb0 = (const LAS char*)smem + G1_BOFF + lw * 1024 + (8 * (l >> 5) + ((l & 15) >> 2)) * 64 + ((l >> 4) & 1) * 32 + (l & 3) * 8;
  u32x2 t0[NP], t1[NP];
  auto cs_read = [&](int slot) {
#pragma unroll
    for (int p = 0; p < NP; ++p) {
      t0[p] = __builtin_bit_cast(u32x2, lds_tr(cb0 + slot * G1_STAGE + p * 4096));
      t1[p] = __builtin_bit_cast(u32x2, lds_tr(cb0 + slot * G1_STAGE + p * 4096 + 256));
    }
    SBAR();
  };
  auto cs_add = [&](int p) {
    cs[p] = fadd_s(cs[p], fadd_s(fadd_s(pair_sum(t0[p][0]), pair_sum(t0[p][1])), fadd_s(pair_sum(t1[p][0]), pair_sum(t1[p][1]))));
    PIN(cs[p]); SBAR();
  };
  auto issue = [&](int st, int slot, bool sum) {
    const int kt = (st + krot) & 15;
    const unsigned sa = lds0 + slot * G1_STAGE + lw * 8192;
    const char* a0 = abase + kt * 128;
    const unsigned sb = lds0 + slot * G1_STAGE + G1_BOFF + lw * 1024;
#pragma unroll
    for (int p = 0; p < NP; ++p) glds16<0>(bvoff, bbase[p] + (size_t)kt * (64 * LD * 2), sb + p * 4096);
#pragma unroll
    for (int i = 0; i < 8; ++i) {
      if (sum && i < NP) { SBAR(); cs_add(i); }
      glds16<0>((i & 1) ? avo : ave, a0 + i * 16384, sa + i * 1024);
    }
  };
  auto colsum = [&](int slot) {
    cs_read(slot);
#pragma unroll
    for (int p = 0; p < NP; ++p) cs_add(p);
  };
  unsigned* flags = (unsigned*)(ws + WS_FLAG);
  const bool coord = (NP == G1_KV) && blockIdx.x == COORD_BLOCK && lw == 1;
  if (coord) {
#pragma unroll
    for (int i = 0; i < 6; ++i) if (i * 64 + l < FL_N) __hip_atomic_store((gu32*)flags + (i * 64 + l) * 32, 0u, RLX_AGENT);
    store16_dev(ws + WS_HSC + l * 16, (u32x4){0u, 0u, 0u, 0u});
    asm volatile("s_waitcnt vmcnt(0)" ::: "memory");
  }
  issue(0, 0, false); issue(1, 1, false); issue(2, 2, false);
  asm volatile("s_waitcnt vmcnt(%0)\n\ts_barrier" :: "n"(2 * (8 + NP)) : "memory");
  __builtin_amdgcn_s_setprio(2);
  int state = (lw == 0 || coord) ? 0 : 2;
  bool pend = false, first = true;
  u32x4 last = {0u, 0u, 0u, 0u};
  const unsigned found = rfl(*(const LAS unsigned*)((const LAS char*)smem + G1_HS));
  if (lw == 0 && l < 9) {
    const unsigned v = *(const LAS unsigned*)((const LAS char*)smem + G1_HS + 16 + (l & 7) * 4);
    __hip_atomic_store(l < 8 ? (gu32*)(ws + WS_STAT) + mtile * 256 + cg * 8 + l : (gu32*)(ws + WS_HSA) + blockIdx.x, l < 8 ? v : found + 1u, RLX_AGENT);
  }
  colsum(0);
  auto poll_issue = [&]() {
    if (NP == G1_KV && coord) {
      glds16_dev<0>(l * 16, ws + WS_HSA, lds0 + G1_HS + 1024); glds16_dev<0>(l * 16, ws + WS_HSC, lds0 + G1_HS + 2048);
    } else {
      const char* a_ = state == 0 ? ws + WS_HSB + blockIdx.x * 4 : (const char*)(flags + (FL_S + mtile) * 32);
      peek_dev(a_, lds0 + G1_HS + 256);
    }
  };
  auto poll_check = [&]() {
    if (NP == G1_KV && coord) {
      const u32x4 pa = *(const LAS u32x4*)((const LAS char*)smem + G1_HS + 1024 + l * 16), pc = *(const LAS u32x4*)((const LAS char*)smem + G1_HS + 2048 + l * 16);
      if (first || pa[0] != last[0] || pa[1] != last[1] || pa[2] != last[2] || pa[3] != last[3]) store16_dev(ws + WS_HSB + l * 16, pa);
      last = pa; first = false;
      if (__all((pc[0] & pc[1] & pc[2] & pc[3]) == 1u)) state = 2;
    } else {
      const unsigned v = rfl(*(const LAS unsigned*)((const LAS char*)smem + G1_HS + 256));
      if (state == 0) {
        if (v != found) {
          if (l == 0) {
            __hip_atomic_store((gu32*)(ws + WS_HSC) + blockIdx.x, 1u, RLX_AGENT);
            __hip_atomic_fetch_add((gu32*)flags + (FL_S + mtile) * 32, 1u, RLX_AGENT);
          }
          state = 1;
        }
      } else if (v >= 32u) state = 2;
    }
    state = rfl(state);
  };
  int slot = 0;
#pragma unroll 1
  for (int i = 0; i < 13; ++i) {
    asm volatile("s_waitcnt vmcnt(%0)\n\ts_barrier" :: "n"(8 + NP) : "memory");
    if (state < 2 && (i >= ESTART || coord)) {
      if (pend) poll_check();
      pend = state == 0 || (state == 1 && i >= PSTART);
      if (pend) poll_issue();
    }
    const int nslot = (slot == 2) ? 0 : slot + 1;
    cs_read(nslot);
    issue(i + 3, slot, true);
    slot = nslot;
  }
#pragma unroll 1
  for (unsigned spins = 0; state < 2 && spins < SPIN_CAP; ++spins) {
    if (!pend) poll_issue();
    asm volatile("s_waitcnt vmcnt(0)" ::: "memory");
    poll_check();
    pend = false;
    __builtin_amdgcn_s_sleep(1);
  }
  asm volatile("s_waitcnt vmcnt(%0)\n\ts_barrier" :: "n"(8 + NP) : "memory");
  if (lw == 0) { glds16_dev<0>(l * 16, ws + WS_STAT + mtile * 1024, lds0 + G1_ST); *(LAS unsigned*)((LAS char*)smem + AT_RDY) = 0u; }
  colsum(2);
  asm volatile("s_waitcnt vmcnt(0)" ::: "memory");
  colsum(0);
#pragma unroll
  for (int p = 0; p < NP; ++p) *(LAS float*)((LAS char*)smem + G1_CS + p * 1024 + (l & 31) * 32 + (lw * 2 + (l >> 5)) * 4) = cs[p];
  asm volatile("s_waitcnt lgkmcnt(0)\n\ts_barrier" ::: "memory");
}

template <int NP>
DI void g1_compute(char* __restrict__ ws, const u16* __restrict__ x, const u16* __restrict__ b1, int cg, int mtile, char* smem, int w, int l) {
  const int h = l >> 5;
  unsigned st[2], bias[NP];
  {
    const int row = mtile * 256 + cg * 8 + w * 2 + h;
    u32x4 v[4];
#pragma unroll
    for (int i = 0; i < 4; ++i) v[i] = *(const u32x4*)(x + (size_t)row * DM + (i * 32 + (l & 31)) * 8);
    const unsigned found = __hip_atomic_load((gu32*)(ws + WS_HSB) + blockIdx.x, RLX_AGENT);
#pragma unroll
    for (int p = 0; p < NP; ++p) {
      int cb = (cg - 9) * 5 + p; cb = cb > 111 ? 111 : cb;
      bias[p] = (NP == G1_FFN) ? (unsigned)b1[cb * 32 + (l & 31)] : 0u;
    }
    SBAR();
    const unsigned rs = row_stats(v);
    if ((l & 31) == 0) *(LAS unsigned*)((LAS char*)smem + G1_HS + 16 + (w * 2 + h) * 4) = rs;
    if (w == 0 && l == 0) *(LAS unsigned*)((LAS char*)smem + G1_HS) = found;
  }
  f32x16 acc[2][NP];
#pragma unroll
  for (int p = 0; p < NP; ++p) { acc[0][p] = f32x16{}; acc[1][p] = f32x16{}; }
  const LAS char* lbase = (const LAS char*)smem;
  int aoff[4];
#pragma unroll
  for (int ks = 0; ks < 4; ++ks) aoff[ks] = (w * 64 + (l & 31)) * 128 + (((2 * ks + h) ^ ((l >> 1) & 7)) << 4);
  const int boff = G1_BOFF + (8 * h + ((l & 15) >> 2)) * 64 + ((l >> 4) & 1) * 32 + (l & 3) * 8;
  bf16x8 fa0[2], fb0[NP], fa1[2], fb1[NP];
#define G1_RD(FA, FB, base, ks) do { \
    FA[0] = lds_b128((base) + aoff[ks]); FA[1] = lds_b128((base) + aoff[ks] + 4096); \
    _Pragma("unroll") for (int p_ = 0; p_ < NP; ++p_) \
      FB[p_] = cat8(lds_tr((base) + boff + p_ * 4096 + (ks) * 1024), lds_tr((base) + boff + p_ * 4096 + (ks) * 1024 + 256)); \
  } while (0)
#define G1_MM(FA, FB) do { \
    _Pragma("unroll") for (int p_ = 0; p_ < NP; ++p_) { \
      if (NP == G1_KV && p_ == 1) { acc[0][p_] = MFMA32(FA[0], FB[p_], acc[0][p_]); acc[1][p_] = MFMA32(FA[1], FB[p_], acc[1][p_]); } \
      else { acc[0][p_] = MFMA32(FB[p_], FA[0], acc[0][p_]); acc[1][p_] = MFMA32(FB[p_], FA[1], acc[1][p_]); } } \
  } while (0)
#define G1_SCHED() do { \
    _Pragma("unroll") for (int q_ = 0; q_ < NP + 1; ++q_) { \
      __builtin_amdgcn_sched_group_barrier(0x008, 1, 0); __builtin_amdgcn_sched_group_barrier(0x100, 2, 0); } \
    __builtin_amdgcn_sched_group_barrier(0x008, NP - 1, 0); SBAR(); \
  } while (0)

  asm volatile("s_waitcnt lgkmcnt(0)\n\ts_barrier" ::: "memory");
  G1_RD(fa0, fb0, lbase, 0); SBAR();
  G1_RD(fa1, fb1, lbase, 1); G1_MM(fa0, fb0); G1_SCHED();
  G1_RD(fa0, fb0, lbase, 2); G1_MM(fa1, fb1); G1_SCHED();
  G1_RD(fa1, fb1, lbase, 3); G1_MM(fa0, fb0); G1_SCHED();
  int slot = 1;
#pragma unroll 1
  for (int i = 0; i < 15; ++i) {
    const LAS char* nb = lbase + slot * G1_STAGE;
    asm volatile("s_waitcnt lgkmcnt(0)\n\ts_barrier" ::: "memory"); SBAR();
    G1_RD(fa0, fb0, nb, 0); G1_MM(fa1, fb1); G1_SCHED();
    G1_RD(fa1, fb1, nb, 1); G1_MM(fa0, fb0); G1_SCHED();
    G1_RD(fa0, fb0, nb, 2); G1_MM(fa1, fb1); G1_SCHED();
    G1_RD(fa1, fb1, nb, 3); G1_MM(fa0, fb0); G1_SCHED();
    slot = (slot == 2) ? 0 : slot + 1;
  }
  __builtin_amdgcn_s_setprio(3);
  {
    f32x4 t[NP][2];
#pragma unroll
    for (int m = 0; m < 2; ++m) st[m] = *(const LAS unsigned*)(lbase + G1_ST + (w * 64 + m * 32 + (l & 31)) * 4);
#pragma unroll
    for (int p = 0; p < NP; ++p) {
      t[p][0] = *(const LAS f32x4*)(lbase + G1_CS + p * 1024 + (l & 31) * 32);
      t[p][1] = *(const LAS f32x4*)(lbase + G1_CS + p * 1024 + (l & 31) * 32 + 16);
    }
    SBAR();
    G1_MM(fa1, fb1);
    const unsigned msk = h ? 0u : ~0u;
#pragma unroll
    for (int m = 0; m < 2; ++m) {
      const unsigned nm = st[m] & 0xffffu;
      fa0[m] = __builtin_bit_cast(bf16x8, (u32x4){(nm | (nm << 16)) & msk, st[m] & msk, 0u, 0u});
    }
#pragma unroll
    for (int p = 0; p < NP; ++p) {
      const f32x4 u = t[p][0] + t[p][1];
      const float c = (u[0] + u[1]) + (u[2] + u[3]);
      const unsigned c0 = cvtpk(c, c) & 0xffffu; const float r1 = c - bf_lo(c0);
      const unsigned c1 = cvtpk(r1, r1) & 0xffffu; const float r2 = r1 - bf_lo(c1);
      const unsigned c2 = cvtpk(r2, r2) & 0xffffu;
      fb0[p] = __builtin_bit_cast(bf16x8, (u32x4){c0 | (c1 << 16), c2 | (bias[p] << 16), 0u, 0u});
    }
  }
  G1_MM(fa0, fb0);
  float rden[2];
#pragma unroll
  for (int m = 0; m < 2; ++m) rden[m] = 1.0f / bf_hi(st[m]);
  SBAR();

#pragma unroll
  for (int p = 0; p < NP; ++p) {
    const int cb = (NP == G1_FFN) ? (cg - 9) * 5 + p : cg * 4 + p;
    if (NP != G1_FFN || cb < 112) {
#pragma unroll
      for (int m = 0; m < 2; ++m) {
        const int tt = mtile * 8 + w * 2 + m;
        char* dst = (NP == G1_FFN) ? ws + WS_H + (size_t)(cb >> 1) * KT + (size_t)tt * 4096 + (cb & 1) * 2048 + l * 16
                  : (NP == G1_Q)   ? ws + WS_Q + (size_t)(tt * 32 + cb) * 2048 + l * 16
                                   : ws + WS_KV + (size_t)tt * 4096 + p * 2048 + l * 16;
        const float sc = (NP == G1_Q) ? rden[m] * (0.17677669529663687f * 1.4426950408889634f) : rden[m];
#pragma unroll
        for (int s = 0; s < 2; ++s) {
          u32x4 o;
#pragma unroll
          for (int j = 0; j < 4; ++j) {
            float a = acc[m][p][8 * s + 2 * j], b = acc[m][p][8 * s + 2 * j + 1];
            if (NP == G1_FFN) { a = relu_s(a); b = relu_s(b); a *= a; b *= b; }
            else if (NP == G1_KV && p == 1) {
              const int r = 8 * s + 2 * j, tk = (r & 3) + 8 * (r >> 2) + 4 * h;
              a *= __shfl(rden[m], tk); b *= __shfl(rden[m], tk + 1);
            } else { a *= sc; b *= sc; }
            o[j] = cvtpk(a, b);
          }
          store16_dev(dst + s * 1024, o);
        }
      }
    }
  }
#undef G1_RD
#undef G1_MM
#undef G1_SCHED
}

constexpr int AT_NCH = 8;
constexpr int AT_BUF = 32768;
constexpr int AT_BASE = 98304;
constexpr int AT_Q = 65536;
constexpr int AT_DUMP = 90112;
constexpr int MAIN_LDS = 163840;

DI float xhalf_max(float m) {
  auto rr = __builtin_amdgcn_permlane32_swap(__float_as_uint(m), __float_as_uint(m), false, false);
  return __builtin_fmaxf(__uint_as_float(rr[0]), __uint_as_float(rr[1]));
}
DI float xhalf_sum(float m) {
  auto rr = __builtin_amdgcn_permlane32_swap(__float_as_uint(m), __float_as_uint(m), false, false);
  return __uint_as_float(rr[0]) + __uint_as_float(rr[1]);
}
DI float max16(const f32x16& s) {
  float a = __builtin_fmaxf(s[0], s[1]);
#pragma unroll
  for (int r = 2; r < 16; ++r) a = __builtin_fmaxf(a, s[r]);
  return a;
}
DI bf16x8 pack8(const f32x16& p, int s) {
  u32x4 o;
#pragma unroll
  for (int j = 0; j < 4; ++j) o[j] = cvtpk(p[8 * s + 2 * j], p[8 * s + 2 * j + 1]);
  return __builtin_bit_cast(bf16x8, o);
}

DI void attn_fallback(const char* __restrict__ kv, bf16x8 q0, bf16x8 q1, int l, f32x16& o_out, float& l_out) {
  f32x16 o = {};
  float m = -INFINITY, lsum = 0.f;
  for (int t = 0; t < 64; ++t) {
    const char* p = kv + (size_t)t * 4096 + l * 16;
    const bf16x8 k0 = load16_dev(p), k1 = load16_dev(p + 1024), v0 = load16_dev(p + 2048), v1 = load16_dev(p + 3072);
    f32x16 s = {};
    s = MFMA32(k0, q0, s); s = MFMA32(k1, q1, s);
    const float mn = __builtin_fmaxf(m, xhalf_max(max16(s)));
    const float ms = (mn == -INFINITY) ? 0.f : mn;
    const float alpha = __builtin_amdgcn_exp2f(m - ms);
    m = mn;
    float ps = 0.f;
#pragma unroll
    for (int r = 0; r < 16; ++r) { s[r] = __builtin_amdgcn_exp2f(s[r] - ms); ps += s[r]; }
    lsum = lsum * alpha + ps;
#pragma unroll
    for (int r = 0; r < 16; ++r) o[r] *= alpha;
    o = MFMA32(v0, pack8(s, 0), o); o = MFMA32(v1, pack8(s, 1), o);
  }
  o_out = o;
  l_out = xhalf_sum(lsum);
}

constexpr int G2_SLOT = 32768, G2_BOFF = 16384, G2_PART = 131072;
struct G2Ld {
  const char* hb; const char* ob; const char* wb; const char* wb2; unsigned lds; unsigned voff, voffb; int rot, frot;
  DI void init(const char* ws, const u16* w2, const u16* wo, unsigned lds0, int lw, int l) {
    const int xcd = blockIdx.x & 7, idx = blockIdx.x >> 3, kh = xcd & 1;
    const int mt = ((xcd >> 1) & 1) * 8 + (idx & 7), nt = (xcd >> 2) * 4 + (idx >> 3);
    hb = ws + WS_H + (size_t)(mt * 4 + lw) * 4096 + (size_t)kh * (28 * KT);
    ob = ws + WS_O + (size_t)(mt * 4 + lw) * 4096;
    wb = (const char*)(w2 + (size_t)(kh * 1792 + lw * 16) * DM + nt * 128);
    wb2 = (const char*)(wo + (size_t)(lw * 16) * DM + nt * 128 + kh * 64);
    voffb = (unsigned)((l >> 2) * DM * 2 + (l & 3) * 16);
    lds = lds0 + lw * 4096; voff = l * 16;
    frot = (7 * (mt & 7)) >> 1;
    rot = 2 * (mt & 7);
  }
  DI void ffn(int s) const {
    const unsigned d = lds + (s & 3) * G2_SLOT;
    s += frot; s = s >= 28 ? s - 28 : s;
    const char* a = hb + (size_t)s * KT;
    glds16_dev<0>(voff, a, d); glds16_dev<1024>(voff, a, d); glds16_dev<2048>(voff, a, d); glds16_dev<3072>(voff, a, d);
    const char* b = wb + (size_t)s * 131072;
    glds16<0>(voffb, b, d + G2_BOFF); glds16<0>(voffb, b + 64, d + G2_BOFF + 1024); glds16<0>(voffb, b + 128, d + G2_BOFF + 2048); glds16<0>(voffb, b + 192, d + G2_BOFF + 3072);
  }
  template <int J> DI void ffn_piece(int s) const {
    const unsigned d = lds + (s & 3) * G2_SLOT;
    s += frot; s = s >= 28 ? s - 28 : s;
    if (J < 4) glds16_dev<(J & 3) * 1024>(voff, hb + (size_t)s * KT, d);
    else glds16<0>(voffb, wb + (size_t)s * 131072 + (J & 3) * 64, d + G2_BOFF + (J & 3) * 1024);
  }
  DI void wo(int s) const {
    const unsigned d = lds + (s & 3) * G2_SLOT;
    const int k = (s - 28 + rot) & 15;
    const char* a = ob + (size_t)k * KT;
    glds16_dev<0>(voff, a, d); glds16_dev<1024>(voff, a, d); glds16_dev<2048>(voff, a, d); glds16_dev<3072>(voff, a, d);
    const char* b = wb2 + (size_t)k * 131072;
    glds16<0>(voffb, b, d + G2_BOFF); glds16<0>(voffb, b + 64, d + G2_BOFF + 1024);
  }
};

DI void attn_finish(char* __restrict__ ws, f32x16 o, f32x4 lacc, bf16x8 q0, bf16x8 q1, int l, char* dst) {
  const float l0 = __shfl(lacc[0] + lacc[1], l & 15), l1 = __shfl(lacc[2] + lacc[3], l & 15);
  float lsum = (l & 16) ? l1 : l0;
  bool ok = (lsum >= 9.5367431640625e-07f) && (lsum <= 1.8446744073709552e19f);
#pragma unroll
  for (int r = 0; r < 16; ++r) ok = ok && (__builtin_fabsf(o[r]) <= 3.0e38f);
  if (!__all(ok)) attn_fallback(ws + WS_KV, q0, q1, l, o, lsum);
  const float rl = 1.0f / lsum;
#pragma unroll
  for (int s = 0; s < 2; ++s) {
    u32x4 ov;
#pragma unroll
    for (int j = 0; j < 4; ++j) ov[j] = cvtpk(o[8 * s + 2 * j] * rl, o[8 * s + 2 * j + 1] * rl);
    store16_dev(dst + s * 1024, ov);
  }
}

template <bool LOADER>
DI void attn_phase(char* __restrict__ ws, const u16* w2, const u16* wo, char* smem, const int w, const int l, unsigned* late_pub) {
  const int tt = blockIdx.x >> 2, hg = blockIdx.x & 3;
  const int head = hg * 8 + w, lw = w & 3;
  const unsigned lds0 = (unsigned)(size_t)(LAS char*)smem;
  unsigned* flags = (unsigned*)(ws + WS_FLAG);
  const unsigned voff = l * 16;
  const char* ksrc = ws + WS_KV + (size_t)lw * 4096;
  const unsigned kdst = lds0 + AT_BASE + lw * 4096;
  auto ld = [&](int c, int half) {
    const char* s_ = ksrc + (size_t)c * AT_BUF + half * 16384;
    const unsigned d_ = kdst + (c & 1) * AT_BUF + half * 16384;
    glds16_dev<0>(voff, s_, d_); glds16_dev<1024>(voff, s_, d_); glds16_dev<2048>(voff, s_, d_); glds16_dev<3072>(voff, s_, d_);
  };
  G2Ld g2;
  const unsigned tdump = lds0 + AT_DUMP + lw * 256;
  const int xcd = blockIdx.x & 7, idx = blockIdx.x >> 3, gk = xcd & 1;
  const int mt = ((xcd >> 1) & 1) * 8 + (idx & 7), nt = (xcd >> 2) * 4 + (idx >> 3);
  if (LOADER) {
    const unsigned* word = flags + ((l & 1) ? FL_Q + (tt >> 3) * 8 + hg * 2 + (lw >> 1) : FL_KV) * 32;
    const unsigned need = (l & 1) ? 4u : 32u;
#pragma unroll 1
    for (unsigned spins = 0; spins < SPIN_CAP; ++spins) {
      if (__all(__hip_atomic_load((gu32*)word, RLX_AGENT) >= need)) break;
      __builtin_amdgcn_s_sleep(2);
    }
    __builtin_amdgcn_fence(__ATOMIC_ACQUIRE, "wavefront");
    const char* qs = ws + WS_Q + (size_t)(tt * 32 + hg * 8 + lw * 2) * 2048;
    const unsigned qd = lds0 + AT_Q + lw * 4096;
    glds16_dev<0>(voff, qs, qd); glds16_dev<1024>(voff, qs, qd); glds16_dev<2048>(voff, qs, qd); glds16_dev<3072>(voff, qs, qd);
    ld(0, 0); ld(0, 1); ld(1, 0); ld(1, 1);
    l2_touch(wo, tdump);
    g2.init(ws, w2, wo, lds0, lw, l);
  }
  const unsigned s_lo = (l & 14) == 0 ? 0x3f803f80u : 0u, s_hi = (l & 14) == 2 ? 0x3f803f80u : 0u;
  const bf16x8_t sel = __builtin_bit_cast(bf16x8_t, (u32x4){s_lo, s_lo, s_hi, s_hi});
  const int sidx = (l & 1) ? 0xeeee : 0x4444;
  f32x16 o = {}, sa, sb;
  f32x4 ls = {};
  u32x8 pa, pb = {};
  PIN(o);
  if (LOADER) {
    asm volatile("s_waitcnt vmcnt(9)" ::: "memory");
    if (l == 0) __hip_atomic_fetch_add((LAS unsigned*)((LAS char*)smem + AT_RDY), 1u, __ATOMIC_RELAXED, __HIP_MEMORY_SCOPE_WORKGROUP);
  }
#pragma unroll 1
  for (unsigned spins = 0; spins < 64u * SPIN_CAP; ++spins) {
    if (rfl(__hip_atomic_load((LAS unsigned*)((LAS char*)smem + AT_RDY), __ATOMIC_RELAXED, __HIP_MEMORY_SCOPE_WORKGROUP)) >= 4u) break;
    __builtin_amdgcn_s_sleep(1);
  }
  asm volatile("" ::: "memory");

  const LAS char* qp = (const LAS char*)smem + AT_Q + w * 2048 + l * 16;
  const bf16x8 q0 = lds_b128(qp), q1 = lds_b128(qp + 1024);
  const LAS char* kvp = (const LAS char*)smem + AT_BASE + l * 16;
  bf16x8 kx0, kx1, ky0, ky1, va0, va1, vb0, vb1, vc0, vc1, vd0, vd1;
  kx0 = lds_b128(kvp); kx1 = lds_b128(kvp + 1024); ky0 = lds_b128(kvp + 4096); ky1 = lds_b128(kvp + 4096 + 1024);
  va0 = lds_b128(kvp + 2048); va1 = lds_b128(kvp + 3072); vb0 = lds_b128(kvp + 4096 + 2048); vb1 = lds_b128(kvp + 4096 + 3072);
  vd0 = va0; vd1 = va1;
  f32x16 nm;
  {
    sa = MFMA32(kx0, q0, o); sa = MFMA32(kx1, q1, sa);
    const float ma = -xhalf_max(max16(sa));
#pragma unroll
    for (int r = 0; r < 16; ++r) { nm[r] = ma; sa[r] += ma; }
    PIN(nm);
  }
#define EX(S, i) S[i] = __builtin_amdgcn_exp2f(S[i])
#define CV(P, j, S, i) P[j] = cvtpk(S[i], S[(i) + 1])
#define PLO(x) __builtin_bit_cast(bf16x8, __builtin_shufflevector(x, x, 0, 1, 2, 3))
#define PHI(x) __builtin_bit_cast(bf16x8, __builtin_shufflevector(x, x, 4, 5, 6, 7))
#define RSUM(x) ls = __builtin_amdgcn_smfmac_f32_16x16x64_bf16(sel, __builtin_bit_cast(bf16x16_t, x), ls, sidx, 0, 0)
#define STEP(S, P, SN, PP, KN0, KN1, VP0, VP1, KL0, KL1, VL0, VL1, NP) do { \
    SN = MFMA32(KN0, q0, nm); SBAR(); KL0 = lds_b128(NP); EX(S, 0); EX(S, 1); EX(S, 2); EX(S, 3); PIN(S); SBAR(); \
    SN = MFMA32(KN1, q1, SN); SBAR(); KL1 = lds_b128((NP) + 1024); CV(P, 0, S, 0); CV(P, 1, S, 2); EX(S, 4); EX(S, 5); EX(S, 6); EX(S, 7); PIN(S); PIN(P); SBAR(); \
    o = MFMA32(VP0, PLO(PP), o); SBAR(); VL0 = lds_b128((NP) + 2048); CV(P, 2, S, 4); EX(S, 8); CV(P, 3, S, 6); EX(S, 9); EX(S, 10); EX(S, 11); PIN(S); PIN(P); SBAR(); \
    o = MFMA32(VP1, PHI(PP), o); SBAR(); VL1 = lds_b128((NP) + 3072); CV(P, 4, S, 8); CV(P, 5, S, 10); EX(S, 12); EX(S, 13); EX(S, 14); PIN(S); PIN(P); SBAR(); \
    RSUM(PP); SBAR(); EX(S, 15); CV(P, 6, S, 12); CV(P, 7, S, 14); PIN(P); SBAR(); \
  } while (0)
#define XP(st, j) do { if (LOADER && (unsigned)((st) + 1) < 4u) g2.template ffn_piece<j>((st) + 1); SBAR(); } while (0)
#define PRIO(n) do { __builtin_amdgcn_s_setprio(n); SBAR(); } while (0)
#pragma unroll 1
  for (int c = 0; c < AT_NCH; ++c) {
    const LAS char* cb = kvp + (c & 1) * AT_BUF;
    const LAS char* nb = kvp + ((c + 1) & 1) * AT_BUF;
    PRIO(2);
    STEP(sa, pa, sb, pb, ky0, ky1, vd0, vd1, kx0, kx1, vc0, vc1, cb + 2 * 4096); XP(c - 5, 2);
    STEP(sb, pb, sa, pa, kx0, kx1, va0, va1, ky0, ky1, vd0, vd1, cb + 3 * 4096); XP(c - 5, 3);
    PRIO(1);
    STEP(sa, pa, sb, pb, ky0, ky1, vb0, vb1, kx0, kx1, va0, va1, cb + 4 * 4096); XP(c - 5, 4);
    STEP(sb, pb, sa, pa, kx0, kx1, vc0, vc1, ky0, ky1, vb0, vb1, cb + 5 * 4096); XP(c - 5, 5);
    STEP(sa, pa, sb, pb, ky0, ky1, vd0, vd1, kx0, kx1, vc0, vc1, cb + 6 * 4096); XP(c - 5, 6);
    PRIO(0);
    STEP(sb, pb, sa, pa, kx0, kx1, va0, va1, ky0, ky1, vd0, vd1, cb + 7 * 4096); XP(c - 5, 7);
    if (c < AT_NCH - 1) {
      if (LOADER) {
        if (c < 4) asm volatile("s_waitcnt vmcnt(1) lgkmcnt(0)\n\ts_barrier" ::: "memory");
        else asm volatile("s_waitcnt vmcnt(8) lgkmcnt(0)\n\ts_barrier" ::: "memory");
        if (c == 2)
          peek_dev(flags + (FL_H + (mt >> 1)) * 32, lds0 + G1_CS + lw * 256);
        if (c + 2 < AT_NCH) { ld(c + 2, 0); ld(c + 2, 1); }
        if (c < 2) {
          const int j = c * 4 + lw;
          l2_touch(j < 7 ? (const char*)(w2 + (size_t)(gk * 1792 + j * 256 + (mt & 7) * 32 + (l >> 1)) * DM + nt * 128 + (l & 1) * 64)
                         : (const char*)(wo + (size_t)((mt & 7) * 128 + l) * DM + nt * 128 + gk * 64), tdump);
        } else if (c == 2) {
          l2_touch(lw == 0 ? (const char*)(wo + (size_t)((mt & 7) * 128 + 64 + l) * DM + nt * 128 + gk * 64) : (const char*)wo, tdump);
        } else if (c == 3) {
          if (rfl(*(const LAS unsigned*)((const LAS char*)smem + G1_CS + lw * 256)) < 92u) wait_ge(flags + (FL_H + (mt >> 1)) * 32, 92u);
        }
      } else {
        asm volatile("s_waitcnt lgkmcnt(0)\n\ts_barrier" ::: "memory");
        if (c == 0 && late_pub) publish(late_pub, l);
      }
      PRIO(3);
    }
    SBAR();
    STEP(sa, pa, sb, pb, ky0, ky1, vb0, vb1, kx0, kx1, va0, va1, nb); XP(c - 4, 0);
    STEP(sb, pb, sa, pa, kx0, kx1, vc0, vc1, ky0, ky1, vb0, vb1, nb + 4096); XP(c - 4, 1);
  }
  o = MFMA32(vd0, PLO(pb), o); o = MFMA32(vd1, PHI(pb), o); RSUM(pb);
#undef EX
#undef CV
#undef PLO
#undef PHI
#undef RSUM
#undef STEP
#undef PRIO
#undef XP
  __builtin_amdgcn_s_setprio(0);
  attn_finish(ws, o, ls, q0, q1, l, ws + WS_O + (size_t)(head >> 1) * KT + (size_t)tt * 4096 + (head & 1) * 2048 + l * 16);
}

DI void g2_load(const u16* w2, const u16* wo, char* __restrict__ ws, unsigned lds0, const int lw, const int l) {
  const int xcd = blockIdx.x & 7, idx = blockIdx.x >> 3;
  const int mt = ((xcd >> 1) & 1) * 8 + (idx & 7);
  unsigned* flags = (unsigned*)(ws + WS_FLAG);
  G2Ld g; g.init(ws, w2, wo, lds0, lw, l);
  asm volatile("s_waitcnt vmcnt(10) lgkmcnt(0)\n\ts_barrier" ::: "memory");
  g.ffn(4); g.ffn(5);
  const LAS unsigned* peek = (const LAS unsigned*)(size_t)(lds0 + G2_PART);
#pragma unroll 1
  for (int i = 2; i < 24; ++i) {
    asm volatile("s_waitcnt vmcnt(16)\n\ts_barrier" ::: "memory");
    if (i == 6 && l == 0) __hip_atomic_fetch_add((gu32*)flags + (FL_O + (blockIdx.x >> 4)) * 32, 1u, RLX_AGENT);
    if (i == 20 && lw == 0) peek_dev(flags + (FL_O + mt) * 32, lds0 + G2_PART);
    g.ffn(i + 4);
  }
  asm volatile("s_waitcnt vmcnt(16)\n\ts_barrier" ::: "memory");
  if (rfl(peek[0]) < 128u) wait_ge(flags + (FL_O + mt) * 32, 128u);
  g.wo(28);
  peek = (const LAS unsigned*)(size_t)(lds0 + G2_BOFF + 2048);
#pragma unroll 1
  for (int i = 25; i < 37; ++i) {
    asm volatile("s_waitcnt vmcnt(12)\n\ts_barrier" ::: "memory");
    if (i == 35 && lw == 0) peek_dev(flags + (FL_P + blockIdx.x) * 32, lds0 + G2_BOFF + 2048);
    g.wo(i + 4);
  }
  auto part = [&]() {
    const char* src = ws + WS_PART + (size_t)(blockIdx.x ^ 1) * 32768 + lw * 8192;
    const unsigned d = lds0 + G2_PART + lw * 8192;
    glds16_dev<0>(g.voff, src, d); glds16_dev<1024>(g.voff, src, d); glds16_dev<2048>(g.voff, src, d); glds16_dev<3072>(g.voff, src, d);
    glds16_dev<0>(g.voff, src + 4096, d + 4096); glds16_dev<1024>(g.voff, src + 4096, d + 4096); glds16_dev<2048>(g.voff, src + 4096, d + 4096); glds16_dev<3072>(g.voff, src + 4096, d + 4096);
  };
  asm volatile("s_waitcnt vmcnt(12)\n\ts_barrier" ::: "memory");
  const bool early = rfl(peek[0]) >= 4u;
  g.wo(41);
  if (early) part();
#pragma unroll 1
  for (int i = 38; i < 41; ++i) {
    if (early) asm volatile("s_waitcnt vmcnt(20)\n\ts_barrier" ::: "memory");
    else asm volatile("s_waitcnt vmcnt(12)\n\ts_barrier" ::: "memory");
    if (i < 40) g.wo(i + 4);
  }
  asm volatile("s_waitcnt vmcnt(6)\n\ts_barrier" ::: "memory");
  if (!early) { wait_ge(flags + (FL_P + blockIdx.x) * 32, 4u); part(); }
  asm volatile("s_waitcnt vmcnt(0)\n\ts_barrier" ::: "memory");
}

DI void g2_compute(const u16* __restrict__ x, const u16* __restrict__ b2, char* __restrict__ ws, u16* __restrict__ out,
                   char* smem, const int w, const int l) {
  const int xcd = blockIdx.x & 7, idx = blockIdx.x >> 3;
  const int kh = xcd & 1, mt = ((xcd >> 1) & 1) * 8 + (idx & 7), nt = (xcd >> 2) * 4 + (idx >> 3);
  unsigned* flags = (unsigned*)(ws + WS_FLAG);
  const int h = l >> 5, wm = w & 1, wn = w >> 1;
  f32x16 accg[2] = {}, accp[2] = {}, accs[2] = {};
  const int col0 = nt * 128 + kh * 64 + wn * 32;
  const int row0 = mt * 128 + wm * 64 + (l & 31);
  unsigned stw[2];
  u32x4 xv[4];
  u32x2 bv[4];
#pragma unroll
  for (int m = 0; m < 2; ++m) stw[m] = __hip_atomic_load((gu32*)(ws + WS_STAT) + row0 + m * 32, RLX_AGENT);
#pragma unroll
  for (int k = 0; k < 4; ++k) xv[k] = *(const u32x4*)(x + (size_t)(row0 + (k >> 1) * 32) * DM + col0 + 16 * (k & 1) + 8 * h);
#pragma unroll
  for (int k = 0; k < 4; ++k) bv[k] = *(const u32x2*)(b2 + col0 + 8 * k + 4 * h);
  const LAS char* lbase = (const LAS char*)smem + l * 16;
  const int btr = (4 * h + ((l & 15) >> 2)) * 64 + ((l >> 4) & 1) * 32 + (l & 3) * 8 - l * 16;
#define LDB(p_) cat8(lds_tr(p_), lds_tr((p_) + 512))
  const int ao = wm * 8192;
  const int bG = G2_BOFF + (kh * 2 + wn) * 1024 + btr, bP = G2_BOFF + ((kh ^ 1) * 2 + wn) * 1024 + btr;
  const int bW = G2_BOFF + wn * 1024 + btr;
  bf16x8 fa0[2], fb0[2], fa1[2], fb1[2];
#define G2_RD(FA, FB, base, ks) do { \
    FA[0] = lds_b128((base) + ao + (ks) * 1024); FB[0] = LDB((base) + bG + (ks) * 4096); \
    FA[1] = lds_b128((base) + ao + 4096 + (ks) * 1024); FB[1] = LDB((base) + bP + (ks) * 4096); \
  } while (0)
#define G2_MM(FA, FB) do { \
    accg[0] = MFMA32(FB[0], FA[0], accg[0]); accg[1] = MFMA32(FB[0], FA[1], accg[1]); \
    accp[0] = MFMA32(FB[1], FA[0], accp[0]); accp[1] = MFMA32(FB[1], FA[1], accp[1]); \
  } while (0)
#define G2_SCHED(NR) do { \
    __builtin_amdgcn_sched_group_barrier(0x008, 1, 0); __builtin_amdgcn_sched_group_barrier(0x100, NR, 0); \
    __builtin_amdgcn_sched_group_barrier(0x008, 1, 0); __builtin_amdgcn_sched_group_barrier(0x100, NR, 0); \
    __builtin_amdgcn_sched_group_barrier(0x008, 2, 0); SBAR(); \
  } while (0)
#define G2_BAR() do { asm volatile("s_waitcnt lgkmcnt(0)\n\ts_barrier" ::: "memory"); SBAR(); } while (0)

  SBAR();
  G2_RD(fa0, fb0, lbase, 0); SBAR();
  G2_RD(fa1, fb1, lbase, 1); G2_MM(fa0, fb0); G2_SCHED(3);
  G2_RD(fa0, fb0, lbase, 2); G2_MM(fa1, fb1); G2_SCHED(3);
  G2_RD(fa1, fb1, lbase, 3); G2_MM(fa0, fb0); G2_SCHED(3);
#pragma unroll 1
  for (int i = 0; i < 27; ++i) {
    const LAS char* nb = lbase + ((i + 1) & 3) * G2_SLOT;
    if (i > 0) G2_BAR();
    G2_RD(fa0, fb0, nb, 0); G2_MM(fa1, fb1); G2_SCHED(3);
    G2_RD(fa1, fb1, nb, 1); G2_MM(fa0, fb0); G2_SCHED(3);
    G2_RD(fa0, fb0, nb, 2); G2_MM(fa1, fb1); G2_SCHED(3);
    G2_RD(fa1, fb1, nb, 3); G2_MM(fa0, fb0); G2_SCHED(3);
    if (i == 3) { publish(flags + (FL_O + (blockIdx.x >> 4)) * 32, l); SBAR(); }
  }
  bf16x8 ga0[4], gb0[2], ga1[4], gb1[2];
#define W_RD(GA, GB, base, ks) do { \
    GA[0] = lds_b128((base) + ao + (ks) * 1024); GB[0] = LDB((base) + bW + (ks) * 4096); GA[1] = lds_b128((base) + ao + 4096 + (ks) * 1024); \
    GA[2] = lds_b128((base) + ao + (ks) * 1024 + 1024); GB[1] = LDB((base) + bW + (ks) * 4096 + 4096); GA[3] = lds_b128((base) + ao + 4096 + (ks) * 1024 + 1024); \
  } while (0)
#define W_MM(GA, GB) do { \
    accs[0] = MFMA32(GB[0], GA[0], accs[0]); accs[1] = MFMA32(GB[0], GA[1], accs[1]); \
    accs[0] = MFMA32(GB[1], GA[2], accs[0]); accs[1] = MFMA32(GB[1], GA[3], accs[1]); \
  } while (0)
#define W_STEP(i) do { const LAS char* nb_ = lbase + (((i) + 1) & 3) * G2_SLOT; G2_BAR(); \
    W_RD(ga0, gb0, nb_, 0); W_MM(ga1, gb1); G2_SCHED(4); \
    W_RD(ga1, gb1, nb_, 2); W_MM(ga0, gb0); G2_SCHED(4); } while (0)
  char* pdst = ws + WS_PART + (size_t)blockIdx.x * 32768 + w * 8192 + l * 16;
#define G2_SEND(q) do { asm volatile("s_nop 7" ::: "memory"); \
    _Pragma("unroll") for (int i_ = 2 * ((q) & 1); i_ < 2 * ((q) & 1) + 2; ++i_) \
      store16_dev(pdst + ((q) >> 1) * 4096 + i_ * 1024, (u32x4){__float_as_uint(accp[(q) >> 1][4 * i_]), __float_as_uint(accp[(q) >> 1][4 * i_ + 1]), \
                                                               __float_as_uint(accp[(q) >> 1][4 * i_ + 2]), __float_as_uint(accp[(q) >> 1][4 * i_ + 3])}); SBAR(); } while (0)
  G2_BAR();
  W_RD(ga0, gb0, lbase, 0); G2_MM(fa1, fb1); G2_SCHED(4);
  W_RD(ga1, gb1, lbase, 2); W_MM(ga0, gb0); G2_SCHED(4);
  G2_SEND(0);
  W_STEP(28); G2_SEND(1);
  W_STEP(29); G2_SEND(2);
  W_STEP(30); G2_SEND(3);
#pragma unroll 1
  for (int i = 31; i < 42; ++i) {
    W_STEP(i);
    if (i == 33) { publish(flags + (FL_P + (blockIdx.x ^ 1)) * 32, l); SBAR(); }
  }
  W_STEP(42);
  W_MM(ga1, gb1);
  {
    const LAS char* xp = (const LAS char*)smem + G2_PART + w * 8192 + l * 16;
#pragma unroll
    for (int m = 0; m < 2; ++m) {
      const float den = bf_hi(stw[m]), rd2 = 1.0f / (den * den);
#pragma unroll
      for (int k = 0; k < 2; ++k) {
        u32x4 xx = xv[m * 2 + k];
        { auto r0 = __builtin_amdgcn_permlane32_swap(xx[0], xx[2], false, false); xx[0] = r0[0]; xx[2] = r0[1];
          auto r1 = __builtin_amdgcn_permlane32_swap(xx[1], xx[3], false, false); xx[1] = r1[0]; xx[3] = r1[1]; }
        unsigned pk[4];
#pragma unroll
        for (int ii = 0; ii < 2; ++ii) {
          const int i = 2 * k + ii;
          const f32x4 tp = *(const LAS f32x4*)(xp + m * 4096 + i * 1024);
          float r[4];
#pragma unroll
          for (int j = 0; j < 4; ++j) {
            const unsigned xw_ = xx[2 * ii + (j >> 1)], bw_ = bv[i][j >> 1];
            const float z = (accg[m][4 * i + j] + tp[j]) * rd2 + ((j & 1) ? bf_hi(bw_) : bf_lo(bw_));
            const float sg = z * __builtin_amdgcn_rcpf(1.0f + __builtin_amdgcn_exp2f(-1.4426950408889634f * z));
            r[j] = ((j & 1) ? bf_hi(xw_) : bf_lo(xw_)) + accs[m][4 * i + j] * sg;
          }
          pk[2 * ii] = cvtpk(r[0], r[1]); pk[2 * ii + 1] = cvtpk(r[2], r[3]);
        }
        auto r0 = __builtin_amdgcn_permlane32_swap(pk[0], pk[2], false, false);
        auto r1 = __builtin_amdgcn_permlane32_swap(pk[1], pk[3], false, false);
        *(u32x4*)(out + (size_t)(row0 + m * 32) * DM + col0 + 16 * k + 8 * h) = (u32x4){r0[0], r1[0], r0[1], r1[1]};
      }
    }
  }
#undef G2_RD
#undef G2_MM
#undef G2_SCHED
#undef G2_BAR
#undef G2_SEND
#undef W_RD
#undef W_MM
#undef W_STEP
#undef LDB
}

__global__ __launch_bounds__(512, 2) void main_kernel(const u16* __restrict__ x, const u16* __restrict__ wq, const u16* __restrict__ wk,
                                                      const u16* __restrict__ wv, const u16* __restrict__ wo, const u16* __restrict__ w1,
                                                      const u16* __restrict__ b1, const u16* __restrict__ w2, const u16* __restrict__ b2,
                                                      char* __restrict__ ws, u16* __restrict__ out) {
  extern __shared__ __attribute__((aligned(16))) char smem[];
  const int l = threadIdx.x & 63, w = rfl(threadIdx.x >> 6);
  const unsigned lds0 = (unsigned)(size_t)(LAS char*)smem;
  const int xcd = blockIdx.x & 7, idx = blockIdx.x >> 3;
  const int mtile = (xcd & 1) * 4 + (idx & 3), cg = (idx >> 2) * 4 + (xcd >> 1);
  unsigned* flags = (unsigned*)(ws + WS_FLAG);
  if (w == 3) {
    unsigned long long pc; unsigned len;
    asm volatile("s_getpc_b64 %0\n.Lmk_code_begin:\n\ts_mov_b32 %1, .Lmk_code_end-.Lmk_code_begin" : "=s"(pc), "=s"(len));
    unsigned off = (unsigned)(idx * 16 + (l & 15)) * 128u;
    off = off < len ? off : 0u;
    l2_touch((const char*)pc + off, lds0 + G1_HS + 3072);
  }
  if (w >= 4) {
    if (cg > 8) g1_load<G1_FFN>(ws, x, w1, nullptr, cg, mtile, smem, w - 4, l);
    else if (cg < 8) g1_load<G1_Q>(ws, x, wq, nullptr, cg, mtile, smem, w - 4, l);
    else g1_load<G1_KV>(ws, x, wk, wv, cg, mtile, smem, w - 4, l);
    attn_phase<true>(ws, w2, wo, smem, w, l, nullptr);
    g2_load(w2, wo, ws, lds0, w - 4, l);
    asm volatile(".Lmk_code_end:");
  } else {
    unsigned* late = nullptr;
    if (cg > 8) { g1_compute<G1_FFN>(ws, x, b1, cg, mtile, smem, w, l); late = flags + (FL_H + mtile) * 32; }
    else if (cg < 8) { g1_compute<G1_Q>(ws, x, b1, cg, mtile, smem, w, l); publish(flags + (FL_Q + mtile * 8 + cg) * 32, l); }
    else { g1_compute<G1_KV>(ws, x, b1, cg, mtile, smem, w, l); publish(flags + FL_KV * 32, l); }
    attn_phase<false>(ws, w2, wo, smem, w, l, late);
    g2_compute(x, b2, ws, out, smem, w, l);
  }
}

extern "C" void kernel_launch(void* const* d_in, const int* in_sizes, int n_in, void* d_out, int out_size, void* d_ws,
                              size_t ws_size, hipStream_t stream) {
  const u16* x = (const u16*)d_in[0];
  const u16* wq = (const u16*)d_in[1];
  const u16* wk = (const u16*)d_in[2];
  const u16* wv = (const u16*)d_in[3];
  const u16* wo = (const u16*)d_in[4];
  const u16* w1 = (const u16*)d_in[6];
  const u16* b1 = (const u16*)d_in[7];
  const u16* w2 = (const u16*)d_in[8];
  const u16* b2 = (const u16*)d_in[9];
  char* ws = (char*)d_ws;
  (void)hipFuncSetAttribute((const void*)main_kernel, hipFuncAttributeMaxDynamicSharedMemorySize, MAIN_LDS);
  main_kernel<<<256, 512, MAIN_LDS, stream>>>(x, wq, wk, wv, wo, w1, b1, w2, b2, ws, (u16*)d_out);
}
```

```cpp
#include <hip/hip_runtime.h>
#include <hip/hip_bf16.h>

typedef short bf16x8 __attribute__((ext_vector_type(8)));
typedef short s16x4 __attribute__((ext_vector_type(4)));
typedef float f32x16 __attribute__((ext_vector_type(16)));
typedef float f32x4 __attribute__((ext_vector_type(4)));
typedef float f32x2 __attribute__((ext_vector_type(2)));
typedef __bf16 bf16x2_t __attribute__((ext_vector_type(2)));
typedef unsigned u32x4 __attribute__((ext_vector_type(4)));
typedef unsigned u32x2 __attribute__((ext_vector_type(2)));
typedef unsigned u32x8 __attribute__((ext_vector_type(8)));
typedef __bf16 bf16x8_t __attribute__((ext_vector_type(8)));
typedef __bf16 bf16x16_t __attribute__((ext_vector_type(16)));
typedef unsigned short u16;
#define LAS __attribute__((address_space(3)))
#define DI __device__ __forceinline__
#define SBAR() __builtin_amdgcn_sched_barrier(0)
#define PIN(x) asm volatile("" : "+v"(x))
#define MFMA32(a, b, c) __builtin_amdgcn_mfma_f32_32x32x16_bf16((a), (b), (c), 0, 0, 0)

constexpr int DM = 1024, FF = 3584;
constexpr size_t KT = 262144;
constexpr size_t WS_STAT = 0;
constexpr size_t WS_Q = 4u << 20;
constexpr size_t WS_KV = 8u << 20;
constexpr size_t WS_O = 9u << 20;
constexpr size_t WS_H = 13u << 20;
constexpr size_t WS_PART = 28u << 20;
constexpr size_t WS_FLAG = 37u << 20;
constexpr int FL_P = 0, FL_O = 256, FL_H = 272, FL_Q = 280, FL_KV = 344, FL_S = 345, FL_N = 353;
constexpr size_t WS_HSA = WS_FLAG + 65536, WS_HSB = WS_HSA + 4096, WS_HSC = WS_HSB + 4096;
constexpr int COORD_BLOCK = 64;

#define STAMP(kid, idx) do {} while (0)
DI unsigned rfl(unsigned x) { return __builtin_amdgcn_readfirstlane(x); }
DI unsigned cvtpk(float lo, float hi) {
  f32x2 v = {lo, hi};
  bf16x2_t b = __builtin_convertvector(v, bf16x2_t);
  return __builtin_bit_cast(unsigned, b);
}
DI float fadd_s(float a, float b) { float r; asm("v_add_f32_e32 %0, %1, %2" : "=v"(r) : "v"(a), "v"(b)); return r; }
DI float bf_lo(unsigned u) { return __uint_as_float(u << 16); }
DI float bf_hi(unsigned u) { return __uint_as_float(u & 0xffff0000u); }
DI float bf_round(float x) { return bf_lo(cvtpk(x, x)); }

template <int IMM>
DI void glds16(unsigned voff, const void* sbase, unsigned ldsaddr) {
  asm volatile("s_mov_b32 m0, %2\n\ts_nop 0\n\tglobal_load_lds_dwordx4 %0, %1 offset:%3"
               :: "v"(voff), "s"(sbase), "s"(ldsaddr), "i"(IMM) : "memory");
}
template <int IMM>
DI void glds16_dev(unsigned voff, const void* sbase, unsigned ldsaddr) {
  asm volatile("s_mov_b32 m0, %2\n\ts_nop 0\n\tglobal_load_lds_dwordx4 %0, %1 offset:%3 sc1"
               :: "v"(voff), "s"(sbase), "s"(ldsaddr), "i"(IMM) : "memory");
}
DI void peek_dev(const void* word, unsigned ldsaddr) {
  asm volatile("s_mov_b32 m0, %2\n\ts_nop 0\n\tglobal_load_lds_dword %0, %1 sc1" :: "v"(0u), "s"(word), "s"(ldsaddr) : "memory");
}
DI void store16_dev(void* p, u32x4 v) {
  asm volatile("global_store_dwordx4 %0, %1, off sc1\n\ts_nop 1" :: "v"(p), "v"(v) : "memory");
}
DI void l2_touch(const void* p, unsigned ldsaddr) {
  asm volatile("s_mov_b32 m0, %1\n\ts_nop 0\n\tglobal_load_lds_dword %0, off" :: "v"(p), "s"(ldsaddr) : "memory");
}
constexpr unsigned SPIN_CAP = 1u << 20;
typedef __attribute__((address_space(1))) unsigned gu32;
#define RLX_AGENT __ATOMIC_RELAXED, __HIP_MEMORY_SCOPE_AGENT
DI void wait_ge(const unsigned* word, unsigned want) {
#pragma unroll 1
  for (unsigned spins = 0; spins < SPIN_CAP; ++spins) {
    if (rfl(__hip_atomic_load((gu32*)word, RLX_AGENT)) >= want) break;
    __builtin_amdgcn_s_sleep(2);
  }
  __builtin_amdgcn_fence(__ATOMIC_ACQUIRE, "wavefront");
}
DI void publish(unsigned* word, int l) {
  asm volatile("s_waitcnt vmcnt(0)" ::: "memory");
  if (l == 0) __hip_atomic_fetch_add((gu32*)word, 1u, RLX_AGENT);
}
DI bf16x8 load16_dev(const char* p) {
  typedef __attribute__((address_space(1))) unsigned long long gu64;
  const unsigned long long a = __hip_atomic_load((gu64*)p, RLX_AGENT), b = __hip_atomic_load((gu64*)p + 1, RLX_AGENT);
  return __builtin_bit_cast(bf16x8, (u32x4){(unsigned)a, (unsigned)(a >> 32), (unsigned)b, (unsigned)(b >> 32)});
}
DI float relu_s(float a) { return __builtin_amdgcn_fmed3f(a, 0.0f, 3.4028234663852886e38f); }
DI bf16x8 lds_b128(const LAS char* p) { return *(const LAS bf16x8*)p; }
DI s16x4 lds_tr(const LAS char* p) {
  return __builtin_amdgcn_ds_read_tr16_b64_v4i16((LAS s16x4*)p);
}
DI bf16x8 cat8(s16x4 lo, s16x4 hi) { return __builtin_shufflevector(lo, hi, 0, 1, 2, 3, 4, 5, 6, 7); }

DI float half_sum(float v) {
  v += __uint_as_float(__builtin_amdgcn_update_dpp(0u, __float_as_uint(v), 0xB1, 0xf, 0xf, true));
  v += __uint_as_float(__builtin_amdgcn_update_dpp(0u, __float_as_uint(v), 0x4E, 0xf, 0xf, true));
  v += __uint_as_float(__builtin_amdgcn_update_dpp(0u, __float_as_uint(v), 0x141, 0xf, 0xf, true));
  v += __uint_as_float(__builtin_amdgcn_update_dpp(0u, __float_as_uint(v), 0x140, 0xf, 0xf, true));
  auto r = __builtin_amdgcn_permlane16_swap(__float_as_uint(v), __float_as_uint(v), false, false);
  return __uint_as_float(r[0]) + __uint_as_float(r[1]);
}
DI unsigned row_stats(const u32x4 (&v)[4]) {
  f32x2 e[16], s2 = {0.f, 0.f};
#pragma unroll
  for (int i = 0; i < 16; ++i) { e[i] = (f32x2){bf_lo(v[i >> 2][i & 3]), bf_hi(v[i >> 2][i & 3])}; s2 += e[i]; }
  const float mean = half_sum(s2[0] + s2[1]) * (1.0f / DM);
  f32x2 q2 = {0.f, 0.f};
#pragma unroll
  for (int i = 0; i < 16; ++i) { const f32x2 d = e[i] - mean; q2 += d * d; }
  const float q = half_sum(q2[0] + q2[1]);
  const float den = __builtin_sqrtf(bf_round(bf_round(q * (1.0f / DM)) + 1e-5f));
  return cvtpk(-mean, den);
}

constexpr int G1_STAGE = 53248, G1_BOFF = 32768;
enum { G1_FFN = 5, G1_Q = 4, G1_KV = 2 };

constexpr int G1_ST = 87040;
constexpr int AT_RDY = 89088;
constexpr int G1_HS = 159744;
constexpr int G1_CS = 81920;
constexpr int ESTART = 3, PSTART = 9;
DI float pair_sum(unsigned v) { return fadd_s(bf_lo(v), bf_hi(v)); }

template <int NP>
DI void g1_load(char* ws, const u16* x, const u16* wsrc, const u16* wv, int cg, int mtile, char* smem, int lw, int l) {
  constexpr int LD = (NP == G1_FFN) ? FF : (NP == G1_Q) ? DM : 32;
  const unsigned lds0 = (unsigned)(size_t)(LAS char*)smem;
  const char* abase = (const char*)(x + (size_t)(mtile * 256 + lw * 64) * DM);
  const unsigned ave = (unsigned)((l >> 3) * 2048 + (((l & 7) ^ (l >> 4)) << 4)), avo = ave ^ 64u;
  const unsigned bvoff = (unsigned)((lw * 16 + (l >> 2)) * LD * 2 + (l & 3) * 16);
  const int krot = NP == G1_Q ? (mtile & 3) * 4 : 0;
  const char* bbase[NP];
#pragma unroll
  for (int p = 0; p < NP; ++p) {
    if (NP == G1_KV) bbase[p] = (const char*)(p ? wv : wsrc);
    else if (NP == G1_Q) bbase[p] = (const char*)(wsrc + (cg * 4 + p) * 32);
    else { const int cb = (cg - 9) * 5 + p; bbase[p] = (const char*)(wsrc + (cb > 111 ? 111 : cb) * 32); }
  }
  float cs[NP];
#pragma unroll
  for (int p = 0; p < NP; ++p) cs[p] = 0.f;
  const LAS char* cb0 = (const LAS char*)smem + G1_BOFF + lw * 1024 + (8 * (l >> 5) + ((l & 15) >> 2)) * 64 + ((l >> 4) & 1) * 32 + (l & 3) * 8;
  u32x2 t0[NP], t1[NP];
  auto cs_read = [&](int slot) {
#pragma unroll
    for (int p = 0; p < NP; ++p) {
      t0[p] = __builtin_bit_cast(u32x2, lds_tr(cb0 + slot * G1_STAGE + p * 4096));
      t1[p] = __builtin_bit_cast(u32x2, lds_tr(cb0 + slot * G1_STAGE + p * 4096 + 256));
    }
    SBAR();
  };
  auto cs_add = [&](int p) {
    cs[p] = fadd_s(cs[p], fadd_s(fadd_s(pair_sum(t0[p][0]), pair_sum(t0[p][1])), fadd_s(pair_sum(t1[p][0]), pair_sum(t1[p][1]))));
    PIN(cs[p]); SBAR();
  };
  auto issue = [&](int st, int slot, bool sum) {
    const int kt = (st + krot) & 15;
    const unsigned sa = lds0 + slot * G1_STAGE + lw * 8192;
    const char* a0 = abase + kt * 128;
    const unsigned sb = lds0 + slot * G1_STAGE + G1_BOFF + lw * 1024;
#pragma unroll
    for (int p = 0; p < NP; ++p) glds16<0>(bvoff, bbase[p] + (size_t)kt * (64 * LD * 2), sb + p * 4096);
#pragma unroll
    for (int i = 0; i < 8; ++i) {
      if (sum && i < NP) { SBAR(); cs_add(i); }
      glds16<0>((i & 1) ? avo : ave, a0 + i * 16384, sa + i * 1024);
    }
  };
  auto colsum = [&](int slot) {
    cs_read(slot);
#pragma unroll
    for (int p = 0; p < NP; ++p) cs_add(p);
  };
  unsigned* flags = (unsigned*)(ws + WS_FLAG);
  const bool coord = (NP == G1_KV) && blockIdx.x == COORD_BLOCK && lw == 1;
  if (coord) {
#pragma unroll
    for (int i = 0; i < 6; ++i) if (i * 64 + l < FL_N) __hip_atomic_store((gu32*)flags + (i * 64 + l) * 32, 0u, RLX_AGENT);
    store16_dev(ws + WS_HSC + l * 16, (u32x4){0u, 0u, 0u, 0u});
    asm volatile("s_waitcnt vmcnt(0)" ::: "memory");
  }
  issue(0, 0, false); issue(1, 1, false); issue(2, 2, false);
  asm volatile("s_waitcnt vmcnt(%0)\n\ts_barrier" :: "n"(2 * (8 + NP)) : "memory");
  __builtin_amdgcn_s_setprio(2);
  int state = (lw == 0 || coord) ? 0 : 2;
  bool pend = false, first = true;
  u32x4 last = {0u, 0u, 0u, 0u};
  const unsigned found = rfl(*(const LAS unsigned*)((const LAS char*)smem + G1_HS));
  if (lw == 0 && l < 9) {
    const unsigned v = *(const LAS unsigned*)((const LAS char*)smem + G1_HS + 16 + (l & 7) * 4);
    __hip_atomic_store(l < 8 ? (gu32*)(ws + WS_STAT) + mtile * 256 + cg * 8 + l : (gu32*)(ws + WS_HSA) + blockIdx.x, l < 8 ? v : found + 1u, RLX_AGENT);
  }
  colsum(0);
  auto poll_issue = [&]() {
    if (NP == G1_KV && coord) {
      glds16_dev<0>(l * 16, ws + WS_HSA, lds0 + G1_HS + 1024); glds16_dev<0>(l * 16, ws + WS_HSC, lds0 + G1_HS + 2048);
    } else {
      const char* a_ = state == 0 ? ws + WS_HSB + blockIdx.x * 4 : (const char*)(flags + (FL_S + mtile) * 32);
      peek_dev(a_, lds0 + G1_HS + 256);
    }
  };
  auto poll_check = [&]() {
    if (NP == G1_KV && coord) {
      const u32x4 pa = *(const LAS u32x4*)((const LAS char*)smem + G1_HS + 1024 + l * 16), pc = *(const LAS u32x4*)((const LAS char*)smem + G1_HS + 2048 + l * 16);
      if (first || pa[0] != last[0] || pa[1] != last[1] || pa[2] != last[2] || pa[3] != last[3]) store16_dev(ws + WS_HSB + l * 16, pa);
      last = pa; first = false;
      if (__all((pc[0] & pc[1] & pc[2] & pc[3]) == 1u)) state = 2;
    } else {
      const unsigned v = rfl(*(const LAS unsigned*)((const LAS char*)smem + G1_HS + 256));
      if (state == 0) {
        if (v != found) {
          if (l == 0) {
            __hip_atomic_store((gu32*)(ws + WS_HSC) + blockIdx.x, 1u, RLX_AGENT);
            __hip_atomic_fetch_add((gu32*)flags + (FL_S + mtile) * 32, 1u, RLX_AGENT);
          }
          state = 1;
        }
      } else if (v >= 32u) state = 2;
    }
    state = rfl(state);
  };
  int slot = 0;
#pragma unroll 1
  for (int i = 0; i < 13; ++i) {
    asm volatile("s_waitcnt vmcnt(%0)\n\ts_barrier" :: "n"(8 + NP) : "memory");
    if (state < 2 && (i >= ESTART || coord)) {
      if (pend) poll_check();
      pend = state == 0 || (state == 1 && i >= PSTART);
      if (pend) poll_issue();
    }
    const int nslot = (slot == 2) ? 0 : slot + 1;
    cs_read(nslot);
    issue(i + 3, slot, true);
    slot = nslot;
  }
#pragma unroll 1
  for (unsigned spins = 0; state < 2 && spins < SPIN_CAP; ++spins) {
    if (!pend) poll_issue();
    asm volatile("s_waitcnt vmcnt(0)" ::: "memory");
    poll_check();
    pend = false;
    __builtin_amdgcn_s_sleep(1);
  }
  asm volatile("s_waitcnt vmcnt(%0)\n\ts_barrier" :: "n"(8 + NP) : "memory");
  if (lw == 0) { glds16_dev<0>(l * 16, ws + WS_STAT + mtile * 1024, lds0 + G1_ST); *(LAS unsigned*)((LAS char*)smem + AT_RDY) = 0u; }
  colsum(2);
  asm volatile("s_waitcnt vmcnt(0)" ::: "memory");
  colsum(0);
#pragma unroll
  for (int p = 0; p < NP; ++p) *(LAS float*)((LAS char*)smem + G1_CS + p * 1024 + (l & 31) * 32 + (lw * 2 + (l >> 5)) * 4) = cs[p];
  asm volatile("s_waitcnt lgkmcnt(0)\n\ts_barrier" ::: "memory");
}

template <int NP>
DI void g1_compute(char* __restrict__ ws, const u16* __restrict__ x, const u16* __restrict__ b1, int cg, int mtile, char* smem, int w, int l) {
  STAMP(1, 0);
  const int h = l >> 5;
  unsigned st[2], bias[NP];
  {
    const int row = mtile * 256 + cg * 8 + w * 2 + h;
    u32x4 v[4];
#pragma unroll
    for (int i = 0; i < 4; ++i) v[i] = *(const u32x4*)(x + (size_t)row * DM + (i * 32 + (l & 31)) * 8);
    const unsigned found = __hip_atomic_load((gu32*)(ws + WS_HSB) + blockIdx.x, RLX_AGENT);
#pragma unroll
    for (int p = 0; p < NP; ++p) {
      int cb = (cg - 9) * 5 + p; cb = cb > 111 ? 111 : cb;
      bias[p] = (NP == G1_FFN) ? (unsigned)b1[cb * 32 + (l & 31)] : 0u;
    }
    SBAR();
    const unsigned rs = row_stats(v);
    if ((l & 31) == 0) *(LAS unsigned*)((LAS char*)smem + G1_HS + 16 + (w * 2 + h) * 4) = rs;
    if (w == 0 && l == 0) *(LAS unsigned*)((LAS char*)smem + G1_HS) = found;
  }
  f32x16 acc[2][NP];
#pragma unroll
  for (int p = 0; p < NP; ++p) { acc[0][p] = f32x16{}; acc[1][p] = f32x16{}; }
  const LAS char* lbase = (const LAS char*)smem;
  int aoff[4];
#pragma unroll
  for (int ks = 0; ks < 4; ++ks) aoff[ks] = (w * 64 + (l & 31)) * 128 + (((2 * ks + h) ^ ((l >> 1) & 7)) << 4);
  const int boff = G1_BOFF + (8 * h + ((l & 15) >> 2)) * 64 + ((l >> 4) & 1) * 32 + (l & 3) * 8;
  bf16x8 fa0[2], fb0[NP], fa1[2], fb1[NP];
#define G1_RD(FA, FB, base, ks) do { \
    FA[0] = lds_b128((base) + aoff[ks]); FA[1] = lds_b128((base) + aoff[ks] + 4096); \
    _Pragma("unroll") for (int p_ = 0; p_ < NP; ++p_) \
      FB[p_] = cat8(lds_tr((base) + boff + p_ * 4096 + (ks) * 1024), lds_tr((base) + boff + p_ * 4096 + (ks) * 1024 + 256)); \
  } while (0)
#define G1_MM(FA, FB) do { \
    _Pragma("unroll") for (int p_ = 0; p_ < NP; ++p_) { \
      if (NP == G1_KV && p_ == 1) { acc[0][p_] = MFMA32(FA[0], FB[p_], acc[0][p_]); acc[1][p_] = MFMA32(FA[1], FB[p_], acc[1][p_]); } \
      else { acc[0][p_] = MFMA32(FB[p_], FA[0], acc[0][p_]); acc[1][p_] = MFMA32(FB[p_], FA[1], acc[1][p_]); } } \
  } while (0)
#define G1_SCHED() do { \
    _Pragma("unroll") for (int q_ = 0; q_ < NP + 1; ++q_) { \
      __builtin_amdgcn_sched_group_barrier(0x008, 1, 0); __builtin_amdgcn_sched_group_barrier(0x100, 2, 0); } \
    __builtin_amdgcn_sched_group_barrier(0x008, NP - 1, 0); SBAR(); \
  } while (0)

  asm volatile("s_waitcnt lgkmcnt(0)\n\ts_barrier" ::: "memory");
  STAMP(1, 1);
  G1_RD(fa0, fb0, lbase, 0); SBAR();
  G1_RD(fa1, fb1, lbase, 1); G1_MM(fa0, fb0); G1_SCHED();
  G1_RD(fa0, fb0, lbase, 2); G1_MM(fa1, fb1); G1_SCHED();
  G1_RD(fa1, fb1, lbase, 3); G1_MM(fa0, fb0); G1_SCHED();
  int slot = 1;
#pragma unroll 1
  for (int i = 0; i < 15; ++i) {
    const LAS char* nb = lbase + slot * G1_STAGE;
    asm volatile("s_waitcnt lgkmcnt(0)\n\ts_barrier" ::: "memory"); SBAR();
    G1_RD(fa0, fb0, nb, 0); G1_MM(fa1, fb1); G1_SCHED();
    G1_RD(fa1, fb1, nb, 1); G1_MM(fa0, fb0); G1_SCHED();
    G1_RD(fa0, fb0, nb, 2); G1_MM(fa1, fb1); G1_SCHED();
    G1_RD(fa1, fb1, nb, 3); G1_MM(fa0, fb0); G1_SCHED();
    slot = (slot == 2) ? 0 : slot + 1;
  }
  STAMP(1, 4);
  __builtin_amdgcn_s_setprio(3);
  {
    f32x4 t[NP][2];
#pragma unroll
    for (int m = 0; m < 2; ++m) st[m] = *(const LAS unsigned*)(lbase + G1_ST + (w * 64 + m * 32 + (l & 31)) * 4);
#pragma unroll
    for (int p = 0; p < NP; ++p) {
      t[p][0] = *(const LAS f32x4*)(lbase + G1_CS + p * 1024 + (l & 31) * 32);
      t[p][1] = *(const LAS f32x4*)(lbase + G1_CS + p * 1024 + (l & 31) * 32 + 16);
    }
    SBAR();
    G1_MM(fa1, fb1);
    const unsigned msk = h ? 0u : ~0u;
#pragma unroll
    for (int m = 0; m < 2; ++m) {
      const unsigned nm = st[m] & 0xffffu;
      fa0[m] = __builtin_bit_cast(bf16x8, (u32x4){(nm | (nm << 16)) & msk, st[m] & msk, 0u, 0u});
    }
#pragma unroll
    for (int p = 0; p < NP; ++p) {
      const f32x4 u = t[p][0] + t[p][1];
      const float c = (u[0] + u[1]) + (u[2] + u[3]);
      const unsigned c0 = cvtpk(c, c) & 0xffffu; const float r1 = c - bf_lo(c0);
      const unsigned c1 = cvtpk(r1, r1) & 0xffffu; const float r2 = r1 - bf_lo(c1);
      const unsigned c2 = cvtpk(r2, r2) & 0xffffu;
      fb0[p] = __builtin_bit_cast(bf16x8, (u32x4){c0 | (c1 << 16), c2 | (bias[p] << 16), 0u, 0u});
    }
  }
  G1_MM(fa0, fb0);
  float rden[2];
#pragma unroll
  for (int m = 0; m < 2; ++m) rden[m] = 1.0f / bf_hi(st[m]);
  SBAR();
  STAMP(1, 2);

#pragma unroll
  for (int p = 0; p < NP; ++p) {
    const int cb = (NP == G1_FFN) ? (cg - 9) * 5 + p : cg * 4 + p;
    if (NP != G1_FFN || cb < 112) {
#pragma unroll
      for (int m = 0; m < 2; ++m) {
        const int tt = mtile * 8 + w * 2 + m;
        char* dst = (NP == G1_FFN) ? ws + WS_H + (size_t)(cb >> 1) * KT + (size_t)tt * 4096 + (cb & 1) * 2048 + l * 16
                  : (NP == G1_Q)   ? ws + WS_Q + (size_t)(tt * 32 + cb) * 2048 + l * 16
                                   : ws + WS_KV + (size_t)tt * 4096 + p * 2048 + l * 16;
        const float sc = (NP == G1_Q) ? rden[m] * (0.17677669529663687f * 1.4426950408889634f) : rden[m];
#pragma unroll
        for (int s = 0; s < 2; ++s) {
          u32x4 o;
#pragma unroll
          for (int j = 0; j < 4; ++j) {
            float a = acc[m][p][8 * s + 2 * j], b = acc[m][p][8 * s + 2 * j + 1];
            if (NP == G1_FFN) { a = relu_s(a); b = relu_s(b); a *= a; b *= b; }
            else if (NP == G1_KV && p == 1) {
              const int r = 8 * s + 2 * j, tk = (r & 3) + 8 * (r >> 2) + 4 * h;
              a *= __shfl(rden[m], tk); b *= __shfl(rden[m], tk + 1);
            } else { a *= sc; b *= sc; }
            o[j] = cvtpk(a, b);
          }
          store16_dev(dst + s * 1024, o);
        }
      }
    }
  }
  STAMP(1, 3);
#undef G1_RD
#undef G1_MM
#undef G1_SCHED
}

constexpr int AT_NCH = 8;
constexpr int AT_BUF = 32768;
constexpr int AT_BASE = 98304;
constexpr int AT_Q = 65536;
constexpr int AT_DUMP = 90112;
constexpr int MAIN_LDS = 163840;

DI float xhalf_max(float m) {
  auto rr = __builtin_amdgcn_permlane32_swap(__float_as_uint(m), __float_as_uint(m), false, false);
  return __builtin_fmaxf(__uint_as_float(rr[0]), __uint_as_float(rr[1]));
}
DI float xhalf_sum(float m) {
  auto rr = __builtin_amdgcn_permlane32_swap(__float_as_uint(m), __float_as_uint(m), false, false);
  return __uint_as_float(rr[0]) + __uint_as_float(rr[1]);
}
DI float max16(const f32x16& s) {
  float a = __builtin_fmaxf(s[0], s[1]);
#pragma unroll
  for (int r = 2; r < 16; ++r) a = __builtin_fmaxf(a, s[r]);
  return a;
}
DI bf16x8 pack8(const f32x16& p, int s) {
  u32x4 o;
#pragma unroll
  for (int j = 0; j < 4; ++j) o[j] = cvtpk(p[8 * s + 2 * j], p[8 * s + 2 * j + 1]);
  return __builtin_bit_cast(bf16x8, o);
}

DI void attn_fallback(const char* __restrict__ kv, bf16x8 q0, bf16x8 q1, int l, f32x16& o_out, float& l_out) {
  f32x16 o = {};
  float m = -INFINITY, lsum = 0.f;
  for (int t = 0; t < 64; ++t) {
    const char* p = kv + (size_t)t * 4096 + l * 16;
    const bf16x8 k0 = load16_dev(p), k1 = load16_dev(p + 1024), v0 = load16_dev(p + 2048), v1 = load16_dev(p + 3072);
    f32x16 s = {};
    s = MFMA32(k0, q0, s); s = MFMA32(k1, q1, s);
    const float mn = __builtin_fmaxf(m, xhalf_max(max16(s)));
    const float ms = (mn == -INFINITY) ? 0.f : mn;
    const float alpha = __builtin_amdgcn_exp2f(m - ms);
    m = mn;
    float ps = 0.f;
#pragma unroll
    for (int r = 0; r < 16; ++r) { s[r] = __builtin_amdgcn_exp2f(s[r] - ms); ps += s[r]; }
    lsum = lsum * alpha + ps;
#pragma unroll
    for (int r = 0; r < 16; ++r) o[r] *= alpha;
    o = MFMA32(v0, pack8(s, 0), o); o = MFMA32(v1, pack8(s, 1), o);
  }
  o_out = o;
  l_out = xhalf_sum(lsum);
}

constexpr int G2_SLOT = 32768, G2_BOFF = 16384, G2_PART = 131072;
struct G2Ld {
  const char* hb; const char* ob; const char* wb; const char* wb2; unsigned lds; unsigned voff, voffb; int rot, frot;
  DI void init(const char* ws, const u16* w2, const u16* wo, unsigned lds0, int lw, int l) {
    const int xcd = blockIdx.x & 7, idx = blockIdx.x >> 3, kh = xcd & 1;
    const int mt = ((xcd >> 1) & 1) * 8 + (idx & 7), nt = (xcd >> 2) * 4 + (idx >> 3);
    hb = ws + WS_H + (size_t)(mt * 4 + lw) * 4096 + (size_t)kh * (28 * KT);
    ob = ws + WS_O + (size_t)(mt * 4 + lw) * 4096;
    wb = (const char*)(w2 + (size_t)(kh * 1792 + lw * 16) * DM + nt * 128);
    wb2 = (const char*)(wo + (size_t)(lw * 16) * DM + nt * 128 + kh * 64);
    voffb = (unsigned)((l >> 2) * DM * 2 + (l & 3) * 16);
    lds = lds0 + lw * 4096; voff = l * 16;
    frot = (7 * (mt & 7)) >> 1;
    rot = 2 * (mt & 7);
  }
  DI void ffn(int s) const {
    const unsigned d = lds + (s & 3) * G2_SLOT;
    s += frot; s = s >= 28 ? s - 28 : s;
    const char* a = hb + (size_t)s * KT;
    glds16_dev<0>(voff, a, d); glds16_dev<1024>(voff, a, d); glds16_dev<2048>(voff, a, d); glds16_dev<3072>(voff, a, d);
    const char* b = wb + (size_t)s * 131072;
    glds16<0>(voffb, b, d + G2_BOFF); glds16<0>(voffb, b + 64, d + G2_BOFF + 1024); glds16<0>(voffb, b + 128, d + G2_BOFF + 2048); glds16<0>(voffb, b + 192, d + G2_BOFF + 3072);
  }
  template <int J> DI void ffn_piece(int s) const {
    const unsigned d = lds + (s & 3) * G2_SLOT;
    s += frot; s = s >= 28 ? s - 28 : s;
    if (J < 4) glds16_dev<(J & 3) * 1024>(voff, hb + (size_t)s * KT, d);
    else glds16<0>(voffb, wb + (size_t)s * 131072 + (J & 3) * 64, d + G2_BOFF + (J & 3) * 1024);
  }
  DI void wo(int s) const {
    const unsigned d = lds + (s & 3) * G2_SLOT;
    const int k = (s - 28 + rot) & 15;
    const char* a = ob + (size_t)k * KT;
    glds16_dev<0>(voff, a, d); glds16_dev<1024>(voff, a, d); glds16_dev<2048>(voff, a, d); glds16_dev<3072>(voff, a, d);
    const char* b = wb2 + (size_t)k * 131072;
    glds16<0>(voffb, b, d + G2_BOFF); glds16<0>(voffb, b + 64, d + G2_BOFF + 1024);
  }
};

DI void attn_finish(char* __restrict__ ws, f32x16 o, f32x4 lacc, bf16x8 q0, bf16x8 q1, int l, char* dst) {
  const float l0 = __shfl(lacc[0] + lacc[1], l & 15), l1 = __shfl(lacc[2] + lacc[3], l & 15);
  float lsum = (l & 16) ? l1 : l0;
  bool ok = (lsum >= 9.5367431640625e-07f) && (lsum <= 1.8446744073709552e19f);
#pragma unroll
  for (int r = 0; r < 16; ++r) ok = ok && (__builtin_fabsf(o[r]) <= 3.0e38f);
  if (!__all(ok)) attn_fallback(ws + WS_KV, q0, q1, l, o, lsum);
  const float rl = 1.0f / lsum;
#pragma unroll
  for (int s = 0; s < 2; ++s) {
    u32x4 ov;
#pragma unroll
    for (int j = 0; j < 4; ++j) ov[j] = cvtpk(o[8 * s + 2 * j] * rl, o[8 * s + 2 * j + 1] * rl);
    store16_dev(dst + s * 1024, ov);
  }
}

template <bool LOADER>
DI void attn_phase(char* __restrict__ ws, const u16* w2, const u16* wo, char* smem, const int w, const int l, unsigned* late_pub) {
  const int tt = blockIdx.x >> 2, hg = blockIdx.x & 3;
  const int head = hg * 8 + w, lw = w & 3;
  const unsigned lds0 = (unsigned)(size_t)(LAS char*)smem;
  unsigned* flags = (unsigned*)(ws + WS_FLAG);
  const unsigned voff = l * 16;
  const char* ksrc = ws + WS_KV + (size_t)lw * 4096;
  const unsigned kdst = lds0 + AT_BASE + lw * 4096;
  auto ld = [&](int c, int half) {
    const char* s_ = ksrc + (size_t)c * AT_BUF + half * 16384;
    const unsigned d_ = kdst + (c & 1) * AT_BUF + half * 16384;
    glds16_dev<0>(voff, s_, d_); glds16_dev<1024>(voff, s_, d_); glds16_dev<2048>(voff, s_, d_); glds16_dev<3072>(voff, s_, d_);
  };
  G2Ld g2;
  const unsigned tdump = lds0 + AT_DUMP + lw * 256;
  const int xcd = blockIdx.x & 7, idx = blockIdx.x >> 3, gk = xcd & 1;
  const int mt = ((xcd >> 1) & 1) * 8 + (idx & 7), nt = (xcd >> 2) * 4 + (idx >> 3);
  STAMP(2, 0);
  if (LOADER) {
    const unsigned* word = flags + ((l & 1) ? FL_Q + (tt >> 3) * 8 + hg * 2 + (lw >> 1) : FL_KV) * 32;
    const unsigned need = (l & 1) ? 4u : 32u;
#pragma unroll 1
    for (unsigned spins = 0; spins < SPIN_CAP; ++spins) {
      if (__all(__hip_atomic_load((gu32*)word, RLX_AGENT) >= need)) break;
      __builtin_amdgcn_s_sleep(2);
    }
    __builtin_amdgcn_fence(__ATOMIC_ACQUIRE, "wavefront");
    const char* qs = ws + WS_Q + (size_t)(tt * 32 + hg * 8 + lw * 2) * 2048;
    const unsigned qd = lds0 + AT_Q + lw * 4096;
    glds16_dev<0>(voff, qs, qd); glds16_dev<1024>(voff, qs, qd); glds16_dev<2048>(voff, qs, qd); glds16_dev<3072>(voff, qs, qd);
    ld(0, 0); ld(0, 1); ld(1, 0); ld(1, 1);
    l2_touch(wo, tdump);
    g2.init(ws, w2, wo, lds0, lw, l);
  }
  const unsigned s_lo = (l & 14) == 0 ? 0x3f803f80u : 0u, s_hi = (l & 14) == 2 ? 0x3f803f80u : 0u;
  const bf16x8_t sel = __builtin_bit_cast(bf16x8_t, (u32x4){s_lo, s_lo, s_hi, s_hi});
  const int sidx = (l & 1) ? 0xeeee : 0x4444;
  f32x16 o = {}, sa, sb;
  f32x4 ls = {};
  u32x8 pa, pb = {};
  PIN(o);
  if (LOADER) {
    asm volatile("s_waitcnt vmcnt(9)" ::: "memory");
    if (l == 0) __hip_atomic_fetch_add((LAS unsigned*)((LAS char*)smem + AT_RDY), 1u, __ATOMIC_RELAXED, __HIP_MEMORY_SCOPE_WORKGROUP);
  }
#pragma unroll 1
  for (unsigned spins = 0; spins < 64u * SPIN_CAP; ++spins) {
    if (rfl(__hip_atomic_load((LAS unsigned*)((LAS char*)smem + AT_RDY), __ATOMIC_RELAXED, __HIP_MEMORY_SCOPE_WORKGROUP)) >= 4u) break;
    __builtin_amdgcn_s_sleep(1);
  }
  asm volatile("" ::: "memory");
  STAMP(2, 1);

  const LAS char* qp = (const LAS char*)smem + AT_Q + w * 2048 + l * 16;
  const bf16x8 q0 = lds_b128(qp), q1 = lds_b128(qp + 1024);
  const LAS char* kvp = (const LAS char*)smem + AT_BASE + l * 16;
  bf16x8 kx0, kx1, ky0, ky1, va0, va1, vb0, vb1, vc0, vc1, vd0, vd1;
  kx0 = lds_b128(kvp); kx1 = lds_b128(kvp + 1024); ky0 = lds_b128(kvp + 4096); ky1 = lds_b128(kvp + 4096 + 1024);
  va0 = lds_b128(kvp + 2048); va1 = lds_b128(kvp + 3072); vb0 = lds_b128(kvp + 4096 + 2048); vb1 = lds_b128(kvp + 4096 + 3072);
  vd0 = va0; vd1 = va1;
  f32x16 nm;
  {
    sa = MFMA32(kx0, q0, o); sa = MFMA32(kx1, q1, sa);
    const float ma = -xhalf_max(max16(sa));
#pragma unroll
    for (int r = 0; r < 16; ++r) { nm[r] = ma; sa[r] += ma; }
    PIN(nm);
  }
#define EX(S, i) S[i] = __builtin_amdgcn_exp2f(S[i])
#define CV(P, j, S, i) P[j] = cvtpk(S[i], S[(i) + 1])
#define PLO(x) __builtin_bit_cast(bf16x8, __builtin_shufflevector(x, x, 0, 1, 2, 3))
#define PHI(x) __builtin_bit_cast(bf16x8, __builtin_shufflevector(x, x, 4, 5, 6, 7))
#define RSUM(x) ls = __builtin_amdgcn_smfmac_f32_16x16x64_bf16(sel, __builtin_bit_cast(bf16x16_t, x), ls, sidx, 0, 0)
#define STEP(S, P, SN, PP, KN0, KN1, VP0, VP1, KL0, KL1, VL0, VL1, NP) do { \
    SN = MFMA32(KN0, q0, nm); SBAR(); KL0 = lds_b128(NP); EX(S, 0); EX(S, 1); EX(S, 2); EX(S, 3); PIN(S); SBAR(); \
    SN = MFMA32(KN1, q1, SN); SBAR(); KL1 = lds_b128((NP) + 1024); CV(P, 0, S, 0); CV(P, 1, S, 2); EX(S, 4); EX(S, 5); EX(S, 6); EX(S, 7); PIN(S); PIN(P); SBAR(); \
    o = MFMA32(VP0, PLO(PP), o); SBAR(); VL0 = lds_b128((NP) + 2048); CV(P, 2, S, 4); EX(S, 8); CV(P, 3, S, 6); EX(S, 9); EX(S, 10); EX(S, 11); PIN(S); PIN(P); SBAR(); \
    o = MFMA32(VP1, PHI(PP), o); SBAR(); VL1 = lds_b128((NP) + 3072); CV(P, 4, S, 8); CV(P, 5, S, 10); EX(S, 12); EX(S, 13); EX(S, 14); PIN(S); PIN(P); SBAR(); \
    RSUM(PP); SBAR(); EX(S, 15); CV(P, 6, S, 12); CV(P, 7, S, 14); PIN(P); SBAR(); \
  } while (0)
#define XP(st, j) do { if (LOADER && (unsigned)((st) + 1) < 4u) g2.template ffn_piece<j>((st) + 1); SBAR(); } while (0)
#define PRIO(n) do { __builtin_amdgcn_s_setprio(n); SBAR(); } while (0)
#pragma unroll 1
  for (int c = 0; c < AT_NCH; ++c) {
    const LAS char* cb = kvp + (c & 1) * AT_BUF;
    const LAS char* nb = kvp + ((c + 1) & 1) * AT_BUF;
    PRIO(2);
    STEP(sa, pa, sb, pb, ky0, ky1, vd0, vd1, kx0, kx1, vc0, vc1, cb + 2 * 4096); XP(c - 5, 2);
    STEP(sb, pb, sa, pa, kx0, kx1, va0, va1, ky0, ky1, vd0, vd1, cb + 3 * 4096); XP(c - 5, 3);
    PRIO(1);
    STEP(sa, pa, sb, pb, ky0, ky1, vb0, vb1, kx0, kx1, va0, va1, cb + 4 * 4096); XP(c - 5, 4);
    STEP(sb, pb, sa, pa, kx0, kx1, vc0, vc1, ky0, ky1, vb0, vb1, cb + 5 * 4096); XP(c - 5, 5);
    STEP(sa, pa, sb, pb, ky0, ky1, vd0, vd1, kx0, kx1, vc0, vc1, cb + 6 * 4096); XP(c - 5, 6);
    PRIO(0);
    STEP(sb, pb, sa, pa, kx0, kx1, va0, va1, ky0, ky1, vd0, vd1, cb + 7 * 4096); XP(c - 5, 7);
    if (c < AT_NCH - 1) {
      if (LOADER) {
        if (c < 4) asm volatile("s_waitcnt vmcnt(1) lgkmcnt(0)\n\ts_barrier" ::: "memory");
        else asm volatile("s_waitcnt vmcnt(8) lgkmcnt(0)\n\ts_barrier" ::: "memory");
        if (c == 2)
          peek_dev(flags + (FL_H + (mt >> 1)) * 32, lds0 + G1_CS + lw * 256);
        if (c + 2 < AT_NCH) { ld(c + 2, 0); ld(c + 2, 1); }
        if (c < 2) {
          const int j = c * 4 + lw;
          l2_touch(j < 7 ? (const char*)(w2 + (size_t)(gk * 1792 + j * 256 + (mt & 7) * 32 + (l >> 1)) * DM + nt * 128 + (l & 1) * 64)
                         : (const char*)(wo + (size_t)((mt & 7) * 128 + l) * DM + nt * 128 + gk * 64), tdump);
        } else if (c == 2) {
          l2_touch(lw == 0 ? (const char*)(wo + (size_t)((mt & 7) * 128 + 64 + l) * DM + nt * 128 + gk * 64) : (const char*)wo, tdump);
        } else if (c == 3) {
          if (rfl(*(const LAS unsigned*)((const LAS char*)smem + G1_CS + lw * 256)) < 92u) wait_ge(flags + (FL_H + (mt >> 1)) * 32, 92u);
        }
      } else {
        asm volatile("s_waitcnt lgkmcnt(0)\n\ts_barrier" ::: "memory");
        if (c == 0 && late_pub) publish(late_pub, l);
        if (!(c & 1)) STAMP(2, 4 + (c >> 1));
      }
      PRIO(3);
    }
    SBAR();
    STEP(sa, pa, sb, pb, ky0, ky1, vb0, vb1, kx0, kx1, va0, va1, nb); XP(c - 4, 0);
    STEP(sb, pb, sa, pa, kx0, kx1, vc0, vc1, ky0, ky1, vb0, vb1, nb + 4096); XP(c - 4, 1);
  }
  o = MFMA32(vd0, PLO(pb), o); o = MFMA32(vd1, PHI(pb), o); RSUM(pb);
#undef EX
#undef CV
#undef PLO
#undef PHI
#undef RSUM
#undef STEP
#undef PRIO
#undef XP
  STAMP(2, 2);
  __builtin_amdgcn_s_setprio(0);
  attn_finish(ws, o, ls, q0, q1, l, ws + WS_O + (size_t)(head >> 1) * KT + (size_t)tt * 4096 + (head & 1) * 2048 + l * 16);
  STAMP(2, 3);
}

DI void g2_load(const u16* w2, const u16* wo, char* __restrict__ ws, unsigned lds0, const int lw, const int l) {
  const int xcd = blockIdx.x & 7, idx = blockIdx.x >> 3;
  const int mt = ((xcd >> 1) & 1) * 8 + (idx & 7);
  unsigned* flags = (unsigned*)(ws + WS_FLAG);
  G2Ld g; g.init(ws, w2, wo, lds0, lw, l);
  asm volatile("s_waitcnt vmcnt(10) lgkmcnt(0)\n\ts_barrier" ::: "memory");
  g.ffn(4); g.ffn(5);
  const LAS unsigned* peek = (const LAS unsigned*)(size_t)(lds0 + G2_PART);
#pragma unroll 1
  for (int i = 2; i < 24; ++i) {
    asm volatile("s_waitcnt vmcnt(16)\n\ts_barrier" ::: "memory");
    if (i == 6 && l == 0) __hip_atomic_fetch_add((gu32*)flags + (FL_O + (blockIdx.x >> 4)) * 32, 1u, RLX_AGENT);
    if (i == 20 && lw == 0) peek_dev(flags + (FL_O + mt) * 32, lds0 + G2_PART);
    g.ffn(i + 4);
  }
  asm volatile("s_waitcnt vmcnt(16)\n\ts_barrier" ::: "memory");
  if (rfl(peek[0]) < 128u) wait_ge(flags + (FL_O + mt) * 32, 128u);
  g.wo(28);
  peek = (const LAS unsigned*)(size_t)(lds0 + G2_BOFF + 2048);
#pragma unroll 1
  for (int i = 25; i < 37; ++i) {
    asm volatile("s_waitcnt vmcnt(12)\n\ts_barrier" ::: "memory");
    if (i == 35 && lw == 0) peek_dev(flags + (FL_P + blockIdx.x) * 32, lds0 + G2_BOFF + 2048);
    g.wo(i + 4);
  }
  auto part = [&]() {
    const char* src = ws + WS_PART + (size_t)(blockIdx.x ^ 1) * 16384 + lw * 4096;
    const unsigned d = lds0 + G2_PART + lw * 4096;
    glds16_dev<0>(g.voff, src, d); glds16_dev<1024>(g.voff, src, d); glds16_dev<2048>(g.voff, src, d); glds16_dev<3072>(g.voff, src, d);
  };
  asm volatile("s_waitcnt vmcnt(12)\n\ts_barrier" ::: "memory");
  const bool early = rfl(peek[0]) >= 4u;
  g.wo(41);
  if (early) part();
#pragma unroll 1
  for (int i = 38; i < 41; ++i) {
    if (early) asm volatile("s_waitcnt vmcnt(16)\n\ts_barrier" ::: "memory");
    else asm volatile("s_waitcnt vmcnt(12)\n\ts_barrier" ::: "memory");
    if (i < 40) g.wo(i + 4);
  }
  asm volatile("s_waitcnt vmcnt(6)\n\ts_barrier" ::: "memory");
  if (!early) { wait_ge(flags + (FL_P + blockIdx.x) * 32, 4u); part(); }
  asm volatile("s_waitcnt vmcnt(0)\n\ts_barrier" ::: "memory");
}

DI void g2_compute(const u16* __restrict__ x, const u16* __restrict__ b2, char* __restrict__ ws, u16* __restrict__ out,
                   char* smem, const int w, const int l) {
  const int xcd = blockIdx.x & 7, idx = blockIdx.x >> 3;
  const int kh = xcd & 1, mt = ((xcd >> 1) & 1) * 8 + (idx & 7), nt = (xcd >> 2) * 4 + (idx >> 3);
  unsigned* flags = (unsigned*)(ws + WS_FLAG);
  STAMP(3, 0);
  const int h = l >> 5, wm = w & 1, wn = w >> 1;
  f32x16 accg[2] = {}, accp[2] = {}, accs[2] = {};
  const int col0 = nt * 128 + kh * 64 + wn * 32;
  const int row0 = mt * 128 + wm * 64 + (l & 31);
  unsigned stw[2];
  u32x4 xv[4];
  u32x2 bv[4];
#pragma unroll
  for (int m = 0; m < 2; ++m) stw[m] = __hip_atomic_load((gu32*)(ws + WS_STAT) + row0 + m * 32, RLX_AGENT);
#pragma unroll
  for (int k = 0; k < 4; ++k) xv[k] = *(const u32x4*)(x + (size_t)(row0 + (k >> 1) * 32) * DM + col0 + 16 * (k & 1) + 8 * h);
#pragma unroll
  for (int k = 0; k < 4; ++k) bv[k] = *(const u32x2*)(b2 + col0 + 8 * k + 4 * h);
  const LAS char* lbase = (const LAS char*)smem + l * 16;
  const int btr = (4 * h + ((l & 15) >> 2)) * 64 + ((l >> 4) & 1) * 32 + (l & 3) * 8 - l * 16;
#define LDB(p_) cat8(lds_tr(p_), lds_tr((p_) + 512))
  const int ao = wm * 8192;
  const int bG = G2_BOFF + (kh * 2 + wn) * 1024 + btr, bP = G2_BOFF + ((kh ^ 1) * 2 + wn) * 1024 + btr;
  const int bW = G2_BOFF + wn * 1024 + btr;
  bf16x8 fa0[2], fb0[2], fa1[2], fb1[2];
#define G2_RD(FA, FB, base, ks) do { \
    FA[0] = lds_b128((base) + ao + (ks) * 1024); FB[0] = LDB((base) + bG + (ks) * 4096); \
    FA[1] = lds_b128((base) + ao + 4096 + (ks) * 1024); FB[1] = LDB((base) + bP + (ks) * 4096); \
  } while (0)
#define G2_MM(FA, FB) do { \
    accg[0] = MFMA32(FB[0], FA[0], accg[0]); accg[1] = MFMA32(FB[0], FA[1], accg[1]); \
    accp[0] = MFMA32(FB[1], FA[0], accp[0]); accp[1] = MFMA32(FB[1], FA[1], accp[1]); \
  } while (0)
#define G2_SCHED(NR) do { \
    __builtin_amdgcn_sched_group_barrier(0x008, 1, 0); __builtin_amdgcn_sched_group_barrier(0x100, NR, 0); \
    __builtin_amdgcn_sched_group_barrier(0x008, 1, 0); __builtin_amdgcn_sched_group_barrier(0x100, NR, 0); \
    __builtin_amdgcn_sched_group_barrier(0x008, 2, 0); SBAR(); \
  } while (0)
#define G2_BAR() do { asm volatile("s_waitcnt lgkmcnt(0)\n\ts_barrier" ::: "memory"); SBAR(); } while (0)

  SBAR();
  STAMP(3, 1);
  G2_RD(fa0, fb0, lbase, 0); SBAR();
  G2_RD(fa1, fb1, lbase, 1); G2_MM(fa0, fb0); G2_SCHED(3);
  G2_RD(fa0, fb0, lbase, 2); G2_MM(fa1, fb1); G2_SCHED(3);
  G2_RD(fa1, fb1, lbase, 3); G2_MM(fa0, fb0); G2_SCHED(3);
#pragma unroll 1
  for (int i = 0; i < 27; ++i) {
    const LAS char* nb = lbase + ((i + 1) & 3) * G2_SLOT;
    if (i > 0) G2_BAR();
    G2_RD(fa0, fb0, nb, 0); G2_MM(fa1, fb1); G2_SCHED(3);
    G2_RD(fa1, fb1, nb, 1); G2_MM(fa0, fb0); G2_SCHED(3);
    G2_RD(fa0, fb0, nb, 2); G2_MM(fa1, fb1); G2_SCHED(3);
    G2_RD(fa1, fb1, nb, 3); G2_MM(fa0, fb0); G2_SCHED(3);
    if (i == 3) { publish(flags + (FL_O + (blockIdx.x >> 4)) * 32, l); SBAR(); }
  }
  STAMP(3, 4);
  bf16x8 ga0[4], gb0[2], ga1[4], gb1[2];
#define W_RD(GA, GB, base, ks) do { \
    GA[0] = lds_b128((base) + ao + (ks) * 1024); GB[0] = LDB((base) + bW + (ks) * 4096); GA[1] = lds_b128((base) + ao + 4096 + (ks) * 1024); \
    GA[2] = lds_b128((base) + ao + (ks) * 1024 + 1024); GB[1] = LDB((base) + bW + (ks) * 4096 + 4096); GA[3] = lds_b128((base) + ao + 4096 + (ks) * 1024 + 1024); \
  } while (0)
#define W_MM(GA, GB) do { \
    accs[0] = MFMA32(GB[0], GA[0], accs[0]); accs[1] = MFMA32(GB[0], GA[1], accs[1]); \
    accs[0] = MFMA32(GB[1], GA[2], accs[0]); accs[1] = MFMA32(GB[1], GA[3], accs[1]); \
  } while (0)
#define W_STEP(i) do { const LAS char* nb_ = lbase + (((i) + 1) & 3) * G2_SLOT; G2_BAR(); \
    W_RD(ga0, gb0, nb_, 0); W_MM(ga1, gb1); G2_SCHED(4); \
    W_RD(ga1, gb1, nb_, 2); W_MM(ga0, gb0); G2_SCHED(4); } while (0)
  char* pdst = ws + WS_PART + (size_t)blockIdx.x * 16384 + w * 4096 + l * 16;
#define G2_SEND(q) do { const int b_ = 8 * ((q) & 1); \
    store16_dev(pdst + (q) * 1024, (u32x4){cvtpk(accp[(q) >> 1][b_], accp[(q) >> 1][b_ + 1]), cvtpk(accp[(q) >> 1][b_ + 2], accp[(q) >> 1][b_ + 3]), \
                                           cvtpk(accp[(q) >> 1][b_ + 4], accp[(q) >> 1][b_ + 5]), cvtpk(accp[(q) >> 1][b_ + 6], accp[(q) >> 1][b_ + 7])}); SBAR(); } while (0)
  G2_BAR();
  W_RD(ga0, gb0, lbase, 0); G2_MM(fa1, fb1); G2_SCHED(4);
  W_RD(ga1, gb1, lbase, 2); W_MM(ga0, gb0); G2_SCHED(4);
  G2_SEND(0); G2_SEND(1);
  STAMP(3, 5);
  W_STEP(28); G2_SEND(2); G2_SEND(3);
#pragma unroll 1
  for (int i = 29; i < 42; ++i) {
    if (i == 40) STAMP(3, 6);
    W_STEP(i);
    if (i == 32) { publish(flags + (FL_P + (blockIdx.x ^ 1)) * 32, l); SBAR(); }
  }
  W_STEP(42);
  W_MM(ga1, gb1);
  STAMP(3, 2);
  {
    const LAS char* xp = (const LAS char*)smem + G2_PART + w * 4096 + l * 16;
#pragma unroll
    for (int m = 0; m < 2; ++m) {
      const float den = bf_hi(stw[m]), rd2 = 1.0f / (den * den);
#pragma unroll
      for (int k = 0; k < 2; ++k) {
        u32x4 xx = xv[m * 2 + k];
        { auto r0 = __builtin_amdgcn_permlane32_swap(xx[0], xx[2], false, false); xx[0] = r0[0]; xx[2] = r0[1];
          auto r1 = __builtin_amdgcn_permlane32_swap(xx[1], xx[3], false, false); xx[1] = r1[0]; xx[3] = r1[1]; }
        unsigned pk[4];
        const u32x4 tp = *(const LAS u32x4*)(xp + (m * 2 + k) * 1024);
#pragma unroll
        for (int ii = 0; ii < 2; ++ii) {
          const int i = 2 * k + ii;
          float r[4];
#pragma unroll
          for (int j = 0; j < 4; ++j) {
            const unsigned xw_ = xx[2 * ii + (j >> 1)], bw_ = bv[i][j >> 1], pw_ = tp[2 * ii + (j >> 1)];
            const float z = (accg[m][4 * i + j] + ((j & 1) ? bf_hi(pw_) : bf_lo(pw_))) * rd2 + ((j & 1) ? bf_hi(bw_) : bf_lo(bw_));
            const float sg = z * __builtin_amdgcn_rcpf(1.0f + __builtin_amdgcn_exp2f(-1.4426950408889634f * z));
            r[j] = ((j & 1) ? bf_hi(xw_) : bf_lo(xw_)) + accs[m][4 * i + j] * sg;
          }
          pk[2 * ii] = cvtpk(r[0], r[1]); pk[2 * ii + 1] = cvtpk(r[2], r[3]);
        }
        auto r0 = __builtin_amdgcn_permlane32_swap(pk[0], pk[2], false, false);
        auto r1 = __builtin_amdgcn_permlane32_swap(pk[1], pk[3], false, false);
        *(u32x4*)(out + (size_t)(row0 + m * 32) * DM + col0 + 16 * k + 8 * h) = (u32x4){r0[0], r1[0], r0[1], r1[1]};
      }
    }
  }
  STAMP(3, 3);
#undef G2_RD
#undef G2_MM
#undef G2_SCHED
#undef G2_BAR
#undef G2_SEND
#undef W_RD
#undef W_MM
#undef W_STEP
#undef LDB
}

__global__ __launch_bounds__(512, 2) void main_kernel(const u16* __restrict__ x, const u16* __restrict__ wq, const u16* __restrict__ wk,
                                                      const u16* __restrict__ wv, const u16* __restrict__ wo, const u16* __restrict__ w1,
                                                      const u16* __restrict__ b1, const u16* __restrict__ w2, const u16* __restrict__ b2,
                                                      char* __restrict__ ws, u16* __restrict__ out) {
  extern __shared__ __attribute__((aligned(16))) char smem[];
  const int l = threadIdx.x & 63, w = rfl(threadIdx.x >> 6);
  const unsigned lds0 = (unsigned)(size_t)(LAS char*)smem;
  const int xcd = blockIdx.x & 7, idx = blockIdx.x >> 3;
  const int mtile = (xcd & 1) * 4 + (idx & 3), cg = (idx >> 2) * 4 + (xcd >> 1);
  unsigned* flags = (unsigned*)(ws + WS_FLAG);
  if (w == 3) {
    unsigned long long pc; unsigned len;
    asm volatile("s_getpc_b64 %0\n.Lmk_code_begin:\n\ts_mov_b32 %1, .Lmk_code_end-.Lmk_code_begin" : "=s"(pc), "=s"(len));
    unsigned off = (unsigned)(idx * 16 + (l & 15)) * 128u;
    off = off < len ? off : 0u;
    l2_touch((const char*)pc + off, lds0 + G1_HS + 3072);
  }
  if (w >= 4) {
    if (cg > 8) g1_load<G1_FFN>(ws, x, w1, nullptr, cg, mtile, smem, w - 4, l);
    else if (cg < 8) g1_load<G1_Q>(ws, x, wq, nullptr, cg, mtile, smem, w - 4, l);
    else g1_load<G1_KV>(ws, x, wk, wv, cg, mtile, smem, w - 4, l);
    attn_phase<true>(ws, w2, wo, smem, w, l, nullptr);
    g2_load(w2, wo, ws, lds0, w - 4, l);
    asm volatile(".Lmk_code_end:");
  } else {
    unsigned* late = nullptr;
    if (cg > 8) { g1_compute<G1_FFN>(ws, x, b1, cg, mtile, smem, w, l); late = flags + (FL_H + mtile) * 32; }
    else if (cg < 8) { g1_compute<G1_Q>(ws, x, b1, cg, mtile, smem, w, l); publish(flags + (FL_Q + mtile * 8 + cg) * 32, l); }
    else { g1_compute<G1_KV>(ws, x, b1, cg, mtile, smem, w, l); publish(flags + FL_KV * 32, l); }
    attn_phase<false>(ws, w2, wo, smem, w, l, late);
    g2_compute(x, b2, ws, out, smem, w, l);
  }
}

extern "C" void kernel_launch(void* const* d_in, const int* in_sizes, int n_in, void* d_out, int out_size, void* d_ws,
                              size_t ws_size, hipStream_t stream) {
  const u16* x = (const u16*)d_in[0];
  const u16* wq = (const u16*)d_in[1];
  const u16* wk = (const u16*)d_in[2];
  const u16* wv = (const u16*)d_in[3];
  const u16* wo = (const u16*)d_in[4];
  const u16* w1 = (const u16*)d_in[6];
  const u16* b1 = (const u16*)d_in[7];
  const u16* w2 = (const u16*)d_in[8];
  const u16* b2 = (const u16*)d_in[9];
  char* ws = (char*)d_ws;
  (void)hipFuncSetAttribute((const void*)main_kernel, hipFuncAttributeMaxDynamicSharedMemorySize, MAIN_LDS);
  main_kernel<<<256, 512, MAIN_LDS, stream>>>(x, wq, wk, wv, wo, w1, b1, w2, b2, ws, (u16*)d_out);
}
```

```cpp
#include <hip/hip_runtime.h>
#include <hip/hip_bf16.h>

typedef short bf16x8 __attribute__((ext_vector_type(8)));
typedef short s16x4 __attribute__((ext_vector_type(4)));
typedef float f32x16 __attribute__((ext_vector_type(16)));
typedef float f32x4 __attribute__((ext_vector_type(4)));
typedef float f32x2 __attribute__((ext_vector_type(2)));
typedef __bf16 bf16x2_t __attribute__((ext_vector_type(2)));
typedef unsigned u32x4 __attribute__((ext_vector_type(4)));
typedef unsigned u32x2 __attribute__((ext_vector_type(2)));
typedef unsigned u32x8 __attribute__((ext_vector_type(8)));
typedef __bf16 bf16x8_t __attribute__((ext_vector_type(8)));
typedef __bf16 bf16x16_t __attribute__((ext_vector_type(16)));
typedef unsigned short u16;
#define LAS __attribute__((address_space(3)))
#define DI __device__ __forceinline__
#define SBAR() __builtin_amdgcn_sched_barrier(0)
#define PIN(x) asm volatile("" : "+v"(x))
#define MFMA32(a, b, c) __builtin_amdgcn_mfma_f32_32x32x16_bf16((a), (b), (c), 0, 0, 0)

constexpr int DM = 1024, FF = 3584;
constexpr size_t KT = 262144;
constexpr size_t WS_STAT = 0;
constexpr size_t WS_Q = 4u << 20;
constexpr size_t WS_KV = 8u << 20;
constexpr size_t WS_O = 9u << 20;
constexpr size_t WS_H = 13u << 20;
constexpr size_t WS_PART = 28u << 20;
constexpr size_t WS_FLAG = 37u << 20;
constexpr int FL_P = 0, FL_O = 256, FL_H = 272, FL_Q = 280, FL_KV = 344, FL_S = 345, FL_N = 353;
constexpr size_t WS_HSA = WS_FLAG + 65536, WS_HSB = WS_HSA + 4096, WS_HSC = WS_HSB + 4096;
constexpr int COORD_BLOCK = 64;

DI unsigned rfl(unsigned x) { return __builtin_amdgcn_readfirstlane(x); }
DI unsigned cvtpk(float lo, float hi) {
  f32x2 v = {lo, hi};
  bf16x2_t b = __builtin_convertvector(v, bf16x2_t);
  return __builtin_bit_cast(unsigned, b);
}
DI float fadd_s(float a, float b) { float r; asm("v_add_f32_e32 %0, %1, %2" : "=v"(r) : "v"(a), "v"(b)); return r; }
DI float bf_lo(unsigned u) { return __uint_as_float(u << 16); }
DI float bf_hi(unsigned u) { return __uint_as_float(u & 0xffff0000u); }
DI float bf_round(float x) { return bf_lo(cvtpk(x, x)); }

template <int IMM>
DI void glds16(unsigned voff, const void* sbase, unsigned ldsaddr) {
  asm volatile("s_mov_b32 m0, %2\n\ts_nop 0\n\tglobal_load_lds_dwordx4 %0, %1 offset:%3"
               :: "v"(voff), "s"(sbase), "s"(ldsaddr), "i"(IMM) : "memory");
}
template <int IMM>
DI void glds16_dev(unsigned voff, const void* sbase, unsigned ldsaddr) {
  asm volatile("s_mov_b32 m0, %2\n\ts_nop 0\n\tglobal_load_lds_dwordx4 %0, %1 offset:%3 sc1"
               :: "v"(voff), "s"(sbase), "s"(ldsaddr), "i"(IMM) : "memory");
}
DI void peek_dev(const void* word, unsigned ldsaddr) {
  asm volatile("s_mov_b32 m0, %2\n\ts_nop 0\n\tglobal_load_lds_dword %0, %1 sc1" :: "v"(0u), "s"(word), "s"(ldsaddr) : "memory");
}
DI void store16_dev(void* p, u32x4 v) {
  asm volatile("global_store_dwordx4 %0, %1, off sc1\n\ts_nop 1" :: "v"(p), "v"(v) : "memory");
}
DI void l2_touch(const void* p, unsigned ldsaddr) {
  asm volatile("s_mov_b32 m0, %1\n\ts_nop 0\n\tglobal_load_lds_dword %0, off" :: "v"(p), "s"(ldsaddr) : "memory");
}
constexpr unsigned SPIN_CAP = 1u << 20;
typedef __attribute__((address_space(1))) unsigned gu32;
#define RLX_AGENT __ATOMIC_RELAXED, __HIP_MEMORY_SCOPE_AGENT
DI void wait_ge(const unsigned* word, unsigned want) {
#pragma unroll 1
  for (unsigned spins = 0; spins < SPIN_CAP; ++spins) {
    if (rfl(__hip_atomic_load((gu32*)word, RLX_AGENT)) >= want) break;
    __builtin_amdgcn_s_sleep(2);
  }
  __builtin_amdgcn_fence(__ATOMIC_ACQUIRE, "wavefront");
}
DI void publish(unsigned* word, int l) {
  asm volatile("s_waitcnt vmcnt(0)" ::: "memory");
  if (l == 0) __hip_atomic_fetch_add((gu32*)word, 1u, RLX_AGENT);
}
DI bf16x8 load16_dev(const char* p) {
  typedef __attribute__((address_space(1))) unsigned long long gu64;
  const unsigned long long a = __hip_atomic_load((gu64*)p, RLX_AGENT), b = __hip_atomic_load((gu64*)p + 1, RLX_AGENT);
  return __builtin_bit_cast(bf16x8, (u32x4){(unsigned)a, (unsigned)(a >> 32), (unsigned)b, (unsigned)(b >> 32)});
}
DI float relu_s(float a) { return __builtin_amdgcn_fmed3f(a, 0.0f, 3.4028234663852886e38f); }
DI bf16x8 lds_b128(const LAS char* p) { return *(const LAS bf16x8*)p; }
DI s16x4 lds_tr(const LAS char* p) {
  return __builtin_amdgcn_ds_read_tr16_b64_v4i16((LAS s16x4*)p);
}
DI bf16x8 cat8(s16x4 lo, s16x4 hi) { return __builtin_shufflevector(lo, hi, 0, 1, 2, 3, 4, 5, 6, 7); }

DI float half_sum(float v) {
  v += __uint_as_float(__builtin_amdgcn_update_dpp(0u, __float_as_uint(v), 0xB1, 0xf, 0xf, true));
  v += __uint_as_float(__builtin_amdgcn_update_dpp(0u, __float_as_uint(v), 0x4E, 0xf, 0xf, true));
  v += __uint_as_float(__builtin_amdgcn_update_dpp(0u, __float_as_uint(v), 0x141, 0xf, 0xf, true));
  v += __uint_as_float(__builtin_amdgcn_update_dpp(0u, __float_as_uint(v), 0x140, 0xf, 0xf, true));
  auto r = __builtin_amdgcn_permlane16_swap(__float_as_uint(v), __float_as_uint(v), false, false);
  return __uint_as_float(r[0]) + __uint_as_float(r[1]);
}
DI unsigned row_stats(const u32x4 (&v)[4]) {
  f32x2 e[16], s2 = {0.f, 0.f};
#pragma unroll
  for (int i = 0; i < 16; ++i) { e[i] = (f32x2){bf_lo(v[i >> 2][i & 3]), bf_hi(v[i >> 2][i & 3])}; s2 += e[i]; }
  const float mean = half_sum(s2[0] + s2[1]) * (1.0f / DM);
  f32x2 q2 = {0.f, 0.f};
#pragma unroll
  for (int i = 0; i < 16; ++i) { const f32x2 d = e[i] - mean; q2 += d * d; }
  const float q = half_sum(q2[0] + q2[1]);
  const float den = __builtin_sqrtf(bf_round(bf_round(q * (1.0f / DM)) + 1e-5f));
  return cvtpk(-mean, den);
}

constexpr int G1_STAGE = 53248, G1_BOFF = 32768;
enum { G1_FFN = 5, G1_Q = 4, G1_KV = 2 };

constexpr int G1_ST = 87040;
constexpr int AT_RDY = 89088;
constexpr int G1_HS = 159744;
constexpr int G1_CS = 81920;
constexpr int ESTART = 3, PSTART = 9;
DI float pair_sum(unsigned v) { return fadd_s(bf_lo(v), bf_hi(v)); }

template <int NP>
DI void g1_load(char* ws, const u16* x, const u16* wsrc, const u16* wv, int cg, int mtile, char* smem, int lw, int l) {
  constexpr int LD = (NP == G1_FFN) ? FF : (NP == G1_Q) ? DM : 32;
  const unsigned lds0 = (unsigned)(size_t)(LAS char*)smem;
  const char* abase = (const char*)(x + (size_t)(mtile * 256 + lw * 64) * DM);
  const unsigned ave = (unsigned)((l >> 3) * 2048 + (((l & 7) ^ (l >> 4)) << 4)), avo = ave ^ 64u;
  const unsigned bvoff = (unsigned)((lw * 16 + (l >> 2)) * LD * 2 + (l & 3) * 16);
  const int krot = NP == G1_Q ? (mtile & 3) * 4 : 0;
  const char* bbase[NP];
#pragma unroll
  for (int p = 0; p < NP; ++p) {
    if (NP == G1_KV) bbase[p] = (const char*)(p ? wv : wsrc);
    else if (NP == G1_Q) bbase[p] = (const char*)(wsrc + (cg * 4 + p) * 32);
    else { const int cb = (cg - 9) * 5 + p; bbase[p] = (const char*)(wsrc + (cb > 111 ? 111 : cb) * 32); }
  }
  float cs[NP];
#pragma unroll
  for (int p = 0; p < NP; ++p) cs[p] = 0.f;
  const LAS char* cb0 = (const LAS char*)smem + G1_BOFF + lw * 1024 + (8 * (l >> 5) + ((l & 15) >> 2)) * 64 + ((l >> 4) & 1) * 32 + (l & 3) * 8;
  u32x2 t0[NP], t1[NP];
  auto cs_read = [&](int slot) {
#pragma unroll
    for (int p = 0; p < NP; ++p) {
      t0[p] = __builtin_bit_cast(u32x2, lds_tr(cb0 + slot * G1_STAGE + p * 4096));
      t1[p] = __builtin_bit_cast(u32x2, lds_tr(cb0 + slot * G1_STAGE + p * 4096 + 256));
    }
    SBAR();
  };
  auto cs_add = [&](int p) {
    cs[p] = fadd_s(cs[p], fadd_s(fadd_s(pair_sum(t0[p][0]), pair_sum(t0[p][1])), fadd_s(pair_sum(t1[p][0]), pair_sum(t1[p][1]))));
    PIN(cs[p]); SBAR();
  };
  auto issue = [&](int st, int slot, bool sum) {
    const int kt = (st + krot) & 15;
    const unsigned sa = lds0 + slot * G1_STAGE + lw * 8192;
    const char* a0 = abase + kt * 128;
    const unsigned sb = lds0 + slot * G1_STAGE + G1_BOFF + lw * 1024;
#pragma unroll
    for (int p = 0; p < NP; ++p) glds16<0>(bvoff, bbase[p] + (size_t)kt * (64 * LD * 2), sb + p * 4096);
#pragma unroll
    for (int i = 0; i < 8; ++i) {
      if (sum && i < NP) { SBAR(); cs_add(i); }
      glds16<0>((i & 1) ? avo : ave, a0 + i * 16384, sa + i * 1024);
    }
  };
  auto colsum = [&](int slot) {
    cs_read(slot);
#pragma unroll
    for (int p = 0; p < NP; ++p) cs_add(p);
  };
  unsigned* flags = (unsigned*)(ws + WS_FLAG);
  const bool coord = (NP == G1_KV) && blockIdx.x == COORD_BLOCK && lw == 1;
  if (coord) {
#pragma unroll
    for (int i = 0; i < 6; ++i) if (i * 64 + l < FL_N) __hip_atomic_store((gu32*)flags + (i * 64 + l) * 32, 0u, RLX_AGENT);
    store16_dev(ws + WS_HSC + l * 16, (u32x4){0u, 0u, 0u, 0u});
    asm volatile("s_waitcnt vmcnt(0)" ::: "memory");
  }
  issue(0, 0, false); issue(1, 1, false); issue(2, 2, false);
  asm volatile("s_waitcnt vmcnt(%0)\n\ts_barrier" :: "n"(2 * (8 + NP)) : "memory");
  __builtin_amdgcn_s_setprio(2);
  int state = (lw == 0 || coord) ? 0 : 2;
  bool pend = false, first = true;
  u32x4 last = {0u, 0u, 0u, 0u};
  const unsigned found = rfl(*(const LAS unsigned*)((const LAS char*)smem + G1_HS));
  if (lw == 0 && l < 9) {
    const unsigned v = *(const LAS unsigned*)((const LAS char*)smem + G1_HS + 16 + (l & 7) * 4);
    __hip_atomic_store(l < 8 ? (gu32*)(ws + WS_STAT) + mtile * 256 + cg * 8 + l : (gu32*)(ws + WS_HSA) + blockIdx.x, l < 8 ? v : found + 1u, RLX_AGENT);
  }
  colsum(0);
  auto poll_issue = [&]() {
    if (NP == G1_KV && coord) {
      glds16_dev<0>(l * 16, ws + WS_HSA, lds0 + G1_HS + 1024); glds16_dev<0>(l * 16, ws + WS_HSC, lds0 + G1_HS + 2048);
    } else {
      const char* a_ = state == 0 ? ws + WS_HSB + blockIdx.x * 4 : (const char*)(flags + (FL_S + mtile) * 32);
      peek_dev(a_, lds0 + G1_HS + 256);
    }
  };
  auto poll_check = [&]() {
    if (NP == G1_KV && coord) {
      const u32x4 pa = *(const LAS u32x4*)((const LAS char*)smem + G1_HS + 1024 + l * 16), pc = *(const LAS u32x4*)((const LAS char*)smem + G1_HS + 2048 + l * 16);
      if (first || pa[0] != last[0] || pa[1] != last[1] || pa[2] != last[2] || pa[3] != last[3]) store16_dev(ws + WS_HSB + l * 16, pa);
      last = pa; first = false;
      if (__all((pc[0] & pc[1] & pc[2] & pc[3]) == 1u)) state = 2;
    } else {
      const unsigned v = rfl(*(const LAS unsigned*)((const LAS char*)smem + G1_HS + 256));
      if (state == 0) {
        if (v != found) {
          if (l == 0) {
            __hip_atomic_store((gu32*)(ws + WS_HSC) + blockIdx.x, 1u, RLX_AGENT);
            __hip_atomic_fetch_add((gu32*)flags + (FL_S + mtile) * 32, 1u, RLX_AGENT);
          }
          state = 1;
        }
      } else if (v >= 32u) state = 2;
    }
    state = rfl(state);
  };
  int slot = 0;
#pragma unroll 1
  for (int i = 0; i < 13; ++i) {
    asm volatile("s_waitcnt vmcnt(%0)\n\ts_barrier" :: "n"(8 + NP) : "memory");
    if (state < 2 && (i >= ESTART || coord)) {
      if (pend) poll_check();
      pend = state == 0 || (state == 1 && i >= PSTART);
      if (pend) poll_issue();
    }
    const int nslot = (slot == 2) ? 0 : slot + 1;
    cs_read(nslot);
    issue(i + 3, slot, true);
    slot = nslot;
  }
#pragma unroll 1
  for (unsigned spins = 0; state < 2 && spins < SPIN_CAP; ++spins) {
    if (!pend) poll_issue();
    asm volatile("s_waitcnt vmcnt(0)" ::: "memory");
    poll_check();
    pend = false;
    __builtin_amdgcn_s_sleep(1);
  }
  asm volatile("s_waitcnt vmcnt(%0)\n\ts_barrier" :: "n"(8 + NP) : "memory");
  if (lw == 0) { glds16_dev<0>(l * 16, ws + WS_STAT + mtile * 1024, lds0 + G1_ST); *(LAS unsigned*)((LAS char*)smem + AT_RDY) = 0u; }
  colsum(2);
  asm volatile("s_waitcnt vmcnt(0)" ::: "memory");
  colsum(0);
#pragma unroll
  for (int p = 0; p < NP; ++p) *(LAS float*)((LAS char*)smem + G1_CS + p * 1024 + (l & 31) * 32 + (lw * 2 + (l >> 5)) * 4) = cs[p];
  asm volatile("s_waitcnt lgkmcnt(0)\n\ts_barrier" ::: "memory");
}

template <int NP>
DI void g1_compute(char* __restrict__ ws, const u16* __restrict__ x, const u16* __restrict__ b1, int cg, int mtile, char* smem, int w, int l) {
  const int h = l >> 5;
  unsigned st[2], bias[NP];
  {
    const int row = mtile * 256 + cg * 8 + w * 2 + h;
    u32x4 v[4];
#pragma unroll
    for (int i = 0; i < 4; ++i) v[i] = *(const u32x4*)(x + (size_t)row * DM + (i * 32 + (l & 31)) * 8);
    const unsigned found = __hip_atomic_load((gu32*)(ws + WS_HSB) + blockIdx.x, RLX_AGENT);
#pragma unroll
    for (int p = 0; p < NP; ++p) {
      int cb = (cg - 9) * 5 + p; cb = cb > 111 ? 111 : cb;
      bias[p] = (NP == G1_FFN) ? (unsigned)b1[cb * 32 + (l & 31)] : 0u;
    }
    SBAR();
    const unsigned rs = row_stats(v);
    if ((l & 31) == 0) *(LAS unsigned*)((LAS char*)smem + G1_HS + 16 + (w * 2 + h) * 4) = rs;
    if (w == 0 && l == 0) *(LAS unsigned*)((LAS char*)smem + G1_HS) = found;
  }
  f32x16 acc[2][NP];
#pragma unroll
  for (int p = 0; p < NP; ++p) { acc[0][p] = f32x16{}; acc[1][p] = f32x16{}; }
  const LAS char* lbase = (const LAS char*)smem;
  int aoff[4];
#pragma unroll
  for (int ks = 0; ks < 4; ++ks) aoff[ks] = (w * 64 + (l & 31)) * 128 + (((2 * ks + h) ^ ((l >> 1) & 7)) << 4);
  const int boff = G1_BOFF + (8 * h + ((l & 15) >> 2)) * 64 + ((l >> 4) & 1) * 32 + (l & 3) * 8;
  bf16x8 fa0[2], fb0[NP], fa1[2], fb1[NP];
#define G1_RD(FA, FB, base, ks) do { \
    FA[0] = lds_b128((base) + aoff[ks]); FA[1] = lds_b128((base) + aoff[ks] + 4096); \
    _Pragma("unroll") for (int p_ = 0; p_ < NP; ++p_) \
      FB[p_] = cat8(lds_tr((base) + boff + p_ * 4096 + (ks) * 1024), lds_tr((base) + boff + p_ * 4096 + (ks) * 1024 + 256)); \
  } while (0)
#define G1_MM(FA, FB) do { \
    _Pragma("unroll") for (int p_ = 0; p_ < NP; ++p_) { \
      if (NP == G1_KV && p_ == 1) { acc[0][p_] = MFMA32(FA[0], FB[p_], acc[0][p_]); acc[1][p_] = MFMA32(FA[1], FB[p_], acc[1][p_]); } \
      else { acc[0][p_] = MFMA32(FB[p_], FA[0], acc[0][p_]); acc[1][p_] = MFMA32(FB[p_], FA[1], acc[1][p_]); } } \
  } while (0)
#define G1_SCHED() do { \
    _Pragma("unroll") for (int q_ = 0; q_ < NP + 1; ++q_) { \
      __builtin_amdgcn_sched_group_barrier(0x008, 1, 0); __builtin_amdgcn_sched_group_barrier(0x100, 2, 0); } \
    __builtin_amdgcn_sched_group_barrier(0x008, NP - 1, 0); SBAR(); \
  } while (0)

  asm volatile("s_waitcnt lgkmcnt(0)\n\ts_barrier" ::: "memory");
  G1_RD(fa0, fb0, lbase, 0); SBAR();
  G1_RD(fa1, fb1, lbase, 1); G1_MM(fa0, fb0); G1_SCHED();
  G1_RD(fa0, fb0, lbase, 2); G1_MM(fa1, fb1); G1_SCHED();
  G1_RD(fa1, fb1, lbase, 3); G1_MM(fa0, fb0); G1_SCHED();
  int slot = 1;
#pragma unroll 1
  for (int i = 0; i < 15; ++i) {
    const LAS char* nb = lbase + slot * G1_STAGE;
    asm volatile("s_waitcnt lgkmcnt(0)\n\ts_barrier" ::: "memory"); SBAR();
    G1_RD(fa0, fb0, nb, 0); G1_MM(fa1, fb1); G1_SCHED();
    G1_RD(fa1, fb1, nb, 1); G1_MM(fa0, fb0); G1_SCHED();
    G1_RD(fa0, fb0, nb, 2); G1_MM(fa1, fb1); G1_SCHED();
    G1_RD(fa1, fb1, nb, 3); G1_MM(fa0, fb0); G1_SCHED();
    slot = (slot == 2) ? 0 : slot + 1;
  }
  __builtin_amdgcn_s_setprio(3);
  {
    f32x4 t[NP][2];
#pragma unroll
    for (int m = 0; m < 2; ++m) st[m] = *(const LAS unsigned*)(lbase + G1_ST + (w * 64 + m * 32 + (l & 31)) * 4);
#pragma unroll
    for (int p = 0; p < NP; ++p) {
      t[p][0] = *(const LAS f32x4*)(lbase + G1_CS + p * 1024 + (l & 31) * 32);
      t[p][1] = *(const LAS f32x4*)(lbase + G1_CS + p * 1024 + (l & 31) * 32 + 16);
    }
    SBAR();
    G1_MM(fa1, fb1);
    const unsigned msk = h ? 0u : ~0u;
#pragma unroll
    for (int m = 0; m < 2; ++m) {
      const unsigned nm = st[m] & 0xffffu;
      fa0[m] = __builtin_bit_cast(bf16x8, (u32x4){(nm | (nm << 16)) & msk, st[m] & msk, 0u, 0u});
    }
#pragma unroll
    for (int p = 0; p < NP; ++p) {
      const f32x4 u = t[p][0] + t[p][1];
      const float c = (u[0] + u[1]) + (u[2] + u[3]);
      const unsigned c0 = cvtpk(c, c) & 0xffffu; const float r1 = c - bf_lo(c0);
      const unsigned c1 = cvtpk(r1, r1) & 0xffffu; const float r2 = r1 - bf_lo(c1);
      const unsigned c2 = cvtpk(r2, r2) & 0xffffu;
      fb0[p] = __builtin_bit_cast(bf16x8, (u32x4){c0 | (c1 << 16), c2 | (bias[p] << 16), 0u, 0u});
    }
  }
  G1_MM(fa0, fb0);
  float rden[2];
#pragma unroll
  for (int m = 0; m < 2; ++m) rden[m] = 1.0f / bf_hi(st[m]);
  SBAR();

#pragma unroll
  for (int p = 0; p < NP; ++p) {
    const int cb = (NP == G1_FFN) ? (cg - 9) * 5 + p : cg * 4 + p;
    if (NP != G1_FFN || cb < 112) {
#pragma unroll
      for (int m = 0; m < 2; ++m) {
        const int tt = mtile * 8 + w * 2 + m;
        char* dst = (NP == G1_FFN) ? ws + WS_H + (size_t)(cb >> 1) * KT + (size_t)tt * 4096 + (cb & 1) * 2048 + l * 16
                  : (NP == G1_Q)   ? ws + WS_Q + (size_t)(tt * 32 + cb) * 2048 + l * 16
                                   : ws + WS_KV + (size_t)tt * 4096 + p * 2048 + l * 16;
        const float sc = (NP == G1_Q) ? rden[m] * (0.17677669529663687f * 1.4426950408889634f) : rden[m];
#pragma unroll
        for (int s = 0; s < 2; ++s) {
          u32x4 o;
#pragma unroll
          for (int j = 0; j < 4; ++j) {
            float a = acc[m][p][8 * s + 2 * j], b = acc[m][p][8 * s + 2 * j + 1];
            if (NP == G1_FFN) { a = relu_s(a); b = relu_s(b); a *= a; b *= b; }
            else if (NP == G1_KV && p == 1) {
              const int r = 8 * s + 2 * j, tk = (r & 3) + 8 * (r >> 2) + 4 * h;
              a *= __shfl(rden[m], tk); b *= __shfl(rden[m], tk + 1);
            } else { a *= sc; b *= sc; }
            o[j] = cvtpk(a, b);
          }
          store16_dev(dst + s * 1024, o);
        }
      }
    }
  }
#undef G1_RD
#undef G1_MM
#undef G1_SCHED
}

constexpr int AT_NCH = 8;
constexpr int AT_BUF = 32768;
constexpr int AT_BASE = 98304;
constexpr int AT_Q = 65536;
constexpr int AT_DUMP = 90112;
constexpr int MAIN_LDS = 163840;

DI float xhalf_max(float m) {
  auto rr = __builtin_amdgcn_permlane32_swap(__float_as_uint(m), __float_as_uint(m), false, false);
  return __builtin_fmaxf(__uint_as_float(rr[0]), __uint_as_float(rr[1]));
}
DI float xhalf_sum(float m) {
  auto rr = __builtin_amdgcn_permlane32_swap(__float_as_uint(m), __float_as_uint(m), false, false);
  return __uint_as_float(rr[0]) + __uint_as_float(rr[1]);
}
DI float max16(const f32x16& s) {
  float a = __builtin_fmaxf(s[0], s[1]);
#pragma unroll
  for (int r = 2; r < 16; ++r) a = __builtin_fmaxf(a, s[r]);
  return a;
}
DI bf16x8 pack8(const f32x16& p, int s) {
  u32x4 o;
#pragma unroll
  for (int j = 0; j < 4; ++j) o[j] = cvtpk(p[8 * s + 2 * j], p[8 * s + 2 * j + 1]);
  return __builtin_bit_cast(bf16x8, o);
}

DI void attn_fallback(const char* __restrict__ kv, bf16x8 q0, bf16x8 q1, int l, f32x16& o_out, float& l_out) {
  f32x16 o = {};
  float m = -INFINITY, lsum = 0.f;
  for (int t = 0; t < 64; ++t) {
    const char* p = kv + (size_t)t * 4096 + l * 16;
    const bf16x8 k0 = load16_dev(p), k1 = load16_dev(p + 1024), v0 = load16_dev(p + 2048), v1 = load16_dev(p + 3072);
    f32x16 s = {};
    s = MFMA32(k0, q0, s); s = MFMA32(k1, q1, s);
    const float mn = __builtin_fmaxf(m, xhalf_max(max16(s)));
    const float ms = (mn == -INFINITY) ? 0.f : mn;
    const float alpha = __builtin_amdgcn_exp2f(m - ms);
    m = mn;
    float ps = 0.f;
#pragma unroll
    for (int r = 0; r < 16; ++r) { s[r] = __builtin_amdgcn_exp2f(s[r] - ms); ps += s[r]; }
    lsum = lsum * alpha + ps;
#pragma unroll
    for (int r = 0; r < 16; ++r) o[r] *= alpha;
    o = MFMA32(v0, pack8(s, 0), o); o = MFMA32(v1, pack8(s, 1), o);
  }
  o_out = o;
  l_out = xhalf_sum(lsum);
}

constexpr int G2_SLOT = 32768, G2_BOFF = 16384, G2_PART = 131072;
struct G2Ld {
  const char* hb; const char* ob; const char* wb; const char* wb2; unsigned lds; unsigned voff, voffb; int rot, frot;
  DI void init(const char* ws, const u16* w2, const u16* wo, unsigned lds0, int lw, int l) {
    const int xcd = blockIdx.x & 7, idx = blockIdx.x >> 3, kh = xcd & 1;
    const int mt = ((xcd >> 1) & 1) * 8 + (idx & 7), nt = (xcd >> 2) * 4 + (idx >> 3);
    hb = ws + WS_H + (size_t)(mt * 4 + lw) * 4096 + (size_t)kh * (28 * KT);
    ob = ws + WS_O + (size_t)(mt * 4 + lw) * 4096;
    wb = (const char*)(w2 + (size_t)(kh * 1792 + lw * 16) * DM + nt * 128);
    wb2 = (const char*)(wo + (size_t)(lw * 16) * DM + nt * 128 + kh * 64);
    voffb = (unsigned)((l >> 2) * DM * 2 + (l & 3) * 16);
    lds = lds0 + lw * 4096; voff = l * 16;
    frot = (7 * (mt & 7)) >> 1;
    rot = 2 * (mt & 7);
  }
  DI void ffn(int s) const {
    const unsigned d = lds + (s & 3) * G2_SLOT;
    s += frot; s = s >= 28 ? s - 28 : s;
    const char* a = hb + (size_t)s * KT;
    glds16_dev<0>(voff, a, d); glds16_dev<1024>(voff, a, d); glds16_dev<2048>(voff, a, d); glds16_dev<3072>(voff, a, d);
    const char* b = wb + (size_t)s * 131072;
    glds16<0>(voffb, b, d + G2_BOFF); glds16<0>(voffb, b + 64, d + G2_BOFF + 1024); glds16<0>(voffb, b + 128, d + G2_BOFF + 2048); glds16<0>(voffb, b + 192, d + G2_BOFF + 3072);
  }
  template <int J> DI void ffn_piece(int s) const {
    const unsigned d = lds + (s & 3) * G2_SLOT;
    s += frot; s = s >= 28 ? s - 28 : s;
    if (J < 4) glds16_dev<(J & 3) * 1024>(voff, hb + (size_t)s * KT, d);
    else glds16<0>(voffb, wb + (size_t)s * 131072 + (J & 3) * 64, d + G2_BOFF + (J & 3) * 1024);
  }
  DI void wo(int s) const {
    const unsigned d = lds + (s & 3) * G2_SLOT;
    const int k = (s - 28 + rot) & 15;
    const char* a = ob + (size_t)k * KT;
    glds16_dev<0>(voff, a, d); glds16_dev<1024>(voff, a, d); glds16_dev<2048>(voff, a, d); glds16_dev<3072>(voff, a, d);
    const char* b = wb2 + (size_t)k * 131072;
    glds16<0>(voffb, b, d + G2_BOFF); glds16<0>(voffb, b + 64, d + G2_BOFF + 1024);
  }
};

DI void attn_finish(char* __restrict__ ws, f32x16 o, f32x4 lacc, bf16x8 q0, bf16x8 q1, int l, char* dst) {
  const float l0 = __shfl(lacc[0] + lacc[1], l & 15), l1 = __shfl(lacc[2] + lacc[3], l & 15);
  float lsum = (l & 16) ? l1 : l0;
  bool ok = (lsum >= 9.5367431640625e-07f) && (lsum <= 1.8446744073709552e19f);
#pragma unroll
  for (int r = 0; r < 16; ++r) ok = ok && (__builtin_fabsf(o[r]) <= 3.0e38f);
  if (!__all(ok)) attn_fallback(ws + WS_KV, q0, q1, l, o, lsum);
  const float rl = 1.0f / lsum;
#pragma unroll
  for (int s = 0; s < 2; ++s) {
    u32x4 ov;
#pragma unroll
    for (int j = 0; j < 4; ++j) ov[j] = cvtpk(o[8 * s + 2 * j] * rl, o[8 * s + 2 * j + 1] * rl);
    store16_dev(dst + s * 1024, ov);
  }
}

template <bool LOADER>
DI void attn_phase(char* __restrict__ ws, const u16* w2, const u16* wo, char* smem, const int w, const int l, unsigned* late_pub) {
  const int tt = blockIdx.x >> 2, hg = blockIdx.x & 3;
  const int head = hg * 8 + w, lw = w & 3;
  const unsigned lds0 = (unsigned)(size_t)(LAS char*)smem;
  unsigned* flags = (unsigned*)(ws + WS_FLAG);
  const unsigned voff = l * 16;
  const char* ksrc = ws + WS_KV + (size_t)lw * 4096;
  const unsigned kdst = lds0 + AT_BASE + lw * 4096;
  auto ld = [&](int c, int half) {
    const char* s_ = ksrc + (size_t)c * AT_BUF + half * 16384;
    const unsigned d_ = kdst + (c & 1) * AT_BUF + half * 16384;
    glds16_dev<0>(voff, s_, d_); glds16_dev<1024>(voff, s_, d_); glds16_dev<2048>(voff, s_, d_); glds16_dev<3072>(voff, s_, d_);
  };
  G2Ld g2;
  const unsigned tdump = lds0 + AT_DUMP + lw * 256;
  const int xcd = blockIdx.x & 7, idx = blockIdx.x >> 3, gk = xcd & 1;
  const int mt = ((xcd >> 1) & 1) * 8 + (idx & 7), nt = (xcd >> 2) * 4 + (idx >> 3);
  if (LOADER) {
    const unsigned* word = flags + ((l & 1) ? FL_Q + (tt >> 3) * 8 + hg * 2 + (lw >> 1) : FL_KV) * 32;
    const unsigned need = (l & 1) ? 4u : 32u;
#pragma unroll 1
    for (unsigned spins = 0; spins < SPIN_CAP; ++spins) {
      if (__all(__hip_atomic_load((gu32*)word, RLX_AGENT) >= need)) break;
      __builtin_amdgcn_s_sleep(2);
    }
    __builtin_amdgcn_fence(__ATOMIC_ACQUIRE, "wavefront");
    const char* qs = ws + WS_Q + (size_t)(tt * 32 + hg * 8 + lw * 2) * 2048;
    const unsigned qd = lds0 + AT_Q + lw * 4096;
    glds16_dev<0>(voff, qs, qd); glds16_dev<1024>(voff, qs, qd); glds16_dev<2048>(voff, qs, qd); glds16_dev<3072>(voff, qs, qd);
    ld(0, 0); ld(0, 1); ld(1, 0); ld(1, 1);
    l2_touch(wo, tdump);
    g2.init(ws, w2, wo, lds0, lw, l);
  }
  const unsigned s_lo = (l & 14) == 0 ? 0x3f803f80u : 0u, s_hi = (l & 14) == 2 ? 0x3f803f80u : 0u;
  const bf16x8_t sel = __builtin_bit_cast(bf16x8_t, (u32x4){s_lo, s_lo, s_hi, s_hi});
  const int sidx = (l & 1) ? 0xeeee : 0x4444;
  f32x16 o = {}, sa, sb;
  f32x4 ls = {};
  u32x8 pa, pb = {};
  PIN(o);
  if (LOADER) {
    asm volatile("s_waitcnt vmcnt(9)" ::: "memory");
    if (l == 0) __hip_atomic_fetch_add((LAS unsigned*)((LAS char*)smem + AT_RDY), 1u, __ATOMIC_RELAXED, __HIP_MEMORY_SCOPE_WORKGROUP);
  }
#pragma unroll 1
  for (unsigned spins = 0; spins < 64u * SPIN_CAP; ++spins) {
    if (rfl(__hip_atomic_load((LAS unsigned*)((LAS char*)smem + AT_RDY), __ATOMIC_RELAXED, __HIP_MEMORY_SCOPE_WORKGROUP)) >= 4u) break;
    __builtin_amdgcn_s_sleep(1);
  }
  asm volatile("" ::: "memory");

  const LAS char* qp = (const LAS char*)smem + AT_Q + w * 2048 + l * 16;
  const bf16x8 q0 = lds_b128(qp), q1 = lds_b128(qp + 1024);
  const LAS char* kvp = (const LAS char*)smem + AT_BASE + l * 16;
  bf16x8 kx0, kx1, ky0, ky1, va0, va1, vb0, vb1, vc0, vc1, vd0, vd1;
  kx0 = lds_b128(kvp); kx1 = lds_b128(kvp + 1024); ky0 = lds_b128(kvp + 4096); ky1 = lds_b128(kvp + 4096 + 1024);
  va0 = lds_b128(kvp + 2048); va1 = lds_b128(kvp + 3072); vb0 = lds_b128(kvp + 4096 + 2048); vb1 = lds_b128(kvp + 4096 + 3072);
  vd0 = va0; vd1 = va1;
  f32x16 nm;
  {
    sa = MFMA32(kx0, q0, o); sa = MFMA32(kx1, q1, sa);
    const float ma = -xhalf_max(max16(sa));
#pragma unroll
    for (int r = 0; r < 16; ++r) { nm[r] = ma; sa[r] += ma; }
    PIN(nm);
  }
#define EX(S, i) S[i] = __builtin_amdgcn_exp2f(S[i])
#define CV(P, j, S, i) P[j] = cvtpk(S[i], S[(i) + 1])
#define PLO(x) __builtin_bit_cast(bf16x8, __builtin_shufflevector(x, x, 0, 1, 2, 3))
#define PHI(x) __builtin_bit_cast(bf16x8, __builtin_shufflevector(x, x, 4, 5, 6, 7))
#define RSUM(x) ls = __builtin_amdgcn_smfmac_f32_16x16x64_bf16(sel, __builtin_bit_cast(bf16x16_t, x), ls, sidx, 0, 0)
#define STEP(S, P, SN, PP, KN0, KN1, VP0, VP1, KL0, KL1, VL0, VL1, NP) do { \
    SN = MFMA32(KN0, q0, nm); SBAR(); KL0 = lds_b128(NP); EX(S, 0); EX(S, 1); EX(S, 2); EX(S, 3); PIN(S); SBAR(); \
    SN = MFMA32(KN1, q1, SN); SBAR(); KL1 = lds_b128((NP) + 1024); CV(P, 0, S, 0); CV(P, 1, S, 2); EX(S, 4); EX(S, 5); EX(S, 6); EX(S, 7); PIN(S); PIN(P); SBAR(); \
    o = MFMA32(VP0, PLO(PP), o); SBAR(); VL0 = lds_b128((NP) + 2048); CV(P, 2, S, 4); EX(S, 8); CV(P, 3, S, 6); EX(S, 9); EX(S, 10); EX(S, 11); PIN(S); PIN(P); SBAR(); \
    o = MFMA32(VP1, PHI(PP), o); SBAR(); VL1 = lds_b128((NP) + 3072); CV(P, 4, S, 8); CV(P, 5, S, 10); EX(S, 12); EX(S, 13); EX(S, 14); PIN(S); PIN(P); SBAR(); \
    RSUM(PP); SBAR(); EX(S, 15); CV(P, 6, S, 12); CV(P, 7, S, 14); PIN(P); SBAR(); \
  } while (0)
#define XP(st, j) do { if (LOADER && (unsigned)((st) + 1) < 4u) g2.template ffn_piece<j>((st) + 1); SBAR(); } while (0)
#define PRIO(n) do { __builtin_amdgcn_s_setprio(n); SBAR(); } while (0)
#pragma unroll 1
  for (int c = 0; c < AT_NCH; ++c) {
    const LAS char* cb = kvp + (c & 1) * AT_BUF;
    const LAS char* nb = kvp + ((c + 1) & 1) * AT_BUF;
    PRIO(2);
    STEP(sa, pa, sb, pb, ky0, ky1, vd0, vd1, kx0, kx1, vc0, vc1, cb + 2 * 4096); XP(c - 5, 2);
    STEP(sb, pb, sa, pa, kx0, kx1, va0, va1, ky0, ky1, vd0, vd1, cb + 3 * 4096); XP(c - 5, 3);
    PRIO(1);
    STEP(sa, pa, sb, pb, ky0, ky1, vb0, vb1, kx0, kx1, va0, va1, cb + 4 * 4096); XP(c - 5, 4);
    STEP(sb, pb, sa, pa, kx0, kx1, vc0, vc1, ky0, ky1, vb0, vb1, cb + 5 * 4096); XP(c - 5, 5);
    STEP(sa, pa, sb, pb, ky0, ky1, vd0, vd1, kx0, kx1, vc0, vc1, cb + 6 * 4096); XP(c - 5, 6);
    PRIO(0);
    STEP(sb, pb, sa, pa, kx0, kx1, va0, va1, ky0, ky1, vd0, vd1, cb + 7 * 4096); XP(c - 5, 7);
    if (c < AT_NCH - 1) {
      if (LOADER) {
        if (c < 4) asm volatile("s_waitcnt vmcnt(1) lgkmcnt(0)\n\ts_barrier" ::: "memory");
        else asm volatile("s_waitcnt vmcnt(8) lgkmcnt(0)\n\ts_barrier" ::: "memory");
        if (c == 2)
          peek_dev(flags + (FL_H + (mt >> 1)) * 32, lds0 + G1_CS + lw * 256);
        if (c + 2 < AT_NCH) { ld(c + 2, 0); ld(c + 2, 1); }
        if (c < 2) {
          const int j = c * 4 + lw;
          l2_touch(j < 7 ? (const char*)(w2 + (size_t)(gk * 1792 + j * 256 + (mt & 7) * 32 + (l >> 1)) * DM + nt * 128 + (l & 1) * 64)
                         : (const char*)(wo + (size_t)((mt & 7) * 128 + l) * DM + nt * 128 + gk * 64), tdump);
        } else if (c == 2) {
          l2_touch(lw == 0 ? (const char*)(wo + (size_t)((mt & 7) * 128 + 64 + l) * DM + nt * 128 + gk * 64) : (const char*)wo, tdump);
        } else if (c == 3) {
          if (rfl(*(const LAS unsigned*)((const LAS char*)smem + G1_CS + lw * 256)) < 92u) wait_ge(flags + (FL_H + (mt >> 1)) * 32, 92u);
        }
      } else {
        asm volatile("s_waitcnt lgkmcnt(0)\n\ts_barrier" ::: "memory");
        if (c == 0 && late_pub) publish(late_pub, l);
      }
      PRIO(3);
    }
    SBAR();
    STEP(sa, pa, sb, pb, ky0, ky1, vb0, vb1, kx0, kx1, va0, va1, nb); XP(c - 4, 0);
    STEP(sb, pb, sa, pa, kx0, kx1, vc0, vc1, ky0, ky1, vb0, vb1, nb + 4096); XP(c - 4, 1);
  }
  o = MFMA32(vd0, PLO(pb), o); o = MFMA32(vd1, PHI(pb), o); RSUM(pb);
#undef EX
#undef CV
#undef PLO
#undef PHI
#undef RSUM
#undef STEP
#undef PRIO
#undef XP
  __builtin_amdgcn_s_setprio(0);
  attn_finish(ws, o, ls, q0, q1, l, ws + WS_O + (size_t)(head >> 1) * KT + (size_t)tt * 4096 + (head & 1) * 2048 + l * 16);
}

DI void g2_load(const u16* w2, const u16* wo, char* __restrict__ ws, unsigned lds0, const int lw, const int l) {
  const int xcd = blockIdx.x & 7, idx = blockIdx.x >> 3;
  const int mt = ((xcd >> 1) & 1) * 8 + (idx & 7);
  unsigned* flags = (unsigned*)(ws + WS_FLAG);
  G2Ld g; g.init(ws, w2, wo, lds0, lw, l);
  asm volatile("s_waitcnt vmcnt(10) lgkmcnt(0)\n\ts_barrier" ::: "memory");
  g.ffn(4); g.ffn(5);
  const LAS unsigned* peek = (const LAS unsigned*)(size_t)(lds0 + G2_PART);
#pragma unroll 1
  for (int i = 2; i < 24; ++i) {
    asm volatile("s_waitcnt vmcnt(16)\n\ts_barrier" ::: "memory");
    if (i == 6 && l == 0) __hip_atomic_fetch_add((gu32*)flags + (FL_O + (blockIdx.x >> 4)) * 32, 1u, RLX_AGENT);
    if (i == 20 && lw == 0) peek_dev(flags + (FL_O + mt) * 32, lds0 + G2_PART);
    g.ffn(i + 4);
  }
  asm volatile("s_waitcnt vmcnt(16)\n\ts_barrier" ::: "memory");
  if (rfl(peek[0]) < 128u) wait_ge(flags + (FL_O + mt) * 32, 128u);
  g.wo(28);
  peek = (const LAS unsigned*)(size_t)(lds0 + G2_BOFF + 2048);
#pragma unroll 1
  for (int i = 25; i < 37; ++i) {
    asm volatile("s_waitcnt vmcnt(12)\n\ts_barrier" ::: "memory");
    if (i == 35 && lw == 0) peek_dev(flags + (FL_P + blockIdx.x) * 32, lds0 + G2_BOFF + 2048);
    g.wo(i + 4);
  }
  auto part = [&]() {
    const char* src = ws + WS_PART + (size_t)(blockIdx.x ^ 1) * 16384 + lw * 4096;
    const unsigned d = lds0 + G2_PART + lw * 4096;
    glds16_dev<0>(g.voff, src, d); glds16_dev<1024>(g.voff, src, d); glds16_dev<2048>(g.voff, src, d); glds16_dev<3072>(g.voff, src, d);
  };
  asm volatile("s_waitcnt vmcnt(12)\n\ts_barrier" ::: "memory");
  const bool early = rfl(peek[0]) >= 4u;
  g.wo(41);
  if (early) part();
#pragma unroll 1
  for (int i = 38; i < 41; ++i) {
    if (early) asm volatile("s_waitcnt vmcnt(16)\n\ts_barrier" ::: "memory");
    else asm volatile("s_waitcnt vmcnt(12)\n\ts_barrier" ::: "memory");
    if (i < 40) g.wo(i + 4);
  }
  asm volatile("s_waitcnt vmcnt(6)\n\ts_barrier" ::: "memory");
  if (!early) { wait_ge(flags + (FL_P + blockIdx.x) * 32, 4u); part(); }
  asm volatile("s_waitcnt vmcnt(0)\n\ts_barrier" ::: "memory");
}

DI void g2_compute(const u16* __restrict__ x, const u16* __restrict__ b2, char* __restrict__ ws, u16* __restrict__ out,
                   char* smem, const int w, const int l) {
  const int xcd = blockIdx.x & 7, idx = blockIdx.x >> 3;
  const int kh = xcd & 1, mt = ((xcd >> 1) & 1) * 8 + (idx & 7), nt = (xcd >> 2) * 4 + (idx >> 3);
  unsigned* flags = (unsigned*)(ws + WS_FLAG);
  const int h = l >> 5, wm = w & 1, wn = w >> 1;
  f32x16 accg[2] = {}, accp[2] = {}, accs[2] = {};
  const int col0 = nt * 128 + kh * 64 + wn * 32;
  const int row0 = mt * 128 + wm * 64 + (l & 31);
  unsigned stw[2];
  u32x4 xv[4];
  u32x2 bv[4];
#pragma unroll
  for (int m = 0; m < 2; ++m) stw[m] = __hip_atomic_load((gu32*)(ws + WS_STAT) + row0 + m * 32, RLX_AGENT);
#pragma unroll
  for (int k = 0; k < 4; ++k) xv[k] = *(const u32x4*)(x + (size_t)(row0 + (k >> 1) * 32) * DM + col0 + 16 * (k & 1) + 8 * h);
#pragma unroll
  for (int k = 0; k < 4; ++k) bv[k] = *(const u32x2*)(b2 + col0 + 8 * k + 4 * h);
  const LAS char* lbase = (const LAS char*)smem + l * 16;
  const int btr = (4 * h + ((l & 15) >> 2)) * 64 + ((l >> 4) & 1) * 32 + (l & 3) * 8 - l * 16;
#define LDB(p_) cat8(lds_tr(p_), lds_tr((p_) + 512))
  const int ao = wm * 8192;
  const int bG = G2_BOFF + (kh * 2 + wn) * 1024 + btr, bP = G2_BOFF + ((kh ^ 1) * 2 + wn) * 1024 + btr;
  const int bW = G2_BOFF + wn * 1024 + btr;
  bf16x8 fa0[2], fb0[2], fa1[2], fb1[2];
#define G2_RD(FA, FB, base, ks) do { \
    FA[0] = lds_b128((base) + ao + (ks) * 1024); FB[0] = LDB((base) + bG + (ks) * 4096); \
    FA[1] = lds_b128((base) + ao + 4096 + (ks) * 1024); FB[1] = LDB((base) + bP + (ks) * 4096); \
  } while (0)
#define G2_MM(FA, FB) do { \
    accg[0] = MFMA32(FB[0], FA[0], accg[0]); accg[1] = MFMA32(FB[0], FA[1], accg[1]); \
    accp[0] = MFMA32(FB[1], FA[0], accp[0]); accp[1] = MFMA32(FB[1], FA[1], accp[1]); \
  } while (0)
#define G2_SCHED(NR) do { \
    __builtin_amdgcn_sched_group_barrier(0x008, 1, 0); __builtin_amdgcn_sched_group_barrier(0x100, NR, 0); \
    __builtin_amdgcn_sched_group_barrier(0x008, 1, 0); __builtin_amdgcn_sched_group_barrier(0x100, NR, 0); \
    __builtin_amdgcn_sched_group_barrier(0x008, 2, 0); SBAR(); \
  } while (0)
#define G2_BAR() do { asm volatile("s_waitcnt lgkmcnt(0)\n\ts_barrier" ::: "memory"); SBAR(); } while (0)

  SBAR();
  G2_RD(fa0, fb0, lbase, 0); SBAR();
  G2_RD(fa1, fb1, lbase, 1); G2_MM(fa0, fb0); G2_SCHED(3);
  G2_RD(fa0, fb0, lbase, 2); G2_MM(fa1, fb1); G2_SCHED(3);
  G2_RD(fa1, fb1, lbase, 3); G2_MM(fa0, fb0); G2_SCHED(3);
#pragma unroll 1
  for (int i = 0; i < 27; ++i) {
    const LAS char* nb = lbase + ((i + 1) & 3) * G2_SLOT;
    if (i > 0) G2_BAR();
    G2_RD(fa0, fb0, nb, 0); G2_MM(fa1, fb1); G2_SCHED(3);
    G2_RD(fa1, fb1, nb, 1); G2_MM(fa0, fb0); G2_SCHED(3);
    G2_RD(fa0, fb0, nb, 2); G2_MM(fa1, fb1); G2_SCHED(3);
    G2_RD(fa1, fb1, nb, 3); G2_MM(fa0, fb0); G2_SCHED(3);
    if (i == 3) { publish(flags + (FL_O + (blockIdx.x >> 4)) * 32, l); SBAR(); }
  }
  bf16x8 ga0[4], gb0[2], ga1[4], gb1[2];
#define W_RD(GA, GB, base, ks) do { \
    GA[0] = lds_b128((base) + ao + (ks) * 1024); GB[0] = LDB((base) + bW + (ks) * 4096); GA[1] = lds_b128((base) + ao + 4096 + (ks) * 1024); \
    GA[2] = lds_b128((base) + ao + (ks) * 1024 + 1024); GB[1] = LDB((base) + bW + (ks) * 4096 + 4096); GA[3] = lds_b128((base) + ao + 4096 + (ks) * 1024 + 1024); \
  } while (0)
#define W_MM(GA, GB) do { \
    accs[0] = MFMA32(GB[0], GA[0], accs[0]); accs[1] = MFMA32(GB[0], GA[1], accs[1]); \
    accs[0] = MFMA32(GB[1], GA[2], accs[0]); accs[1] = MFMA32(GB[1], GA[3], accs[1]); \
  } while (0)
#define W_STEP(i) do { const LAS char* nb_ = lbase + (((i) + 1) & 3) * G2_SLOT; G2_BAR(); \
    W_RD(ga0, gb0, nb_, 0); W_MM(ga1, gb1); G2_SCHED(4); \
    W_RD(ga1, gb1, nb_, 2); W_MM(ga0, gb0); G2_SCHED(4); } while (0)
  char* pdst = ws + WS_PART + (size_t)blockIdx.x * 16384 + w * 4096 + l * 16;
#define G2_SEND(q) do { const int b_ = 8 * ((q) & 1); \
    store16_dev(pdst + (q) * 1024, (u32x4){cvtpk(accp[(q) >> 1][b_], accp[(q) >> 1][b_ + 1]), cvtpk(accp[(q) >> 1][b_ + 2], accp[(q) >> 1][b_ + 3]), \
                                           cvtpk(accp[(q) >> 1][b_ + 4], accp[(q) >> 1][b_ + 5]), cvtpk(accp[(q) >> 1][b_ + 6], accp[(q) >> 1][b_ + 7])}); SBAR(); } while (0)
  G2_BAR();
  W_RD(ga0, gb0, lbase, 0); G2_MM(fa1, fb1); G2_SCHED(4);
  W_RD(ga1, gb1, lbase, 2); W_MM(ga0, gb0); G2_SCHED(4);
  G2_SEND(0); G2_SEND(1);
  W_STEP(28); G2_SEND(2); G2_SEND(3);
#pragma unroll 1
  for (int i = 29; i < 42; ++i) {
    W_STEP(i);
    if (i == 32) { publish(flags + (FL_P + (blockIdx.x ^ 1)) * 32, l); SBAR(); }
  }
  W_STEP(42);
  W_MM(ga1, gb1);
  {
    const LAS char* xp = (const LAS char*)smem + G2_PART + w * 4096 + l * 16;
#pragma unroll
    for (int m = 0; m < 2; ++m) {
      const float den = bf_hi(stw[m]), rd2 = 1.0f / (den * den);
#pragma unroll
      for (int k = 0; k < 2; ++k) {
        u32x4 xx = xv[m * 2 + k];
        { auto r0 = __builtin_amdgcn_permlane32_swap(xx[0], xx[2], false, false); xx[0] = r0[0]; xx[2] = r0[1];
          auto r1 = __builtin_amdgcn_permlane32_swap(xx[1], xx[3], false, false); xx[1] = r1[0]; xx[3] = r1[1]; }
        unsigned pk[4];
        const u32x4 tp = *(const LAS u32x4*)(xp + (m * 2 + k) * 1024);
#pragma unroll
        for (int ii = 0; ii < 2; ++ii) {
          const int i = 2 * k + ii;
          float r[4];
#pragma unroll
          for (int j = 0; j < 4; ++j) {
            const unsigned xw_ = xx[2 * ii + (j >> 1)], bw_ = bv[i][j >> 1], pw_ = tp[2 * ii + (j >> 1)];
            const float z = (accg[m][4 * i + j] + ((j & 1) ? bf_hi(pw_) : bf_lo(pw_))) * rd2 + ((j & 1) ? bf_hi(bw_) : bf_lo(bw_));
            const float sg = z * __builtin_amdgcn_rcpf(1.0f + __builtin_amdgcn_exp2f(-1.4426950408889634f * z));
            r[j] = ((j & 1) ? bf_hi(xw_) : bf_lo(xw_)) + accs[m][4 * i + j] * sg;
          }
          pk[2 * ii] = cvtpk(r[0], r[1]); pk[2 * ii + 1] = cvtpk(r[2], r[3]);
        }
        auto r0 = __builtin_amdgcn_permlane32_swap(pk[0], pk[2], false, false);
        auto r1 = __builtin_amdgcn_permlane32_swap(pk[1], pk[3], false, false);
        *(u32x4*)(out + (size_t)(row0 + m * 32) * DM + col0 + 16 * k + 8 * h) = (u32x4){r0[0], r1[0], r0[1], r1[1]};
      }
    }
  }
#undef G2_RD
#undef G2_MM
#undef G2_SCHED
#undef G2_BAR
#undef G2_SEND
#undef W_RD
#undef W_MM
#undef W_STEP
#undef LDB
}

__global__ __launch_bounds__(512, 2) void main_kernel(const u16* __restrict__ x, const u16* __restrict__ wq, const u16* __restrict__ wk,
                                                      const u16* __restrict__ wv, const u16* __restrict__ wo, const u16* __restrict__ w1,
                                                      const u16* __restrict__ b1, const u16* __restrict__ w2, const u16* __restrict__ b2,
                                                      char* __restrict__ ws, u16* __restrict__ out) {
  extern __shared__ __attribute__((aligned(16))) char smem[];
  const int l = threadIdx.x & 63, w = rfl(threadIdx.x >> 6);
  const unsigned lds0 = (unsigned)(size_t)(LAS char*)smem;
  const int xcd = blockIdx.x & 7, idx = blockIdx.x >> 3;
  const int mtile = (xcd & 1) * 4 + (idx & 3), cg = (idx >> 2) * 4 + (xcd >> 1);
  unsigned* flags = (unsigned*)(ws + WS_FLAG);
  if (w == 3) {
    unsigned long long pc; unsigned len;
    asm volatile("s_getpc_b64 %0\n.Lmk_code_begin:\n\ts_mov_b32 %1, .Lmk_code_end-.Lmk_code_begin" : "=s"(pc), "=s"(len));
    unsigned off = (unsigned)(idx * 16 + (l & 15)) * 128u;
    off = (off < len && len <= 65536u) ? off : 0u;
    l2_touch((const char*)pc + off, lds0 + G1_HS + 3072);
  }
  if (w >= 4) {
    if (cg > 8) g1_load<G1_FFN>(ws, x, w1, nullptr, cg, mtile, smem, w - 4, l);
    else if (cg < 8) g1_load<G1_Q>(ws, x, wq, nullptr, cg, mtile, smem, w - 4, l);
    else g1_load<G1_KV>(ws, x, wk, wv, cg, mtile, smem, w - 4, l);
    attn_phase<true>(ws, w2, wo, smem, w, l, nullptr);
    g2_load(w2, wo, ws, lds0, w - 4, l);
    asm volatile(".Lmk_code_end:");
  } else {
    unsigned* late = nullptr;
    if (cg > 8) { g1_compute<G1_FFN>(ws, x, b1, cg, mtile, smem, w, l); late = flags + (FL_H + mtile) * 32; }
    else if (cg < 8) { g1_compute<G1_Q>(ws, x, b1, cg, mtile, smem, w, l); publish(flags + (FL_Q + mtile * 8 + cg) * 32, l); }
    else { g1_compute<G1_KV>(ws, x, b1, cg, mtile, smem, w, l); publish(flags + FL_KV * 32, l); }
    attn_phase<false>(ws, w2, wo, smem, w, l, late);
    g2_compute(x, b2, ws, out, smem, w, l);
  }
}

extern "C" void kernel_launch(void* const* d_in, const int* in_sizes, int n_in, void* d_out, int out_size, void* d_ws,
                              size_t ws_size, hipStream_t stream) {
  const u16* x = (const u16*)d_in[0];
  const u16* wq = (const u16*)d_in[1];
  const u16* wk = (const u16*)d_in[2];
  const u16* wv = (const u16*)d_in[3];
  const u16* wo = (const u16*)d_in[4];
  const u16* w1 = (const u16*)d_in[6];
  const u16* b1 = (const u16*)d_in[7];
  const u16* w2 = (const u16*)d_in[8];
  const u16* b2 = (const u16*)d_in[9];
  char* ws = (char*)d_ws;
  (void)hipFuncSetAttribute((const void*)main_kernel, hipFuncAttributeMaxDynamicSharedMemorySize, MAIN_LDS);
  main_kernel<<<256, 512, MAIN_LDS, stream>>>(x, wq, wk, wv, wo, w1, b1, w2, b2, ws, (u16*)d_out);
}
```

```cpp
#include <hip/hip_runtime.h>
#include <hip/hip_bf16.h>

typedef short bf16x8 __attribute__((ext_vector_type(8)));
typedef short s16x4 __attribute__((ext_vector_type(4)));
typedef float f32x16 __attribute__((ext_vector_type(16)));
typedef float f32x4 __attribute__((ext_vector_type(4)));
typedef float f32x2 __attribute__((ext_vector_type(2)));
typedef __bf16 bf16x2_t __attribute__((ext_vector_type(2)));
typedef unsigned u32x4 __attribute__((ext_vector_type(4)));
typedef unsigned u32x2 __attribute__((ext_vector_type(2)));
typedef unsigned u32x8 __attribute__((ext_vector_type(8)));
typedef __bf16 bf16x8_t __attribute__((ext_vector_type(8)));
typedef __bf16 bf16x16_t __attribute__((ext_vector_type(16)));
typedef unsigned short u16;
#define LAS __attribute__((address_space(3)))
#define DI __device__ __forceinline__
#define SBAR() __builtin_amdgcn_sched_barrier(0)
#define PIN(x) asm volatile("" : "+v"(x))
#define MFMA32(a, b, c) __builtin_amdgcn_mfma_f32_32x32x16_bf16((a), (b), (c), 0, 0, 0)

constexpr int DM = 1024, FF = 3584;
constexpr size_t KT = 262144;
constexpr size_t WS_STAT = 0;
constexpr size_t WS_Q = 4u << 20;
constexpr size_t WS_KV = 8u << 20;
constexpr size_t WS_O = 9u << 20;
constexpr size_t WS_H = 13u << 20;
constexpr size_t WS_PART = 28u << 20;
constexpr size_t WS_FLAG = 37u << 20;
constexpr int FL_P = 0, FL_O = 256, FL_H = 272, FL_Q = 280, FL_KV = 344, FL_S = 345, FL_N = 353;
constexpr size_t WS_HSA = WS_FLAG + 65536, WS_HSB = WS_HSA + 4096, WS_HSC = WS_HSB + 4096;
constexpr int COORD_BLOCK = 64;

DI unsigned rfl(unsigned x) { return __builtin_amdgcn_readfirstlane(x); }
DI unsigned cvtpk(float lo, float hi) {
  f32x2 v = {lo, hi};
  bf16x2_t b = __builtin_convertvector(v, bf16x2_t);
  return __builtin_bit_cast(unsigned, b);
}
DI float fadd_s(float a, float b) { float r; asm("v_add_f32_e32 %0, %1, %2" : "=v"(r) : "v"(a), "v"(b)); return r; }
DI float bf_lo(unsigned u) { return __uint_as_float(u << 16); }
DI float bf_hi(unsigned u) { return __uint_as_float(u & 0xffff0000u); }
DI float bf_round(float x) { return bf_lo(cvtpk(x, x)); }

template <int IMM>
DI void glds16(unsigned voff, const void* sbase, unsigned ldsaddr) {
  asm volatile("s_mov_b32 m0, %2\n\ts_nop 0\n\tglobal_load_lds_dwordx4 %0, %1 offset:%3"
               :: "v"(voff), "s"(sbase), "s"(ldsaddr), "i"(IMM) : "memory");
}
template <int IMM>
DI void glds16_dev(unsigned voff, const void* sbase, unsigned ldsaddr) {
  asm volatile("s_mov_b32 m0, %2\n\ts_nop 0\n\tglobal_load_lds_dwordx4 %0, %1 offset:%3 sc1"
               :: "v"(voff), "s"(sbase), "s"(ldsaddr), "i"(IMM) : "memory");
}
DI void peek_dev(const void* word, unsigned ldsaddr) {
  asm volatile("s_mov_b32 m0, %2\n\ts_nop 0\n\tglobal_load_lds_dword %0, %1 sc1" :: "v"(0u), "s"(word), "s"(ldsaddr) : "memory");
}
DI void store16_dev(void* p, u32x4 v) {
  asm volatile("global_store_dwordx4 %0, %1, off sc1\n\ts_nop 1" :: "v"(p), "v"(v) : "memory");
}
DI void l2_touch(const void* p, unsigned ldsaddr) {
  asm volatile("s_mov_b32 m0, %1\n\ts_nop 0\n\tglobal_load_lds_dword %0, off" :: "v"(p), "s"(ldsaddr) : "memory");
}
constexpr unsigned SPIN_CAP = 1u << 20;
typedef __attribute__((address_space(1))) unsigned gu32;
#define RLX_AGENT __ATOMIC_RELAXED, __HIP_MEMORY_SCOPE_AGENT
DI void wait_ge(const unsigned* word, unsigned want) {
#pragma unroll 1
  for (unsigned spins = 0; spins < SPIN_CAP; ++spins) {
    if (rfl(__hip_atomic_load((gu32*)word, RLX_AGENT)) >= want) break;
    __builtin_amdgcn_s_sleep(8);
  }
  __builtin_amdgcn_fence(__ATOMIC_ACQUIRE, "wavefront");
}
DI void publish(unsigned* word, int l) {
  asm volatile("s_waitcnt vmcnt(0)" ::: "memory");
  if (l == 0) __hip_atomic_fetch_add((gu32*)word, 1u, RLX_AGENT);
}
DI bf16x8 load16_dev(const char* p) {
  typedef __attribute__((address_space(1))) unsigned long long gu64;
  const unsigned long long a = __hip_atomic_load((gu64*)p, RLX_AGENT), b = __hip_atomic_load((gu64*)p + 1, RLX_AGENT);
  return __builtin_bit_cast(bf16x8, (u32x4){(unsigned)a, (unsigned)(a >> 32), (unsigned)b, (unsigned)(b >> 32)});
}
DI float relu_s(float a) { return __builtin_amdgcn_fmed3f(a, 0.0f, 3.4028234663852886e38f); }
DI bf16x8 lds_b128(const LAS char* p) { return *(const LAS bf16x8*)p; }
DI s16x4 lds_tr(const LAS char* p) {
  return __builtin_amdgcn_ds_read_tr16_b64_v4i16((LAS s16x4*)p);
}
DI bf16x8 cat8(s16x4 lo, s16x4 hi) { return __builtin_shufflevector(lo, hi, 0, 1, 2, 3, 4, 5, 6, 7); }

DI float half_sum(float v) {
  v += __uint_as_float(__builtin_amdgcn_update_dpp(0u, __float_as_uint(v), 0xB1, 0xf, 0xf, true));
  v += __uint_as_float(__builtin_amdgcn_update_dpp(0u, __float_as_uint(v), 0x4E, 0xf, 0xf, true));
  v += __uint_as_float(__builtin_amdgcn_update_dpp(0u, __float_as_uint(v), 0x141, 0xf, 0xf, true));
  v += __uint_as_float(__builtin_amdgcn_update_dpp(0u, __float_as_uint(v), 0x140, 0xf, 0xf, true));
  auto r = __builtin_amdgcn_permlane16_swap(__float_as_uint(v), __float_as_uint(v), false, false);
  return __uint_as_float(r[0]) + __uint_as_float(r[1]);
}
DI unsigned row_stats(const u32x4 (&v)[4]) {
  f32x2 e[16], s2 = {0.f, 0.f};
#pragma unroll
  for (int i = 0; i < 16; ++i) { e[i] = (f32x2){bf_lo(v[i >> 2][i & 3]), bf_hi(v[i >> 2][i & 3])}; s2 += e[i]; }
  const float mean = half_sum(s2[0] + s2[1]) * (1.0f / DM);
  f32x2 q2 = {0.f, 0.f};
#pragma unroll
  for (int i = 0; i < 16; ++i) { const f32x2 d = e[i] - mean; q2 += d * d; }
  const float q = half_sum(q2[0] + q2[1]);
  const float den = __builtin_sqrtf(bf_round(bf_round(q * (1.0f / DM)) + 1e-5f));
  return cvtpk(-mean, den);
}

constexpr int G1_STAGE = 53248, G1_BOFF = 32768;
enum { G1_FFN = 5, G1_Q = 4, G1_KV = 2 };

constexpr int G1_ST = 87040;
constexpr int AT_RDY = 89088;
constexpr int G1_HS = 159744;
constexpr int G1_CS = 81920;
constexpr int ESTART = 3, PSTART = 9;
DI float pair_sum(unsigned v) { return fadd_s(bf_lo(v), bf_hi(v)); }

template <int NP>
DI void g1_load(char* ws, const u16* x, const u16* wsrc, const u16* wv, int cg, int mtile, char* smem, int lw, int l) {
  constexpr int LD = (NP == G1_FFN) ? FF : (NP == G1_Q) ? DM : 32;
  const unsigned lds0 = (unsigned)(size_t)(LAS char*)smem;
  const char* abase = (const char*)(x + (size_t)(mtile * 256 + lw * 64) * DM);
  const unsigned ave = (unsigned)((l >> 3) * 2048 + (((l & 7) ^ (l >> 4)) << 4)), avo = ave ^ 64u;
  const unsigned bvoff = (unsigned)((lw * 16 + (l >> 2)) * LD * 2 + (l & 3) * 16);
  const int krot = NP == G1_Q ? (mtile & 3) * 4 : 0;
  const char* bbase[NP];
#pragma unroll
  for (int p = 0; p < NP; ++p) {
    if (NP == G1_KV) bbase[p] = (const char*)(p ? wv : wsrc);
    else if (NP == G1_Q) bbase[p] = (const char*)(wsrc + (cg * 4 + p) * 32);
    else { const int cb = (cg - 9) * 5 + p; bbase[p] = (const char*)(wsrc + (cb > 111 ? 111 : cb) * 32); }
  }
  float cs[NP];
#pragma unroll
  for (int p = 0; p < NP; ++p) cs[p] = 0.f;
  const LAS char* cb0 = (const LAS char*)smem + G1_BOFF + lw * 1024 + (8 * (l >> 5) + ((l & 15) >> 2)) * 64 + ((l >> 4) & 1) * 32 + (l & 3) * 8;
  u32x2 t0[NP], t1[NP];
  auto cs_read = [&](int slot) {
#pragma unroll
    for (int p = 0; p < NP; ++p) {
      t0[p] = __builtin_bit_cast(u32x2, lds_tr(cb0 + slot * G1_STAGE + p * 4096));
      t1[p] = __builtin_bit_cast(u32x2, lds_tr(cb0 + slot * G1_STAGE + p * 4096 + 256));
    }
    SBAR();
  };
  auto cs_add = [&](int p) {
    cs[p] = fadd_s(cs[p], fadd_s(fadd_s(pair_sum(t0[p][0]), pair_sum(t0[p][1])), fadd_s(pair_sum(t1[p][0]), pair_sum(t1[p][1]))));
    PIN(cs[p]); SBAR();
  };
  auto issue = [&](int st, int slot, bool sum) {
    const int kt = (st + krot) & 15;
    const unsigned sa = lds0 + slot * G1_STAGE + lw * 8192;
    const char* a0 = abase + kt * 128;
    const unsigned sb = lds0 + slot * G1_STAGE + G1_BOFF + lw * 1024;
#pragma unroll
    for (int p = 0; p < NP; ++p) glds16<0>(bvoff, bbase[p] + (size_t)kt * (64 * LD * 2), sb + p * 4096);
#pragma unroll
    for (int i = 0; i < 8; ++i) {
      if (sum && i < NP) { SBAR(); cs_add(i); }
      glds16<0>((i & 1) ? avo : ave, a0 + i * 16384, sa + i * 1024);
    }
  };
  auto colsum = [&](int slot) {
    cs_read(slot);
#pragma unroll
    for (int p = 0; p < NP; ++p) cs_add(p);
  };
  unsigned* flags = (unsigned*)(ws + WS_FLAG);
  const bool coord = (NP == G1_KV) && blockIdx.x == COORD_BLOCK && lw == 1;
  if (coord) {
#pragma unroll
    for (int i = 0; i < 6; ++i) if (i * 64 + l < FL_N) __hip_atomic_store((gu32*)flags + (i * 64 + l) * 32, 0u, RLX_AGENT);
    store16_dev(ws + WS_HSC + l * 16, (u32x4){0u, 0u, 0u, 0u});
    asm volatile("s_waitcnt vmcnt(0)" ::: "memory");
  }
  issue(0, 0, false); issue(1, 1, false); issue(2, 2, false);
  asm volatile("s_waitcnt vmcnt(%0)\n\ts_barrier" :: "n"(2 * (8 + NP)) : "memory");
  __builtin_amdgcn_s_setprio(2);
  int state = (lw == 0 || coord) ? 0 : 2;
  bool pend = false, first = true;
  u32x4 last = {0u, 0u, 0u, 0u};
  const unsigned found = rfl(*(const LAS unsigned*)((const LAS char*)smem + G1_HS));
  if (lw == 0 && l < 9) {
    const unsigned v = *(const LAS unsigned*)((const LAS char*)smem + G1_HS + 16 + (l & 7) * 4);
    __hip_atomic_store(l < 8 ? (gu32*)(ws + WS_STAT) + mtile * 256 + cg * 8 + l : (gu32*)(ws + WS_HSA) + blockIdx.x, l < 8 ? v : found + 1u, RLX_AGENT);
  }
  colsum(0);
  auto poll_issue = [&]() {
    if (NP == G1_KV && coord) {
      glds16_dev<0>(l * 16, ws + WS_HSA, lds0 + G1_HS + 1024); glds16_dev<0>(l * 16, ws + WS_HSC, lds0 + G1_HS + 2048);
    } else {
      const char* a_ = state == 0 ? ws + WS_HSB + blockIdx.x * 4 : (const char*)(flags + (FL_S + mtile) * 32);
      peek_dev(a_, lds0 + G1_HS + 256);
    }
  };
  auto poll_check = [&]() {
    if (NP == G1_KV && coord) {
      const u32x4 pa = *(const LAS u32x4*)((const LAS char*)smem + G1_HS + 1024 + l * 16), pc = *(const LAS u32x4*)((const LAS char*)smem + G1_HS + 2048 + l * 16);
      if (first || pa[0] != last[0] || pa[1] != last[1] || pa[2] != last[2] || pa[3] != last[3]) store16_dev(ws + WS_HSB + l * 16, pa);
      last = pa; first = false;
      if (__all((pc[0] & pc[1] & pc[2] & pc[3]) == 1u)) state = 2;
    } else {
      const unsigned v = rfl(*(const LAS unsigned*)((const LAS char*)smem + G1_HS + 256));
      if (state == 0) {
        if (v != found) {
          if (l == 0) {
            __hip_atomic_store((gu32*)(ws + WS_HSC) + blockIdx.x, 1u, RLX_AGENT);
            __hip_atomic_fetch_add((gu32*)flags + (FL_S + mtile) * 32, 1u, RLX_AGENT);
          }
          state = 1;
        }
      } else if (v >= 32u) state = 2;
    }
    state = rfl(state);
  };
  int slot = 0;
#pragma unroll 1
  for (int i = 0; i < 13; ++i) {
    asm volatile("s_waitcnt vmcnt(%0)\n\ts_barrier" :: "n"(8 + NP) : "memory");
    if (state < 2 && (i >= ESTART || coord)) {
      if (pend) poll_check();
      pend = state == 0 || (state == 1 && i >= PSTART);
      if (pend) poll_issue();
    }
    const int nslot = (slot == 2) ? 0 : slot + 1;
    cs_read(nslot);
    issue(i + 3, slot, true);
    slot = nslot;
  }
#pragma unroll 1
  for (unsigned spins = 0; state < 2 && spins < SPIN_CAP; ++spins) {
    if (!pend) poll_issue();
    asm volatile("s_waitcnt vmcnt(0)" ::: "memory");
    poll_check();
    pend = false;
    __builtin_amdgcn_s_sleep(8);
  }
  asm volatile("s_waitcnt vmcnt(%0)\n\ts_barrier" :: "n"(8 + NP) : "memory");
  if (lw == 0) { glds16_dev<0>(l * 16, ws + WS_STAT + mtile * 1024, lds0 + G1_ST); *(LAS u32x2*)((LAS char*)smem + AT_RDY) = (u32x2){0u, 0u}; }
  colsum(2);
  asm volatile("s_waitcnt vmcnt(0)" ::: "memory");
  colsum(0);
#pragma unroll
  for (int p = 0; p < NP; ++p) *(LAS float*)((LAS char*)smem + G1_CS + p * 1024 + (l & 31) * 32 + (lw * 2 + (l >> 5)) * 4) = cs[p];
  asm volatile("s_waitcnt lgkmcnt(0)\n\ts_barrier" ::: "memory");
}

template <int NP>
DI void g1_compute(char* __restrict__ ws, const u16* __restrict__ x, const u16* __restrict__ b1, int cg, int mtile, char* smem, int w, int l) {
  const int h = l >> 5;
  unsigned st[2], bias[NP];
  {
    const int row = mtile * 256 + cg * 8 + w * 2 + h;
    u32x4 v[4];
#pragma unroll
    for (int i = 0; i < 4; ++i) v[i] = *(const u32x4*)(x + (size_t)row * DM + (i * 32 + (l & 31)) * 8);
    const unsigned found = __hip_atomic_load((gu32*)(ws + WS_HSB) + blockIdx.x, RLX_AGENT);
#pragma unroll
    for (int p = 0; p < NP; ++p) {
      int cb = (cg - 9) * 5 + p; cb = cb > 111 ? 111 : cb;
      bias[p] = (NP == G1_FFN) ? (unsigned)b1[cb * 32 + (l & 31)] : 0u;
    }
    SBAR();
    const unsigned rs = row_stats(v);
    if ((l & 31) == 0) *(LAS unsigned*)((LAS char*)smem + G1_HS + 16 + (w * 2 + h) * 4) = rs;
    if (w == 0 && l == 0) *(LAS unsigned*)((LAS char*)smem + G1_HS) = found;
  }
  f32x16 acc[2][NP];
#pragma unroll
  for (int p = 0; p < NP; ++p) { acc[0][p] = f32x16{}; acc[1][p] = f32x16{}; }
  const LAS char* lbase = (const LAS char*)smem;
  int aoff[4];
#pragma unroll
  for (int ks = 0; ks < 4; ++ks) aoff[ks] = (w * 64 + (l & 31)) * 128 + (((2 * ks + h) ^ ((l >> 1) & 7)) << 4);
  const int boff = G1_BOFF + (8 * h + ((l & 15) >> 2)) * 64 + ((l >> 4) & 1) * 32 + (l & 3) * 8;
  bf16x8 fa0[2], fb0[NP], fa1[2], fb1[NP];
#define G1_RD(FA, FB, base, ks) do { \
    FA[0] = lds_b128((base) + aoff[ks]); FA[1] = lds_b128((base) + aoff[ks] + 4096); \
    _Pragma("unroll") for (int p_ = 0; p_ < NP; ++p_) \
      FB[p_] = cat8(lds_tr((base) + boff + p_ * 4096 + (ks) * 1024), lds_tr((base) + boff + p_ * 4096 + (ks) * 1024 + 256)); \
  } while (0)
#define G1_MM(FA, FB) do { \
    _Pragma("unroll") for (int p_ = 0; p_ < NP; ++p_) { \
      if (NP == G1_KV && p_ == 1) { acc[0][p_] = MFMA32(FA[0], FB[p_], acc[0][p_]); acc[1][p_] = MFMA32(FA[1], FB[p_], acc[1][p_]); } \
      else { acc[0][p_] = MFMA32(FB[p_], FA[0], acc[0][p_]); acc[1][p_] = MFMA32(FB[p_], FA[1], acc[1][p_]); } } \
  } while (0)
#define G1_SCHED() do { \
    _Pragma("unroll") for (int q_ = 0; q_ < NP + 1; ++q_) { \
      __builtin_amdgcn_sched_group_barrier(0x008, 1, 0); __builtin_amdgcn_sched_group_barrier(0x100, 2, 0); } \
    __builtin_amdgcn_sched_group_barrier(0x008, NP - 1, 0); SBAR(); \
  } while (0)

  asm volatile("s_waitcnt lgkmcnt(0)\n\ts_barrier" ::: "memory");
  G1_RD(fa0, fb0, lbase, 0); SBAR();
  G1_RD(fa1, fb1, lbase, 1); G1_MM(fa0, fb0); G1_SCHED();
  G1_RD(fa0, fb0, lbase, 2); G1_MM(fa1, fb1); G1_SCHED();
  G1_RD(fa1, fb1, lbase, 3); G1_MM(fa0, fb0); G1_SCHED();
  int slot = 1;
#pragma unroll 1
  for (int i = 0; i < 15; ++i) {
    const LAS char* nb = lbase + slot * G1_STAGE;
    asm volatile("s_waitcnt lgkmcnt(0)\n\ts_barrier" ::: "memory"); SBAR();
    G1_RD(fa0, fb0, nb, 0); G1_MM(fa1, fb1); G1_SCHED();
    G1_RD(fa1, fb1, nb, 1); G1_MM(fa0, fb0); G1_SCHED();
    G1_RD(fa0, fb0, nb, 2); G1_MM(fa1, fb1); G1_SCHED();
    G1_RD(fa1, fb1, nb, 3); G1_MM(fa0, fb0); G1_SCHED();
    slot = (slot == 2) ? 0 : slot + 1;
  }
  __builtin_amdgcn_s_setprio(3);
  {
    f32x4 t[NP][2];
#pragma unroll
    for (int m = 0; m < 2; ++m) st[m] = *(const LAS unsigned*)(lbase + G1_ST + (w * 64 + m * 32 + (l & 31)) * 4);
#pragma unroll
    for (int p = 0; p < NP; ++p) {
      t[p][0] = *(const LAS f32x4*)(lbase + G1_CS + p * 1024 + (l & 31) * 32);
      t[p][1] = *(const LAS f32x4*)(lbase + G1_CS + p * 1024 + (l & 31) * 32 + 16);
    }
    SBAR();
    G1_MM(fa1, fb1);
    const unsigned msk = h ? 0u : ~0u;
#pragma unroll
    for (int m = 0; m < 2; ++m) {
      const unsigned nm = st[m] & 0xffffu;
      fa0[m] = __builtin_bit_cast(bf16x8, (u32x4){(nm | (nm << 16)) & msk, st[m] & msk, 0u, 0u});
    }
#pragma unroll
    for (int p = 0; p < NP; ++p) {
      const f32x4 u = t[p][0] + t[p][1];
      const float c = (u[0] + u[1]) + (u[2] + u[3]);
      const unsigned c0 = cvtpk(c, c) & 0xffffu; const float r1 = c - bf_lo(c0);
      const unsigned c1 = cvtpk(r1, r1) & 0xffffu; const float r2 = r1 - bf_lo(c1);
      const unsigned c2 = cvtpk(r2, r2) & 0xffffu;
      fb0[p] = __builtin_bit_cast(bf16x8, (u32x4){c0 | (c1 << 16), c2 | (bias[p] << 16), 0u, 0u});
    }
  }
  G1_MM(fa0, fb0);
  float rden[2];
#pragma unroll
  for (int m = 0; m < 2; ++m) rden[m] = 1.0f / bf_hi(st[m]);
  SBAR();

#pragma unroll
  for (int p = 0; p < NP; ++p) {
    const int cb = (NP == G1_FFN) ? (cg - 9) * 5 + p : cg * 4 + p;
    if (NP != G1_FFN || cb < 112) {
#pragma unroll
      for (int m = 0; m < 2; ++m) {
        const int tt = mtile * 8 + w * 2 + m;
        char* dst = (NP == G1_FFN) ? ws + WS_H + (size_t)(cb >> 1) * KT + (size_t)tt * 4096 + (cb & 1) * 2048 + l * 16
                  : (NP == G1_Q)   ? ws + WS_Q + (size_t)(tt * 32 + cb) * 2048 + l * 16
                                   : ws + WS_KV + (size_t)tt * 4096 + p * 2048 + l * 16;
        const float sc = (NP == G1_Q) ? rden[m] * (0.17677669529663687f * 1.4426950408889634f) : rden[m];
#pragma unroll
        for (int s = 0; s < 2; ++s) {
          u32x4 o;
#pragma unroll
          for (int j = 0; j < 4; ++j) {
            float a = acc[m][p][8 * s + 2 * j], b = acc[m][p][8 * s + 2 * j + 1];
            if (NP == G1_FFN) { a = relu_s(a); b = relu_s(b); a *= a; b *= b; }
            else if (NP == G1_KV && p == 1) {
              const int r = 8 * s + 2 * j, tk = (r & 3) + 8 * (r >> 2) + 4 * h;
              a *= __shfl(rden[m], tk); b *= __shfl(rden[m], tk + 1);
            } else { a *= sc; b *= sc; }
            o[j] = cvtpk(a, b);
          }
          store16_dev(dst + s * 1024, o);
        }
      }
    }
  }
#undef G1_RD
#undef G1_MM
#undef G1_SCHED
}

constexpr int AT_NCH = 8;
constexpr int AT_BUF = 32768;
constexpr int AT_BASE = 98304;
constexpr int AT_Q = 65536;
constexpr int AT_DUMP = 90112;
constexpr int MAIN_LDS = 163840;

DI float xhalf_max(float m) {
  auto rr = __builtin_amdgcn_permlane32_swap(__float_as_uint(m), __float_as_uint(m), false, false);
  return __builtin_fmaxf(__uint_as_float(rr[0]), __uint_as_float(rr[1]));
}
DI float xhalf_sum(float m) {
  auto rr = __builtin_amdgcn_permlane32_swap(__float_as_uint(m), __float_as_uint(m), false, false);
  return __uint_as_float(rr[0]) + __uint_as_float(rr[1]);
}
DI float max16(const f32x16& s) {
  float a = __builtin_fmaxf(s[0], s[1]);
#pragma unroll
  for (int r = 2; r < 16; ++r) a = __builtin_fmaxf(a, s[r]);
  return a;
}
DI bf16x8 pack8(const f32x16& p, int s) {
  u32x4 o;
#pragma unroll
  for (int j = 0; j < 4; ++j) o[j] = cvtpk(p[8 * s + 2 * j], p[8 * s + 2 * j + 1]);
  return __builtin_bit_cast(bf16x8, o);
}

DI void attn_fallback(const char* __restrict__ kv, bf16x8 q0, bf16x8 q1, int l, f32x16& o_out, float& l_out) {
  f32x16 o = {};
  float m = -INFINITY, lsum = 0.f;
  for (int t = 0; t < 64; ++t) {
    const char* p = kv + (size_t)t * 4096 + l * 16;
    const bf16x8 k0 = load16_dev(p), k1 = load16_dev(p + 1024), v0 = load16_dev(p + 2048), v1 = load16_dev(p + 3072);
    f32x16 s = {};
    s = MFMA32(k0, q0, s); s = MFMA32(k1, q1, s);
    const float mn = __builtin_fmaxf(m, xhalf_max(max16(s)));
    const float ms = (mn == -INFINITY) ? 0.f : mn;
    const float alpha = __builtin_amdgcn_exp2f(m - ms);
    m = mn;
    float ps = 0.f;
#pragma unroll
    for (int r = 0; r < 16; ++r) { s[r] = __builtin_amdgcn_exp2f(s[r] - ms); ps += s[r]; }
    lsum = lsum * alpha + ps;
#pragma unroll
    for (int r = 0; r < 16; ++r) o[r] *= alpha;
    o = MFMA32(v0, pack8(s, 0), o); o = MFMA32(v1, pack8(s, 1), o);
  }
  o_out = o;
  l_out = xhalf_sum(lsum);
}

constexpr int G2_SLOT = 32768, G2_BOFF = 16384, G2_PART = 131072;
struct G2Ld {
  const char* hb; const char* ob; const char* wb; const char* wb2; unsigned lds; unsigned voff, voffb; int rot, frot;
  DI void init(const char* ws, const u16* w2, const u16* wo, unsigned lds0, int lw, int l) {
    const int xcd = blockIdx.x & 7, idx = blockIdx.x >> 3, kh = xcd & 1;
    const int mt = ((xcd >> 1) & 1) * 8 + (idx & 7), nt = (xcd >> 2) * 4 + (idx >> 3);
    hb = ws + WS_H + (size_t)(mt * 4 + lw) * 4096 + (size_t)kh * (28 * KT);
    ob = ws + WS_O + (size_t)(mt * 4 + lw) * 4096;
    wb = (const char*)(w2 + (size_t)(kh * 1792 + lw * 16) * DM + nt * 128);
    wb2 = (const char*)(wo + (size_t)(lw * 16) * DM + nt * 128 + kh * 64);
    voffb = (unsigned)((l >> 2) * DM * 2 + (l & 3) * 16);
    lds = lds0 + lw * 4096; voff = l * 16;
    frot = (7 * (mt & 7)) >> 1;
    rot = 2 * (mt & 7);
  }
  DI void ffn(int s) const {
    const unsigned d = lds + (s & 3) * G2_SLOT;
    s += frot; s = s >= 28 ? s - 28 : s;
    const char* a = hb + (size_t)s * KT;
    glds16_dev<0>(voff, a, d); glds16_dev<1024>(voff, a, d); glds16_dev<2048>(voff, a, d); glds16_dev<3072>(voff, a, d);
    const char* b = wb + (size_t)s * 131072;
    glds16<0>(voffb, b, d + G2_BOFF); glds16<0>(voffb, b + 64, d + G2_BOFF + 1024); glds16<0>(voffb, b + 128, d + G2_BOFF + 2048); glds16<0>(voffb, b + 192, d + G2_BOFF + 3072);
  }
  template <int J> DI void ffn_piece(int s) const {
    const unsigned d = lds + (s & 3) * G2_SLOT;
    s += frot; s = s >= 28 ? s - 28 : s;
    if (J < 4) glds16_dev<(J & 3) * 1024>(voff, hb + (size_t)s * KT, d);
    else glds16<0>(voffb, wb + (size_t)s * 131072 + (J & 3) * 64, d + G2_BOFF + (J & 3) * 1024);
  }
  DI void wo(int s) const {
    const unsigned d = lds + (s & 3) * G2_SLOT;
    const int k = (s - 28 + rot) & 15;
    const char* a = ob + (size_t)k * KT;
    glds16_dev<0>(voff, a, d); glds16_dev<1024>(voff, a, d); glds16_dev<2048>(voff, a, d); glds16_dev<3072>(voff, a, d);
    const char* b = wb2 + (size_t)k * 131072;
    glds16<0>(voffb, b, d + G2_BOFF); glds16<0>(voffb, b + 64, d + G2_BOFF + 1024);
  }
};

DI void attn_finish(char* __restrict__ ws, f32x16 o, f32x4 lacc, bf16x8 q0, bf16x8 q1, int l, char* dst) {
  const float l0 = __shfl(lacc[0] + lacc[1], l & 15), l1 = __shfl(lacc[2] + lacc[3], l & 15);
  float lsum = (l & 16) ? l1 : l0;
  bool ok = (lsum >= 9.5367431640625e-07f) && (lsum <= 1.8446744073709552e19f);
#pragma unroll
  for (int r = 0; r < 16; ++r) ok = ok && (__builtin_fabsf(o[r]) <= 3.0e38f);
  if (!__all(ok)) attn_fallback(ws + WS_KV, q0, q1, l, o, lsum);
  const float rl = 1.0f / lsum;
#pragma unroll
  for (int s = 0; s < 2; ++s) {
    u32x4 ov;
#pragma unroll
    for (int j = 0; j < 4; ++j) ov[j] = cvtpk(o[8 * s + 2 * j] * rl, o[8 * s + 2 * j + 1] * rl);
    store16_dev(dst + s * 1024, ov);
  }
}

template <bool LOADER>
DI void attn_phase(char* __restrict__ ws, const u16* w2, const u16* wo, char* smem, const int w, const int l, unsigned* late_pub) {
  const int tt = blockIdx.x >> 2, hg = blockIdx.x & 3;
  const int head = hg * 8 + w, lw = w & 3;
  const unsigned lds0 = (unsigned)(size_t)(LAS char*)smem;
  unsigned* flags = (unsigned*)(ws + WS_FLAG);
  const unsigned voff = l * 16;
  const char* ksrc = ws + WS_KV + (size_t)lw * 4096;
  const unsigned kdst = lds0 + AT_BASE + lw * 4096;
  auto ld = [&](int c, int half) {
    const char* s_ = ksrc + (size_t)c * AT_BUF + half * 16384;
    const unsigned d_ = kdst + (c & 1) * AT_BUF + half * 16384;
    glds16_dev<0>(voff, s_, d_); glds16_dev<1024>(voff, s_, d_); glds16_dev<2048>(voff, s_, d_); glds16_dev<3072>(voff, s_, d_);
  };
  G2Ld g2;
  const unsigned tdump = lds0 + AT_DUMP + lw * 256;
  const int xcd = blockIdx.x & 7, idx = blockIdx.x >> 3, gk = xcd & 1;
  const int mt = ((xcd >> 1) & 1) * 8 + (idx & 7), nt = (xcd >> 2) * 4 + (idx >> 3);
  if (LOADER) {
    if (lw == 0) {
      const unsigned* word = flags + (l == 0 ? FL_KV : FL_Q + (tt >> 3) * 8 + hg * 2 + (l & 1)) * 32;
      const unsigned need = l == 0 ? 32u : 4u;
      bool done = l > 2;
#pragma unroll 1
      for (unsigned spins = 0; spins < SPIN_CAP; ++spins) {
        if (!done) done = __hip_atomic_load((gu32*)word, RLX_AGENT) >= need;
        if (__all(done)) break;
        __builtin_amdgcn_s_sleep(8);
      }
      if (l == 0) __hip_atomic_store((LAS unsigned*)((LAS char*)smem + AT_RDY + 4), 1u, __ATOMIC_RELAXED, __HIP_MEMORY_SCOPE_WORKGROUP);
    } else {
#pragma unroll 1
      for (unsigned spins = 0; spins < 64u * SPIN_CAP; ++spins) {
        if (rfl(__hip_atomic_load((LAS unsigned*)((LAS char*)smem + AT_RDY + 4), __ATOMIC_RELAXED, __HIP_MEMORY_SCOPE_WORKGROUP))) break;
        __builtin_amdgcn_s_sleep(1);
      }
    }
    asm volatile("" ::: "memory");
    const char* qs = ws + WS_Q + (size_t)(tt * 32 + hg * 8 + lw * 2) * 2048;
    const unsigned qd = lds0 + AT_Q + lw * 4096;
    glds16_dev<0>(voff, qs, qd); glds16_dev<1024>(voff, qs, qd); glds16_dev<2048>(voff, qs, qd); glds16_dev<3072>(voff, qs, qd);
    ld(0, 0); ld(0, 1); ld(1, 0); ld(1, 1);
    l2_touch(wo, tdump);
    g2.init(ws, w2, wo, lds0, lw, l);
  }
  const unsigned s_lo = (l & 14) == 0 ? 0x3f803f80u : 0u, s_hi = (l & 14) == 2 ? 0x3f803f80u : 0u;
  const bf16x8_t sel = __builtin_bit_cast(bf16x8_t, (u32x4){s_lo, s_lo, s_hi, s_hi});
  const int sidx = (l & 1) ? 0xeeee : 0x4444;
  f32x16 o = {}, sa, sb;
  f32x4 ls = {};
  u32x8 pa, pb = {};
  PIN(o);
  if (LOADER) {
    asm volatile("s_waitcnt vmcnt(9)" ::: "memory");
    if (l == 0) __hip_atomic_fetch_add((LAS unsigned*)((LAS char*)smem + AT_RDY), 1u, __ATOMIC_RELAXED, __HIP_MEMORY_SCOPE_WORKGROUP);
  }
#pragma unroll 1
  for (unsigned spins = 0; spins < 64u * SPIN_CAP; ++spins) {
    if (rfl(__hip_atomic_load((LAS unsigned*)((LAS char*)smem + AT_RDY), __ATOMIC_RELAXED, __HIP_MEMORY_SCOPE_WORKGROUP)) >= 4u) break;
    __builtin_amdgcn_s_sleep(1);
  }
  asm volatile("" ::: "memory");

  const LAS char* qp = (const LAS char*)smem + AT_Q + w * 2048 + l * 16;
  const bf16x8 q0 = lds_b128(qp), q1 = lds_b128(qp + 1024);
  const LAS char* kvp = (const LAS char*)smem + AT_BASE + l * 16;
  bf16x8 kx0, kx1, ky0, ky1, va0, va1, vb0, vb1, vc0, vc1, vd0, vd1;
  kx0 = lds_b128(kvp); kx1 = lds_b128(kvp + 1024); ky0 = lds_b128(kvp + 4096); ky1 = lds_b128(kvp + 4096 + 1024);
  va0 = lds_b128(kvp + 2048); va1 = lds_b128(kvp + 3072); vb0 = lds_b128(kvp + 4096 + 2048); vb1 = lds_b128(kvp + 4096 + 3072);
  vd0 = va0; vd1 = va1;
  f32x16 nm;
  {
    sa = MFMA32(kx0, q0, o); sa = MFMA32(kx1, q1, sa);
    const float ma = -xhalf_max(max16(sa));
#pragma unroll
    for (int r = 0; r < 16; ++r) { nm[r] = ma; sa[r] += ma; }
    PIN(nm);
  }
#define EX(S, i) S[i] = __builtin_amdgcn_exp2f(S[i])
#define CV(P, j, S, i) P[j] = cvtpk(S[i], S[(i) + 1])
#define PLO(x) __builtin_bit_cast(bf16x8, __builtin_shufflevector(x, x, 0, 1, 2, 3))
#define PHI(x) __builtin_bit_cast(bf16x8, __builtin_shufflevector(x, x, 4, 5, 6, 7))
#define RSUM(x) ls = __builtin_amdgcn_smfmac_f32_16x16x64_bf16(sel, __builtin_bit_cast(bf16x16_t, x), ls, sidx, 0, 0)
#define STEP(S, P, SN, PP, KN0, KN1, VP0, VP1, KL0, KL1, VL0, VL1, NP) do { \
    SN = MFMA32(KN0, q0, nm); SBAR(); KL0 = lds_b128(NP); EX(S, 0); EX(S, 1); EX(S, 2); EX(S, 3); PIN(S); SBAR(); \
    SN = MFMA32(KN1, q1, SN); SBAR(); KL1 = lds_b128((NP) + 1024); CV(P, 0, S, 0); CV(P, 1, S, 2); EX(S, 4); EX(S, 5); EX(S, 6); EX(S, 7); PIN(S); PIN(P); SBAR(); \
    o = MFMA32(VP0, PLO(PP), o); SBAR(); VL0 = lds_b128((NP) + 2048); CV(P, 2, S, 4); EX(S, 8); CV(P, 3, S, 6); EX(S, 9); EX(S, 10); EX(S, 11); PIN(S); PIN(P); SBAR(); \
    o = MFMA32(VP1, PHI(PP), o); SBAR(); VL1 = lds_b128((NP) + 3072); CV(P, 4, S, 8); CV(P, 5, S, 10); EX(S, 12); EX(S, 13); EX(S, 14); PIN(S); PIN(P); SBAR(); \
    RSUM(PP); SBAR(); EX(S, 15); CV(P, 6, S, 12); CV(P, 7, S, 14); PIN(P); SBAR(); \
  } while (0)
#define XP(st, j) do { if (LOADER && (unsigned)((st) + 1) < 4u) g2.template ffn_piece<j>((st) + 1); SBAR(); } while (0)
#define PRIO(n) do { __builtin_amdgcn_s_setprio(n); SBAR(); } while (0)
#pragma unroll 1
  for (int c = 0; c < AT_NCH; ++c) {
    const LAS char* cb = kvp + (c & 1) * AT_BUF;
    const LAS char* nb = kvp + ((c + 1) & 1) * AT_BUF;
    PRIO(2);
    STEP(sa, pa, sb, pb, ky0, ky1, vd0, vd1, kx0, kx1, vc0, vc1, cb + 2 * 4096); XP(c - 5, 2);
    STEP(sb, pb, sa, pa, kx0, kx1, va0, va1, ky0, ky1, vd0, vd1, cb + 3 * 4096); XP(c - 5, 3);
    PRIO(1);
    STEP(sa, pa, sb, pb, ky0, ky1, vb0, vb1, kx0, kx1, va0, va1, cb + 4 * 4096); XP(c - 5, 4);
    STEP(sb, pb, sa, pa, kx0, kx1, vc0, vc1, ky0, ky1, vb0, vb1, cb + 5 * 4096); XP(c - 5, 5);
    STEP(sa, pa, sb, pb, ky0, ky1, vd0, vd1, kx0, kx1, vc0, vc1, cb + 6 * 4096); XP(c - 5, 6);
    PRIO(0);
    STEP(sb, pb, sa, pa, kx0, kx1, va0, va1, ky0, ky1, vd0, vd1, cb + 7 * 4096); XP(c - 5, 7);
    if (c < AT_NCH - 1) {
      if (LOADER) {
        if (c == 3 && lw == 0) {
          asm volatile("s_waitcnt vmcnt(1)" ::: "memory");
          if (rfl(*(const LAS unsigned*)((const LAS char*)smem + G1_CS)) < 92u) wait_ge(flags + (FL_H + (mt >> 1)) * 32, 92u);
        }
        if (c < 4) asm volatile("s_waitcnt vmcnt(1) lgkmcnt(0)\n\ts_barrier" ::: "memory");
        else asm volatile("s_waitcnt vmcnt(8) lgkmcnt(0)\n\ts_barrier" ::: "memory");
        if (c == 2 && lw == 0) peek_dev(flags + (FL_H + (mt >> 1)) * 32, lds0 + G1_CS);
        if (c + 2 < AT_NCH) { ld(c + 2, 0); ld(c + 2, 1); }
        if (c < 2) {
          const int j = c * 4 + lw;
          l2_touch(j < 7 ? (const char*)(w2 + (size_t)(gk * 1792 + j * 256 + (mt & 7) * 32 + (l >> 1)) * DM + nt * 128 + (l & 1) * 64)
                         : (const char*)(wo + (size_t)((mt & 7) * 128 + l) * DM + nt * 128 + gk * 64), tdump);
        } else if (c == 2) {
          l2_touch(lw == 0 ? (const char*)(wo + (size_t)((mt & 7) * 128 + 64 + l) * DM + nt * 128 + gk * 64) : (const char*)wo, tdump);
        }
      } else {
        asm volatile("s_waitcnt lgkmcnt(0)\n\ts_barrier" ::: "memory");
        if (c == 0 && late_pub) publish(late_pub, l);
      }
      PRIO(3);
    }
    SBAR();
    STEP(sa, pa, sb, pb, ky0, ky1, vb0, vb1, kx0, kx1, va0, va1, nb); XP(c - 4, 0);
    STEP(sb, pb, sa, pa, kx0, kx1, vc0, vc1, ky0, ky1, vb0, vb1, nb + 4096); XP(c - 4, 1);
  }
  o = MFMA32(vd0, PLO(pb), o); o = MFMA32(vd1, PHI(pb), o); RSUM(pb);
#undef EX
#undef CV
#undef PLO
#undef PHI
#undef RSUM
#undef STEP
#undef PRIO
#undef XP
  __builtin_amdgcn_s_setprio(0);
  attn_finish(ws, o, ls, q0, q1, l, ws + WS_O + (size_t)(head >> 1) * KT + (size_t)tt * 4096 + (head & 1) * 2048 + l * 16);
}

DI void g2_load(const u16* w2, const u16* wo, char* __restrict__ ws, unsigned lds0, const int lw, const int l) {
  const int xcd = blockIdx.x & 7, idx = blockIdx.x >> 3;
  const int mt = ((xcd >> 1) & 1) * 8 + (idx & 7);
  unsigned* flags = (unsigned*)(ws + WS_FLAG);
  G2Ld g; g.init(ws, w2, wo, lds0, lw, l);
  asm volatile("s_waitcnt vmcnt(10) lgkmcnt(0)\n\ts_barrier" ::: "memory");
  g.ffn(4); g.ffn(5);
  const LAS unsigned* peek = (const LAS unsigned*)(size_t)(lds0 + G2_PART);
#pragma unroll 1
  for (int i = 2; i < 24; ++i) {
    asm volatile("s_waitcnt vmcnt(16)\n\ts_barrier" ::: "memory");
    if (i == 6 && l == 0) __hip_atomic_fetch_add((gu32*)flags + (FL_O + (blockIdx.x >> 4)) * 32, 1u, RLX_AGENT);
    if (i == 20 && lw == 0) peek_dev(flags + (FL_O + mt) * 32, lds0 + G2_PART);
    g.ffn(i + 4);
  }
  if (lw == 0 && rfl(peek[0]) < 128u) wait_ge(flags + (FL_O + mt) * 32, 128u);
  asm volatile("s_waitcnt vmcnt(16)\n\ts_barrier" ::: "memory");
  g.wo(28);
  peek = (const LAS unsigned*)(size_t)(lds0 + G2_BOFF + 2048);
#pragma unroll 1
  for (int i = 25; i < 37; ++i) {
    asm volatile("s_waitcnt vmcnt(12)\n\ts_barrier" ::: "memory");
    if (i == 35 && lw == 0) peek_dev(flags + (FL_P + blockIdx.x) * 32, lds0 + G2_BOFF + 2048);
    g.wo(i + 4);
  }
  auto part = [&]() {
    const char* src = ws + WS_PART + (size_t)(blockIdx.x ^ 1) * 16384 + lw * 4096;
    const unsigned d = lds0 + G2_PART + lw * 4096;
    glds16_dev<0>(g.voff, src, d); glds16_dev<1024>(g.voff, src, d); glds16_dev<2048>(g.voff, src, d); glds16_dev<3072>(g.voff, src, d);
  };
  asm volatile("s_waitcnt vmcnt(12)\n\ts_barrier" ::: "memory");
  const bool early = rfl(peek[0]) >= 4u;
  g.wo(41);
  if (early) part();
#pragma unroll 1
  for (int i = 38; i < 41; ++i) {
    if (early) asm volatile("s_waitcnt vmcnt(16)\n\ts_barrier" ::: "memory");
    else asm volatile("s_waitcnt vmcnt(12)\n\ts_barrier" ::: "memory");
    if (i < 40) g.wo(i + 4);
  }
  if (!early && lw == 0) wait_ge(flags + (FL_P + blockIdx.x) * 32, 4u);
  asm volatile("s_waitcnt vmcnt(6)\n\ts_barrier" ::: "memory");
  if (!early) part();
  asm volatile("s_waitcnt vmcnt(0)\n\ts_barrier" ::: "memory");
}

DI void g2_compute(const u16* __restrict__ x, const u16* __restrict__ b2, char* __restrict__ ws, u16* __restrict__ out,
                   char* smem, const int w, const int l) {
  const int xcd = blockIdx.x & 7, idx = blockIdx.x >> 3;
  const int kh = xcd & 1, mt = ((xcd >> 1) & 1) * 8 + (idx & 7), nt = (xcd >> 2) * 4 + (idx >> 3);
  unsigned* flags = (unsigned*)(ws + WS_FLAG);
  const int h = l >> 5, wm = w & 1, wn = w >> 1;
  f32x16 accg[2] = {}, accp[2] = {}, accs[2] = {};
  const int col0 = nt * 128 + kh * 64 + wn * 32;
  const int row0 = mt * 128 + wm * 64 + (l & 31);
  unsigned stw[2];
  u32x4 xv[4];
  u32x2 bv[4];
#pragma unroll
  for (int m = 0; m < 2; ++m) stw[m] = __hip_atomic_load((gu32*)(ws + WS_STAT) + row0 + m * 32, RLX_AGENT);
#pragma unroll
  for (int k = 0; k < 4; ++k) xv[k] = *(const u32x4*)(x + (size_t)(row0 + (k >> 1) * 32) * DM + col0 + 16 * (k & 1) + 8 * h);
#pragma unroll
  for (int k = 0; k < 4; ++k) bv[k] = *(const u32x2*)(b2 + col0 + 8 * k + 4 * h);
  const LAS char* lbase = (const LAS char*)smem + l * 16;
  const int btr = (4 * h + ((l & 15) >> 2)) * 64 + ((l >> 4) & 1) * 32 + (l & 3) * 8 - l * 16;
#define LDB(p_) cat8(lds_tr(p_), lds_tr((p_) + 512))
  const int ao = wm * 8192;
  const int bG = G2_BOFF + (kh * 2 + wn) * 1024 + btr, bP = G2_BOFF + ((kh ^ 1) * 2 + wn) * 1024 + btr;
  const int bW = G2_BOFF + wn * 1024 + btr;
  bf16x8 fa0[2], fb0[2], fa1[2], fb1[2];
#define G2_RD(FA, FB, base, ks) do { \
    FA[0] = lds_b128((base) + ao + (ks) * 1024); FB[0] = LDB((base) + bG + (ks) * 4096); \
    FA[1] = lds_b128((base) + ao + 4096 + (ks) * 1024); FB[1] = LDB((base) + bP + (ks) * 4096); \
  } while (0)
#define G2_MM(FA, FB) do { \
    accg[0] = MFMA32(FB[0], FA[0], accg[0]); accg[1] = MFMA32(FB[0], FA[1], accg[1]); \
    accp[0] = MFMA32(FB[1], FA[0], accp[0]); accp[1] = MFMA32(FB[1], FA[1], accp[1]); \
  } while (0)
#define G2_SCHED(NR) do { \
    __builtin_amdgcn_sched_group_barrier(0x008, 1, 0); __builtin_amdgcn_sched_group_barrier(0x100, NR, 0); \
    __builtin_amdgcn_sched_group_barrier(0x008, 1, 0); __builtin_amdgcn_sched_group_barrier(0x100, NR, 0); \
    __builtin_amdgcn_sched_group_barrier(0x008, 2, 0); SBAR(); \
  } while (0)
#define G2_BAR() do { asm volatile("s_waitcnt lgkmcnt(0)\n\ts_barrier" ::: "memory"); SBAR(); } while (0)

  SBAR();
  G2_RD(fa0, fb0, lbase, 0); SBAR();
  G2_RD(fa1, fb1, lbase, 1); G2_MM(fa0, fb0); G2_SCHED(3);
  G2_RD(fa0, fb0, lbase, 2); G2_MM(fa1, fb1); G2_SCHED(3);
  G2_RD(fa1, fb1, lbase, 3); G2_MM(fa0, fb0); G2_SCHED(3);
#pragma unroll 1
  for (int i = 0; i < 27; ++i) {
    const LAS char* nb = lbase + ((i + 1) & 3) * G2_SLOT;
    if (i > 0) G2_BAR();
    G2_RD(fa0, fb0, nb, 0); G2_MM(fa1, fb1); G2_SCHED(3);
    G2_RD(fa1, fb1, nb, 1); G2_MM(fa0, fb0); G2_SCHED(3);
    G2_RD(fa0, fb0, nb, 2); G2_MM(fa1, fb1); G2_SCHED(3);
    G2_RD(fa1, fb1, nb, 3); G2_MM(fa0, fb0); G2_SCHED(3);
    if (i == 3) { publish(flags + (FL_O + (blockIdx.x >> 4)) * 32, l); SBAR(); }
  }
  bf16x8 ga0[4], gb0[2], ga1[4], gb1[2];
#define W_RD(GA, GB, base, ks) do { \
    GA[0] = lds_b128((base) + ao + (ks) * 1024); GB[0] = LDB((base) + bW + (ks) * 4096); GA[1] = lds_b128((base) + ao + 4096 + (ks) * 1024); \
    GA[2] = lds_b128((base) + ao + (ks) * 1024 + 1024); GB[1] = LDB((base) + bW + (ks) * 4096 + 4096); GA[3] = lds_b128((base) + ao + 4096 + (ks) * 1024 + 1024); \
  } while (0)
#define W_MM(GA, GB) do { \
    accs[0] = MFMA32(GB[0], GA[0], accs[0]); accs[1] = MFMA32(GB[0], GA[1], accs[1]); \
    accs[0] = MFMA32(GB[1], GA[2], accs[0]); accs[1] = MFMA32(GB[1], GA[3], accs[1]); \
  } while (0)
#define W_STEP(i) do { const LAS char* nb_ = lbase + (((i) + 1) & 3) * G2_SLOT; G2_BAR(); \
    W_RD(ga0, gb0, nb_, 0); W_MM(ga1, gb1); G2_SCHED(4); \
    W_RD(ga1, gb1, nb_, 2); W_MM(ga0, gb0); G2_SCHED(4); } while (0)
  char* pdst = ws + WS_PART + (size_t)blockIdx.x * 16384 + w * 4096 + l * 16;
#define G2_SEND(q) do { const int b_ = 8 * ((q) & 1); \
    store16_dev(pdst + (q) * 1024, (u32x4){cvtpk(accp[(q) >> 1][b_], accp[(q) >> 1][b_ + 1]), cvtpk(accp[(q) >> 1][b_ + 2], accp[(q) >> 1][b_ + 3]), \
                                           cvtpk(accp[(q) >> 1][b_ + 4], accp[(q) >> 1][b_ + 5]), cvtpk(accp[(q) >> 1][b_ + 6], accp[(q) >> 1][b_ + 7])}); SBAR(); } while (0)
  G2_BAR();
  W_RD(ga0, gb0, lbase, 0); G2_MM(fa1, fb1); G2_SCHED(4);
  W_RD(ga1, gb1, lbase, 2); W_MM(ga0, gb0); G2_SCHED(4);
  G2_SEND(0); G2_SEND(1);
  W_STEP(28); G2_SEND(2); G2_SEND(3);
#pragma unroll 1
  for (int i = 29; i < 42; ++i) {
    W_STEP(i);
    if (i == 32) { publish(flags + (FL_P + (blockIdx.x ^ 1)) * 32, l); SBAR(); }
  }
  W_STEP(42);
  W_MM(ga1, gb1);
  {
    const LAS char* xp = (const LAS char*)smem + G2_PART + w * 4096 + l * 16;
#pragma unroll
    for (int m = 0; m < 2; ++m) {
      const float den = bf_hi(stw[m]), rd2 = 1.0f / (den * den);
#pragma unroll
      for (int k = 0; k < 2; ++k) {
        u32x4 xx = xv[m * 2 + k];
        { auto r0 = __builtin_amdgcn_permlane32_swap(xx[0], xx[2], false, false); xx[0] = r0[0]; xx[2] = r0[1];
          auto r1 = __builtin_amdgcn_permlane32_swap(xx[1], xx[3], false, false); xx[1] = r1[0]; xx[3] = r1[1]; }
        unsigned pk[4];
        const u32x4 tp = *(const LAS u32x4*)(xp + (m * 2 + k) * 1024);
#pragma unroll
        for (int ii = 0; ii < 2; ++ii) {
          const int i = 2 * k + ii;
          float r[4];
#pragma unroll
          for (int j = 0; j < 4; ++j) {
            const unsigned xw_ = xx[2 * ii + (j >> 1)], bw_ = bv[i][j >> 1], pw_ = tp[2 * ii + (j >> 1)];
            const float z = (accg[m][4 * i + j] + ((j & 1) ? bf_hi(pw_) : bf_lo(pw_))) * rd2 + ((j & 1) ? bf_hi(bw_) : bf_lo(bw_));
            const float sg = z * __builtin_amdgcn_rcpf(1.0f + __builtin_amdgcn_exp2f(-1.4426950408889634f * z));
            r[j] = ((j & 1) ? bf_hi(xw_) : bf_lo(xw_)) + accs[m][4 * i + j] * sg;
          }
          pk[2 * ii] = cvtpk(r[0], r[1]); pk[2 * ii + 1] = cvtpk(r[2], r[3]);
        }
        auto r0 = __builtin_amdgcn_permlane32_swap(pk[0], pk[2], false, false);
        auto r1 = __builtin_amdgcn_permlane32_swap(pk[1], pk[3], false, false);
        *(u32x4*)(out + (size_t)(row0 + m * 32) * DM + col0 + 16 * k + 8 * h) = (u32x4){r0[0], r1[0], r0[1], r1[1]};
      }
    }
  }
#undef G2_RD
#undef G2_MM
#undef G2_SCHED
#undef G2_BAR
#undef G2_SEND
#undef W_RD
#undef W_MM
#undef W_STEP
#undef LDB
}

__global__ __launch_bounds__(512, 2) void main_kernel(const u16* __restrict__ x, const u16* __restrict__ wq, const u16* __restrict__ wk,
                                                      const u16* __restrict__ wv, const u16* __restrict__ wo, const u16* __restrict__ w1,
                                                      const u16* __restrict__ b1, const u16* __restrict__ w2, const u16* __restrict__ b2,
                                                      char* __restrict__ ws, u16* __restrict__ out) {
  extern __shared__ __attribute__((aligned(16))) char smem[];
  const int l = threadIdx.x & 63, w = rfl(threadIdx.x >> 6);
  const unsigned lds0 = (unsigned)(size_t)(LAS char*)smem;
  const int xcd = blockIdx.x & 7, idx = blockIdx.x >> 3;
  const int mtile = (xcd & 1) * 4 + (idx & 3), cg = (idx >> 2) * 4 + (xcd >> 1);
  unsigned* flags = (unsigned*)(ws + WS_FLAG);
  if (w == 3) {
    unsigned long long pc; unsigned len;
    asm volatile("s_getpc_b64 %0\n.Lmk_code_begin:\n\ts_mov_b32 %1, .Lmk_code_end-.Lmk_code_begin" : "=s"(pc), "=s"(len));
    unsigned off = (unsigned)(idx * 16 + (l & 15)) * 128u;
    off = (off < len && len <= 65536u) ? off : 0u;
    l2_touch((const char*)pc + off, lds0 + G1_HS + 3072);
  }
  if (w >= 4) {
    if (cg > 8) g1_load<G1_FFN>(ws, x, w1, nullptr, cg, mtile, smem, w - 4, l);
    else if (cg < 8) g1_load<G1_Q>(ws, x, wq, nullptr, cg, mtile, smem, w - 4, l);
    else g1_load<G1_KV>(ws, x, wk, wv, cg, mtile, smem, w - 4, l);
    attn_phase<true>(ws, w2, wo, smem, w, l, nullptr);
    g2_load(w2, wo, ws, lds0, w - 4, l);
    asm volatile(".Lmk_code_end:");
  } else {
    unsigned* late = nullptr;
    if (cg > 8) { g1_compute<G1_FFN>(ws, x, b1, cg, mtile, smem, w, l); late = flags + (FL_H + mtile) * 32; }
    else if (cg < 8) { g1_compute<G1_Q>(ws, x, b1, cg, mtile, smem, w, l); publish(flags + (FL_Q + mtile * 8 + cg) * 32, l); }
    else { g1_compute<G1_KV>(ws, x, b1, cg, mtile, smem, w, l); publish(flags + FL_KV * 32, l); }
    attn_phase<false>(ws, w2, wo, smem, w, l, late);
    g2_compute(x, b2, ws, out, smem, w, l);
  }
}

extern "C" void kernel_launch(void* const* d_in, const int* in_sizes, int n_in, void* d_out, int out_size, void* d_ws,
                              size_t ws_size, hipStream_t stream) {
  const u16* x = (const u16*)d_in[0];
  const u16* wq = (const u16*)d_in[1];
  const u16* wk = (const u16*)d_in[2];
  const u16* wv = (const u16*)d_in[3];
  const u16* wo = (const u16*)d_in[4];
  const u16* w1 = (const u16*)d_in[6];
  const u16* b1 = (const u16*)d_in[7];
  const u16* w2 = (const u16*)d_in[8];
  const u16* b2 = (const u16*)d_in[9];
  char* ws = (char*)d_ws;
  (void)hipFuncSetAttribute((const void*)main_kernel, hipFuncAttributeMaxDynamicSharedMemorySize, MAIN_LDS);
  main_kernel<<<256, 512, MAIN_LDS, stream>>>(x, wq, wk, wv, wo, w1, b1, w2, b2, ws, (u16*)d_out);
}
```

```cpp
#include <hip/hip_runtime.h>
#include <hip/hip_bf16.h>

typedef short bf16x8 __attribute__((ext_vector_type(8)));
typedef short s16x4 __attribute__((ext_vector_type(4)));
typedef float f32x16 __attribute__((ext_vector_type(16)));
typedef float f32x4 __attribute__((ext_vector_type(4)));
typedef float f32x2 __attribute__((ext_vector_type(2)));
typedef __bf16 bf16x2_t __attribute__((ext_vector_type(2)));
typedef unsigned u32x4 __attribute__((ext_vector_type(4)));
typedef unsigned u32x2 __attribute__((ext_vector_type(2)));
typedef unsigned u32x8 __attribute__((ext_vector_type(8)));
typedef __bf16 bf16x8_t __attribute__((ext_vector_type(8)));
typedef __bf16 bf16x16_t __attribute__((ext_vector_type(16)));
typedef unsigned short u16;
#define LAS __attribute__((address_space(3)))
#define DI __device__ __forceinline__
#define SBAR() __builtin_amdgcn_sched_barrier(0)
#define PIN(x) asm volatile("" : "+v"(x))
#define MFMA16(a, b, c) __builtin_amdgcn_mfma_f32_16x16x32_bf16((a), (b), (c), 0, 0, 0)
#define MFMA32(a, b, c) __builtin_amdgcn_mfma_f32_32x32x16_bf16((a), (b), (c), 0, 0, 0)

constexpr int DM = 1024, FF = 3584;
constexpr size_t KT = 262144;
constexpr size_t WS_STAT = 0;
constexpr size_t WS_Q = 4u << 20;
constexpr size_t WS_KV = 8u << 20;
constexpr size_t WS_O = 9u << 20;
constexpr size_t WS_H = 13u << 20;
constexpr size_t WS_PART = 28u << 20;
constexpr size_t WS_FLAG = 37u << 20;
constexpr int FL_P = 0, FL_O = 256, FL_H = 272, FL_Q = 280, FL_KV = 344, FL_S = 345, FL_N = 353;
constexpr size_t WS_HSA = WS_FLAG + 65536, WS_HSB = WS_HSA + 4096, WS_HSC = WS_HSB + 4096;
constexpr int COORD_BLOCK = 64;

#define STAMP(kid, idx) do {} while (0)
DI unsigned rfl(unsigned x) { return __builtin_amdgcn_readfirstlane(x); }
DI unsigned cvtpk(float lo, float hi) {
  f32x2 v = {lo, hi};
  bf16x2_t b = __builtin_convertvector(v, bf16x2_t);
  return __builtin_bit_cast(unsigned, b);
}
DI float bf_lo(unsigned u) { return __uint_as_float(u << 16); }
DI float bf_hi(unsigned u) { return __uint_as_float(u & 0xffff0000u); }
DI float bf_round(float x) { return bf_lo(cvtpk(x, x)); }

template <int IMM>
DI void glds16(unsigned voff, const void* sbase, unsigned ldsaddr) {
  asm volatile("s_mov_b32 m0, %2\n\ts_nop 0\n\tglobal_load_lds_dwordx4 %0, %1 offset:%3"
               :: "v"(voff), "s"(sbase), "s"(ldsaddr), "i"(IMM) : "memory");
}
template <int IMM>
DI void glds16_dev(unsigned voff, const void* sbase, unsigned ldsaddr) {
  asm volatile("s_mov_b32 m0, %2\n\ts_nop 0\n\tglobal_load_lds_dwordx4 %0, %1 offset:%3 sc1"
               :: "v"(voff), "s"(sbase), "s"(ldsaddr), "i"(IMM) : "memory");
}
DI void set_m0(unsigned ldsaddr) { asm volatile("s_mov_b32 m0, %0\n\ts_nop 0" :: "s"(ldsaddr) : "memory"); }
template <int IMM> DI void gl16(unsigned voff, const void* sbase) {
  asm volatile("global_load_lds_dwordx4 %0, %1 offset:%2" :: "v"(voff), "s"(sbase), "i"(IMM) : "memory");
}
template <int IMM> DI void gl16_dev(unsigned voff, const void* sbase) {
  asm volatile("global_load_lds_dwordx4 %0, %1 offset:%2 sc1" :: "v"(voff), "s"(sbase), "i"(IMM) : "memory");
}
DI void peek_dev(const void* word, unsigned ldsaddr) {
  asm volatile("s_mov_b32 m0, %2\n\ts_nop 0\n\tglobal_load_lds_dword %0, %1 sc1" :: "v"(0u), "s"(word), "s"(ldsaddr) : "memory");
}
DI void store16_dev(void* p, u32x4 v) {
  asm volatile("global_store_dwordx4 %0, %1, off sc1\n\ts_nop 1" :: "v"(p), "v"(v) : "memory");
}
DI void l2_touch(const void* p, unsigned ldsaddr) {
  asm volatile("s_mov_b32 m0, %1\n\ts_nop 0\n\tglobal_load_lds_dword %0, off" :: "v"(p), "s"(ldsaddr) : "memory");
}
#define QSLP 8
#define WSLP QSLP
constexpr unsigned SPIN_CAP = 1u << 20;
typedef __attribute__((address_space(1))) unsigned gu32;
#define RLX_AGENT __ATOMIC_RELAXED, __HIP_MEMORY_SCOPE_AGENT
DI void wait_ge(const unsigned* word, unsigned want) {
#pragma unroll 1
  for (unsigned spins = 0; spins < SPIN_CAP; ++spins) {
    if (rfl(__hip_atomic_load((gu32*)word, RLX_AGENT)) >= want) break;
    __builtin_amdgcn_s_sleep(WSLP);
  }
  __builtin_amdgcn_fence(__ATOMIC_ACQUIRE, "wavefront");
}
DI void publish(unsigned* word, int l) {
  asm volatile("s_waitcnt vmcnt(0)" ::: "memory");
  if (l == 0) __hip_atomic_fetch_add((gu32*)word, 1u, RLX_AGENT);
}
DI bf16x8 load16_dev(const char* p) {
  typedef __attribute__((address_space(1))) unsigned long long gu64;
  const unsigned long long a = __hip_atomic_load((gu64*)p, RLX_AGENT), b = __hip_atomic_load((gu64*)p + 1, RLX_AGENT);
  return __builtin_bit_cast(bf16x8, (u32x4){(unsigned)a, (unsigned)(a >> 32), (unsigned)b, (unsigned)(b >> 32)});
}
DI float relu_s(float a) { return __builtin_amdgcn_fmed3f(a, 0.0f, 3.4028234663852886e38f); }
DI bf16x8 lds_b128(const LAS char* p) { return *(const LAS bf16x8*)p; }
DI s16x4 lds_tr(const LAS char* p) {
  return __builtin_amdgcn_ds_read_tr16_b64_v4i16((LAS s16x4*)p);
}
DI bf16x8 cat8(s16x4 lo, s16x4 hi) { return __builtin_shufflevector(lo, hi, 0, 1, 2, 3, 4, 5, 6, 7); }

DI float half_sum(float v) {
  v += __uint_as_float(__builtin_amdgcn_update_dpp(0u, __float_as_uint(v), 0xB1, 0xf, 0xf, true));
  v += __uint_as_float(__builtin_amdgcn_update_dpp(0u, __float_as_uint(v), 0x4E, 0xf, 0xf, true));
  v += __uint_as_float(__builtin_amdgcn_update_dpp(0u, __float_as_uint(v), 0x141, 0xf, 0xf, true));
  v += __uint_as_float(__builtin_amdgcn_update_dpp(0u, __float_as_uint(v), 0x140, 0xf, 0xf, true));
  auto r = __builtin_amdgcn_permlane16_swap(__float_as_uint(v), __float_as_uint(v), false, false);
  return __uint_as_float(r[0]) + __uint_as_float(r[1]);
}
DI unsigned row_stats(const u32x4 (&v)[4]) {
  f32x2 e[16], s2 = {0.f, 0.f};
#pragma unroll
  for (int i = 0; i < 16; ++i) { e[i] = (f32x2){bf_lo(v[i >> 2][i & 3]), bf_hi(v[i >> 2][i & 3])}; s2 += e[i]; }
  const float mean = half_sum(s2[0] + s2[1]) * (1.0f / DM);
  f32x2 q2 = {0.f, 0.f};
#pragma unroll
  for (int i = 0; i < 16; ++i) { const f32x2 d = e[i] - mean; q2 += d * d; }
  const float q = half_sum(q2[0] + q2[1]);
  const float den = __builtin_sqrtf(bf_round(bf_round(q * (1.0f / DM)) + 1e-5f));
  return cvtpk(-mean, den);
}

constexpr int G1_STAGE = 53248, G1_BOFF = 32768;
enum { G1_FFN = 5, G1_Q = 4, G1_KV = 2 };

constexpr int G1_ST = 87040;
constexpr int G1_CF = 53248;
constexpr int AT_RDY = 89088;
constexpr int G1_HS = 159744;
constexpr int G1_CS = 81920;
constexpr int ESTART = 3, PSTART = 9;

DI float xhalf_max(float m) {
  auto rr = __builtin_amdgcn_permlane32_swap(__float_as_uint(m), __float_as_uint(m), false, false);
  return __builtin_fmaxf(__uint_as_float(rr[0]), __uint_as_float(rr[1]));
}
DI float xhalf_sum(float m) {
  auto rr = __builtin_amdgcn_permlane32_swap(__float_as_uint(m), __float_as_uint(m), false, false);
  return __uint_as_float(rr[0]) + __uint_as_float(rr[1]);
}
template <int NP>
DI void g1_load(char* ws, const u16* x, const u16* wsrc, const u16* wv, const u16* b1, int cg, int mtile, char* smem, int lw, int l) {
  constexpr int LD = (NP == G1_FFN) ? FF : (NP == G1_Q) ? DM : 32;
  const unsigned lds0 = (unsigned)(size_t)(LAS char*)smem;
  const char* abase = (const char*)(x + (size_t)(mtile * 256 + lw * 64) * DM);
  const unsigned ave = (unsigned)((l >> 3) * 2048 + (((l & 7) ^ (l >> 4)) << 4)), avo = ave ^ 64u;
  const unsigned bvoff = (unsigned)((lw * 16 + (l >> 2)) * LD * 2 + (((l & 3) ^ ((l >> 5) << 1)) << 4));
  const int krot = NP == G1_Q ? (mtile & 3) * 4 : 0;
  unsigned va[8], vb[NP];
#pragma unroll
  for (int i = 0; i < 8; ++i) va[i] = ((i & 1) ? avo : ave) + i * 16384 - (i - 4) * 1024;
#pragma unroll
  for (int p = 0; p < NP; ++p) {
    int cb = (NP == G1_FFN) ? (cg - 9) * 5 + p : (NP == G1_Q) ? cg * 4 + p : 0;
    cb = cb > 111 ? 111 : cb;
    vb[p] = bvoff + cb * 64 - (p - 2) * 1024 + 4096;
  }
  unsigned bias[NP];
#pragma unroll
  for (int p = 0; p < NP; ++p) {
    int cb = (cg - 9) * 5 + p; cb = cb > 111 ? 111 : cb;
    bias[p] = (NP == G1_FFN) ? (unsigned)b1[cb * 32 + (l & 31)] : 0u;
  }
  float cs[NP];
#pragma unroll
  for (int p = 0; p < NP; ++p) cs[p] = 0.f;
  const LAS char* cb0 = (const LAS char*)smem + G1_BOFF + lw * (NP * 1024) + (8 * (l >> 5) + ((l & 15) >> 2)) * 64 + (((l >> 4) ^ (l >> 5)) & 1) * 32 + (l & 3) * 8;
  u32x2 t0[NP], t1[NP];
  auto cs_read = [&](int slot) {
#pragma unroll
    for (int p = 0; p < NP; ++p) {
      t0[p] = __builtin_bit_cast(u32x2, lds_tr(cb0 + slot * G1_STAGE + p * 1024));
      t1[p] = __builtin_bit_cast(u32x2, lds_tr(cb0 + slot * G1_STAGE + p * 1024 + 256));
    }
    SBAR();
  };
  auto cs_add = [&](int p) {
    float a, b, c, d;
    asm volatile("v_lshlrev_b32 %1, 16, %5\n\tv_and_b32 %2, 0xffff0000, %5\n\tv_lshlrev_b32 %3, 16, %6\n\tv_and_b32 %4, 0xffff0000, %6\n\t"
                 "v_add_f32 %1, %1, %2\n\tv_add_f32 %3, %3, %4\n\t"
                 "v_lshlrev_b32 %2, 16, %7\n\tv_and_b32 %4, 0xffff0000, %7\n\tv_add_f32 %1, %1, %2\n\tv_add_f32 %3, %3, %4\n\t"
                 "v_lshlrev_b32 %2, 16, %8\n\tv_and_b32 %4, 0xffff0000, %8\n\tv_add_f32 %1, %1, %2\n\tv_add_f32 %3, %3, %4\n\t"
                 "v_add_f32 %1, %1, %3\n\tv_add_f32 %0, %0, %1"
                 : "+v"(cs[p]), "=&v"(a), "=&v"(b), "=&v"(c), "=&v"(d) : "v"(t0[p][0]), "v"(t0[p][1]), "v"(t1[p][0]), "v"(t1[p][1]));
    SBAR();
  };
  auto issue = [&](int st, int slot, bool sum) {
    const int kt = (st + krot) & 15;
    const char* b0 = (const char*)wsrc + (size_t)kt * (64 * LD * 2) - 4096;
    set_m0(lds0 + slot * G1_STAGE + G1_BOFF + lw * (NP * 1024) + 2048);
    gl16<-2048>(vb[0], b0);
    gl16<-1024>(vb[1], NP == G1_KV ? (const char*)wv + (size_t)kt * (64 * LD * 2) - 4096 : b0);
    if (NP > 2) { gl16<0>(vb[2 % NP], b0); gl16<1024>(vb[3 % NP], b0); }
    if (NP > 4) gl16<2048>(vb[4 % NP], b0);
    const char* a0 = abase + kt * 128;
    set_m0(lds0 + slot * G1_STAGE + lw * 8192 + 4096);
#define AL(i) do { if (sum && (i) < NP) cs_add((i) % NP); gl16<((i) - 4) * 1024>(va[i], a0); } while (0)
    AL(0); AL(1); AL(2); AL(3); AL(4); AL(5); AL(6); AL(7);
#undef AL
  };
  auto colsum = [&](int slot) {
    cs_read(slot);
#pragma unroll
    for (int p = 0; p < NP; ++p) cs_add(p);
  };
  unsigned* flags = (unsigned*)(ws + WS_FLAG);
  const bool coord = (NP == G1_KV) && blockIdx.x == COORD_BLOCK && lw == 1;
  if (coord) {
#pragma unroll
    for (int i = 0; i < 6; ++i) if (i * 64 + l < FL_N) __hip_atomic_store((gu32*)flags + (i * 64 + l) * 32, 0u, RLX_AGENT);
    store16_dev(ws + WS_HSC + l * 16, (u32x4){0u, 0u, 0u, 0u});
    asm volatile("s_waitcnt vmcnt(0)" ::: "memory");
  }
  issue(0, 0, false); issue(1, 1, false); issue(2, 2, false);
  asm volatile("s_waitcnt vmcnt(%0)\n\ts_barrier" :: "n"(2 * (8 + NP)) : "memory");
  __builtin_amdgcn_s_setprio(2);
  int state = (lw == 0 || coord) ? 0 : 2;
  bool pend = false, first = true;
  u32x4 last = {0u, 0u, 0u, 0u};
  const unsigned found = rfl(*(const LAS unsigned*)((const LAS char*)smem + G1_HS));
  if (lw == 0 && l < 9) {
    const unsigned v = *(const LAS unsigned*)((const LAS char*)smem + G1_HS + 16 + (l & 7) * 4);
    __hip_atomic_store(l < 8 ? (gu32*)(ws + WS_STAT) + mtile * 256 + cg * 8 + l : (gu32*)(ws + WS_HSA) + blockIdx.x, l < 8 ? v : found + 1u, RLX_AGENT);
  }
  colsum(0);
  auto poll_issue = [&]() {
    if (NP == G1_KV && coord) {
      glds16_dev<0>(l * 16, ws + WS_HSA, lds0 + G1_HS + 1024); glds16_dev<0>(l * 16, ws + WS_HSC, lds0 + G1_HS + 2048);
    } else {
      const char* a_ = state == 0 ? ws + WS_HSB + blockIdx.x * 4 : (const char*)(flags + (FL_S + mtile) * 32);
      peek_dev(a_, lds0 + G1_HS + 256);
    }
  };
  auto poll_check = [&]() {
    if (NP == G1_KV && coord) {
      const u32x4 pa = *(const LAS u32x4*)((const LAS char*)smem + G1_HS + 1024 + l * 16), pc = *(const LAS u32x4*)((const LAS char*)smem + G1_HS + 2048 + l * 16);
      if (first || pa[0] != last[0] || pa[1] != last[1] || pa[2] != last[2] || pa[3] != last[3]) store16_dev(ws + WS_HSB + l * 16, pa);
      last = pa; first = false;
      if (__all((pc[0] & pc[1] & pc[2] & pc[3]) == 1u)) state = 2;
    } else {
      const unsigned v = rfl(*(const LAS unsigned*)((const LAS char*)smem + G1_HS + 256));
      if (state == 0) {
        if (v != found) {
          if (l == 0) {
            __hip_atomic_store((gu32*)(ws + WS_HSC) + blockIdx.x, 1u, RLX_AGENT);
            __hip_atomic_fetch_add((gu32*)flags + (FL_S + mtile) * 32, 1u, RLX_AGENT);
          }
          state = 1;
        }
      } else if (v >= 32u) state = 2;
    }
    state = rfl(state);
  };
  int slot = 0;
#pragma unroll 1
  for (int i = 0; i < 13; ++i) {
    asm volatile("s_waitcnt vmcnt(%0)\n\ts_barrier" :: "n"(8 + NP) : "memory");
    if (state < 2 && (i >= ESTART || coord)) {
      if (pend) poll_check();
      pend = state == 0 || (state == 1 && i >= PSTART);
      if (pend) poll_issue();
    }
    const int nslot = (slot == 2) ? 0 : slot + 1;
    cs_read(nslot);
    issue(i + 3, slot, true);
    slot = nslot;
  }
#pragma unroll 1
  for (unsigned spins = 0; state < 2 && spins < SPIN_CAP; ++spins) {
    if (!pend) poll_issue();
    asm volatile("s_waitcnt vmcnt(0)" ::: "memory");
    poll_check();
    pend = false;
    __builtin_amdgcn_s_sleep(8);
  }
  asm volatile("s_waitcnt vmcnt(%0)\n\ts_barrier" :: "n"(8 + NP) : "memory");
  if (lw == 0) { glds16_dev<0>(l * 16, ws + WS_STAT + mtile * 1024, lds0 + G1_ST); *(LAS u32x2*)((LAS char*)smem + AT_RDY) = (u32x2){0u, 0u}; }
  colsum(2);
  asm volatile("s_waitcnt vmcnt(0)" ::: "memory");
  colsum(0);
#pragma unroll
  for (int p = 0; p < NP; ++p) {
    const float c = xhalf_sum(cs[p]);
    const unsigned c0 = cvtpk(c, c) & 0xffffu; const float r1 = c - bf_lo(c0);
    const unsigned c1 = cvtpk(r1, r1) & 0xffffu; const float r2 = r1 - bf_lo(c1);
    const unsigned c2 = cvtpk(r2, r2) & 0xffffu;
    if (l < 32) *(LAS u32x4*)((LAS char*)smem + G1_CF + (p * 2 + (l >> 4)) * 1024 + (lw * 16 + (l & 15)) * 16) = (u32x4){c0 | (c1 << 16), c2 | (lw ? 0u : bias[p] << 16), 0u, 0u};
  }
  asm volatile("s_waitcnt lgkmcnt(0)\n\ts_barrier" ::: "memory");
}

template <int NP>
DI void g1_compute(char* __restrict__ ws, const u16* __restrict__ x, int cg, int mtile, char* smem, int w, int l) {
  STAMP(1, 0);
  int g = l >> 4, c16 = l & 15;
  {
    const int row = mtile * 256 + cg * 8 + w * 2 + (l >> 5);
    u32x4 v[4];
#pragma unroll
    for (int i = 0; i < 4; ++i) v[i] = *(const u32x4*)(x + (size_t)row * DM + (i * 32 + (l & 31)) * 8);
    const unsigned found = __hip_atomic_load((gu32*)(ws + WS_HSB) + blockIdx.x, RLX_AGENT);
    SBAR();
    const unsigned rs = row_stats(v);
    if ((l & 31) == 0) *(LAS unsigned*)((LAS char*)smem + G1_HS + 16 + (w * 2 + (l >> 5)) * 4) = rs;
    if (w == 0 && l == 0) *(LAS unsigned*)((LAS char*)smem + G1_HS) = found;
  }
  f32x4 acc[4][NP][2];
#pragma unroll
  for (int m = 0; m < 4; ++m)
#pragma unroll
    for (int p = 0; p < NP; ++p) { acc[m][p][0] = f32x4{}; acc[m][p][1] = f32x4{}; }
  const LAS char* lbase = (const LAS char*)smem;
  int aoff[2], boff[2];
#pragma unroll
  for (int k = 0; k < 2; ++k) {
    aoff[k] = (w * 64 + c16) * 128 + (((4 * k + g) ^ (c16 >> 1)) << 4);
    boff[k] = G1_BOFF + (g >> 1) * (NP * 1024) + (8 * (g & 1) + (c16 >> 2)) * 64 + ((k ^ g) & 1) * 32 + (l & 3) * 8;
  }
  bf16x8 fa0[4], fa1[4], fb0[NP], fb1[NP];
#define G1_RA(FA, base, k) do { _Pragma("unroll") for (int m_ = 0; m_ < 4; ++m_) FA[m_] = lds_b128((base) + aoff[k] + m_ * 2048); } while (0)
#define G1_RB(FB, base, k, t) do { _Pragma("unroll") for (int p_ = 0; p_ < NP; ++p_) \
      FB[p_] = cat8(lds_tr((base) + boff[t] + p_ * 1024 + (k) * (NP * 2048)), lds_tr((base) + boff[t] + p_ * 1024 + (k) * (NP * 2048) + 256)); } while (0)
#define G1_MM(FA, FB, t) do { \
    _Pragma("unroll") for (int m_ = 0; m_ < 4; ++m_) _Pragma("unroll") for (int p_ = 0; p_ < NP; ++p_) { \
      if (NP == G1_KV && p_ == 1) acc[m_][p_][t] = MFMA16(FA[m_], FB[p_], acc[m_][p_][t]); \
      else acc[m_][p_][t] = MFMA16(FB[p_], FA[m_], acc[m_][p_][t]); } \
  } while (0)
#define G1_SCHED(NR) do { \
    _Pragma("unroll") for (int q_ = 0; q_ < (NR); ++q_) { \
      __builtin_amdgcn_sched_group_barrier(0x008, 1, 0); __builtin_amdgcn_sched_group_barrier(0x100, 1, 0); } \
    __builtin_amdgcn_sched_group_barrier(0x008, 4 * NP - (NR), 0); SBAR(); \
  } while (0)

#pragma unroll
  for (int m = 0; m < 4; ++m) fa1[m] = bf16x8{};
#pragma unroll
  for (int p = 0; p < NP; ++p) fb1[p] = bf16x8{};
  int slot = 0;
#pragma unroll 1
  for (int i = 0; i < 16; ++i) {
    const LAS char* nb = lbase + slot * G1_STAGE;
    asm volatile("s_waitcnt lgkmcnt(0)\n\ts_barrier" ::: "memory"); SBAR();
    if (i == 0) STAMP(1, 1);
    G1_RB(fb0, nb, 0, 0); G1_RA(fa0, nb, 0); G1_MM(fa1, fb1, 1); G1_SCHED(2 * NP + 4);
    G1_RB(fb1, nb, 0, 1);                    G1_MM(fa0, fb0, 0); G1_SCHED(2 * NP);
    G1_RB(fb0, nb, 1, 0); G1_RA(fa1, nb, 1); G1_MM(fa0, fb1, 1); G1_SCHED(2 * NP + 4);
    G1_RB(fb1, nb, 1, 1);                    G1_MM(fa1, fb0, 0); G1_SCHED(2 * NP);
    slot = (slot == 2) ? 0 : slot + 1;
  }
  STAMP(1, 4);
  __builtin_amdgcn_s_setprio(3);
  asm volatile("" : "+v"(l));
  g = l >> 4; c16 = l & 15;
#define G1_MMA(FA, FB, t) do { \
    _Pragma("unroll") for (int m_ = 0; m_ < 4; ++m_) _Pragma("unroll") for (int p_ = 0; p_ < NP; ++p_) { \
      if (NP == G1_KV && p_ == 1) asm volatile("v_mfma_f32_16x16x32_bf16 %0, %1, %2, %0" : "+v"(acc[m_][p_][t]) : "v"(FA[m_]), "v"(FB[p_])); \
      else asm volatile("v_mfma_f32_16x16x32_bf16 %0, %1, %2, %0" : "+v"(acc[m_][p_][t]) : "v"(FB[p_]), "v"(FA[m_])); } \
  } while (0)
  unsigned st[4];
  {
#pragma unroll
    for (int m = 0; m < 4; ++m) st[m] = *(const LAS unsigned*)(lbase + G1_ST + (w * 64 + m * 16 + c16) * 4);
#pragma unroll
    for (int p = 0; p < NP; ++p) fb0[p] = lds_b128(lbase + G1_CF + p * 2048 + l * 16);
    SBAR();
    G1_MMA(fa1, fb1, 1);
    SBAR();
#pragma unroll
    for (int p = 0; p < NP; ++p) fb1[p] = lds_b128(lbase + G1_CF + p * 2048 + 1024 + l * 16);
#pragma unroll
    for (int m = 0; m < 4; ++m) {
      const unsigned nm = st[m] & 0xffffu;
      fa0[m] = __builtin_bit_cast(bf16x8, (u32x4){nm | (nm << 16), st[m], 0u, 0u});
      PIN(fa0[m]);
    }
    SBAR();
    asm volatile("s_nop 1");
    G1_MMA(fa0, fb0, 0);
    G1_MMA(fa0, fb1, 1);
    asm volatile("s_nop 7");
  }
#undef G1_MMA
  SBAR();
  STAMP(1, 2);

#pragma unroll
  for (int p = 0; p < NP; ++p) {
    const int cb = (NP == G1_FFN) ? (cg - 9) * 5 + p : cg * 4 + p;
    if (NP == G1_KV && p == 1) {
#pragma unroll
      for (int mm = 0; mm < 2; ++mm) {
        const u32x4 s0 = *(const LAS u32x4*)(lbase + G1_ST + (w * 64 + mm * 32 + 4 * g) * 4), s1 = *(const LAS u32x4*)(lbase + G1_ST + (w * 64 + mm * 32 + 16 + 4 * g) * 4);
#pragma unroll
        for (int t = 0; t < 2; ++t) {
          const f32x4 a = acc[2 * mm][p][t], b = acc[2 * mm + 1][p][t];
          const u32x4 o = {cvtpk(a[0] / bf_hi(s0[0]), a[1] / bf_hi(s0[1])), cvtpk(a[2] / bf_hi(s0[2]), a[3] / bf_hi(s0[3])),
                           cvtpk(b[0] / bf_hi(s1[0]), b[1] / bf_hi(s1[1])), cvtpk(b[2] / bf_hi(s1[2]), b[3] / bf_hi(s1[3]))};
          store16_dev(ws + WS_KV + (size_t)(mtile * 8 + w * 2 + mm) * 4096 + 2048 + (g >> 1) * 1024 + ((g & 1) * 32 + t * 16 + c16) * 16, o);
        }
      }
    } else if (NP != G1_FFN || cb < 112) {
#pragma unroll
      for (int m = 0; m < 4; ++m) {
        const int tt = mtile * 8 + w * 2 + (m >> 1);
        const int pos = (NP == G1_KV) ? (c16 & 7) + (m & 1) * 8 + (c16 >> 3) * 16 : (m & 1) * 16 + c16;
        char* dst = (NP == G1_FFN) ? ws + WS_H + (size_t)(cb >> 1) * KT + (size_t)tt * 4096 + ((m & 1) * 2 + (cb & 1)) * 1024 + l * 16
                  : ((NP == G1_Q) ? ws + WS_Q + (size_t)(tt * 32 + cb) * 2048 : ws + WS_KV + (size_t)tt * 4096) + (g >> 1) * 1024 + ((g & 1) * 32 + pos) * 16;
        float sc = 1.0f;
        if (NP != G1_FFN) { sc = 1.0f / bf_hi(st[m]); if (NP == G1_Q) sc *= 0.17677669529663687f * 1.4426950408889634f; }
        u32x4 o;
#pragma unroll
        for (int j = 0; j < 4; ++j) {
          float a = acc[m][p][j >> 1][2 * (j & 1)], b = acc[m][p][j >> 1][2 * (j & 1) + 1];
          if (NP == G1_FFN) { a = relu_s(a); b = relu_s(b); a *= a; b *= b; }
          else { a *= sc; b *= sc; }
          o[j] = cvtpk(a, b);
        }
        store16_dev(dst, o);
      }
    }
  }
  STAMP(1, 3);
#undef G1_RA
#undef G1_RB
#undef G1_MM
#undef G1_SCHED
}

constexpr int AT_NCH = 8;
constexpr int AT_BUF = 32768;
constexpr int AT_BASE = 98304;
constexpr int AT_Q = 65536;
constexpr int AT_DUMP = 90112;
constexpr int MAIN_LDS = 163840;

DI float max16(const f32x16& s) {
  float a = __builtin_fmaxf(s[0], s[1]);
#pragma unroll
  for (int r = 2; r < 16; ++r) a = __builtin_fmaxf(a, s[r]);
  return a;
}
DI bf16x8 pack8(const f32x16& p, int s) {
  u32x4 o;
#pragma unroll
  for (int j = 0; j < 4; ++j) o[j] = cvtpk(p[8 * s + 2 * j], p[8 * s + 2 * j + 1]);
  return __builtin_bit_cast(bf16x8, o);
}

DI void attn_fallback(const char* __restrict__ kv, bf16x8 q0, bf16x8 q1, int l, f32x16& o_out, float& l_out) {
  f32x16 o = {};
  float m = -INFINITY, lsum = 0.f;
  for (int t = 0; t < 64; ++t) {
    const char* p = kv + (size_t)t * 4096 + l * 16;
    const bf16x8 k0 = load16_dev(p), k1 = load16_dev(p + 1024), v0 = load16_dev(p + 2048), v1 = load16_dev(p + 3072);
    f32x16 s = {};
    s = MFMA32(k0, q0, s); s = MFMA32(k1, q1, s);
    const float mn = __builtin_fmaxf(m, xhalf_max(max16(s)));
    const float ms = (mn == -INFINITY) ? 0.f : mn;
    const float alpha = __builtin_amdgcn_exp2f(m - ms);
    m = mn;
    float ps = 0.f;
#pragma unroll
    for (int r = 0; r < 16; ++r) { s[r] = __builtin_amdgcn_exp2f(s[r] - ms); ps += s[r]; }
    lsum = lsum * alpha + ps;
#pragma unroll
    for (int r = 0; r < 16; ++r) o[r] *= alpha;
    o = MFMA32(v0, pack8(s, 0), o); o = MFMA32(v1, pack8(s, 1), o);
  }
  o_out = o;
  l_out = xhalf_sum(lsum);
}

constexpr int G2_SLOT = 32768, G2_BOFF = 16384, G2_PART = 131072;
struct G2Ld {
  const char* hb; const char* ob; const char* wb; const char* wb2; unsigned lds; unsigned voff, voffb; int rot, frot;
  DI void init(const char* ws, const u16* w2, const u16* wo, unsigned lds0, int lw, int l) {
    const int xcd = blockIdx.x & 7, idx = blockIdx.x >> 3, kh = xcd & 1;
    const int mt = ((xcd >> 1) & 1) * 8 + (idx & 7), nt = (xcd >> 2) * 4 + (idx >> 3);
    hb = ws + WS_H + (size_t)(mt * 4 + lw) * 4096 + (size_t)kh * (28 * KT);
    ob = ws + WS_O + (size_t)(mt * 4 + lw) * 4096;
    wb = (const char*)(w2 + (size_t)(kh * 1792 + lw * 16) * DM + nt * 128);
    wb2 = (const char*)(wo + (size_t)(lw * 16) * DM + nt * 128 + kh * 64);
    voffb = (unsigned)((l >> 2) * DM * 2 + (l & 3) * 16);
    lds = lds0 + lw * 4096; voff = l * 16;
    frot = (7 * (mt & 7)) >> 1;
    rot = 2 * (mt & 7);
  }
  DI void ffn(int s) const {
    const unsigned d = lds + (s & 3) * G2_SLOT;
    s += frot; s = s >= 28 ? s - 28 : s;
    const char* a = hb + (size_t)s * KT;
    glds16_dev<0>(voff, a, d); glds16_dev<1024>(voff, a, d); glds16_dev<2048>(voff, a, d); glds16_dev<3072>(voff, a, d);
    const char* b = wb + (size_t)s * 131072;
    glds16<0>(voffb, b, d + G2_BOFF); glds16<0>(voffb, b + 64, d + G2_BOFF + 1024); glds16<0>(voffb, b + 128, d + G2_BOFF + 2048); glds16<0>(voffb, b + 192, d + G2_BOFF + 3072);
  }
  template <int J> DI void ffn_piece(int s) const {
    const unsigned d = lds + (s & 3) * G2_SLOT;
    s += frot; s = s >= 28 ? s - 28 : s;
    if (J < 4) glds16_dev<(J & 3) * 1024>(voff, hb + (size_t)s * KT, d);
    else glds16<0>(voffb, wb + (size_t)s * 131072 + (J & 3) * 64, d + G2_BOFF + (J & 3) * 1024);
  }
  DI void wo(int s) const {
    const unsigned d = lds + (s & 3) * G2_SLOT;
    const int k = (s - 28 + rot) & 15;
    const char* a = ob + (size_t)k * KT;
    glds16_dev<0>(voff, a, d); glds16_dev<1024>(voff, a, d); glds16_dev<2048>(voff, a, d); glds16_dev<3072>(voff, a, d);
    const char* b = wb2 + (size_t)k * 131072;
    glds16<0>(voffb, b, d + G2_BOFF); glds16<0>(voffb, b + 64, d + G2_BOFF + 1024);
  }
};

DI void attn_finish(char* __restrict__ ws, f32x16 o, f32x4 lacc, bf16x8 q0, bf16x8 q1, int l, char* dst) {
  const float l0 = __shfl(lacc[0] + lacc[1], l & 15), l1 = __shfl(lacc[2] + lacc[3], l & 15);
  float lsum = (l & 16) ? l1 : l0;
  bool ok = (lsum >= 9.5367431640625e-07f) && (lsum <= 1.8446744073709552e19f);
#pragma unroll
  for (int r = 0; r < 16; ++r) ok = ok && (__builtin_fabsf(o[r]) <= 3.0e38f);
  if (!__all(ok)) attn_fallback(ws + WS_KV, q0, q1, l, o, lsum);
  const float rl = 1.0f / lsum;
#pragma unroll
  for (int s = 0; s < 2; ++s) {
    u32x4 ov;
#pragma unroll
    for (int j = 0; j < 4; ++j) ov[j] = cvtpk(o[8 * s + 2 * j] * rl, o[8 * s + 2 * j + 1] * rl);
    store16_dev(dst + s * 1024, ov);
  }
}

template <bool LOADER>
DI void attn_phase(char* __restrict__ ws, const u16* w2, const u16* wo, char* smem, const int w, const int l, unsigned* late_pub) {
  const int tt = blockIdx.x >> 2, hg = blockIdx.x & 3;
  const int head = hg * 8 + w, lw = w & 3;
  const unsigned lds0 = (unsigned)(size_t)(LAS char*)smem;
  unsigned* flags = (unsigned*)(ws + WS_FLAG);
  const unsigned voff = l * 16;
  const char* ksrc = ws + WS_KV + (size_t)lw * 4096;
  const unsigned kdst = lds0 + AT_BASE + lw * 4096;
  auto ld = [&](int c, int half) {
    const char* s_ = ksrc + (size_t)c * AT_BUF + half * 16384;
    const unsigned d_ = kdst + (c & 1) * AT_BUF + half * 16384;
    glds16_dev<0>(voff, s_, d_); glds16_dev<1024>(voff, s_, d_); glds16_dev<2048>(voff, s_, d_); glds16_dev<3072>(voff, s_, d_);
  };
  G2Ld g2;
  const unsigned tdump = lds0 + AT_DUMP + lw * 256;
  const int xcd = blockIdx.x & 7, idx = blockIdx.x >> 3, gk = xcd & 1;
  const int mt = ((xcd >> 1) & 1) * 8 + (idx & 7), nt = (xcd >> 2) * 4 + (idx >> 3);
  STAMP(2, 0);
  if (LOADER) {
    if (lw == 0) {
      const unsigned* word = flags + (l == 0 ? FL_KV : FL_Q + (tt >> 3) * 8 + hg * 2 + (l & 1)) * 32;
      const unsigned need = l == 0 ? 32u : 4u;
      bool done = l > 2;
#pragma unroll 1
      for (unsigned spins = 0; spins < SPIN_CAP; ++spins) {
        if (!done) done = __hip_atomic_load((gu32*)word, RLX_AGENT) >= need;
        if (__all(done)) break;
        __builtin_amdgcn_s_sleep(QSLP);
      }
      if (l == 0) __hip_atomic_store((LAS unsigned*)((LAS char*)smem + AT_RDY + 4), 1u, __ATOMIC_RELAXED, __HIP_MEMORY_SCOPE_WORKGROUP);
    } else {
#pragma unroll 1
      for (unsigned spins = 0; spins < 64u * SPIN_CAP; ++spins) {
        if (rfl(__hip_atomic_load((LAS unsigned*)((LAS char*)smem + AT_RDY + 4), __ATOMIC_RELAXED, __HIP_MEMORY_SCOPE_WORKGROUP))) break;
        __builtin_amdgcn_s_sleep(1);
      }
    }
    asm volatile("" ::: "memory");
    const char* qs = ws + WS_Q + (size_t)(tt * 32 + hg * 8 + lw * 2) * 2048;
    const unsigned qd = lds0 + AT_Q + lw * 4096;
    glds16_dev<0>(voff, qs, qd); glds16_dev<1024>(voff, qs, qd); glds16_dev<2048>(voff, qs, qd); glds16_dev<3072>(voff, qs, qd);
    ld(0, 0); ld(0, 1); ld(1, 0); ld(1, 1);
    l2_touch(wo, tdump);
    g2.init(ws, w2, wo, lds0, lw, l);
  }
  const unsigned s_lo = (l & 14) == 0 ? 0x3f803f80u : 0u, s_hi = (l & 14) == 2 ? 0x3f803f80u : 0u;
  const bf16x8_t sel = __builtin_bit_cast(bf16x8_t, (u32x4){s_lo, s_lo, s_hi, s_hi});
  const int sidx = (l & 1) ? 0xeeee : 0x4444;
  f32x16 o = {}, sa, sb;
  f32x4 ls = {};
  u32x8 pa, pb = {};
  PIN(o);
  if (LOADER) {
    asm volatile("s_waitcnt vmcnt(9)" ::: "memory");
    if (l == 0) __hip_atomic_fetch_add((LAS unsigned*)((LAS char*)smem + AT_RDY), 1u, __ATOMIC_RELAXED, __HIP_MEMORY_SCOPE_WORKGROUP);
  }
#pragma unroll 1
  for (unsigned spins = 0; spins < 64u * SPIN_CAP; ++spins) {
    if (rfl(__hip_atomic_load((LAS unsigned*)((LAS char*)smem + AT_RDY), __ATOMIC_RELAXED, __HIP_MEMORY_SCOPE_WORKGROUP)) >= 4u) break;
    __builtin_amdgcn_s_sleep(1);
  }
  asm volatile("" ::: "memory");
  STAMP(2, 1);

  const LAS char* qp = (const LAS char*)smem + AT_Q + w * 2048 + l * 16;
  const bf16x8 q0 = lds_b128(qp), q1 = lds_b128(qp + 1024);
  const LAS char* kvp = (const LAS char*)smem + AT_BASE + l * 16;
  bf16x8 kx0, kx1, ky0, ky1, va0, va1, vb0, vb1, vc0, vc1, vd0, vd1;
  kx0 = lds_b128(kvp); kx1 = lds_b128(kvp + 1024); ky0 = lds_b128(kvp + 4096); ky1 = lds_b128(kvp + 4096 + 1024);
  va0 = lds_b128(kvp + 2048); va1 = lds_b128(kvp + 3072); vb0 = lds_b128(kvp + 4096 + 2048); vb1 = lds_b128(kvp + 4096 + 3072);
  vd0 = va0; vd1 = va1;
  f32x16 nm;
  {
    sa = MFMA32(kx0, q0, o); sa = MFMA32(kx1, q1, sa);
    const float ma = -xhalf_max(max16(sa));
#pragma unroll
    for (int r = 0; r < 16; ++r) { nm[r] = ma; sa[r] += ma; }
    PIN(nm);
  }
#define EX(S, i) S[i] = __builtin_amdgcn_exp2f(S[i])
#define CV(P, j, S, i) P[j] = cvtpk(S[i], S[(i) + 1])
#define PLO(x) __builtin_bit_cast(bf16x8, __builtin_shufflevector(x, x, 0, 1, 2, 3))
#define PHI(x) __builtin_bit_cast(bf16x8, __builtin_shufflevector(x, x, 4, 5, 6, 7))
#define RSUM(x) ls = __builtin_amdgcn_smfmac_f32_16x16x64_bf16(sel, __builtin_bit_cast(bf16x16_t, x), ls, sidx, 0, 0)
#define STEP(S, P, SN, PP, KN0, KN1, VP0, VP1, KL0, KL1, VL0, VL1, NP) do { \
    SN = MFMA32(KN0, q0, nm); SBAR(); KL0 = lds_b128(NP); EX(S, 0); EX(S, 1); EX(S, 2); EX(S, 3); PIN(S); SBAR(); \
    SN = MFMA32(KN1, q1, SN); SBAR(); KL1 = lds_b128((NP) + 1024); CV(P, 0, S, 0); CV(P, 1, S, 2); EX(S, 4); EX(S, 5); EX(S, 6); EX(S, 7); PIN(S); PIN(P); SBAR(); \
    o = MFMA32(VP0, PLO(PP), o); SBAR(); VL0 = lds_b128((NP) + 2048); CV(P, 2, S, 4); EX(S, 8); CV(P, 3, S, 6); EX(S, 9); EX(S, 10); EX(S, 11); PIN(S); PIN(P); SBAR(); \
    o = MFMA32(VP1, PHI(PP), o); SBAR(); VL1 = lds_b128((NP) + 3072); CV(P, 4, S, 8); CV(P, 5, S, 10); EX(S, 12); EX(S, 13); EX(S, 14); PIN(S); PIN(P); SBAR(); \
    RSUM(PP); SBAR(); EX(S, 15); CV(P, 6, S, 12); CV(P, 7, S, 14); PIN(P); SBAR(); \
  } while (0)
#define XP(on, st, j) do { if (LOADER && (on)) g2.template ffn_piece<j>(st); SBAR(); } while (0)
#define PRIO(n) do { __builtin_amdgcn_s_setprio(n); SBAR(); } while (0)
#define HEAD(cb, on, st) do { \
    PRIO(2); \
    STEP(sa, pa, sb, pb, ky0, ky1, vd0, vd1, kx0, kx1, vc0, vc1, (cb) + 2 * 4096); XP(on, st, 2); \
    STEP(sb, pb, sa, pa, kx0, kx1, va0, va1, ky0, ky1, vd0, vd1, (cb) + 3 * 4096); XP(on, st, 3); \
    PRIO(1); \
    STEP(sa, pa, sb, pb, ky0, ky1, vb0, vb1, kx0, kx1, va0, va1, (cb) + 4 * 4096); XP(on, st, 4); \
    STEP(sb, pb, sa, pa, kx0, kx1, vc0, vc1, ky0, ky1, vb0, vb1, (cb) + 5 * 4096); XP(on, st, 5); \
    STEP(sa, pa, sb, pb, ky0, ky1, vd0, vd1, kx0, kx1, vc0, vc1, (cb) + 6 * 4096); XP(on, st, 6); \
    PRIO(0); \
    STEP(sb, pb, sa, pa, kx0, kx1, va0, va1, ky0, ky1, vd0, vd1, (cb) + 7 * 4096); XP(on, st, 7); \
  } while (0)
#define TAIL(nb, on, st) do { \
    STEP(sa, pa, sb, pb, ky0, ky1, vb0, vb1, kx0, kx1, va0, va1, nb); XP(on, st, 0); \
    STEP(sb, pb, sa, pa, kx0, kx1, vc0, vc1, ky0, ky1, vb0, vb1, (nb) + 4096); XP(on, st, 1); \
  } while (0)
  HEAD(kvp, false, 0);
  if (LOADER) {
#pragma unroll 1
    for (int c = 0; c < 3; ++c) {
      asm volatile("s_waitcnt vmcnt(1) lgkmcnt(0)\n\ts_barrier" ::: "memory");
      if (c == 2 && lw == 0) peek_dev(flags + (FL_H + (mt >> 1)) * 32, lds0 + G1_CS);
      ld(c + 2, 0); ld(c + 2, 1);
      const int j = c * 4 + lw;
      l2_touch(j < 7 ? (const char*)(w2 + (size_t)(gk * 1792 + j * 256 + (mt & 7) * 32 + (l >> 1)) * DM + nt * 128 + (l & 1) * 64)
             : j < 9 ? (const char*)(wo + (size_t)((mt & 7) * 128 + (j - 7) * 64 + l) * DM + nt * 128 + gk * 64) : (const char*)wo, tdump);
      PRIO(3);
      const LAS char* nb = kvp + ((c + 1) & 1) * AT_BUF;
      TAIL(nb, false, 0); HEAD(nb, false, 0);
    }
    asm volatile("s_waitcnt vmcnt(1)" ::: "memory");
    if (lw == 0 && rfl(*(const LAS unsigned*)((const LAS char*)smem + G1_CS)) < 92u) wait_ge(flags + (FL_H + (mt >> 1)) * 32, 92u);
#pragma unroll 1
    for (int c = 3; c < 7; ++c) {
      asm volatile("s_waitcnt vmcnt(8) lgkmcnt(0)\n\ts_barrier" ::: "memory");
      if (c < 6) { ld(c + 2, 0); ld(c + 2, 1); }
      PRIO(3);
      const LAS char* nb = kvp + ((c + 1) & 1) * AT_BUF;
      TAIL(nb, true, c - 3); HEAD(nb, true, c - 3);
    }
  } else {
#pragma unroll 1
    for (int c = 0; c < 7; ++c) {
      asm volatile("s_waitcnt lgkmcnt(0)\n\ts_barrier" ::: "memory");
      if (c == 0 && late_pub) publish(late_pub, l);
      if (!(c & 1)) STAMP(2, 4 + (c >> 1));
      PRIO(3);
      const LAS char* nb = kvp + ((c + 1) & 1) * AT_BUF;
      TAIL(nb, false, 0); HEAD(nb, false, 0);
    }
  }
  TAIL(kvp, false, 0);
#undef HEAD
#undef TAIL
  o = MFMA32(vd0, PLO(pb), o); o = MFMA32(vd1, PHI(pb), o); RSUM(pb);
#undef EX
#undef CV
#undef PLO
#undef PHI
#undef RSUM
#undef STEP
#undef PRIO
#undef XP
  STAMP(2, 2);
  __builtin_amdgcn_s_setprio(0);
  attn_finish(ws, o, ls, q0, q1, l, ws + WS_O + (size_t)(head >> 1) * KT + (size_t)tt * 4096 + (head & 1) * 2048 + l * 16);
  STAMP(2, 3);
}

DI void g2_load(const u16* w2, const u16* wo, char* __restrict__ ws, unsigned lds0, const int lw, const int l) {
  const int xcd = blockIdx.x & 7, idx = blockIdx.x >> 3;
  const int mt = ((xcd >> 1) & 1) * 8 + (idx & 7);
  unsigned* flags = (unsigned*)(ws + WS_FLAG);
  G2Ld g; g.init(ws, w2, wo, lds0, lw, l);
  asm volatile("s_waitcnt vmcnt(10) lgkmcnt(0)\n\ts_barrier" ::: "memory");
  g.ffn(4); g.ffn(5);
  const LAS unsigned* peek = (const LAS unsigned*)(size_t)(lds0 + G2_PART);
#pragma unroll 1
  for (int i = 2; i < 24; ++i) {
    asm volatile("s_waitcnt vmcnt(16)\n\ts_barrier" ::: "memory");
    if (i == 6 && l == 0) __hip_atomic_fetch_add((gu32*)flags + (FL_O + (blockIdx.x >> 4)) * 32, 1u, RLX_AGENT);
    if (i == 20 && lw == 0) peek_dev(flags + (FL_O + mt) * 32, lds0 + G2_PART);
    g.ffn(i + 4);
  }
  if (lw == 0 && rfl(peek[0]) < 128u) wait_ge(flags + (FL_O + mt) * 32, 128u);
  asm volatile("s_waitcnt vmcnt(16)\n\ts_barrier" ::: "memory");
  g.wo(28);
  peek = (const LAS unsigned*)(size_t)(lds0 + G2_BOFF + 2048);
#pragma unroll 1
  for (int i = 25; i < 37; ++i) {
    asm volatile("s_waitcnt vmcnt(12)\n\ts_barrier" ::: "memory");
    if (i == 35 && lw == 0) peek_dev(flags + (FL_P + blockIdx.x) * 32, lds0 + G2_BOFF + 2048);
    g.wo(i + 4);
  }
  auto part = [&]() {
    const char* src = ws + WS_PART + (size_t)(blockIdx.x ^ 1) * 16384 + lw * 4096;
    const unsigned d = lds0 + G2_PART + lw * 4096;
    glds16_dev<0>(g.voff, src, d); glds16_dev<1024>(g.voff, src, d); glds16_dev<2048>(g.voff, src, d); glds16_dev<3072>(g.voff, src, d);
  };
  asm volatile("s_waitcnt vmcnt(12)\n\ts_barrier" ::: "memory");
  const bool early = rfl(peek[0]) >= 4u;
  g.wo(41);
  if (early) part();
#pragma unroll 1
  for (int i = 38; i < 41; ++i) {
    if (early) asm volatile("s_waitcnt vmcnt(16)\n\ts_barrier" ::: "memory");
    else asm volatile("s_waitcnt vmcnt(12)\n\ts_barrier" ::: "memory");
    if (i < 40) g.wo(i + 4);
  }
  if (!early && lw == 0) wait_ge(flags + (FL_P + blockIdx.x) * 32, 4u);
  asm volatile("s_waitcnt vmcnt(6)\n\ts_barrier" ::: "memory");
  if (!early) part();
  asm volatile("s_waitcnt vmcnt(0)\n\ts_barrier" ::: "memory");
}

DI void g2_compute(const u16* __restrict__ x, const u16* __restrict__ b2, char* __restrict__ ws, u16* __restrict__ out,
                   char* smem, const int w, const int l) {
  const int xcd = blockIdx.x & 7, idx = blockIdx.x >> 3;
  const int kh = xcd & 1, mt = ((xcd >> 1) & 1) * 8 + (idx & 7), nt = (xcd >> 2) * 4 + (idx >> 3);
  unsigned* flags = (unsigned*)(ws + WS_FLAG);
  STAMP(3, 0);
  const int h = l >> 5, wm = w & 1, wn = w >> 1;
  f32x16 accg[2] = {}, accp[2] = {}, accs[2] = {};
  const int col0 = nt * 128 + kh * 64 + wn * 32;
  const int row0 = mt * 128 + wm * 64 + (l & 31);
  unsigned stw[2];
  u32x4 xv[4];
  u32x2 bv[4];
#pragma unroll
  for (int m = 0; m < 2; ++m) stw[m] = __hip_atomic_load((gu32*)(ws + WS_STAT) + row0 + m * 32, RLX_AGENT);
#pragma unroll
  for (int k = 0; k < 4; ++k) xv[k] = *(const u32x4*)(x + (size_t)(row0 + (k >> 1) * 32) * DM + col0 + 16 * (k & 1) + 8 * h);
#pragma unroll
  for (int k = 0; k < 4; ++k) bv[k] = *(const u32x2*)(b2 + col0 + 8 * k + 4 * h);
  const LAS char* lbase = (const LAS char*)smem + l * 16;
  const int btr = (4 * h + ((l & 15) >> 2)) * 64 + ((l >> 4) & 1) * 32 + (l & 3) * 8 - l * 16;
#define LDB(p_) cat8(lds_tr(p_), lds_tr((p_) + 512))
#define LDH(p_) cat8(lds_tr(p_), lds_tr((p_) + 4096))
  const int ah = wm * 8192 + ((l >> 4) & 1) * 2048 + h * 256 + (l & 15) * 16 - l * 16;
  const int ao = wm * 8192;
  const int bG = G2_BOFF + (kh * 2 + wn) * 1024 + btr, bP = G2_BOFF + ((kh ^ 1) * 2 + wn) * 1024 + btr;
  const int bW = G2_BOFF + wn * 1024 + btr;
  bf16x8 fa0[2], fb0[2], fa1[2], fb1[2];
#define G2_RD(FA, FB, base, ks) do { \
    FA[0] = lds_b128((base) + ah + (ks) * 512); FB[0] = LDH((base) + bG + ((ks) >> 1) * 8192 + ((ks) & 1) * 512); \
    FA[1] = lds_b128((base) + ah + 4096 + (ks) * 512); FB[1] = LDH((base) + bP + ((ks) >> 1) * 8192 + ((ks) & 1) * 512); \
  } while (0)
#define G2_MM(FA, FB) do { \
    accg[0] = MFMA32(FB[0], FA[0], accg[0]); accg[1] = MFMA32(FB[0], FA[1], accg[1]); \
    accp[0] = MFMA32(FB[1], FA[0], accp[0]); accp[1] = MFMA32(FB[1], FA[1], accp[1]); \
  } while (0)
#define G2_SCHED(NR) do { \
    __builtin_amdgcn_sched_group_barrier(0x008, 1, 0); __builtin_amdgcn_sched_group_barrier(0x100, NR, 0); \
    __builtin_amdgcn_sched_group_barrier(0x008, 1, 0); __builtin_amdgcn_sched_group_barrier(0x100, NR, 0); \
    __builtin_amdgcn_sched_group_barrier(0x008, 2, 0); SBAR(); \
  } while (0)
#define G2_BAR() do { asm volatile("s_waitcnt lgkmcnt(0)\n\ts_barrier" ::: "memory"); SBAR(); } while (0)

  SBAR();
  STAMP(3, 1);
  G2_RD(fa0, fb0, lbase, 0); SBAR();
  G2_RD(fa1, fb1, lbase, 1); G2_MM(fa0, fb0); G2_SCHED(3);
  G2_RD(fa0, fb0, lbase, 2); G2_MM(fa1, fb1); G2_SCHED(3);
  G2_RD(fa1, fb1, lbase, 3); G2_MM(fa0, fb0); G2_SCHED(3);
#define F_BODY(nb_) do { \
    G2_RD(fa0, fb0, nb_, 0); G2_MM(fa1, fb1); G2_SCHED(3); \
    G2_RD(fa1, fb1, nb_, 1); G2_MM(fa0, fb0); G2_SCHED(3); \
    G2_RD(fa0, fb0, nb_, 2); G2_MM(fa1, fb1); G2_SCHED(3); \
    G2_RD(fa1, fb1, nb_, 3); G2_MM(fa0, fb0); G2_SCHED(3); } while (0)
  const LAS char* lhi = lbase + 2 * G2_SLOT;
  F_BODY(lbase + G2_SLOT);
  G2_BAR(); F_BODY(lhi);
  G2_BAR(); F_BODY(lhi + G2_SLOT);
  publish(flags + (FL_O + (blockIdx.x >> 4)) * 32, l); SBAR();
#pragma unroll 1
  for (int i = 0; i < 6; ++i) {
    G2_BAR(); F_BODY(lbase);
    G2_BAR(); F_BODY(lbase + G2_SLOT);
    G2_BAR(); F_BODY(lhi);
    G2_BAR(); F_BODY(lhi + G2_SLOT);
  }
#undef F_BODY
  STAMP(3, 4);
  bf16x8 ga0[4], gb0[2], ga1[4], gb1[2];
#define W_RD(GA, GB, base, ks) do { \
    GA[0] = lds_b128((base) + ao + (ks) * 1024); GB[0] = LDB((base) + bW + (ks) * 4096); GA[1] = lds_b128((base) + ao + 4096 + (ks) * 1024); \
    GA[2] = lds_b128((base) + ao + (ks) * 1024 + 1024); GB[1] = LDB((base) + bW + (ks) * 4096 + 4096); GA[3] = lds_b128((base) + ao + 4096 + (ks) * 1024 + 1024); \
  } while (0)
#define W_MM(GA, GB) do { \
    accs[0] = MFMA32(GB[0], GA[0], accs[0]); accs[1] = MFMA32(GB[0], GA[1], accs[1]); \
    accs[0] = MFMA32(GB[1], GA[2], accs[0]); accs[1] = MFMA32(GB[1], GA[3], accs[1]); \
  } while (0)
#define W_STEP(i) do { const LAS char* nb_ = lbase + (((i) + 1) & 3) * G2_SLOT; G2_BAR(); \
    W_RD(ga0, gb0, nb_, 0); W_MM(ga1, gb1); G2_SCHED(4); \
    W_RD(ga1, gb1, nb_, 2); W_MM(ga0, gb0); G2_SCHED(4); } while (0)
  char* pdst = ws + WS_PART + (size_t)blockIdx.x * 16384 + w * 4096 + l * 16;
#define G2_SEND(q) do { const int b_ = 8 * ((q) & 1); \
    store16_dev(pdst + (q) * 1024, (u32x4){cvtpk(accp[(q) >> 1][b_], accp[(q) >> 1][b_ + 1]), cvtpk(accp[(q) >> 1][b_ + 2], accp[(q) >> 1][b_ + 3]), \
                                           cvtpk(accp[(q) >> 1][b_ + 4], accp[(q) >> 1][b_ + 5]), cvtpk(accp[(q) >> 1][b_ + 6], accp[(q) >> 1][b_ + 7])}); SBAR(); } while (0)
  G2_BAR();
  W_RD(ga0, gb0, lbase, 0); G2_MM(fa1, fb1); G2_SCHED(4);
  W_RD(ga1, gb1, lbase, 2); W_MM(ga0, gb0); G2_SCHED(4);
  G2_SEND(0); G2_SEND(1);
  STAMP(3, 5);
  W_STEP(28); G2_SEND(2); G2_SEND(3);
  W_STEP(29); W_STEP(30); W_STEP(31); W_STEP(32);
  publish(flags + (FL_P + (blockIdx.x ^ 1)) * 32, l); SBAR();
  W_STEP(33);
#pragma unroll 1
  for (int i = 0; i < 2; ++i) { W_STEP(34); W_STEP(35); W_STEP(36); W_STEP(37); }
  STAMP(3, 6);
  W_STEP(42);
  W_MM(ga1, gb1);
  STAMP(3, 2);
  {
    const LAS char* xp = (const LAS char*)smem + G2_PART + w * 4096 + l * 16;
#pragma unroll
    for (int m = 0; m < 2; ++m) {
      const float den = bf_hi(stw[m]), rd2 = 1.0f / (den * den);
#pragma unroll
      for (int k = 0; k < 2; ++k) {
        u32x4 xx = xv[m * 2 + k];
        { auto r0 = __builtin_amdgcn_permlane32_swap(xx[0], xx[2], false, false); xx[0] = r0[0]; xx[2] = r0[1];
          auto r1 = __builtin_amdgcn_permlane32_swap(xx[1], xx[3], false, false); xx[1] = r1[0]; xx[3] = r1[1]; }
        unsigned pk[4];
        const u32x4 tp = *(const LAS u32x4*)(xp + (m * 2 + k) * 1024);
#pragma unroll
        for (int ii = 0; ii < 2; ++ii) {
          const int i = 2 * k + ii;
          float r[4];
#pragma unroll
          for (int j = 0; j < 4; ++j) {
            const unsigned xw_ = xx[2 * ii + (j >> 1)], bw_ = bv[i][j >> 1], pw_ = tp[2 * ii + (j >> 1)];
            const float z = (accg[m][4 * i + j] + ((j & 1) ? bf_hi(pw_) : bf_lo(pw_))) * rd2 + ((j & 1) ? bf_hi(bw_) : bf_lo(bw_));
            const float sg = z * __builtin_amdgcn_rcpf(1.0f + __builtin_amdgcn_exp2f(-1.4426950408889634f * z));
            r[j] = ((j & 1) ? bf_hi(xw_) : bf_lo(xw_)) + accs[m][4 * i + j] * sg;
          }
          pk[2 * ii] = cvtpk(r[0], r[1]); pk[2 * ii + 1] = cvtpk(r[2], r[3]);
        }
        auto r0 = __builtin_amdgcn_permlane32_swap(pk[0], pk[2], false, false);
        auto r1 = __builtin_amdgcn_permlane32_swap(pk[1], pk[3], false, false);
        *(u32x4*)(out + (size_t)(row0 + m * 32) * DM + col0 + 16 * k + 8 * h) = (u32x4){r0[0], r1[0], r0[1], r1[1]};
      }
    }
  }
  STAMP(3, 3);
#undef G2_RD
#undef G2_MM
#undef G2_SCHED
#undef G2_BAR
#undef G2_SEND
#undef W_RD
#undef W_MM
#undef W_STEP
#undef LDB
#undef LDH
}

__global__ __launch_bounds__(512, 2) void main_kernel(const u16* __restrict__ x, const u16* __restrict__ wq, const u16* __restrict__ wk,
                                                      const u16* __restrict__ wv, const u16* __restrict__ wo, const u16* __restrict__ w1,
                                                      const u16* __restrict__ b1, const u16* __restrict__ w2, const u16* __restrict__ b2,
                                                      char* __restrict__ ws, u16* __restrict__ out) {
  extern __shared__ __attribute__((aligned(16))) char smem[];
  const int l = threadIdx.x & 63, w = rfl(threadIdx.x >> 6);
  const unsigned lds0 = (unsigned)(size_t)(LAS char*)smem;
  const int xcd = blockIdx.x & 7, idx = blockIdx.x >> 3;
  const int mtile = (xcd & 1) * 4 + (idx & 3), cg = (idx >> 2) * 4 + (xcd >> 1);
  unsigned* flags = (unsigned*)(ws + WS_FLAG);
  if (w == 3) {
    unsigned long long pc; unsigned len;
    asm volatile("s_getpc_b64 %0\n.Lmk_code_begin:\n\ts_mov_b32 %1, .Lmk_code_end-.Lmk_code_begin" : "=s"(pc), "=s"(len));
    unsigned off = (unsigned)((l & 31) * 32 + idx) * 128u;
    off = (off < len && len <= 131072u) ? off : 0u;
    l2_touch((const char*)pc + off, lds0 + G1_HS + 3072);
  }
  if (w >= 4) {
    if (cg > 8) g1_load<G1_FFN>(ws, x, w1, nullptr, b1, cg, mtile, smem, w - 4, l);
    else if (cg < 8) g1_load<G1_Q>(ws, x, wq, nullptr, b1, cg, mtile, smem, w - 4, l);
    else g1_load<G1_KV>(ws, x, wk, wv, b1, cg, mtile, smem, w - 4, l);
    attn_phase<true>(ws, w2, wo, smem, w, l, nullptr);
    g2_load(w2, wo, ws, lds0, w - 4, l);
    asm volatile(".Lmk_code_end:");
  } else {
    unsigned* late = nullptr;
    if (cg > 8) { g1_compute<G1_FFN>(ws, x, cg, mtile, smem, w, l); late = flags + (FL_H + mtile) * 32; }
    else if (cg < 8) { g1_compute<G1_Q>(ws, x, cg, mtile, smem, w, l); publish(flags + (FL_Q + mtile * 8 + cg) * 32, l); }
    else { g1_compute<G1_KV>(ws, x, cg, mtile, smem, w, l); publish(flags + FL_KV * 32, l); }
    attn_phase<false>(ws, w2, wo, smem, w, l, late);
    g2_compute(x, b2, ws, out, smem, w, l);
  }
}

extern "C" void kernel_launch(void* const* d_in, const int* in_sizes, int n_in, void* d_out, int out_size, void* d_ws,
                              size_t ws_size, hipStream_t stream) {
  const u16* x = (const u16*)d_in[0];
  const u16* wq = (const u16*)d_in[1];
  const u16* wk = (const u16*)d_in[2];
  const u16* wv = (const u16*)d_in[3];
  const u16* wo = (const u16*)d_in[4];
  const u16* w1 = (const u16*)d_in[6];
  const u16* b1 = (const u16*)d_in[7];
  const u16* w2 = (const u16*)d_in[8];
  const u16* b2 = (const u16*)d_in[9];
  char* ws = (char*)d_ws;
  (void)hipFuncSetAttribute((const void*)main_kernel, hipFuncAttributeMaxDynamicSharedMemorySize, MAIN_LDS);
  main_kernel<<<256, 512, MAIN_LDS, stream>>>(x, wq, wk, wv, wo, w1, b1, w2, b2, ws, (u16*)d_out);
}
```

```cpp
#include <hip/hip_runtime.h>
#include <hip/hip_bf16.h>

typedef short bf16x8 __attribute__((ext_vector_type(8)));
typedef short s16x4 __attribute__((ext_vector_type(4)));
typedef float f32x16 __attribute__((ext_vector_type(16)));
typedef float f32x4 __attribute__((ext_vector_type(4)));
typedef float f32x2 __attribute__((ext_vector_type(2)));
typedef __bf16 bf16x2_t __attribute__((ext_vector_type(2)));
typedef unsigned u32x4 __attribute__((ext_vector_type(4)));
typedef unsigned u32x2 __attribute__((ext_vector_type(2)));
typedef unsigned u32x8 __attribute__((ext_vector_type(8)));
typedef __bf16 bf16x8_t __attribute__((ext_vector_type(8)));
typedef __bf16 bf16x16_t __attribute__((ext_vector_type(16)));
typedef unsigned short u16;
#define LAS __attribute__((address_space(3)))
#define DI __device__ __forceinline__
#define SBAR() __builtin_amdgcn_sched_barrier(0)
#define PIN(x) asm volatile("" : "+v"(x))
#define MFMA16(a, b, c) __builtin_amdgcn_mfma_f32_16x16x32_bf16((a), (b), (c), 0, 0, 0)
#define MFMA32(a, b, c) __builtin_amdgcn_mfma_f32_32x32x16_bf16((a), (b), (c), 0, 0, 0)

constexpr int DM = 1024, FF = 3584;
constexpr size_t KT = 262144;
constexpr size_t WS_STAT = 0;
constexpr size_t WS_Q = 4u << 20;
constexpr size_t WS_KV = 8u << 20;
constexpr size_t WS_O = 9u << 20;
constexpr size_t WS_H = 13u << 20;
constexpr size_t WS_PART = 28u << 20;
constexpr size_t WS_FLAG = 37u << 20;
constexpr int FL_P = 0, FL_O = 256, FL_H = 272, FL_Q = 280, FL_KV = 344, FL_S = 345, FL_N = 353;
constexpr size_t WS_HSA = WS_FLAG + 65536, WS_HSB = WS_HSA + 4096, WS_HSC = WS_HSB + 4096;
constexpr int COORD_BLOCK = 64;

DI unsigned rfl(unsigned x) { return __builtin_amdgcn_readfirstlane(x); }
DI unsigned cvtpk(float lo, float hi) {
  f32x2 v = {lo, hi};
  bf16x2_t b = __builtin_convertvector(v, bf16x2_t);
  return __builtin_bit_cast(unsigned, b);
}
DI float bf_lo(unsigned u) { return __uint_as_float(u << 16); }
DI float bf_hi(unsigned u) { return __uint_as_float(u & 0xffff0000u); }
DI float bf_round(float x) { return bf_lo(cvtpk(x, x)); }

template <int IMM>
DI void glds16(unsigned voff, const void* sbase, unsigned ldsaddr) {
  asm volatile("s_mov_b32 m0, %2\n\ts_nop 0\n\tglobal_load_lds_dwordx4 %0, %1 offset:%3"
               :: "v"(voff), "s"(sbase), "s"(ldsaddr), "i"(IMM) : "memory");
}
template <int IMM>
DI void glds16_dev(unsigned voff, const void* sbase, unsigned ldsaddr) {
  asm volatile("s_mov_b32 m0, %2\n\ts_nop 0\n\tglobal_load_lds_dwordx4 %0, %1 offset:%3 sc1"
               :: "v"(voff), "s"(sbase), "s"(ldsaddr), "i"(IMM) : "memory");
}
DI void set_m0(unsigned ldsaddr) { asm volatile("s_mov_b32 m0, %0\n\ts_nop 0" :: "s"(ldsaddr) : "memory"); }
template <int IMM> DI void gl16(unsigned voff, const void* sbase) {
  asm volatile("global_load_lds_dwordx4 %0, %1 offset:%2" :: "v"(voff), "s"(sbase), "i"(IMM) : "memory");
}
template <int IMM> DI void gl16_dev(unsigned voff, const void* sbase) {
  asm volatile("global_load_lds_dwordx4 %0, %1 offset:%2 sc1" :: "v"(voff), "s"(sbase), "i"(IMM) : "memory");
}
DI void peek_dev(const void* word, unsigned ldsaddr) {
  asm volatile("s_mov_b32 m0, %2\n\ts_nop 0\n\tglobal_load_lds_dword %0, %1 sc1" :: "v"(0u), "s"(word), "s"(ldsaddr) : "memory");
}
DI void store16_dev(void* p, u32x4 v) {
  asm volatile("global_store_dwordx4 %0, %1, off sc1\n\ts_nop 1" :: "v"(p), "v"(v) : "memory");
}
DI void l2_touch(const void* p, unsigned ldsaddr) {
  asm volatile("s_mov_b32 m0, %1\n\ts_nop 0\n\tglobal_load_lds_dword %0, off" :: "v"(p), "s"(ldsaddr) : "memory");
}
constexpr unsigned SPIN_CAP = 1u << 20;
typedef __attribute__((address_space(1))) unsigned gu32;
#define RLX_AGENT __ATOMIC_RELAXED, __HIP_MEMORY_SCOPE_AGENT
DI void wait_ge(const unsigned* word, unsigned want) {
#pragma unroll 1
  for (unsigned spins = 0; spins < SPIN_CAP; ++spins) {
    if (rfl(__hip_atomic_load((gu32*)word, RLX_AGENT)) >= want) break;
    __builtin_amdgcn_s_sleep(8);
  }
  __builtin_amdgcn_fence(__ATOMIC_ACQUIRE, "wavefront");
}
DI void publish(unsigned* word, int l) {
  asm volatile("s_waitcnt vmcnt(0)" ::: "memory");
  if (l == 0) __hip_atomic_fetch_add((gu32*)word, 1u, RLX_AGENT);
}
DI bf16x8 load16_dev(const char* p) {
  typedef __attribute__((address_space(1))) unsigned long long gu64;
  const unsigned long long a = __hip_atomic_load((gu64*)p, RLX_AGENT), b = __hip_atomic_load((gu64*)p + 1, RLX_AGENT);
  return __builtin_bit_cast(bf16x8, (u32x4){(unsigned)a, (unsigned)(a >> 32), (unsigned)b, (unsigned)(b >> 32)});
}
DI float relu_s(float a) { return __builtin_amdgcn_fmed3f(a, 0.0f, 3.4028234663852886e38f); }
DI bf16x8 lds_b128(const LAS char* p) { return *(const LAS bf16x8*)p; }
DI s16x4 lds_tr(const LAS char* p) {
  return __builtin_amdgcn_ds_read_tr16_b64_v4i16((LAS s16x4*)p);
}
DI bf16x8 cat8(s16x4 lo, s16x4 hi) { return __builtin_shufflevector(lo, hi, 0, 1, 2, 3, 4, 5, 6, 7); }

DI float half_sum(float v) {
  v += __uint_as_float(__builtin_amdgcn_update_dpp(0u, __float_as_uint(v), 0xB1, 0xf, 0xf, true));
  v += __uint_as_float(__builtin_amdgcn_update_dpp(0u, __float_as_uint(v), 0x4E, 0xf, 0xf, true));
  v += __uint_as_float(__builtin_amdgcn_update_dpp(0u, __float_as_uint(v), 0x141, 0xf, 0xf, true));
  v += __uint_as_float(__builtin_amdgcn_update_dpp(0u, __float_as_uint(v), 0x140, 0xf, 0xf, true));
  auto r = __builtin_amdgcn_permlane16_swap(__float_as_uint(v), __float_as_uint(v), false, false);
  return __uint_as_float(r[0]) + __uint_as_float(r[1]);
}
DI unsigned row_stats(const u32x4 (&v)[4]) {
  f32x2 e[16], s2 = {0.f, 0.f};
#pragma unroll
  for (int i = 0; i < 16; ++i) { e[i] = (f32x2){bf_lo(v[i >> 2][i & 3]), bf_hi(v[i >> 2][i & 3])}; s2 += e[i]; }
  const float mean = half_sum(s2[0] + s2[1]) * (1.0f / DM);
  f32x2 q2 = {0.f, 0.f};
#pragma unroll
  for (int i = 0; i < 16; ++i) { const f32x2 d = e[i] - mean; q2 += d * d; }
  const float q = half_sum(q2[0] + q2[1]);
  const float den = __builtin_sqrtf(bf_round(bf_round(q * (1.0f / DM)) + 1e-5f));
  return cvtpk(-mean, den);
}

constexpr int G1_STAGE = 53248, G1_BOFF = 32768;
enum { G1_FFN = 5, G1_Q = 4, G1_KV = 2 };

constexpr int G1_ST = 87040;
constexpr int G1_CF = 53248;
constexpr int AT_RDY = 89088;
constexpr int G1_HS = 159744;
constexpr int G1_CS = 81920;
constexpr int ESTART = 3, PSTART = 9;

DI float xhalf_max(float m) {
  auto rr = __builtin_amdgcn_permlane32_swap(__float_as_uint(m), __float_as_uint(m), false, false);
  return __builtin_fmaxf(__uint_as_float(rr[0]), __uint_as_float(rr[1]));
}
DI float xhalf_sum(float m) {
  auto rr = __builtin_amdgcn_permlane32_swap(__float_as_uint(m), __float_as_uint(m), false, false);
  return __uint_as_float(rr[0]) + __uint_as_float(rr[1]);
}
template <int NP>
DI void g1_load(char* ws, const u16* x, const u16* wsrc, const u16* wv, const u16* b1, int cg, int mtile, char* smem, int lw, int l) {
  constexpr int LD = (NP == G1_FFN) ? FF : (NP == G1_Q) ? DM : 32;
  const unsigned lds0 = (unsigned)(size_t)(LAS char*)smem;
  const char* abase = (const char*)(x + (size_t)(mtile * 256 + lw * 64) * DM);
  const unsigned ave = (unsigned)((l >> 3) * 2048 + (((l & 7) ^ (l >> 4)) << 4)), avo = ave ^ 64u;
  const unsigned bvoff = (unsigned)((lw * 16 + (l >> 2)) * LD * 2 + (((l & 3) ^ ((l >> 5) << 1)) << 4));
  const int krot = NP == G1_Q ? (mtile & 3) * 4 : 0;
  unsigned va[8], vb[NP];
#pragma unroll
  for (int i = 0; i < 8; ++i) va[i] = ((i & 1) ? avo : ave) + i * 16384 - (i - 4) * 1024;
#pragma unroll
  for (int p = 0; p < NP; ++p) {
    int cb = (NP == G1_FFN) ? (cg - 9) * 5 + p : (NP == G1_Q) ? cg * 4 + p : 0;
    cb = cb > 111 ? 111 : cb;
    vb[p] = bvoff + cb * 64 - (p - 2) * 1024 + 4096;
  }
  unsigned bias[NP];
#pragma unroll
  for (int p = 0; p < NP; ++p) {
    int cb = (cg - 9) * 5 + p; cb = cb > 111 ? 111 : cb;
    bias[p] = (NP == G1_FFN) ? (unsigned)b1[cb * 32 + (l & 31)] : 0u;
  }
  float cs[NP];
#pragma unroll
  for (int p = 0; p < NP; ++p) cs[p] = 0.f;
  const LAS char* cb0 = (const LAS char*)smem + G1_BOFF + lw * (NP * 1024) + (8 * (l >> 5) + ((l & 15) >> 2)) * 64 + (((l >> 4) ^ (l >> 5)) & 1) * 32 + (l & 3) * 8;
  u32x2 t0[NP], t1[NP];
  auto cs_read = [&](int slot) {
#pragma unroll
    for (int p = 0; p < NP; ++p) {
      t0[p] = __builtin_bit_cast(u32x2, lds_tr(cb0 + slot * G1_STAGE + p * 1024));
      t1[p] = __builtin_bit_cast(u32x2, lds_tr(cb0 + slot * G1_STAGE + p * 1024 + 256));
    }
    SBAR();
  };
  auto cs_add = [&](int p) {
    float a, b, c, d;
    asm volatile("v_lshlrev_b32 %1, 16, %5\n\tv_and_b32 %2, 0xffff0000, %5\n\tv_lshlrev_b32 %3, 16, %6\n\tv_and_b32 %4, 0xffff0000, %6\n\t"
                 "v_add_f32 %1, %1, %2\n\tv_add_f32 %3, %3, %4\n\t"
                 "v_lshlrev_b32 %2, 16, %7\n\tv_and_b32 %4, 0xffff0000, %7\n\tv_add_f32 %1, %1, %2\n\tv_add_f32 %3, %3, %4\n\t"
                 "v_lshlrev_b32 %2, 16, %8\n\tv_and_b32 %4, 0xffff0000, %8\n\tv_add_f32 %1, %1, %2\n\tv_add_f32 %3, %3, %4\n\t"
                 "v_add_f32 %1, %1, %3\n\tv_add_f32 %0, %0, %1"
                 : "+v"(cs[p]), "=&v"(a), "=&v"(b), "=&v"(c), "=&v"(d) : "v"(t0[p][0]), "v"(t0[p][1]), "v"(t1[p][0]), "v"(t1[p][1]));
    SBAR();
  };
  auto issue = [&](int st, int slot, bool sum) {
    const int kt = (st + krot) & 15;
    const char* b0 = (const char*)wsrc + (size_t)kt * (64 * LD * 2) - 4096;
    set_m0(lds0 + slot * G1_STAGE + G1_BOFF + lw * (NP * 1024) + 2048);
    gl16<-2048>(vb[0], b0);
    gl16<-1024>(vb[1], NP == G1_KV ? (const char*)wv + (size_t)kt * (64 * LD * 2) - 4096 : b0);
    if (NP > 2) { gl16<0>(vb[2 % NP], b0); gl16<1024>(vb[3 % NP], b0); }
    if (NP > 4) gl16<2048>(vb[4 % NP], b0);
    const char* a0 = abase + kt * 128;
    set_m0(lds0 + slot * G1_STAGE + lw * 8192 + 4096);
#define AL(i) do { if (sum && (i) < NP) cs_add((i) % NP); gl16<((i) - 4) * 1024>(va[i], a0); } while (0)
    AL(0); AL(1); AL(2); AL(3); AL(4); AL(5); AL(6); AL(7);
#undef AL
  };
  auto colsum = [&](int slot) {
    cs_read(slot);
#pragma unroll
    for (int p = 0; p < NP; ++p) cs_add(p);
  };
  unsigned* flags = (unsigned*)(ws + WS_FLAG);
  const bool coord = (NP == G1_KV) && blockIdx.x == COORD_BLOCK && lw == 1;
  if (coord) {
#pragma unroll
    for (int i = 0; i < 6; ++i) if (i * 64 + l < FL_N) __hip_atomic_store((gu32*)flags + (i * 64 + l) * 32, 0u, RLX_AGENT);
    store16_dev(ws + WS_HSC + l * 16, (u32x4){0u, 0u, 0u, 0u});
    asm volatile("s_waitcnt vmcnt(0)" ::: "memory");
  }
  issue(0, 0, false); issue(1, 1, false); issue(2, 2, false);
  asm volatile("s_waitcnt vmcnt(%0)\n\ts_barrier" :: "n"(2 * (8 + NP)) : "memory");
  __builtin_amdgcn_s_setprio(2);
  int state = (lw == 0 || coord) ? 0 : 2;
  bool pend = false, first = true;
  u32x4 last = {0u, 0u, 0u, 0u};
  const unsigned found = rfl(*(const LAS unsigned*)((const LAS char*)smem + G1_HS));
  if (lw == 0 && l < 9) {
    const unsigned v = *(const LAS unsigned*)((const LAS char*)smem + G1_HS + 16 + (l & 7) * 4);
    __hip_atomic_store(l < 8 ? (gu32*)(ws + WS_STAT) + mtile * 256 + cg * 8 + l : (gu32*)(ws + WS_HSA) + blockIdx.x, l < 8 ? v : found + 1u, RLX_AGENT);
  }
  colsum(0);
  auto poll_issue = [&]() {
    if (NP == G1_KV && coord) {
      glds16_dev<0>(l * 16, ws + WS_HSA, lds0 + G1_HS + 1024); glds16_dev<0>(l * 16, ws + WS_HSC, lds0 + G1_HS + 2048);
    } else {
      const char* a_ = state == 0 ? ws + WS_HSB + blockIdx.x * 4 : (const char*)(flags + (FL_S + mtile) * 32);
      peek_dev(a_, lds0 + G1_HS + 256);
    }
  };
  auto poll_check = [&]() {
    if (NP == G1_KV && coord) {
      const u32x4 pa = *(const LAS u32x4*)((const LAS char*)smem + G1_HS + 1024 + l * 16), pc = *(const LAS u32x4*)((const LAS char*)smem + G1_HS + 2048 + l * 16);
      if (first || pa[0] != last[0] || pa[1] != last[1] || pa[2] != last[2] || pa[3] != last[3]) store16_dev(ws + WS_HSB + l * 16, pa);
      last = pa; first = false;
      if (__all((pc[0] & pc[1] & pc[2] & pc[3]) == 1u)) state = 2;
    } else {
      const unsigned v = rfl(*(const LAS unsigned*)((const LAS char*)smem + G1_HS + 256));
      if (state == 0) {
        if (v != found) {
          if (l == 0) {
            __hip_atomic_store((gu32*)(ws + WS_HSC) + blockIdx.x, 1u, RLX_AGENT);
            __hip_atomic_fetch_add((gu32*)flags + (FL_S + mtile) * 32, 1u, RLX_AGENT);
          }
          state = 1;
        }
      } else if (v >= 32u) state = 2;
    }
    state = rfl(state);
  };
  int slot = 0;
#pragma unroll 1
  for (int i = 0; i < 13; ++i) {
    asm volatile("s_waitcnt vmcnt(%0)\n\ts_barrier" :: "n"(8 + NP) : "memory");
    if (state < 2 && (i >= ESTART || coord)) {
      if (pend) poll_check();
      pend = state == 0 || (state == 1 && i >= PSTART);
      if (pend) poll_issue();
    }
    const int nslot = (slot == 2) ? 0 : slot + 1;
    cs_read(nslot);
    issue(i + 3, slot, true);
    slot = nslot;
  }
#pragma unroll 1
  for (unsigned spins = 0; state < 2 && spins < SPIN_CAP; ++spins) {
    if (!pend) poll_issue();
    asm volatile("s_waitcnt vmcnt(0)" ::: "memory");
    poll_check();
    pend = false;
    __builtin_amdgcn_s_sleep(8);
  }
  asm volatile("s_waitcnt vmcnt(%0)\n\ts_barrier" :: "n"(8 + NP) : "memory");
  if (lw == 0) { glds16_dev<0>(l * 16, ws + WS_STAT + mtile * 1024, lds0 + G1_ST); *(LAS u32x2*)((LAS char*)smem + AT_RDY) = (u32x2){0u, 0u}; }
  colsum(2);
  asm volatile("s_waitcnt vmcnt(0)" ::: "memory");
  colsum(0);
#pragma unroll
  for (int p = 0; p < NP; ++p) {
    const float c = xhalf_sum(cs[p]);
    const unsigned c0 = cvtpk(c, c) & 0xffffu; const float r1 = c - bf_lo(c0);
    const unsigned c1 = cvtpk(r1, r1) & 0xffffu; const float r2 = r1 - bf_lo(c1);
    const unsigned c2 = cvtpk(r2, r2) & 0xffffu;
    if (l < 32) *(LAS u32x4*)((LAS char*)smem + G1_CF + (p * 2 + (l >> 4)) * 1024 + (lw * 16 + (l & 15)) * 16) = (u32x4){c0 | (c1 << 16), c2 | (lw ? 0u : bias[p] << 16), 0u, 0u};
  }
  asm volatile("s_waitcnt lgkmcnt(0)\n\ts_barrier" ::: "memory");
}

template <int NP>
DI void g1_compute(char* __restrict__ ws, const u16* __restrict__ x, int cg, int mtile, char* smem, int w, int l) {
  int g = l >> 4, c16 = l & 15;
  {
    const int row = mtile * 256 + cg * 8 + w * 2 + (l >> 5);
    u32x4 v[4];
#pragma unroll
    for (int i = 0; i < 4; ++i) v[i] = *(const u32x4*)(x + (size_t)row * DM + (i * 32 + (l & 31)) * 8);
    const unsigned found = __hip_atomic_load((gu32*)(ws + WS_HSB) + blockIdx.x, RLX_AGENT);
    SBAR();
    const unsigned rs = row_stats(v);
    if ((l & 31) == 0) *(LAS unsigned*)((LAS char*)smem + G1_HS + 16 + (w * 2 + (l >> 5)) * 4) = rs;
    if (w == 0 && l == 0) *(LAS unsigned*)((LAS char*)smem + G1_HS) = found;
  }
  f32x4 acc[4][NP][2];
#pragma unroll
  for (int m = 0; m < 4; ++m)
#pragma unroll
    for (int p = 0; p < NP; ++p) { acc[m][p][0] = f32x4{}; acc[m][p][1] = f32x4{}; }
  const LAS char* lbase = (const LAS char*)smem;
  int aoff[2], boff[2];
#pragma unroll
  for (int k = 0; k < 2; ++k) {
    aoff[k] = (w * 64 + c16) * 128 + (((4 * k + g) ^ (c16 >> 1)) << 4);
    boff[k] = G1_BOFF + (g >> 1) * (NP * 1024) + (8 * (g & 1) + (c16 >> 2)) * 64 + ((k ^ g) & 1) * 32 + (l & 3) * 8;
  }
  bf16x8 fa0[4], fa1[4], fb0[NP], fb1[NP];
#define G1_RA(FA, base, k) do { _Pragma("unroll") for (int m_ = 0; m_ < 4; ++m_) FA[m_] = lds_b128((base) + aoff[k] + m_ * 2048); } while (0)
#define G1_RB(FB, base, k, t) do { _Pragma("unroll") for (int p_ = 0; p_ < NP; ++p_) \
      FB[p_] = cat8(lds_tr((base) + boff[t] + p_ * 1024 + (k) * (NP * 2048)), lds_tr((base) + boff[t] + p_ * 1024 + (k) * (NP * 2048) + 256)); } while (0)
#define G1_MM(FA, FB, t) do { \
    _Pragma("unroll") for (int m_ = 0; m_ < 4; ++m_) _Pragma("unroll") for (int p_ = 0; p_ < NP; ++p_) { \
      if (NP == G1_KV && p_ == 1) acc[m_][p_][t] = MFMA16(FA[m_], FB[p_], acc[m_][p_][t]); \
      else acc[m_][p_][t] = MFMA16(FB[p_], FA[m_], acc[m_][p_][t]); } \
  } while (0)
#define G1_SCHED(NR) do { \
    _Pragma("unroll") for (int q_ = 0; q_ < (NR); ++q_) { \
      __builtin_amdgcn_sched_group_barrier(0x008, 1, 0); __builtin_amdgcn_sched_group_barrier(0x100, 1, 0); } \
    __builtin_amdgcn_sched_group_barrier(0x008, 4 * NP - (NR), 0); SBAR(); \
  } while (0)

#pragma unroll
  for (int m = 0; m < 4; ++m) fa1[m] = bf16x8{};
#pragma unroll
  for (int p = 0; p < NP; ++p) fb1[p] = bf16x8{};
#define G1_STG(nb) do { \
    asm volatile("s_waitcnt lgkmcnt(0)\n\ts_barrier" ::: "memory"); SBAR(); \
    G1_RB(fb0, nb, 0, 0); G1_RA(fa0, nb, 0); G1_MM(fa1, fb1, 1); G1_SCHED(2 * NP + 4); \
    G1_RB(fb1, nb, 0, 1);                    G1_MM(fa0, fb0, 0); G1_SCHED(2 * NP); \
    G1_RB(fb0, nb, 1, 0); G1_RA(fa1, nb, 1); G1_MM(fa0, fb1, 1); G1_SCHED(2 * NP + 4); \
    G1_RB(fb1, nb, 1, 1);                    G1_MM(fa1, fb0, 0); G1_SCHED(2 * NP); } while (0)
  int slot = 0;
#pragma unroll 1
  for (int i = 0; i < 8; ++i) {
    const int s1 = (slot == 2) ? 0 : slot + 1;
    G1_STG(lbase + slot * G1_STAGE);
    G1_STG(lbase + s1 * G1_STAGE);
    slot = (s1 == 2) ? 0 : s1 + 1;
  }
#undef G1_STG
  __builtin_amdgcn_s_setprio(3);
  asm volatile("" : "+v"(l));
  g = l >> 4; c16 = l & 15;
#define G1_MMA(FA, FB, t) do { \
    _Pragma("unroll") for (int m_ = 0; m_ < 4; ++m_) _Pragma("unroll") for (int p_ = 0; p_ < NP; ++p_) { \
      if (NP == G1_KV && p_ == 1) asm volatile("v_mfma_f32_16x16x32_bf16 %0, %1, %2, %0" : "+v"(acc[m_][p_][t]) : "v"(FA[m_]), "v"(FB[p_])); \
      else asm volatile("v_mfma_f32_16x16x32_bf16 %0, %1, %2, %0" : "+v"(acc[m_][p_][t]) : "v"(FB[p_]), "v"(FA[m_])); } \
  } while (0)
  unsigned st[4];
  {
#pragma unroll
    for (int m = 0; m < 4; ++m) st[m] = *(const LAS unsigned*)(lbase + G1_ST + (w * 64 + m * 16 + c16) * 4);
#pragma unroll
    for (int p = 0; p < NP; ++p) fb0[p] = lds_b128(lbase + G1_CF + p * 2048 + l * 16);
    SBAR();
    G1_MMA(fa1, fb1, 1);
    SBAR();
#pragma unroll
    for (int p = 0; p < NP; ++p) fb1[p] = lds_b128(lbase + G1_CF + p * 2048 + 1024 + l * 16);
#pragma unroll
    for (int m = 0; m < 4; ++m) {
      const unsigned nm = st[m] & 0xffffu;
      fa0[m] = __builtin_bit_cast(bf16x8, (u32x4){nm | (nm << 16), st[m], 0u, 0u});
      PIN(fa0[m]);
    }
    SBAR();
    asm volatile("s_nop 1");
    G1_MMA(fa0, fb0, 0);
    G1_MMA(fa0, fb1, 1);
    asm volatile("s_nop 7");
  }
#undef G1_MMA
  SBAR();

#pragma unroll
  for (int p = 0; p < NP; ++p) {
    const int cb = (NP == G1_FFN) ? (cg - 9) * 5 + p : cg * 4 + p;
    if (NP == G1_KV && p == 1) {
#pragma unroll
      for (int mm = 0; mm < 2; ++mm) {
        const u32x4 s0 = *(const LAS u32x4*)(lbase + G1_ST + (w * 64 + mm * 32 + 4 * g) * 4), s1 = *(const LAS u32x4*)(lbase + G1_ST + (w * 64 + mm * 32 + 16 + 4 * g) * 4);
#pragma unroll
        for (int t = 0; t < 2; ++t) {
          const f32x4 a = acc[2 * mm][p][t], b = acc[2 * mm + 1][p][t];
          const u32x4 o = {cvtpk(a[0] / bf_hi(s0[0]), a[1] / bf_hi(s0[1])), cvtpk(a[2] / bf_hi(s0[2]), a[3] / bf_hi(s0[3])),
                           cvtpk(b[0] / bf_hi(s1[0]), b[1] / bf_hi(s1[1])), cvtpk(b[2] / bf_hi(s1[2]), b[3] / bf_hi(s1[3]))};
          store16_dev(ws + WS_KV + (size_t)(mtile * 8 + w * 2 + mm) * 4096 + 2048 + (g >> 1) * 1024 + ((g & 1) * 32 + t * 16 + c16) * 16, o);
        }
      }
    } else if (NP != G1_FFN || cb < 112) {
#pragma unroll
      for (int m = 0; m < 4; ++m) {
        const int tt = mtile * 8 + w * 2 + (m >> 1);
        const int pos = (NP == G1_KV) ? (c16 & 7) + (m & 1) * 8 + (c16 >> 3) * 16 : (m & 1) * 16 + c16;
        char* dst = (NP == G1_FFN) ? ws + WS_H + (size_t)(cb >> 1) * KT + (size_t)tt * 4096 + ((m & 1) * 2 + (cb & 1)) * 1024 + l * 16
                  : ((NP == G1_Q) ? ws + WS_Q + (size_t)(tt * 32 + cb) * 2048 : ws + WS_KV + (size_t)tt * 4096) + (g >> 1) * 1024 + ((g & 1) * 32 + pos) * 16;
        float sc = 1.0f;
        if (NP != G1_FFN) { sc = 1.0f / bf_hi(st[m]); if (NP == G1_Q) sc *= 0.17677669529663687f * 1.4426950408889634f; }
        u32x4 o;
#pragma unroll
        for (int j = 0; j < 4; ++j) {
          float a = acc[m][p][j >> 1][2 * (j & 1)], b = acc[m][p][j >> 1][2 * (j & 1) + 1];
          if (NP == G1_FFN) { a = relu_s(a); b = relu_s(b); a *= a; b *= b; }
          else { a *= sc; b *= sc; }
          o[j] = cvtpk(a, b);
        }
        store16_dev(dst, o);
      }
    }
  }
#undef G1_RA
#undef G1_RB
#undef G1_MM
#undef G1_SCHED
}

constexpr int AT_NCH = 8;
constexpr int AT_BUF = 32768;
constexpr int AT_BASE = 98304;
constexpr int AT_Q = 65536;
constexpr int AT_DUMP = 90112;
constexpr int MAIN_LDS = 163840;

DI float max16(const f32x16& s) {
  float a = __builtin_fmaxf(s[0], s[1]);
#pragma unroll
  for (int r = 2; r < 16; ++r) a = __builtin_fmaxf(a, s[r]);
  return a;
}
DI bf16x8 pack8(const f32x16& p, int s) {
  u32x4 o;
#pragma unroll
  for (int j = 0; j < 4; ++j) o[j] = cvtpk(p[8 * s + 2 * j], p[8 * s + 2 * j + 1]);
  return __builtin_bit_cast(bf16x8, o);
}

DI void attn_fallback(const char* __restrict__ kv, bf16x8 q0, bf16x8 q1, int l, f32x16& o_out, float& l_out) {
  f32x16 o = {};
  float m = -INFINITY, lsum = 0.f;
  for (int t = 0; t < 64; ++t) {
    const char* p = kv + (size_t)t * 4096 + l * 16;
    const bf16x8 k0 = load16_dev(p), k1 = load16_dev(p + 1024), v0 = load16_dev(p + 2048), v1 = load16_dev(p + 3072);
    f32x16 s = {};
    s = MFMA32(k0, q0, s); s = MFMA32(k1, q1, s);
    const float mn = __builtin_fmaxf(m, xhalf_max(max16(s)));
    const float ms = (mn == -INFINITY) ? 0.f : mn;
    const float alpha = __builtin_amdgcn_exp2f(m - ms);
    m = mn;
    float ps = 0.f;
#pragma unroll
    for (int r = 0; r < 16; ++r) { s[r] = __builtin_amdgcn_exp2f(s[r] - ms); ps += s[r]; }
    lsum = lsum * alpha + ps;
#pragma unroll
    for (int r = 0; r < 16; ++r) o[r] *= alpha;
    o = MFMA32(v0, pack8(s, 0), o); o = MFMA32(v1, pack8(s, 1), o);
  }
  o_out = o;
  l_out = xhalf_sum(lsum);
}

constexpr int G2_SLOT = 32768, G2_BOFF = 16384, G2_PART = 131072;
struct G2Ld {
  const char* hb; const char* ob; const char* wb; const char* wb2; unsigned lds; unsigned voff, vb[4]; int rot, frot;
  DI void init(const char* ws, const u16* w2, const u16* wo, unsigned lds0, int lw, int l) {
    const int xcd = blockIdx.x & 7, idx = blockIdx.x >> 3, kh = xcd & 1;
    const int mt = ((xcd >> 1) & 1) * 8 + (idx & 7), nt = (xcd >> 2) * 4 + (idx >> 3);
    hb = ws + WS_H + (size_t)(mt * 4 + lw) * 4096 + (size_t)kh * (28 * KT);
    ob = ws + WS_O + (size_t)(mt * 4 + lw) * 4096;
    wb = (const char*)(w2 + (size_t)(kh * 1792 + lw * 16) * DM + nt * 128) - 4096;
    wb2 = (const char*)(wo + (size_t)(lw * 16) * DM + nt * 128 + kh * 64) - 4096;
#pragma unroll
    for (int p = 0; p < 4; ++p) vb[p] = (unsigned)((l >> 2) * DM * 2 + (((l & 3) ^ ((l >> 3) & 2)) << 4) + p * 64 - p * 1024 + 4096);
    lds = lds0 + lw * 4096; voff = l * 16;
    frot = (7 * (mt & 7)) >> 1;
    rot = 2 * (mt & 7);
  }
  DI void ffn(int s) const {
    const unsigned d = lds + (s & 3) * G2_SLOT;
    s += frot; s = s >= 28 ? s - 28 : s;
    const char* a = hb + (size_t)s * KT;
    const char* b = wb + (size_t)s * 131072;
    set_m0(d); gl16_dev<0>(voff, a); gl16_dev<1024>(voff, a); gl16_dev<2048>(voff, a); gl16_dev<3072>(voff, a);
    set_m0(d + G2_BOFF); gl16<0>(vb[0], b); gl16<1024>(vb[1], b); gl16<2048>(vb[2], b); gl16<3072>(vb[3], b);
  }
  template <int J> DI void ffn_piece(int s) const {
    const unsigned d = lds + (s & 3) * G2_SLOT;
    s += frot; s = s >= 28 ? s - 28 : s;
    if (J < 4) glds16_dev<(J & 3) * 1024>(voff, hb + (size_t)s * KT, d);
    else glds16<(J & 3) * 1024>(vb[J & 3], wb + (size_t)s * 131072, d + G2_BOFF);
  }
  DI void wo(int s) const {
    const unsigned d = lds + (s & 3) * G2_SLOT;
    const int k = (s - 28 + rot) & 15;
    const char* a = ob + (size_t)k * KT;
    const char* b = wb2 + (size_t)k * 131072;
    set_m0(d); gl16_dev<0>(voff, a); gl16_dev<1024>(voff, a); gl16_dev<2048>(voff, a); gl16_dev<3072>(voff, a);
    set_m0(d + G2_BOFF); gl16<0>(vb[0], b); gl16<1024>(vb[1], b);
  }
};

DI void attn_finish(char* __restrict__ ws, f32x16 o, f32x4 lacc, bf16x8 q0, bf16x8 q1, int l, char* dst) {
  const float l0 = __shfl(lacc[0] + lacc[1], l & 15), l1 = __shfl(lacc[2] + lacc[3], l & 15);
  float lsum = (l & 16) ? l1 : l0;
  bool ok = (lsum >= 9.5367431640625e-07f) && (lsum <= 1.8446744073709552e19f);
#pragma unroll
  for (int r = 0; r < 16; ++r) ok = ok && (__builtin_fabsf(o[r]) <= 3.0e38f);
  if (!__all(ok)) attn_fallback(ws + WS_KV, q0, q1, l, o, lsum);
  const float rl = 1.0f / lsum;
#pragma unroll
  for (int s = 0; s < 2; ++s) {
    u32x4 ov;
#pragma unroll
    for (int j = 0; j < 4; ++j) ov[j] = cvtpk(o[8 * s + 2 * j] * rl, o[8 * s + 2 * j + 1] * rl);
    store16_dev(dst + s * 1024, ov);
  }
}

template <bool LOADER>
DI void attn_phase(char* __restrict__ ws, const u16* w2, const u16* wo, char* smem, const int w, const int l, unsigned* late_pub) {
  const int tt = blockIdx.x >> 2, hg = blockIdx.x & 3;
  const int head = hg * 8 + w, lw = w & 3;
  const unsigned lds0 = (unsigned)(size_t)(LAS char*)smem;
  unsigned* flags = (unsigned*)(ws + WS_FLAG);
  const unsigned voff = l * 16;
  const char* ksrc = ws + WS_KV + (size_t)lw * 4096;
  const unsigned kdst = lds0 + AT_BASE + lw * 4096;
  auto ld = [&](int c, int half) {
    const char* s_ = ksrc + (size_t)c * AT_BUF + half * 16384;
    const unsigned d_ = kdst + (c & 1) * AT_BUF + half * 16384;
    set_m0(d_); gl16_dev<0>(voff, s_); gl16_dev<1024>(voff, s_); gl16_dev<2048>(voff, s_); gl16_dev<3072>(voff, s_);
  };
  G2Ld g2;
  const unsigned tdump = lds0 + AT_DUMP + lw * 256;
  const int xcd = blockIdx.x & 7, idx = blockIdx.x >> 3, gk = xcd & 1;
  const int mt = ((xcd >> 1) & 1) * 8 + (idx & 7), nt = (xcd >> 2) * 4 + (idx >> 3);
  if (LOADER) {
    if (lw == 0) {
      const unsigned* word = flags + (l == 0 ? FL_KV : FL_Q + (tt >> 3) * 8 + hg * 2 + (l & 1)) * 32;
      const unsigned need = l == 0 ? 32u : 4u;
      bool done = l > 2;
#pragma unroll 1
      for (unsigned spins = 0; spins < SPIN_CAP; ++spins) {
        if (!done) done = __hip_atomic_load((gu32*)word, RLX_AGENT) >= need;
        if (__all(done)) break;
        __builtin_amdgcn_s_sleep(8);
      }
      if (l == 0) __hip_atomic_store((LAS unsigned*)((LAS char*)smem + AT_RDY + 4), 1u, __ATOMIC_RELAXED, __HIP_MEMORY_SCOPE_WORKGROUP);
    } else {
#pragma unroll 1
      for (unsigned spins = 0; spins < 64u * SPIN_CAP; ++spins) {
        if (rfl(__hip_atomic_load((LAS unsigned*)((LAS char*)smem + AT_RDY + 4), __ATOMIC_RELAXED, __HIP_MEMORY_SCOPE_WORKGROUP))) break;
        __builtin_amdgcn_s_sleep(1);
      }
    }
    asm volatile("" ::: "memory");
    const char* qs = ws + WS_Q + (size_t)(tt * 32 + hg * 8 + lw * 2) * 2048;
    const unsigned qd = lds0 + AT_Q + lw * 4096;
    glds16_dev<0>(voff, qs, qd); glds16_dev<1024>(voff, qs, qd); glds16_dev<2048>(voff, qs, qd); glds16_dev<3072>(voff, qs, qd);
    ld(0, 0); ld(0, 1); ld(1, 0); ld(1, 1);
    l2_touch(wo, tdump);
    g2.init(ws, w2, wo, lds0, lw, l);
  }
  const unsigned s_lo = (l & 14) == 0 ? 0x3f803f80u : 0u, s_hi = (l & 14) == 2 ? 0x3f803f80u : 0u;
  const bf16x8_t sel = __builtin_bit_cast(bf16x8_t, (u32x4){s_lo, s_lo, s_hi, s_hi});
  const int sidx = (l & 1) ? 0xeeee : 0x4444;
  f32x16 o = {}, sa, sb;
  f32x4 ls = {};
  u32x8 pa, pb = {};
  PIN(o);
  if (LOADER) {
    asm volatile("s_waitcnt vmcnt(9)" ::: "memory");
    if (l == 0) __hip_atomic_fetch_add((LAS unsigned*)((LAS char*)smem + AT_RDY), 1u, __ATOMIC_RELAXED, __HIP_MEMORY_SCOPE_WORKGROUP);
  }
#pragma unroll 1
  for (unsigned spins = 0; spins < 64u * SPIN_CAP; ++spins) {
    if (rfl(__hip_atomic_load((LAS unsigned*)((LAS char*)smem + AT_RDY), __ATOMIC_RELAXED, __HIP_MEMORY_SCOPE_WORKGROUP)) >= 4u) break;
    __builtin_amdgcn_s_sleep(1);
  }
  asm volatile("" ::: "memory");

  const LAS char* qp = (const LAS char*)smem + AT_Q + w * 2048 + l * 16;
  const bf16x8 q0 = lds_b128(qp), q1 = lds_b128(qp + 1024);
  const LAS char* kvp = (const LAS char*)smem + AT_BASE + l * 16;
  bf16x8 kx0, kx1, ky0, ky1, va0, va1, vb0, vb1, vc0, vc1, vd0, vd1;
  kx0 = lds_b128(kvp); kx1 = lds_b128(kvp + 1024); ky0 = lds_b128(kvp + 4096); ky1 = lds_b128(kvp + 4096 + 1024);
  va0 = lds_b128(kvp + 2048); va1 = lds_b128(kvp + 3072); vb0 = lds_b128(kvp + 4096 + 2048); vb1 = lds_b128(kvp + 4096 + 3072);
  vd0 = va0; vd1 = va1;
  f32x16 nm;
  {
    sa = MFMA32(kx0, q0, o); sa = MFMA32(kx1, q1, sa);
    const float ma = -xhalf_max(max16(sa));
#pragma unroll
    for (int r = 0; r < 16; ++r) { nm[r] = ma; sa[r] += ma; }
    PIN(nm);
  }
#define EX(S, i) S[i] = __builtin_amdgcn_exp2f(S[i])
#define CV(P, j, S, i) P[j] = cvtpk(S[i], S[(i) + 1])
#define PLO(x) __builtin_bit_cast(bf16x8, __builtin_shufflevector(x, x, 0, 1, 2, 3))
#define PHI(x) __builtin_bit_cast(bf16x8, __builtin_shufflevector(x, x, 4, 5, 6, 7))
#define RSUM(x) ls = __builtin_amdgcn_smfmac_f32_16x16x64_bf16(sel, __builtin_bit_cast(bf16x16_t, x), ls, sidx, 0, 0)
#define STEP(S, P, SN, PP, KN0, KN1, VP0, VP1, KL0, KL1, VL0, VL1, NP) do { \
    SN = MFMA32(KN0, q0, nm); SBAR(); KL0 = lds_b128(NP); EX(S, 0); EX(S, 1); EX(S, 2); EX(S, 3); PIN(S); SBAR(); \
    SN = MFMA32(KN1, q1, SN); SBAR(); KL1 = lds_b128((NP) + 1024); CV(P, 0, S, 0); CV(P, 1, S, 2); EX(S, 4); EX(S, 5); EX(S, 6); EX(S, 7); PIN(S); PIN(P); SBAR(); \
    o = MFMA32(VP0, PLO(PP), o); SBAR(); VL0 = lds_b128((NP) + 2048); CV(P, 2, S, 4); EX(S, 8); CV(P, 3, S, 6); EX(S, 9); EX(S, 10); EX(S, 11); PIN(S); PIN(P); SBAR(); \
    o = MFMA32(VP1, PHI(PP), o); SBAR(); VL1 = lds_b128((NP) + 3072); CV(P, 4, S, 8); CV(P, 5, S, 10); EX(S, 12); EX(S, 13); EX(S, 14); PIN(S); PIN(P); SBAR(); \
    RSUM(PP); SBAR(); EX(S, 15); CV(P, 6, S, 12); CV(P, 7, S, 14); PIN(P); SBAR(); \
  } while (0)
#define XP(on, st, j) do { if (LOADER && (on)) g2.template ffn_piece<j>(st); SBAR(); } while (0)
#define PRIO(n) do { __builtin_amdgcn_s_setprio(n); SBAR(); } while (0)
#define HEAD(cb, on, st) do { \
    PRIO(2); \
    STEP(sa, pa, sb, pb, ky0, ky1, vd0, vd1, kx0, kx1, vc0, vc1, (cb) + 2 * 4096); XP(on, st, 2); \
    STEP(sb, pb, sa, pa, kx0, kx1, va0, va1, ky0, ky1, vd0, vd1, (cb) + 3 * 4096); XP(on, st, 3); \
    PRIO(1); \
    STEP(sa, pa, sb, pb, ky0, ky1, vb0, vb1, kx0, kx1, va0, va1, (cb) + 4 * 4096); XP(on, st, 4); \
    STEP(sb, pb, sa, pa, kx0, kx1, vc0, vc1, ky0, ky1, vb0, vb1, (cb) + 5 * 4096); XP(on, st, 5); \
    STEP(sa, pa, sb, pb, ky0, ky1, vd0, vd1, kx0, kx1, vc0, vc1, (cb) + 6 * 4096); XP(on, st, 6); \
    PRIO(0); \
    STEP(sb, pb, sa, pa, kx0, kx1, va0, va1, ky0, ky1, vd0, vd1, (cb) + 7 * 4096); XP(on, st, 7); \
  } while (0)
#define TAIL(nb, on, st) do { \
    STEP(sa, pa, sb, pb, ky0, ky1, vb0, vb1, kx0, kx1, va0, va1, nb); XP(on, st, 0); \
    STEP(sb, pb, sa, pa, kx0, kx1, vc0, vc1, ky0, ky1, vb0, vb1, (nb) + 4096); XP(on, st, 1); \
  } while (0)
  HEAD(kvp, false, 0);
  if (LOADER) {
#pragma unroll 1
    for (int c = 0; c < 3; ++c) {
      asm volatile("s_waitcnt vmcnt(1) lgkmcnt(0)\n\ts_barrier" ::: "memory");
      if (c == 2 && lw == 0) peek_dev(flags + (FL_H + (mt >> 1)) * 32, lds0 + G1_CS);
      ld(c + 2, 0); ld(c + 2, 1);
      const int j = c * 4 + lw;
      l2_touch(j < 7 ? (const char*)(w2 + (size_t)(gk * 1792 + j * 256 + (mt & 7) * 32 + (l >> 1)) * DM + nt * 128 + (l & 1) * 64)
             : j < 9 ? (const char*)(wo + (size_t)((mt & 7) * 128 + (j - 7) * 64 + l) * DM + nt * 128 + gk * 64) : (const char*)wo, tdump);
      PRIO(3);
      const LAS char* nb = kvp + ((c + 1) & 1) * AT_BUF;
      TAIL(nb, false, 0); HEAD(nb, false, 0);
    }
    asm volatile("s_waitcnt vmcnt(1)" ::: "memory");
    if (lw == 0 && rfl(*(const LAS unsigned*)((const LAS char*)smem + G1_CS)) < 92u) wait_ge(flags + (FL_H + (mt >> 1)) * 32, 92u);
#pragma unroll 1
    for (int c = 3; c < 7; ++c) {
      asm volatile("s_waitcnt vmcnt(8) lgkmcnt(0)\n\ts_barrier" ::: "memory");
      if (c < 6) { ld(c + 2, 0); ld(c + 2, 1); }
      PRIO(3);
      const LAS char* nb = kvp + ((c + 1) & 1) * AT_BUF;
      TAIL(nb, true, c - 3); HEAD(nb, true, c - 3);
    }
  } else {
#pragma unroll 1
    for (int c = 0; c < 7; ++c) {
      asm volatile("s_waitcnt lgkmcnt(0)\n\ts_barrier" ::: "memory");
      if (c == 0 && late_pub) publish(late_pub, l);
      PRIO(3);
      const LAS char* nb = kvp + ((c + 1) & 1) * AT_BUF;
      TAIL(nb, false, 0); HEAD(nb, false, 0);
    }
  }
  TAIL(kvp, false, 0);
#undef HEAD
#undef TAIL
  o = MFMA32(vd0, PLO(pb), o); o = MFMA32(vd1, PHI(pb), o); RSUM(pb);
#undef EX
#undef CV
#undef PLO
#undef PHI
#undef RSUM
#undef STEP
#undef PRIO
#undef XP
  __builtin_amdgcn_s_setprio(0);
  attn_finish(ws, o, ls, q0, q1, l, ws + WS_O + (size_t)(head >> 1) * KT + (size_t)tt * 4096 + (head & 1) * 2048 + l * 16);
}

DI void g2_load(const u16* w2, const u16* wo, char* __restrict__ ws, unsigned lds0, const int lw, const int l) {
  const int xcd = blockIdx.x & 7, idx = blockIdx.x >> 3;
  const int mt = ((xcd >> 1) & 1) * 8 + (idx & 7);
  unsigned* flags = (unsigned*)(ws + WS_FLAG);
  G2Ld g; g.init(ws, w2, wo, lds0, lw, l);
  asm volatile("s_waitcnt vmcnt(10) lgkmcnt(0)\n\ts_barrier" ::: "memory");
  g.ffn(4); g.ffn(5);
  const LAS unsigned* peek = (const LAS unsigned*)(size_t)(lds0 + G2_PART);
#pragma unroll 1
  for (int i = 2; i < 24; ++i) {
    asm volatile("s_waitcnt vmcnt(16)\n\ts_barrier" ::: "memory");
    if (i == 6 && l == 0) __hip_atomic_fetch_add((gu32*)flags + (FL_O + (blockIdx.x >> 4)) * 32, 1u, RLX_AGENT);
    if (i == 20 && lw == 0) peek_dev(flags + (FL_O + mt) * 32, lds0 + G2_PART);
    g.ffn(i + 4);
  }
  if (lw == 0 && rfl(peek[0]) < 128u) wait_ge(flags + (FL_O + mt) * 32, 128u);
  asm volatile("s_waitcnt vmcnt(16)\n\ts_barrier" ::: "memory");
  g.wo(28);
  peek = (const LAS unsigned*)(size_t)(lds0 + G2_BOFF + 2048);
#pragma unroll 1
  for (int i = 25; i < 37; ++i) {
    asm volatile("s_waitcnt vmcnt(12)\n\ts_barrier" ::: "memory");
    if (i == 35 && lw == 0) peek_dev(flags + (FL_P + blockIdx.x) * 32, lds0 + G2_BOFF + 2048);
    g.wo(i + 4);
  }
  auto part = [&]() {
    const char* src = ws + WS_PART + (size_t)(blockIdx.x ^ 1) * 16384 + lw * 4096;
    const unsigned d = lds0 + G2_PART + lw * 4096;
    glds16_dev<0>(g.voff, src, d); glds16_dev<1024>(g.voff, src, d); glds16_dev<2048>(g.voff, src, d); glds16_dev<3072>(g.voff, src, d);
  };
  asm volatile("s_waitcnt vmcnt(12)\n\ts_barrier" ::: "memory");
  const bool early = rfl(peek[0]) >= 4u;
  g.wo(41);
  if (early) part();
#pragma unroll 1
  for (int i = 38; i < 41; ++i) {
    if (early) asm volatile("s_waitcnt vmcnt(16)\n\ts_barrier" ::: "memory");
    else asm volatile("s_waitcnt vmcnt(12)\n\ts_barrier" ::: "memory");
    if (i < 40) g.wo(i + 4);
  }
  if (!early && lw == 0) wait_ge(flags + (FL_P + blockIdx.x) * 32, 4u);
  asm volatile("s_waitcnt vmcnt(6)\n\ts_barrier" ::: "memory");
  if (!early) part();
  asm volatile("s_waitcnt vmcnt(0)\n\ts_barrier" ::: "memory");
}

DI void g2_compute(const u16* __restrict__ x, const u16* __restrict__ b2, char* __restrict__ ws, u16* __restrict__ out,
                   char* smem, const int w, const int l) {
  const int xcd = blockIdx.x & 7, idx = blockIdx.x >> 3;
  const int kh = xcd & 1, mt = ((xcd >> 1) & 1) * 8 + (idx & 7), nt = (xcd >> 2) * 4 + (idx >> 3);
  unsigned* flags = (unsigned*)(ws + WS_FLAG);
  const int g = l >> 4, c16 = l & 15, wm = w & 1, wn = w >> 1;
  f32x4 accg[4][2] = {}, accp[4][2] = {}, accs[4][2] = {};
  const int col0 = nt * 128 + kh * 64 + wn * 32;
  const int row0 = mt * 128 + wm * 64 + c16;
  const int colx = col0 + (g & 1) * 16 + (g >> 1) * 8;
  unsigned stw[4];
  u32x4 xv[4];
  u32x2 bv[2];
#pragma unroll
  for (int m = 0; m < 4; ++m) stw[m] = __hip_atomic_load((gu32*)(ws + WS_STAT) + row0 + m * 16, RLX_AGENT);
#pragma unroll
  for (int m = 0; m < 4; ++m) xv[m] = *(const u32x4*)(x + (size_t)(row0 + m * 16) * DM + colx);
#pragma unroll
  for (int t = 0; t < 2; ++t) bv[t] = *(const u32x2*)(b2 + col0 + 16 * t + 4 * g);
  const LAS char* lbase = (const LAS char*)smem;
  const int ah = wm * 8192 + l * 16;
  const int ao = wm * 8192 + (g >> 1) * 1024 + ((g & 1) * 32 + c16) * 16;
  int bF[2][2], bW[2];
#pragma unroll
  for (int t = 0; t < 2; ++t) {
    const int e = ((t ^ g) & 1) * 32 + (l & 3) * 8;
    bF[0][t] = G2_BOFF + (kh * 2 + wn) * 1024 + (4 * g + (c16 >> 2)) * 64 + e;
    bF[1][t] = G2_BOFF + ((kh ^ 1) * 2 + wn) * 1024 + (4 * g + (c16 >> 2)) * 64 + e;
    bW[t] = G2_BOFF + wn * 1024 + (g >> 1) * 4096 + (4 * (g & 1) + (c16 >> 2)) * 64 + e;
  }
  bf16x8 fa0[4], fb0[4], fa1[4], fb1[4];
#define F_RD(FA, FB, base, k) do { \
    _Pragma("unroll") for (int j_ = 0; j_ < 4; ++j_) { \
      FB[j_] = cat8(lds_tr((base) + bF[j_ >> 1][j_ & 1] + (k) * 8192), lds_tr((base) + bF[j_ >> 1][j_ & 1] + (k) * 8192 + 4096)); \
      FA[j_] = lds_b128((base) + ah + j_ * 2048 + (k) * 1024); } \
  } while (0)
#define F_MM(FA, FB) do { \
    _Pragma("unroll") for (int m_ = 0; m_ < 4; ++m_) { \
      accg[m_][0] = MFMA16(FB[0], FA[m_], accg[m_][0]); accg[m_][1] = MFMA16(FB[1], FA[m_], accg[m_][1]); \
      accp[m_][0] = MFMA16(FB[2], FA[m_], accp[m_][0]); accp[m_][1] = MFMA16(FB[3], FA[m_], accp[m_][1]); } \
  } while (0)
#define W_RD(FA, FB, base, k) do { \
    _Pragma("unroll") for (int j_ = 0; j_ < 2; ++j_) FB[j_] = cat8(lds_tr((base) + bW[j_] + (k) * 8192), lds_tr((base) + bW[j_] + (k) * 8192 + 512)); \
    _Pragma("unroll") for (int j_ = 0; j_ < 4; ++j_) FA[j_] = lds_b128((base) + ao + (j_ >> 1) * 4096 + (j_ & 1) * 256 + (k) * 2048); \
  } while (0)
#define W_MM(FA, FB) do { \
    _Pragma("unroll") for (int m_ = 0; m_ < 4; ++m_) { accs[m_][0] = MFMA16(FB[0], FA[m_], accs[m_][0]); accs[m_][1] = MFMA16(FB[1], FA[m_], accs[m_][1]); } \
  } while (0)
#define G2_SCHED(NG, NR, NM) do { \
    _Pragma("unroll") for (int q_ = 0; q_ < (NG); ++q_) { \
      __builtin_amdgcn_sched_group_barrier(0x008, 1, 0); __builtin_amdgcn_sched_group_barrier(0x100, NR, 0); } \
    __builtin_amdgcn_sched_group_barrier(0x008, (NM) - (NG), 0); SBAR(); \
  } while (0)
#define G2_BAR() do { asm volatile("s_waitcnt lgkmcnt(0)\n\ts_barrier" ::: "memory"); SBAR(); } while (0)
#define F_BODY(nb_) do { \
    F_RD(fa0, fb0, nb_, 0); F_MM(fa1, fb1); G2_SCHED(12, 1, 16); \
    F_RD(fa1, fb1, nb_, 1); F_MM(fa0, fb0); G2_SCHED(12, 1, 16); } while (0)

  SBAR();
  F_RD(fa0, fb0, lbase, 0); SBAR();
  F_RD(fa1, fb1, lbase, 1); F_MM(fa0, fb0); G2_SCHED(12, 1, 16);
  const LAS char* lhi = lbase + 2 * G2_SLOT;
  F_BODY(lbase + G2_SLOT);
  G2_BAR(); F_BODY(lhi);
  G2_BAR(); F_BODY(lhi + G2_SLOT);
  publish(flags + (FL_O + (blockIdx.x >> 4)) * 32, l); SBAR();
#pragma unroll 1
  for (int i = 0; i < 6; ++i) {
    G2_BAR(); F_BODY(lbase);
    G2_BAR(); F_BODY(lbase + G2_SLOT);
    G2_BAR(); F_BODY(lhi);
    G2_BAR(); F_BODY(lhi + G2_SLOT);
  }
#define W_STEP(i) do { const LAS char* nb_ = lbase + (((i) + 1) & 3) * G2_SLOT; G2_BAR(); \
    W_RD(fa0, fb0, nb_, 0); W_MM(fa1, fb1); G2_SCHED(4, 2, 8); \
    W_RD(fa1, fb1, nb_, 1); W_MM(fa0, fb0); G2_SCHED(4, 2, 8); } while (0)
  char* pdst = ws + WS_PART + (size_t)blockIdx.x * 16384 + w * 4096 + l * 16;
#define G2_SEND(m) do { store16_dev(pdst + (m) * 1024, (u32x4){cvtpk(accp[m][0][0], accp[m][0][1]), cvtpk(accp[m][0][2], accp[m][0][3]), \
                                                               cvtpk(accp[m][1][0], accp[m][1][1]), cvtpk(accp[m][1][2], accp[m][1][3])}); SBAR(); } while (0)
  G2_BAR();
  W_RD(fa0, fb0, lbase, 0); F_MM(fa1, fb1); G2_SCHED(8, 1, 16);
  W_RD(fa1, fb1, lbase, 1); W_MM(fa0, fb0); G2_SCHED(4, 2, 8);
  G2_SEND(0); G2_SEND(1);
  W_STEP(28); G2_SEND(2); G2_SEND(3);
  W_STEP(29); W_STEP(30); W_STEP(31); W_STEP(32);
  publish(flags + (FL_P + (blockIdx.x ^ 1)) * 32, l); SBAR();
  W_STEP(33);
#pragma unroll 1
  for (int i = 0; i < 2; ++i) { W_STEP(34); W_STEP(35); W_STEP(36); W_STEP(37); }
  W_STEP(42);
  W_MM(fa1, fb1);
  {
    const LAS char* xp = (const LAS char*)smem + G2_PART + w * 4096 + l * 16;
#pragma unroll
    for (int m = 0; m < 4; ++m) {
      const float den = bf_hi(stw[m]), rd2 = 1.0f / (den * den);
      const u32x4 tp = *(const LAS u32x4*)(xp + m * 1024);
      u32x4 xx = xv[m];
      { auto r0 = __builtin_amdgcn_permlane16_swap(xx[0], xx[2], false, false); xx[0] = r0[0]; xx[2] = r0[1];
        auto r1 = __builtin_amdgcn_permlane16_swap(xx[1], xx[3], false, false); xx[1] = r1[0]; xx[3] = r1[1]; }
      float r[8];
#pragma unroll
      for (int e = 0; e < 8; ++e) {
        const unsigned xw_ = xx[e >> 1], bw_ = bv[e >> 2][(e >> 1) & 1], pw_ = tp[e >> 1];
        const float z = (accg[m][e >> 2][e & 3] + ((e & 1) ? bf_hi(pw_) : bf_lo(pw_))) * rd2 + ((e & 1) ? bf_hi(bw_) : bf_lo(bw_));
        const float sg = z * __builtin_amdgcn_rcpf(1.0f + __builtin_amdgcn_exp2f(-1.4426950408889634f * z));
        r[e] = ((e & 1) ? bf_hi(xw_) : bf_lo(xw_)) + accs[m][e >> 2][e & 3] * sg;
      }
      auto r0 = __builtin_amdgcn_permlane16_swap(cvtpk(r[0], r[1]), cvtpk(r[4], r[5]), false, false);
      auto r1 = __builtin_amdgcn_permlane16_swap(cvtpk(r[2], r[3]), cvtpk(r[6], r[7]), false, false);
      *(u32x4*)(out + (size_t)(row0 + m * 16) * DM + colx) = (u32x4){r0[0], r1[0], r0[1], r1[1]};
    }
  }
#undef F_RD
#undef F_MM
#undef F_BODY
#undef G2_SCHED
#undef G2_BAR
#undef G2_SEND
#undef W_RD
#undef W_MM
#undef W_STEP
}

__global__ __launch_bounds__(512, 2) void main_kernel(const u16* __restrict__ x, const u16* __restrict__ wq, const u16* __restrict__ wk,
                                                      const u16* __restrict__ wv, const u16* __restrict__ wo, const u16* __restrict__ w1,
                                                      const u16* __restrict__ b1, const u16* __restrict__ w2, const u16* __restrict__ b2,
                                                      char* __restrict__ ws, u16* __restrict__ out) {
  extern __shared__ __attribute__((aligned(16))) char smem[];
  const int l = threadIdx.x & 63, w = rfl(threadIdx.x >> 6);
  const unsigned lds0 = (unsigned)(size_t)(LAS char*)smem;
  const int xcd = blockIdx.x & 7, idx = blockIdx.x >> 3;
  const int mtile = (xcd & 1) * 4 + (idx & 3), cg = (idx >> 2) * 4 + (xcd >> 1);
  unsigned* flags = (unsigned*)(ws + WS_FLAG);
  if (w == 3) {
    unsigned long long pc; unsigned len;
    asm volatile("s_getpc_b64 %0\n.Lmk_code_begin:\n\ts_mov_b32 %1, .Lmk_code_end-.Lmk_code_begin" : "=s"(pc), "=s"(len));
    unsigned off = (unsigned)((l & 31) * 32 + idx) * 128u;
    off = (off < len && len <= 131072u) ? off : 0u;
    l2_touch((const char*)pc + off, lds0 + G1_HS + 3072);
  }
  if (w >= 4) {
    if (cg > 8) g1_load<G1_FFN>(ws, x, w1, nullptr, b1, cg, mtile, smem, w - 4, l);
    else if (cg < 8) g1_load<G1_Q>(ws, x, wq, nullptr, b1, cg, mtile, smem, w - 4, l);
    else g1_load<G1_KV>(ws, x, wk, wv, b1, cg, mtile, smem, w - 4, l);
    attn_phase<true>(ws, w2, wo, smem, w, l, nullptr);
    g2_load(w2, wo, ws, lds0, w - 4, l);
    asm volatile(".Lmk_code_end:");
  } else {
    unsigned* late = nullptr;
    if (cg > 8) { g1_compute<G1_FFN>(ws, x, cg, mtile, smem, w, l); late = flags + (FL_H + mtile) * 32; }
    else if (cg < 8) { g1_compute<G1_Q>(ws, x, cg, mtile, smem, w, l); publish(flags + (FL_Q + mtile * 8 + cg) * 32, l); }
    else { g1_compute<G1_KV>(ws, x, cg, mtile, smem, w, l); publish(flags + FL_KV * 32, l); }
    attn_phase<false>(ws, w2, wo, smem, w, l, late);
    g2_compute(x, b2, ws, out, smem, w, l);
  }
}

extern "C" void kernel_launch(void* const* d_in, const int* in_sizes, int n_in, void* d_out, int out_size, void* d_ws,
                              size_t ws_size, hipStream_t stream) {
  const u16* x = (const u16*)d_in[0];
  const u16* wq = (const u16*)d_in[1];
  const u16* wk = (const u16*)d_in[2];
  const u16* wv = (const u16*)d_in[3];
  const u16* wo = (const u16*)d_in[4];
  const u16* w1 = (const u16*)d_in[6];
  const u16* b1 = (const u16*)d_in[7];
  const u16* w2 = (const u16*)d_in[8];
  const u16* b2 = (const u16*)d_in[9];
  char* ws = (char*)d_ws;
  (void)hipFuncSetAttribute((const void*)main_kernel, hipFuncAttributeMaxDynamicSharedMemorySize, MAIN_LDS);
  main_kernel<<<256, 512, MAIN_LDS, stream>>>(x, wq, wk, wv, wo, w1, b1, w2, b2, ws, (u16*)d_out);
}
```

```cpp
#include <hip/hip_runtime.h>
#include <hip/hip_bf16.h>

typedef short bf16x8 __attribute__((ext_vector_type(8)));
typedef short s16x4 __attribute__((ext_vector_type(4)));
typedef float f32x16 __attribute__((ext_vector_type(16)));
typedef float f32x4 __attribute__((ext_vector_type(4)));
typedef float f32x2 __attribute__((ext_vector_type(2)));
typedef __bf16 bf16x2_t __attribute__((ext_vector_type(2)));
typedef unsigned u32x4 __attribute__((ext_vector_type(4)));
typedef unsigned u32x2 __attribute__((ext_vector_type(2)));
typedef unsigned u32x8 __attribute__((ext_vector_type(8)));
typedef __bf16 bf16x8_t __attribute__((ext_vector_type(8)));
typedef __bf16 bf16x16_t __attribute__((ext_vector_type(16)));
typedef unsigned short u16;
#define LAS __attribute__((address_space(3)))
#define DI __device__ __forceinline__
#define SBAR() __builtin_amdgcn_sched_barrier(0)
#define PIN(x) asm volatile("" : "+v"(x))
#define MFMA16(a, b, c) __builtin_amdgcn_mfma_f32_16x16x32_bf16((a), (b), (c), 0, 0, 0)
#define MFMA32(a, b, c) __builtin_amdgcn_mfma_f32_32x32x16_bf16((a), (b), (c), 0, 0, 0)

constexpr int DM = 1024, FF = 3584;
constexpr size_t KT = 262144;
constexpr size_t WS_STAT = 0;
constexpr size_t WS_Q = 4u << 20;
constexpr size_t WS_KV = 8u << 20;
constexpr size_t WS_O = 9u << 20;
constexpr size_t WS_H = 13u << 20;
constexpr size_t WS_PART = 28u << 20;
constexpr size_t WS_FLAG = 37u << 20;
constexpr int FL_P = 0, FL_O = 256, FL_H = 272, FL_Q = 280, FL_KV = 344, FL_S = 345, FL_N = 353;
constexpr size_t WS_HSA = WS_FLAG + 65536, WS_HSB = WS_HSA + 4096, WS_HSC = WS_HSB + 4096;
constexpr int COORD_BLOCK = 64;

DI unsigned rfl(unsigned x) { return __builtin_amdgcn_readfirstlane(x); }
DI unsigned cvtpk(float lo, float hi) {
  f32x2 v = {lo, hi};
  bf16x2_t b = __builtin_convertvector(v, bf16x2_t);
  return __builtin_bit_cast(unsigned, b);
}
DI float bf_lo(unsigned u) { return __uint_as_float(u << 16); }
DI float bf_hi(unsigned u) { return __uint_as_float(u & 0xffff0000u); }
DI float bf_round(float x) { return bf_lo(cvtpk(x, x)); }

template <int IMM>
DI void glds16(unsigned voff, const void* sbase, unsigned ldsaddr) {
  asm volatile("s_mov_b32 m0, %2\n\ts_nop 0\n\tglobal_load_lds_dwordx4 %0, %1 offset:%3"
               :: "v"(voff), "s"(sbase), "s"(ldsaddr), "i"(IMM) : "memory");
}
template <int IMM>
DI void glds16_dev(unsigned voff, const void* sbase, unsigned ldsaddr) {
  asm volatile("s_mov_b32 m0, %2\n\ts_nop 0\n\tglobal_load_lds_dwordx4 %0, %1 offset:%3 sc1"
               :: "v"(voff), "s"(sbase), "s"(ldsaddr), "i"(IMM) : "memory");
}
DI void set_m0(unsigned ldsaddr) { asm volatile("s_mov_b32 m0, %0\n\ts_nop 0" :: "s"(ldsaddr) : "memory"); }
template <int IMM> DI void gl16(unsigned voff, const void* sbase) {
  asm volatile("global_load_lds_dwordx4 %0, %1 offset:%2" :: "v"(voff), "s"(sbase), "i"(IMM) : "memory");
}
template <int IMM> DI void gl16_dev(unsigned voff, const void* sbase) {
  asm volatile("global_load_lds_dwordx4 %0, %1 offset:%2 sc1" :: "v"(voff), "s"(sbase), "i"(IMM) : "memory");
}
DI void peek_dev(const void* word, unsigned ldsaddr) {
  asm volatile("s_mov_b32 m0, %2\n\ts_nop 0\n\tglobal_load_lds_dword %0, %1 sc1" :: "v"(0u), "s"(word), "s"(ldsaddr) : "memory");
}
DI void store16_dev(void* p, u32x4 v) {
  asm volatile("global_store_dwordx4 %0, %1, off sc1\n\ts_nop 1" :: "v"(p), "v"(v) : "memory");
}
DI void l2_touch(const void* p, unsigned ldsaddr) {
  asm volatile("s_mov_b32 m0, %1\n\ts_nop 0\n\tglobal_load_lds_dword %0, off" :: "v"(p), "s"(ldsaddr) : "memory");
}
constexpr unsigned SPIN_CAP = 1u << 20;
typedef __attribute__((address_space(1))) unsigned gu32;
#define RLX_AGENT __ATOMIC_RELAXED, __HIP_MEMORY_SCOPE_AGENT
DI void wait_ge(const unsigned* word, unsigned want) {
#pragma unroll 1
  for (unsigned spins = 0; spins < SPIN_CAP; ++spins) {
    if (rfl(__hip_atomic_load((gu32*)word, RLX_AGENT)) >= want) break;
    __builtin_amdgcn_s_sleep(8);
  }
  __builtin_amdgcn_fence(__ATOMIC_ACQUIRE, "wavefront");
}
DI void publish(unsigned* word, int l) {
  asm volatile("s_waitcnt vmcnt(0)" ::: "memory");
  if (l == 0) __hip_atomic_fetch_add((gu32*)word, 1u, RLX_AGENT);
}
DI bf16x8 load16_dev(const char* p) {
  typedef __attribute__((address_space(1))) unsigned long long gu64;
  const unsigned long long a = __hip_atomic_load((gu64*)p, RLX_AGENT), b = __hip_atomic_load((gu64*)p + 1, RLX_AGENT);
  return __builtin_bit_cast(bf16x8, (u32x4){(unsigned)a, (unsigned)(a >> 32), (unsigned)b, (unsigned)(b >> 32)});
}
DI float relu_s(float a) { return __builtin_amdgcn_fmed3f(a, 0.0f, 3.4028234663852886e38f); }
DI bf16x8 lds_b128(const LAS char* p) { return *(const LAS bf16x8*)p; }
DI s16x4 lds_tr(const LAS char* p) {
  return __builtin_amdgcn_ds_read_tr16_b64_v4i16((LAS s16x4*)p);
}
DI bf16x8 cat8(s16x4 lo, s16x4 hi) { return __builtin_shufflevector(lo, hi, 0, 1, 2, 3, 4, 5, 6, 7); }

DI float half_sum(float v) {
  v += __uint_as_float(__builtin_amdgcn_update_dpp(0u, __float_as_uint(v), 0xB1, 0xf, 0xf, true));
  v += __uint_as_float(__builtin_amdgcn_update_dpp(0u, __float_as_uint(v), 0x4E, 0xf, 0xf, true));
  v += __uint_as_float(__builtin_amdgcn_update_dpp(0u, __float_as_uint(v), 0x141, 0xf, 0xf, true));
  v += __uint_as_float(__builtin_amdgcn_update_dpp(0u, __float_as_uint(v), 0x140, 0xf, 0xf, true));
  auto r = __builtin_amdgcn_permlane16_swap(__float_as_uint(v), __float_as_uint(v), false, false);
  return __uint_as_float(r[0]) + __uint_as_float(r[1]);
}
DI unsigned row_stats(const u32x4 (&v)[4]) {
  f32x2 e[16], s2 = {0.f, 0.f};
#pragma unroll
  for (int i = 0; i < 16; ++i) { e[i] = (f32x2){bf_lo(v[i >> 2][i & 3]), bf_hi(v[i >> 2][i & 3])}; s2 += e[i]; }
  const float mean = half_sum(s2[0] + s2[1]) * (1.0f / DM);
  f32x2 q2 = {0.f, 0.f};
#pragma unroll
  for (int i = 0; i < 16; ++i) { const f32x2 d = e[i] - mean; q2 += d * d; }
  const float q = half_sum(q2[0] + q2[1]);
  const float den = __builtin_sqrtf(bf_round(bf_round(q * (1.0f / DM)) + 1e-5f));
  return cvtpk(-mean, den);
}

constexpr int G1_STAGE = 53248, G1_BOFF = 32768;
enum { G1_FFN = 5, G1_Q = 4, G1_KV = 2 };

constexpr int G1_ST = 87040;
constexpr int G1_CF = 53248;
constexpr int AT_RDY = 89088;
constexpr int G1_HS = 159744;
constexpr int G1_CS = 81920;
constexpr int ESTART = 3, PSTART = 9;

DI float xhalf_max(float m) {
  auto rr = __builtin_amdgcn_permlane32_swap(__float_as_uint(m), __float_as_uint(m), false, false);
  return __builtin_fmaxf(__uint_as_float(rr[0]), __uint_as_float(rr[1]));
}
DI float xhalf_sum(float m) {
  auto rr = __builtin_amdgcn_permlane32_swap(__float_as_uint(m), __float_as_uint(m), false, false);
  return __uint_as_float(rr[0]) + __uint_as_float(rr[1]);
}
template <int NP>
DI void g1_load(char* ws, const u16* x, const u16* wsrc, const u16* wv, const u16* b1, int cg, int mtile, char* smem, int lw, int l) {
  constexpr int LD = (NP == G1_FFN) ? FF : (NP == G1_Q) ? DM : 32;
  const unsigned lds0 = (unsigned)(size_t)(LAS char*)smem;
  const char* abase = (const char*)(x + (size_t)(mtile * 256 + lw * 64) * DM);
  const unsigned ave = (unsigned)((l >> 3) * 2048 + (((l & 7) ^ (l >> 4)) << 4)), avo = ave ^ 64u;
  const unsigned bvoff = (unsigned)((lw * 16 + (l >> 2)) * LD * 2 + (((l & 3) ^ ((l >> 5) << 1)) << 4));
  const int krot = NP == G1_Q ? (mtile & 3) * 4 : 0;
  unsigned va[8], vb[NP];
#pragma unroll
  for (int i = 0; i < 8; ++i) va[i] = ((i & 1) ? avo : ave) + i * 16384 - (i - 4) * 1024;
#pragma unroll
  for (int p = 0; p < NP; ++p) {
    int cb = (NP == G1_FFN) ? (cg - 9) * 5 + p : (NP == G1_Q) ? cg * 4 + p : 0;
    cb = cb > 111 ? 111 : cb;
    vb[p] = bvoff + cb * 64 - (p - 2) * 1024 + 4096;
  }
  unsigned bias[NP];
#pragma unroll
  for (int p = 0; p < NP; ++p) {
    int cb = (cg - 9) * 5 + p; cb = cb > 111 ? 111 : cb;
    bias[p] = (NP == G1_FFN) ? (unsigned)b1[cb * 32 + (l & 31)] : 0u;
  }
  float cs[NP];
#pragma unroll
  for (int p = 0; p < NP; ++p) cs[p] = 0.f;
  const LAS char* cb0 = (const LAS char*)smem + G1_BOFF + lw * (NP * 1024) + (8 * (l >> 5) + ((l & 15) >> 2)) * 64 + (((l >> 4) ^ (l >> 5)) & 1) * 32 + (l & 3) * 8;
  u32x2 t0[NP], t1[NP];
  auto cs_read = [&](int slot, int p) {
    t0[p] = __builtin_bit_cast(u32x2, lds_tr(cb0 + slot * G1_STAGE + p * 1024));
    t1[p] = __builtin_bit_cast(u32x2, lds_tr(cb0 + slot * G1_STAGE + p * 1024 + 256));
    SBAR();
  };
  auto cs_add = [&](int p) {
    float a, b, c, d;
    asm volatile("v_lshlrev_b32 %1, 16, %5\n\tv_and_b32 %2, 0xffff0000, %5\n\tv_lshlrev_b32 %3, 16, %6\n\tv_and_b32 %4, 0xffff0000, %6\n\t"
                 "v_add_f32 %1, %1, %2\n\tv_add_f32 %3, %3, %4\n\t"
                 "v_lshlrev_b32 %2, 16, %7\n\tv_and_b32 %4, 0xffff0000, %7\n\tv_add_f32 %1, %1, %2\n\tv_add_f32 %3, %3, %4\n\t"
                 "v_lshlrev_b32 %2, 16, %8\n\tv_and_b32 %4, 0xffff0000, %8\n\tv_add_f32 %1, %1, %2\n\tv_add_f32 %3, %3, %4\n\t"
                 "v_add_f32 %1, %1, %3\n\tv_add_f32 %0, %0, %1"
                 : "+v"(cs[p]), "=&v"(a), "=&v"(b), "=&v"(c), "=&v"(d) : "v"(t0[p][0]), "v"(t0[p][1]), "v"(t1[p][0]), "v"(t1[p][1]));
    SBAR();
  };
  auto issue = [&](int st, int slot, bool sum, int sslot) {
    const int kt = (st + krot) & 15;
    const char* b0 = (const char*)wsrc + (size_t)kt * (64 * LD * 2) - 4096;
    set_m0(lds0 + slot * G1_STAGE + G1_BOFF + lw * (NP * 1024) + 2048);
    gl16<-2048>(vb[0], b0);
    gl16<-1024>(vb[1], NP == G1_KV ? (const char*)wv + (size_t)kt * (64 * LD * 2) - 4096 : b0);
    if (NP > 2) { gl16<0>(vb[2 % NP], b0); gl16<1024>(vb[3 % NP], b0); }
    if (NP > 4) gl16<2048>(vb[4 % NP], b0);
    const char* a0 = abase + kt * 128;
    set_m0(lds0 + slot * G1_STAGE + lw * 8192 + 4096);
#define AL(i) do { gl16<((i) - 4) * 1024>(va[i], a0); \
      if (sum && (i) < NP) cs_read(sslot, (i) % NP); if (sum && (i) >= 1 && (i) <= NP) cs_add(((i) + NP - 1) % NP); } while (0)
    AL(0); AL(1); AL(2); AL(3); AL(4); AL(5); AL(6); AL(7);
#undef AL
  };
  auto colsum = [&](int slot) {
#pragma unroll
    for (int p = 0; p < NP; ++p) cs_read(slot, p);
#pragma unroll
    for (int p = 0; p < NP; ++p) cs_add(p);
  };
  unsigned* flags = (unsigned*)(ws + WS_FLAG);
  const bool coord = (NP == G1_KV) && blockIdx.x == COORD_BLOCK && lw == 1;
  if (coord) {
#pragma unroll
    for (int i = 0; i < 6; ++i) if (i * 64 + l < FL_N) __hip_atomic_store((gu32*)flags + (i * 64 + l) * 32, 0u, RLX_AGENT);
    store16_dev(ws + WS_HSC + l * 16, (u32x4){0u, 0u, 0u, 0u});
    asm volatile("s_waitcnt vmcnt(0)" ::: "memory");
  }
  issue(0, 0, false, 0); issue(1, 1, false, 0); issue(2, 2, false, 0);
  asm volatile("s_waitcnt vmcnt(%0)\n\ts_barrier" :: "n"(2 * (8 + NP)) : "memory");
  __builtin_amdgcn_s_setprio(2);
  int state = (lw == 0 || coord) ? 0 : 2;
  bool pend = false, first = true;
  u32x4 last = {0u, 0u, 0u, 0u};
  const unsigned found = rfl(*(const LAS unsigned*)((const LAS char*)smem + G1_HS));
  if (lw == 0 && l < 9) {
    const unsigned v = *(const LAS unsigned*)((const LAS char*)smem + G1_HS + 16 + (l & 7) * 4);
    __hip_atomic_store(l < 8 ? (gu32*)(ws + WS_STAT) + mtile * 256 + cg * 8 + l : (gu32*)(ws + WS_HSA) + blockIdx.x, l < 8 ? v : found + 1u, RLX_AGENT);
  }
  colsum(0);
  auto poll_issue = [&]() {
    if (NP == G1_KV && coord) {
      glds16_dev<0>(l * 16, ws + WS_HSA, lds0 + G1_HS + 1024); glds16_dev<0>(l * 16, ws + WS_HSC, lds0 + G1_HS + 2048);
    } else {
      const char* a_ = state == 0 ? ws + WS_HSB + blockIdx.x * 4 : (const char*)(flags + (FL_S + mtile) * 32);
      peek_dev(a_, lds0 + G1_HS + 256);
    }
  };
  auto poll_check = [&]() {
    if (NP == G1_KV && coord) {
      const u32x4 pa = *(const LAS u32x4*)((const LAS char*)smem + G1_HS + 1024 + l * 16), pc = *(const LAS u32x4*)((const LAS char*)smem + G1_HS + 2048 + l * 16);
      if (first || pa[0] != last[0] || pa[1] != last[1] || pa[2] != last[2] || pa[3] != last[3]) store16_dev(ws + WS_HSB + l * 16, pa);
      last = pa; first = false;
      if (__all((pc[0] & pc[1] & pc[2] & pc[3]) == 1u)) state = 2;
    } else {
      const unsigned v = rfl(*(const LAS unsigned*)((const LAS char*)smem + G1_HS + 256));
      if (state == 0) {
        if (v != found) {
          if (l == 0) {
            __hip_atomic_store((gu32*)(ws + WS_HSC) + blockIdx.x, 1u, RLX_AGENT);
            __hip_atomic_fetch_add((gu32*)flags + (FL_S + mtile) * 32, 1u, RLX_AGENT);
          }
          state = 1;
        }
      } else if (v >= 32u) state = 2;
    }
    state = rfl(state);
  };
  int slot = 0;
#pragma unroll 1
  for (int i = 0; i < 13; ++i) {
    asm volatile("s_waitcnt vmcnt(%0)\n\ts_barrier" :: "n"(8 + NP) : "memory");
    if (state < 2 && (i >= ESTART || coord)) {
      if (pend) poll_check();
      pend = state == 0 || (state == 1 && i >= PSTART);
      if (pend) poll_issue();
    }
    const int nslot = (slot == 2) ? 0 : slot + 1;
    issue(i + 3, slot, true, nslot);
    slot = nslot;
  }
#pragma unroll 1
  for (unsigned spins = 0; state < 2 && spins < SPIN_CAP; ++spins) {
    if (!pend) poll_issue();
    asm volatile("s_waitcnt vmcnt(0)" ::: "memory");
    poll_check();
    pend = false;
    __builtin_amdgcn_s_sleep(8);
  }
  asm volatile("s_waitcnt vmcnt(%0)\n\ts_barrier" :: "n"(8 + NP) : "memory");
  if (lw == 0) { glds16_dev<0>(l * 16, ws + WS_STAT + mtile * 1024, lds0 + G1_ST); *(LAS u32x2*)((LAS char*)smem + AT_RDY) = (u32x2){0u, 0u}; }
  colsum(2);
  asm volatile("s_waitcnt vmcnt(0)" ::: "memory");
  colsum(0);
#pragma unroll
  for (int p = 0; p < NP; ++p) {
    const float c = xhalf_sum(cs[p]);
    const unsigned c0 = cvtpk(c, c) & 0xffffu; const float r1 = c - bf_lo(c0);
    const unsigned c1 = cvtpk(r1, r1) & 0xffffu; const float r2 = r1 - bf_lo(c1);
    const unsigned c2 = cvtpk(r2, r2) & 0xffffu;
    if (l < 32) *(LAS u32x4*)((LAS char*)smem + G1_CF + (p * 2 + (l >> 4)) * 1024 + (lw * 16 + (l & 15)) * 16) = (u32x4){c0 | (c1 << 16), c2 | (lw ? 0u : bias[p] << 16), 0u, 0u};
  }
  asm volatile("s_waitcnt lgkmcnt(0)\n\ts_barrier" ::: "memory");
}

template <int NP>
DI void g1_compute(char* __restrict__ ws, const u16* __restrict__ x, int cg, int mtile, char* smem, int w, int l) {
  int g = l >> 4, c16 = l & 15;
  {
    const int row = mtile * 256 + cg * 8 + w * 2 + (l >> 5);
    u32x4 v[4];
#pragma unroll
    for (int i = 0; i < 4; ++i) v[i] = *(const u32x4*)(x + (size_t)row * DM + (i * 32 + (l & 31)) * 8);
    const unsigned found = __hip_atomic_load((gu32*)(ws + WS_HSB) + blockIdx.x, RLX_AGENT);
    SBAR();
    const unsigned rs = row_stats(v);
    if ((l & 31) == 0) *(LAS unsigned*)((LAS char*)smem + G1_HS + 16 + (w * 2 + (l >> 5)) * 4) = rs;
    if (w == 0 && l == 0) *(LAS unsigned*)((LAS char*)smem + G1_HS) = found;
  }
  f32x4 acc[4][NP][2];
#pragma unroll
  for (int m = 0; m < 4; ++m)
#pragma unroll
    for (int p = 0; p < NP; ++p) { acc[m][p][0] = f32x4{}; acc[m][p][1] = f32x4{}; }
  const LAS char* lbase = (const LAS char*)smem;
  int aoff[2], boff[2];
#pragma unroll
  for (int k = 0; k < 2; ++k) {
    aoff[k] = (w * 64 + c16) * 128 + (((4 * k + g) ^ (c16 >> 1)) << 4);
    boff[k] = G1_BOFF + (g >> 1) * (NP * 1024) + (8 * (g & 1) + (c16 >> 2)) * 64 + ((k ^ g) & 1) * 32 + (l & 3) * 8;
  }
  bf16x8 fa0[4], fa1[4], fb0[NP], fb1[NP];
#define G1_RA(FA, base, k) do { _Pragma("unroll") for (int m_ = 0; m_ < 4; ++m_) FA[m_] = lds_b128((base) + aoff[k] + m_ * 2048); } while (0)
#define G1_RB(FB, base, k, t) do { _Pragma("unroll") for (int p_ = 0; p_ < NP; ++p_) \
      FB[p_] = cat8(lds_tr((base) + boff[t] + p_ * 1024 + (k) * (NP * 2048)), lds_tr((base) + boff[t] + p_ * 1024 + (k) * (NP * 2048) + 256)); } while (0)
#define G1_MM(FA, FB, t) do { \
    _Pragma("unroll") for (int m_ = 0; m_ < 4; ++m_) _Pragma("unroll") for (int p_ = 0; p_ < NP; ++p_) { \
      if (NP == G1_KV && p_ == 1) acc[m_][p_][t] = MFMA16(FA[m_], FB[p_], acc[m_][p_][t]); \
      else acc[m_][p_][t] = MFMA16(FB[p_], FA[m_], acc[m_][p_][t]); } \
  } while (0)
#define G1_SCHED(NR) do { \
    _Pragma("unroll") for (int q_ = 0; q_ < (NR); ++q_) { \
      __builtin_amdgcn_sched_group_barrier(0x008, 1, 0); __builtin_amdgcn_sched_group_barrier(0x100, 1, 0); } \
    __builtin_amdgcn_sched_group_barrier(0x008, 4 * NP - (NR), 0); SBAR(); \
  } while (0)

#pragma unroll
  for (int m = 0; m < 4; ++m) fa1[m] = bf16x8{};
#pragma unroll
  for (int p = 0; p < NP; ++p) fb1[p] = bf16x8{};
#define G1_STG(nb) do { \
    asm volatile("s_waitcnt lgkmcnt(0)\n\ts_barrier" ::: "memory"); SBAR(); \
    G1_RB(fb0, nb, 0, 0); G1_RA(fa0, nb, 0); G1_MM(fa1, fb1, 1); G1_SCHED(2 * NP + 4); \
    G1_RB(fb1, nb, 0, 1);                    G1_MM(fa0, fb0, 0); G1_SCHED(2 * NP); \
    G1_RB(fb0, nb, 1, 0); G1_RA(fa1, nb, 1); G1_MM(fa0, fb1, 1); G1_SCHED(2 * NP + 4); \
    G1_RB(fb1, nb, 1, 1);                    G1_MM(fa1, fb0, 0); G1_SCHED(2 * NP); } while (0)
  int slot = 0;
#pragma unroll 1
  for (int i = 0; i < 8; ++i) {
    const int s1 = (slot == 2) ? 0 : slot + 1;
    G1_STG(lbase + slot * G1_STAGE);
    G1_STG(lbase + s1 * G1_STAGE);
    slot = (s1 == 2) ? 0 : s1 + 1;
  }
#undef G1_STG
  __builtin_amdgcn_s_setprio(3);
  asm volatile("" : "+v"(l));
  g = l >> 4; c16 = l & 15;
#define G1_MMA(FA, FB, t) do { \
    _Pragma("unroll") for (int m_ = 0; m_ < 4; ++m_) _Pragma("unroll") for (int p_ = 0; p_ < NP; ++p_) { \
      if (NP == G1_KV && p_ == 1) asm volatile("v_mfma_f32_16x16x32_bf16 %0, %1, %2, %0" : "+v"(acc[m_][p_][t]) : "v"(FA[m_]), "v"(FB[p_])); \
      else asm volatile("v_mfma_f32_16x16x32_bf16 %0, %1, %2, %0" : "+v"(acc[m_][p_][t]) : "v"(FB[p_]), "v"(FA[m_])); } \
  } while (0)
  unsigned st[4];
  {
#pragma unroll
    for (int m = 0; m < 4; ++m) st[m] = *(const LAS unsigned*)(lbase + G1_ST + (w * 64 + m * 16 + c16) * 4);
#pragma unroll
    for (int p = 0; p < NP; ++p) fb0[p] = lds_b128(lbase + G1_CF + p * 2048 + l * 16);
    SBAR();
    G1_MMA(fa1, fb1, 1);
    SBAR();
#pragma unroll
    for (int p = 0; p < NP; ++p) fb1[p] = lds_b128(lbase + G1_CF + p * 2048 + 1024 + l * 16);
#pragma unroll
    for (int m = 0; m < 4; ++m) {
      const unsigned nm = st[m] & 0xffffu;
      fa0[m] = __builtin_bit_cast(bf16x8, (u32x4){nm | (nm << 16), st[m], 0u, 0u});
      PIN(fa0[m]);
    }
    SBAR();
    asm volatile("s_nop 1");
    G1_MMA(fa0, fb0, 0);
    G1_MMA(fa0, fb1, 1);
    asm volatile("s_nop 7");
  }
#undef G1_MMA
  SBAR();

#pragma unroll
  for (int p = 0; p < NP; ++p) {
    const int cb = (NP == G1_FFN) ? (cg - 9) * 5 + p : cg * 4 + p;
    if (NP == G1_KV && p == 1) {
#pragma unroll
      for (int mm = 0; mm < 2; ++mm) {
        const u32x4 s0 = *(const LAS u32x4*)(lbase + G1_ST + (w * 64 + mm * 32 + 4 * g) * 4), s1 = *(const LAS u32x4*)(lbase + G1_ST + (w * 64 + mm * 32 + 16 + 4 * g) * 4);
#pragma unroll
        for (int t = 0; t < 2; ++t) {
          const f32x4 a = acc[2 * mm][p][t], b = acc[2 * mm + 1][p][t];
          const u32x4 o = {cvtpk(a[0] / bf_hi(s0[0]), a[1] / bf_hi(s0[1])), cvtpk(a[2] / bf_hi(s0[2]), a[3] / bf_hi(s0[3])),
                           cvtpk(b[0] / bf_hi(s1[0]), b[1] / bf_hi(s1[1])), cvtpk(b[2] / bf_hi(s1[2]), b[3] / bf_hi(s1[3]))};
          store16_dev(ws + WS_KV + (size_t)(mtile * 8 + w * 2 + mm) * 4096 + 2048 + (g >> 1) * 1024 + ((g & 1) * 32 + t * 16 + c16) * 16, o);
        }
      }
    } else if (NP != G1_FFN || cb < 112) {
#pragma unroll
      for (int m = 0; m < 4; ++m) {
        const int tt = mtile * 8 + w * 2 + (m >> 1);
        const int pos = (NP == G1_KV) ? (c16 & 7) + (m & 1) * 8 + (c16 >> 3) * 16 : (m & 1) * 16 + c16;
        char* dst = (NP == G1_FFN) ? ws + WS_H + (size_t)(cb >> 1) * KT + (size_t)tt * 4096 + ((m & 1) * 2 + (cb & 1)) * 1024 + l * 16
                  : ((NP == G1_Q) ? ws + WS_Q + (size_t)(tt * 32 + cb) * 2048 : ws + WS_KV + (size_t)tt * 4096) + (g >> 1) * 1024 + ((g & 1) * 32 + pos) * 16;
        float sc = 1.0f;
        if (NP != G1_FFN) { sc = 1.0f / bf_hi(st[m]); if (NP == G1_Q) sc *= 0.17677669529663687f * 1.4426950408889634f; }
        u32x4 o;
#pragma unroll
        for (int j = 0; j < 4; ++j) {
          float a = acc[m][p][j >> 1][2 * (j & 1)], b = acc[m][p][j >> 1][2 * (j & 1) + 1];
          if (NP == G1_FFN) { a = relu_s(a); b = relu_s(b); a *= a; b *= b; }
          else { a *= sc; b *= sc; }
          o[j] = cvtpk(a, b);
        }
        store16_dev(dst, o);
      }
    }
  }
#undef G1_RA
#undef G1_RB
#undef G1_MM
#undef G1_SCHED
}

constexpr int AT_NCH = 8;
constexpr int AT_BUF = 32768;
constexpr int AT_BASE = 98304;
constexpr int AT_Q = 65536;
constexpr int AT_DUMP = 90112;
constexpr int MAIN_LDS = 163840;

DI float max16(const f32x16& s) {
  float a = __builtin_fmaxf(s[0], s[1]);
#pragma unroll
  for (int r = 2; r < 16; ++r) a = __builtin_fmaxf(a, s[r]);
  return a;
}
DI bf16x8 pack8(const f32x16& p, int s) {
  u32x4 o;
#pragma unroll
  for (int j = 0; j < 4; ++j) o[j] = cvtpk(p[8 * s + 2 * j], p[8 * s + 2 * j + 1]);
  return __builtin_bit_cast(bf16x8, o);
}

DI void attn_fallback(const char* __restrict__ kv, bf16x8 q0, bf16x8 q1, int l, f32x16& o_out, float& l_out) {
  f32x16 o = {};
  float m = -INFINITY, lsum = 0.f;
  for (int t = 0; t < 64; ++t) {
    const char* p = kv + (size_t)t * 4096 + l * 16;
    const bf16x8 k0 = load16_dev(p), k1 = load16_dev(p + 1024), v0 = load16_dev(p + 2048), v1 = load16_dev(p + 3072);
    f32x16 s = {};
    s = MFMA32(k0, q0, s); s = MFMA32(k1, q1, s);
    const float mn = __builtin_fmaxf(m, xhalf_max(max16(s)));
    const float ms = (mn == -INFINITY) ? 0.f : mn;
    const float alpha = __builtin_amdgcn_exp2f(m - ms);
    m = mn;
    float ps = 0.f;
#pragma unroll
    for (int r = 0; r < 16; ++r) { s[r] = __builtin_amdgcn_exp2f(s[r] - ms); ps += s[r]; }
    lsum = lsum * alpha + ps;
#pragma unroll
    for (int r = 0; r < 16; ++r) o[r] *= alpha;
    o = MFMA32(v0, pack8(s, 0), o); o = MFMA32(v1, pack8(s, 1), o);
  }
  o_out = o;
  l_out = xhalf_sum(lsum);
}

constexpr int G2_SLOT = 32768, G2_BOFF = 16384, G2_PART = 131072;
struct G2Ld {
  const char* hb; const char* ob; const char* wb; const char* wb2; unsigned lds; unsigned voff, vb[4]; int rot, frot;
  DI void init(const char* ws, const u16* w2, const u16* wo, unsigned lds0, int lw, int l) {
    const int xcd = blockIdx.x & 7, idx = blockIdx.x >> 3, kh = xcd & 1;
    const int mt = ((xcd >> 1) & 1) * 8 + (idx & 7), nt = (xcd >> 2) * 4 + (idx >> 3);
    hb = ws + WS_H + (size_t)(mt * 4 + lw) * 4096 + (size_t)kh * (28 * KT);
    ob = ws + WS_O + (size_t)(mt * 4 + lw) * 4096;
    wb = (const char*)(w2 + (size_t)(kh * 1792 + lw * 16) * DM + nt * 128) - 4096;
    wb2 = (const char*)(wo + (size_t)(lw * 16) * DM + nt * 128 + kh * 64) - 4096;
#pragma unroll
    for (int p = 0; p < 4; ++p) vb[p] = (unsigned)((l >> 2) * DM * 2 + (((l & 3) ^ ((l >> 3) & 2)) << 4) + p * 64 - p * 1024 + 4096);
    lds = lds0 + lw * 4096; voff = l * 16;
    frot = (7 * (mt & 7)) >> 1;
    rot = 2 * (mt & 7);
  }
  DI void ffn(int s) const {
    const unsigned d = lds + (s & 3) * G2_SLOT;
    s += frot; s = s >= 28 ? s - 28 : s;
    const char* a = hb + (size_t)s * KT;
    const char* b = wb + (size_t)s * 131072;
    set_m0(d); gl16_dev<0>(voff, a); gl16_dev<1024>(voff, a); gl16_dev<2048>(voff, a); gl16_dev<3072>(voff, a);
    set_m0(d + G2_BOFF); gl16<0>(vb[0], b); gl16<1024>(vb[1], b); gl16<2048>(vb[2], b); gl16<3072>(vb[3], b);
  }
  template <int J> DI void ffn_piece(int s) const {
    const unsigned d = lds + (s & 3) * G2_SLOT;
    s += frot; s = s >= 28 ? s - 28 : s;
    if (J < 4) glds16_dev<(J & 3) * 1024>(voff, hb + (size_t)s * KT, d);
    else glds16<(J & 3) * 1024>(vb[J & 3], wb + (size_t)s * 131072, d + G2_BOFF);
  }
  DI void wo(int s) const {
    const unsigned d = lds + (s & 3) * G2_SLOT;
    const int k = (s - 28 + rot) & 15;
    const char* a = ob + (size_t)k * KT;
    const char* b = wb2 + (size_t)k * 131072;
    set_m0(d); gl16_dev<0>(voff, a); gl16_dev<1024>(voff, a); gl16_dev<2048>(voff, a); gl16_dev<3072>(voff, a);
    set_m0(d + G2_BOFF); gl16<0>(vb[0], b); gl16<1024>(vb[1], b);
  }
};

DI void attn_finish(char* __restrict__ ws, f32x16 o, f32x4 lacc, bf16x8 q0, bf16x8 q1, int l, char* dst) {
  const float l0 = __shfl(lacc[0] + lacc[1], l & 15), l1 = __shfl(lacc[2] + lacc[3], l & 15);
  float lsum = (l & 16) ? l1 : l0;
  bool ok = (lsum >= 9.5367431640625e-07f) && (lsum <= 1.8446744073709552e19f);
#pragma unroll
  for (int r = 0; r < 16; ++r) ok = ok && (__builtin_fabsf(o[r]) <= 3.0e38f);
  if (!__all(ok)) attn_fallback(ws + WS_KV, q0, q1, l, o, lsum);
  const float rl = 1.0f / lsum;
#pragma unroll
  for (int s = 0; s < 2; ++s) {
    u32x4 ov;
#pragma unroll
    for (int j = 0; j < 4; ++j) ov[j] = cvtpk(o[8 * s + 2 * j] * rl, o[8 * s + 2 * j + 1] * rl);
    store16_dev(dst + s * 1024, ov);
  }
}

template <bool LOADER>
DI void attn_phase(char* __restrict__ ws, const u16* w2, const u16* wo, char* smem, const int w, const int l, unsigned* late_pub) {
  const int tt = blockIdx.x >> 2, hg = blockIdx.x & 3;
  const int head = hg * 8 + w, lw = w & 3;
  const unsigned lds0 = (unsigned)(size_t)(LAS char*)smem;
  unsigned* flags = (unsigned*)(ws + WS_FLAG);
  const unsigned voff = l * 16;
  const char* ksrc = ws + WS_KV + (size_t)lw * 4096;
  const unsigned kdst = lds0 + AT_BASE + lw * 4096;
  auto ld = [&](int c, int half) {
    const char* s_ = ksrc + (size_t)c * AT_BUF + half * 16384;
    const unsigned d_ = kdst + (c & 1) * AT_BUF + half * 16384;
    set_m0(d_); gl16_dev<0>(voff, s_); gl16_dev<1024>(voff, s_); gl16_dev<2048>(voff, s_); gl16_dev<3072>(voff, s_);
  };
  G2Ld g2;
  const unsigned tdump = lds0 + AT_DUMP + lw * 256;
  const int xcd = blockIdx.x & 7, idx = blockIdx.x >> 3, gk = xcd & 1;
  const int mt = ((xcd >> 1) & 1) * 8 + (idx & 7), nt = (xcd >> 2) * 4 + (idx >> 3);
  if (LOADER) {
    if (lw == 0) {
      const unsigned* word = flags + (l == 0 ? FL_KV : FL_Q + (tt >> 3) * 8 + hg * 2 + (l & 1)) * 32;
      const unsigned need = l == 0 ? 32u : 4u;
      bool done = l > 2;
#pragma unroll 1
      for (unsigned spins = 0; spins < SPIN_CAP; ++spins) {
        if (!done) done = __hip_atomic_load((gu32*)word, RLX_AGENT) >= need;
        if (__all(done)) break;
        __builtin_amdgcn_s_sleep(8);
      }
      if (l == 0) __hip_atomic_store((LAS unsigned*)((LAS char*)smem + AT_RDY + 4), 1u, __ATOMIC_RELAXED, __HIP_MEMORY_SCOPE_WORKGROUP);
    } else {
#pragma unroll 1
      for (unsigned spins = 0; spins < 64u * SPIN_CAP; ++spins) {
        if (rfl(__hip_atomic_load((LAS unsigned*)((LAS char*)smem + AT_RDY + 4), __ATOMIC_RELAXED, __HIP_MEMORY_SCOPE_WORKGROUP))) break;
        __builtin_amdgcn_s_sleep(1);
      }
    }
    asm volatile("" ::: "memory");
    const char* qs = ws + WS_Q + (size_t)(tt * 32 + hg * 8 + lw * 2) * 2048;
    const unsigned qd = lds0 + AT_Q + lw * 4096;
    glds16_dev<0>(voff, qs, qd); glds16_dev<1024>(voff, qs, qd); glds16_dev<2048>(voff, qs, qd); glds16_dev<3072>(voff, qs, qd);
    ld(0, 0); ld(0, 1); ld(1, 0); ld(1, 1);
    l2_touch(wo, tdump);
    g2.init(ws, w2, wo, lds0, lw, l);
  }
  const unsigned s_lo = (l & 14) == 0 ? 0x3f803f80u : 0u, s_hi = (l & 14) == 2 ? 0x3f803f80u : 0u;
  const bf16x8_t sel = __builtin_bit_cast(bf16x8_t, (u32x4){s_lo, s_lo, s_hi, s_hi});
  const int sidx = (l & 1) ? 0xeeee : 0x4444;
  f32x16 o = {}, sa, sb;
  f32x4 ls = {};
  u32x8 pa, pb = {};
  PIN(o);
  if (LOADER) {
    asm volatile("s_waitcnt vmcnt(9)" ::: "memory");
    if (l == 0) __hip_atomic_fetch_add((LAS unsigned*)((LAS char*)smem + AT_RDY), 1u, __ATOMIC_RELAXED, __HIP_MEMORY_SCOPE_WORKGROUP);
  }
#pragma unroll 1
  for (unsigned spins = 0; spins < 64u * SPIN_CAP; ++spins) {
    if (rfl(__hip_atomic_load((LAS unsigned*)((LAS char*)smem + AT_RDY), __ATOMIC_RELAXED, __HIP_MEMORY_SCOPE_WORKGROUP)) >= 4u) break;
    __builtin_amdgcn_s_sleep(1);
  }
  asm volatile("" ::: "memory");

  const LAS char* qp = (const LAS char*)smem + AT_Q + w * 2048 + l * 16;
  const bf16x8 q0 = lds_b128(qp), q1 = lds_b128(qp + 1024);
  const LAS char* kvp = (const LAS char*)smem + AT_BASE + l * 16;
  bf16x8 kx0, kx1, ky0, ky1, va0, va1, vb0, vb1, vc0, vc1, vd0, vd1;
  kx0 = lds_b128(kvp); kx1 = lds_b128(kvp + 1024); ky0 = lds_b128(kvp + 4096); ky1 = lds_b128(kvp + 4096 + 1024);
  va0 = lds_b128(kvp + 2048); va1 = lds_b128(kvp + 3072); vb0 = lds_b128(kvp + 4096 + 2048); vb1 = lds_b128(kvp + 4096 + 3072);
  vd0 = va0; vd1 = va1;
  f32x16 nm;
  {
    sa = MFMA32(kx0, q0, o); sa = MFMA32(kx1, q1, sa);
    const float ma = -xhalf_max(max16(sa));
#pragma unroll
    for (int r = 0; r < 16; ++r) { nm[r] = ma; sa[r] += ma; }
    PIN(nm);
  }
#define EX(S, i) S[i] = __builtin_amdgcn_exp2f(S[i])
#define CV(P, j, S, i) P[j] = cvtpk(S[i], S[(i) + 1])
#define PLO(x) __builtin_bit_cast(bf16x8, __builtin_shufflevector(x, x, 0, 1, 2, 3))
#define PHI(x) __builtin_bit_cast(bf16x8, __builtin_shufflevector(x, x, 4, 5, 6, 7))
#define RSUM(x) ls = __builtin_amdgcn_smfmac_f32_16x16x64_bf16(sel, __builtin_bit_cast(bf16x16_t, x), ls, sidx, 0, 0)
#define STEP(S, P, SN, PP, KN0, KN1, VP0, VP1, KL0, KL1, VL0, VL1, NP) do { \
    SN = MFMA32(KN0, q0, nm); SBAR(); KL0 = lds_b128(NP); EX(S, 0); EX(S, 1); EX(S, 2); EX(S, 3); PIN(S); SBAR(); \
    SN = MFMA32(KN1, q1, SN); SBAR(); KL1 = lds_b128((NP) + 1024); CV(P, 0, S, 0); CV(P, 1, S, 2); EX(S, 4); EX(S, 5); EX(S, 6); EX(S, 7); PIN(S); PIN(P); SBAR(); \
    o = MFMA32(VP0, PLO(PP), o); SBAR(); VL0 = lds_b128((NP) + 2048); CV(P, 2, S, 4); EX(S, 8); CV(P, 3, S, 6); EX(S, 9); EX(S, 10); EX(S, 11); PIN(S); PIN(P); SBAR(); \
    o = MFMA32(VP1, PHI(PP), o); SBAR(); VL1 = lds_b128((NP) + 3072); CV(P, 4, S, 8); CV(P, 5, S, 10); EX(S, 12); EX(S, 13); EX(S, 14); PIN(S); PIN(P); SBAR(); \
    RSUM(PP); SBAR(); EX(S, 15); CV(P, 6, S, 12); CV(P, 7, S, 14); PIN(P); SBAR(); \
  } while (0)
#define XP(on, st, j) do { if (LOADER && (on)) g2.template ffn_piece<j>(st); SBAR(); } while (0)
#define PRIO(n) do { __builtin_amdgcn_s_setprio(n); SBAR(); } while (0)
#define HEAD(cb, on, st) do { \
    PRIO(2); \
    STEP(sa, pa, sb, pb, ky0, ky1, vd0, vd1, kx0, kx1, vc0, vc1, (cb) + 2 * 4096); XP(on, st, 2); \
    STEP(sb, pb, sa, pa, kx0, kx1, va0, va1, ky0, ky1, vd0, vd1, (cb) + 3 * 4096); XP(on, st, 3); \
    PRIO(1); \
    STEP(sa, pa, sb, pb, ky0, ky1, vb0, vb1, kx0, kx1, va0, va1, (cb) + 4 * 4096); XP(on, st, 4); \
    STEP(sb, pb, sa, pa, kx0, kx1, vc0, vc1, ky0, ky1, vb0, vb1, (cb) + 5 * 4096); XP(on, st, 5); \
    STEP(sa, pa, sb, pb, ky0, ky1, vd0, vd1, kx0, kx1, vc0, vc1, (cb) + 6 * 4096); XP(on, st, 6); \
    PRIO(0); \
    STEP(sb, pb, sa, pa, kx0, kx1, va0, va1, ky0, ky1, vd0, vd1, (cb) + 7 * 4096); XP(on, st, 7); \
  } while (0)
#define TAIL(nb, on, st) do { \
    STEP(sa, pa, sb, pb, ky0, ky1, vb0, vb1, kx0, kx1, va0, va1, nb); XP(on, st, 0); \
    STEP(sb, pb, sa, pa, kx0, kx1, vc0, vc1, ky0, ky1, vb0, vb1, (nb) + 4096); XP(on, st, 1); \
  } while (0)
  HEAD(kvp, false, 0);
  if (LOADER) {
#pragma unroll 1
    for (int c = 0; c < 3; ++c) {
      asm volatile("s_waitcnt vmcnt(1) lgkmcnt(0)\n\ts_barrier" ::: "memory");
      if (c == 2 && lw == 0) peek_dev(flags + (FL_H + (mt >> 1)) * 32, lds0 + G1_CS);
      ld(c + 2, 0); ld(c + 2, 1);
      const int j = c * 4 + lw;
      l2_touch(j < 7 ? (const char*)(w2 + (size_t)(gk * 1792 + j * 256 + (mt & 7) * 32 + (l >> 1)) * DM + nt * 128 + (l & 1) * 64)
             : j < 9 ? (const char*)(wo + (size_t)((mt & 7) * 128 + (j - 7) * 64 + l) * DM + nt * 128 + gk * 64) : (const char*)wo, tdump);
      PRIO(3);
      const LAS char* nb = kvp + ((c + 1) & 1) * AT_BUF;
      TAIL(nb, false, 0); HEAD(nb, false, 0);
    }
    asm volatile("s_waitcnt vmcnt(1)" ::: "memory");
    if (lw == 0 && rfl(*(const LAS unsigned*)((const LAS char*)smem + G1_CS)) < 92u) wait_ge(flags + (FL_H + (mt >> 1)) * 32, 92u);
#pragma unroll 1
    for (int c = 3; c < 7; ++c) {
      asm volatile("s_waitcnt vmcnt(8) lgkmcnt(0)\n\ts_barrier" ::: "memory");
      if (c < 6) { ld(c + 2, 0); ld(c + 2, 1); }
      PRIO(3);
      const LAS char* nb = kvp + ((c + 1) & 1) * AT_BUF;
      TAIL(nb, true, c - 3); HEAD(nb, true, c - 3);
    }
  } else {
#pragma unroll 1
    for (int c = 0; c < 7; ++c) {
      asm volatile("s_waitcnt lgkmcnt(0)\n\ts_barrier" ::: "memory");
      if (c == 0 && late_pub) publish(late_pub, l);
      PRIO(3);
      const LAS char* nb = kvp + ((c + 1) & 1) * AT_BUF;
      TAIL(nb, false, 0); HEAD(nb, false, 0);
    }
  }
  TAIL(kvp, false, 0);
#undef HEAD
#undef TAIL
  o = MFMA32(vd0, PLO(pb), o); o = MFMA32(vd1, PHI(pb), o); RSUM(pb);
#undef EX
#undef CV
#undef PLO
#undef PHI
#undef RSUM
#undef STEP
#undef PRIO
#undef XP
  __builtin_amdgcn_s_setprio(0);
  attn_finish(ws, o, ls, q0, q1, l, ws + WS_O + (size_t)(head >> 1) * KT + (size_t)tt * 4096 + (head & 1) * 2048 + l * 16);
}

DI void g2_load(const u16* w2, const u16* wo, char* __restrict__ ws, unsigned lds0, const int lw, const int l) {
  const int xcd = blockIdx.x & 7, idx = blockIdx.x >> 3;
  const int mt = ((xcd >> 1) & 1) * 8 + (idx & 7);
  unsigned* flags = (unsigned*)(ws + WS_FLAG);
  G2Ld g; g.init(ws, w2, wo, lds0, lw, l);
  asm volatile("s_waitcnt vmcnt(10) lgkmcnt(0)\n\ts_barrier" ::: "memory");
  g.ffn(4); g.ffn(5);
  const LAS unsigned* peek = (const LAS unsigned*)(size_t)(lds0 + G2_PART);
#pragma unroll 1
  for (int i = 2; i < 24; ++i) {
    asm volatile("s_waitcnt vmcnt(16)\n\ts_barrier" ::: "memory");
    if (i == 6 && l == 0) __hip_atomic_fetch_add((gu32*)flags + (FL_O + (blockIdx.x >> 4)) * 32, 1u, RLX_AGENT);
    if (i == 20 && lw == 0) peek_dev(flags + (FL_O + mt) * 32, lds0 + G2_PART);
    g.ffn(i + 4);
  }
  if (lw == 0 && rfl(peek[0]) < 128u) wait_ge(flags + (FL_O + mt) * 32, 128u);
  asm volatile("s_waitcnt vmcnt(16)\n\ts_barrier" ::: "memory");
  g.wo(28);
  peek = (const LAS unsigned*)(size_t)(lds0 + G2_BOFF + 2048);
#pragma unroll 1
  for (int i = 25; i < 37; ++i) {
    asm volatile("s_waitcnt vmcnt(12)\n\ts_barrier" ::: "memory");
    if (i == 35 && lw == 0) peek_dev(flags + (FL_P + blockIdx.x) * 32, lds0 + G2_BOFF + 2048);
    g.wo(i + 4);
  }
  auto part = [&]() {
    const char* src = ws + WS_PART + (size_t)(blockIdx.x ^ 1) * 16384 + lw * 4096;
    const unsigned d = lds0 + G2_PART + lw * 4096;
    glds16_dev<0>(g.voff, src, d); glds16_dev<1024>(g.voff, src, d); glds16_dev<2048>(g.voff, src, d); glds16_dev<3072>(g.voff, src, d);
  };
  asm volatile("s_waitcnt vmcnt(12)\n\ts_barrier" ::: "memory");
  const bool early = rfl(peek[0]) >= 4u;
  g.wo(41);
  if (early) part();
#pragma unroll 1
  for (int i = 38; i < 41; ++i) {
    if (early) asm volatile("s_waitcnt vmcnt(16)\n\ts_barrier" ::: "memory");
    else asm volatile("s_waitcnt vmcnt(12)\n\ts_barrier" ::: "memory");
    if (i < 40) g.wo(i + 4);
  }
  if (!early && lw == 0) wait_ge(flags + (FL_P + blockIdx.x) * 32, 4u);
  asm volatile("s_waitcnt vmcnt(6)\n\ts_barrier" ::: "memory");
  if (!early) part();
  asm volatile("s_waitcnt vmcnt(0)\n\ts_barrier" ::: "memory");
}

DI void g2_compute(const u16* __restrict__ x, const u16* __restrict__ b2, char* __restrict__ ws, u16* __restrict__ out,
                   char* smem, const int w, const int l) {
  const int xcd = blockIdx.x & 7, idx = blockIdx.x >> 3;
  const int kh = xcd & 1, mt = ((xcd >> 1) & 1) * 8 + (idx & 7), nt = (xcd >> 2) * 4 + (idx >> 3);
  unsigned* flags = (unsigned*)(ws + WS_FLAG);
  const int g = l >> 4, c16 = l & 15, wm = w & 1, wn = w >> 1;
  f32x4 accg[4][2] = {}, accp[4][2] = {}, accs[4][2] = {};
  const int col0 = nt * 128 + kh * 64 + wn * 32;
  const int row0 = mt * 128 + wm * 64 + c16;
  const int colx = col0 + (g & 1) * 16 + (g >> 1) * 8;
  unsigned stw[4];
  u32x4 xv[4];
  u32x2 bv[2];
#pragma unroll
  for (int m = 0; m < 4; ++m) stw[m] = __hip_atomic_load((gu32*)(ws + WS_STAT) + row0 + m * 16, RLX_AGENT);
#pragma unroll
  for (int m = 0; m < 4; ++m) xv[m] = *(const u32x4*)(x + (size_t)(row0 + m * 16) * DM + colx);
#pragma unroll
  for (int t = 0; t < 2; ++t) bv[t] = *(const u32x2*)(b2 + col0 + 16 * t + 4 * g);
  const LAS char* lbase = (const LAS char*)smem;
  const int ah = wm * 8192 + l * 16;
  const int ao = wm * 8192 + (g >> 1) * 1024 + ((g & 1) * 32 + c16) * 16;
  int bF[2][2], bW[2];
#pragma unroll
  for (int t = 0; t < 2; ++t) {
    const int e = ((t ^ g) & 1) * 32 + (l & 3) * 8;
    bF[0][t] = G2_BOFF + (kh * 2 + wn) * 1024 + (4 * g + (c16 >> 2)) * 64 + e;
    bF[1][t] = G2_BOFF + ((kh ^ 1) * 2 + wn) * 1024 + (4 * g + (c16 >> 2)) * 64 + e;
    bW[t] = G2_BOFF + wn * 1024 + (g >> 1) * 4096 + (4 * (g & 1) + (c16 >> 2)) * 64 + e;
  }
  bf16x8 fa0[4], fb0[4], fa1[4], fb1[4];
#define F_RD(FA, FB, base, k) do { \
    _Pragma("unroll") for (int j_ = 0; j_ < 4; ++j_) { \
      FB[j_] = cat8(lds_tr((base) + bF[j_ >> 1][j_ & 1] + (k) * 8192), lds_tr((base) + bF[j_ >> 1][j_ & 1] + (k) * 8192 + 4096)); \
      FA[j_] = lds_b128((base) + ah + j_ * 2048 + (k) * 1024); } \
  } while (0)
#define F_MM(FA, FB) do { \
    _Pragma("unroll") for (int m_ = 0; m_ < 4; ++m_) { \
      accg[m_][0] = MFMA16(FB[0], FA[m_], accg[m_][0]); accg[m_][1] = MFMA16(FB[1], FA[m_], accg[m_][1]); \
      accp[m_][0] = MFMA16(FB[2], FA[m_], accp[m_][0]); accp[m_][1] = MFMA16(FB[3], FA[m_], accp[m_][1]); } \
  } while (0)
#define W_RD(FA, FB, base, k) do { \
    _Pragma("unroll") for (int j_ = 0; j_ < 2; ++j_) FB[j_] = cat8(lds_tr((base) + bW[j_] + (k) * 8192), lds_tr((base) + bW[j_] + (k) * 8192 + 512)); \
    _Pragma("unroll") for (int j_ = 0; j_ < 4; ++j_) FA[j_] = lds_b128((base) + ao + (j_ >> 1) * 4096 + (j_ & 1) * 256 + (k) * 2048); \
  } while (0)
#define W_MM(FA, FB) do { \
    _Pragma("unroll") for (int m_ = 0; m_ < 4; ++m_) { accs[m_][0] = MFMA16(FB[0], FA[m_], accs[m_][0]); accs[m_][1] = MFMA16(FB[1], FA[m_], accs[m_][1]); } \
  } while (0)
#define G2_SCHED(NG, NR, NM) do { \
    _Pragma("unroll") for (int q_ = 0; q_ < (NG); ++q_) { \
      __builtin_amdgcn_sched_group_barrier(0x008, 1, 0); __builtin_amdgcn_sched_group_barrier(0x100, NR, 0); } \
    __builtin_amdgcn_sched_group_barrier(0x008, (NM) - (NG), 0); SBAR(); \
  } while (0)
#define G2_BAR() do { asm volatile("s_waitcnt lgkmcnt(0)\n\ts_barrier" ::: "memory"); SBAR(); } while (0)
#define F_BODY(nb_) do { \
    F_RD(fa0, fb0, nb_, 0); F_MM(fa1, fb1); G2_SCHED(12, 1, 16); \
    F_RD(fa1, fb1, nb_, 1); F_MM(fa0, fb0); G2_SCHED(12, 1, 16); } while (0)

  SBAR();
  F_RD(fa0, fb0, lbase, 0); SBAR();
  F_RD(fa1, fb1, lbase, 1); F_MM(fa0, fb0); G2_SCHED(12, 1, 16);
  const LAS char* lhi = lbase + 2 * G2_SLOT;
  F_BODY(lbase + G2_SLOT);
  G2_BAR(); F_BODY(lhi);
  G2_BAR(); F_BODY(lhi + G2_SLOT);
  publish(flags + (FL_O + (blockIdx.x >> 4)) * 32, l); SBAR();
#pragma unroll 1
  for (int i = 0; i < 6; ++i) {
    G2_BAR(); F_BODY(lbase);
    G2_BAR(); F_BODY(lbase + G2_SLOT);
    G2_BAR(); F_BODY(lhi);
    G2_BAR(); F_BODY(lhi + G2_SLOT);
  }
#define W_STEP(i) do { const LAS char* nb_ = lbase + (((i) + 1) & 3) * G2_SLOT; G2_BAR(); \
    W_RD(fa0, fb0, nb_, 0); W_MM(fa1, fb1); G2_SCHED(4, 2, 8); \
    W_RD(fa1, fb1, nb_, 1); W_MM(fa0, fb0); G2_SCHED(4, 2, 8); } while (0)
  char* pdst = ws + WS_PART + (size_t)blockIdx.x * 16384 + w * 4096 + l * 16;
#define G2_SEND(m) do { store16_dev(pdst + (m) * 1024, (u32x4){cvtpk(accp[m][0][0], accp[m][0][1]), cvtpk(accp[m][0][2], accp[m][0][3]), \
                                                               cvtpk(accp[m][1][0], accp[m][1][1]), cvtpk(accp[m][1][2], accp[m][1][3])}); SBAR(); } while (0)
  G2_BAR();
  W_RD(fa0, fb0, lbase, 0); F_MM(fa1, fb1); G2_SCHED(8, 1, 16);
  W_RD(fa1, fb1, lbase, 1); W_MM(fa0, fb0); G2_SCHED(4, 2, 8);
  G2_SEND(0); G2_SEND(1);
  W_STEP(28); G2_SEND(2); G2_SEND(3);
  W_STEP(29); W_STEP(30); W_STEP(31); W_STEP(32);
  publish(flags + (FL_P + (blockIdx.x ^ 1)) * 32, l); SBAR();
  W_STEP(33);
#pragma unroll 1
  for (int i = 0; i < 2; ++i) { W_STEP(34); W_STEP(35); W_STEP(36); W_STEP(37); }
  W_STEP(42);
  W_MM(fa1, fb1);
  {
    const LAS char* xp = (const LAS char*)smem + G2_PART + w * 4096 + l * 16;
#pragma unroll
    for (int m = 0; m < 4; ++m) {
      const float den = bf_hi(stw[m]), rd2 = 1.0f / (den * den);
      const u32x4 tp = *(const LAS u32x4*)(xp + m * 1024);
      u32x4 xx = xv[m];
      { auto r0 = __builtin_amdgcn_permlane16_swap(xx[0], xx[2], false, false); xx[0] = r0[0]; xx[2] = r0[1];
        auto r1 = __builtin_amdgcn_permlane16_swap(xx[1], xx[3], false, false); xx[1] = r1[0]; xx[3] = r1[1]; }
      float r[8];
#pragma unroll
      for (int e = 0; e < 8; ++e) {
        const unsigned xw_ = xx[e >> 1], bw_ = bv[e >> 2][(e >> 1) & 1], pw_ = tp[e >> 1];
        const float z = (accg[m][e >> 2][e & 3] + ((e & 1) ? bf_hi(pw_) : bf_lo(pw_))) * rd2 + ((e & 1) ? bf_hi(bw_) : bf_lo(bw_));
        const float sg = z * __builtin_amdgcn_rcpf(1.0f + __builtin_amdgcn_exp2f(-1.4426950408889634f * z));
        r[e] = ((e & 1) ? bf_hi(xw_) : bf_lo(xw_)) + accs[m][e >> 2][e & 3] * sg;
      }
      auto r0 = __builtin_amdgcn_permlane16_swap(cvtpk(r[0], r[1]), cvtpk(r[4], r[5]), false, false);
      auto r1 = __builtin_amdgcn_permlane16_swap(cvtpk(r[2], r[3]), cvtpk(r[6], r[7]), false, false);
      *(u32x4*)(out + (size_t)(row0 + m * 16) * DM + colx) = (u32x4){r0[0], r1[0], r0[1], r1[1]};
    }
  }
#undef F_RD
#undef F_MM
#undef F_BODY
#undef G2_SCHED
#undef G2_BAR
#undef G2_SEND
#undef W_RD
#undef W_MM
#undef W_STEP
}

__global__ __launch_bounds__(512, 2) void main_kernel(const u16* __restrict__ x, const u16* __restrict__ wq, const u16* __restrict__ wk,
                                                      const u16* __restrict__ wv, const u16* __restrict__ wo, const u16* __restrict__ w1,
                                                      const u16* __restrict__ b1, const u16* __restrict__ w2, const u16* __restrict__ b2,
                                                      char* __restrict__ ws, u16* __restrict__ out) {
  extern __shared__ __attribute__((aligned(16))) char smem[];
  const int l = threadIdx.x & 63, w = rfl(threadIdx.x >> 6);
  const unsigned lds0 = (unsigned)(size_t)(LAS char*)smem;
  const int xcd = blockIdx.x & 7, idx = blockIdx.x >> 3;
  const int mtile = (xcd & 1) * 4 + (idx & 3), cg = (idx >> 2) * 4 + (xcd >> 1);
  unsigned* flags = (unsigned*)(ws + WS_FLAG);
  if (w == 3) {
    unsigned long long pc; unsigned len;
    asm volatile("s_getpc_b64 %0\n.Lmk_code_begin:\n\ts_mov_b32 %1, .Lmk_code_end-.Lmk_code_begin" : "=s"(pc), "=s"(len));
    unsigned off = (unsigned)((l & 31) * 32 + idx) * 128u;
    off = (off < len && len <= 131072u) ? off : 0u;
    l2_touch((const char*)pc + off, lds0 + G1_HS + 3072);
  }
  if (w >= 4) {
    if (cg > 8) g1_load<G1_FFN>(ws, x, w1, nullptr, b1, cg, mtile, smem, w - 4, l);
    else if (cg < 8) g1_load<G1_Q>(ws, x, wq, nullptr, b1, cg, mtile, smem, w - 4, l);
    else g1_load<G1_KV>(ws, x, wk, wv, b1, cg, mtile, smem, w - 4, l);
    attn_phase<true>(ws, w2, wo, smem, w, l, nullptr);
    g2_load(w2, wo, ws, lds0, w - 4, l);
    asm volatile(".Lmk_code_end:");
  } else {
    unsigned* late = nullptr;
    if (cg > 8) { g1_compute<G1_FFN>(ws, x, cg, mtile, smem, w, l); late = flags + (FL_H + mtile) * 32; }
    else if (cg < 8) { g1_compute<G1_Q>(ws, x, cg, mtile, smem, w, l); publish(flags + (FL_Q + mtile * 8 + cg) * 32, l); }
    else { g1_compute<G1_KV>(ws, x, cg, mtile, smem, w, l); publish(flags + FL_KV * 32, l); }
    attn_phase<false>(ws, w2, wo, smem, w, l, late);
    g2_compute(x, b2, ws, out, smem, w, l);
  }
}

extern "C" void kernel_launch(void* const* d_in, const int* in_sizes, int n_in, void* d_out, int out_size, void* d_ws,
                              size_t ws_size, hipStream_t stream) {
  const u16* x = (const u16*)d_in[0];
  const u16* wq = (const u16*)d_in[1];
  const u16* wk = (const u16*)d_in[2];
  const u16* wv = (const u16*)d_in[3];
  const u16* wo = (const u16*)d_in[4];
  const u16* w1 = (const u16*)d_in[6];
  const u16* b1 = (const u16*)d_in[7];
  const u16* w2 = (const u16*)d_in[8];
  const u16* b2 = (const u16*)d_in[9];
  char* ws = (char*)d_ws;
  (void)hipFuncSetAttribute((const void*)main_kernel, hipFuncAttributeMaxDynamicSharedMemorySize, MAIN_LDS);
  main_kernel<<<256, 512, MAIN_LDS, stream>>>(x, wq, wk, wv, wo, w1, b1, w2, b2, ws, (u16*)d_out);
}
```

```cpp
#include <hip/hip_runtime.h>
#include <hip/hip_bf16.h>

typedef short bf16x8 __attribute__((ext_vector_type(8)));
typedef short s16x4 __attribute__((ext_vector_type(4)));
typedef float f32x16 __attribute__((ext_vector_type(16)));
typedef float f32x4 __attribute__((ext_vector_type(4)));
typedef float f32x2 __attribute__((ext_vector_type(2)));
typedef __bf16 bf16x2_t __attribute__((ext_vector_type(2)));
typedef unsigned u32x4 __attribute__((ext_vector_type(4)));
typedef unsigned u32x2 __attribute__((ext_vector_type(2)));
typedef unsigned u32x8 __attribute__((ext_vector_type(8)));
typedef __bf16 bf16x8_t __attribute__((ext_vector_type(8)));
typedef __bf16 bf16x16_t __attribute__((ext_vector_type(16)));
typedef unsigned short u16;
#define LAS __attribute__((address_space(3)))
#define DI __device__ __forceinline__
#define SBAR() __builtin_amdgcn_sched_barrier(0)
#define PIN(x) asm volatile("" : "+v"(x))
#define MFMA16(a, b, c) __builtin_amdgcn_mfma_f32_16x16x32_bf16((a), (b), (c), 0, 0, 0)
#define MFMA32(a, b, c) __builtin_amdgcn_mfma_f32_32x32x16_bf16((a), (b), (c), 0, 0, 0)

constexpr int DM = 1024, FF = 3584;
constexpr size_t KT = 262144;
constexpr size_t WS_STAT = 0;
constexpr size_t WS_Q = 4u << 20;
constexpr size_t WS_KV = 8u << 20;
constexpr size_t WS_O = 9u << 20;
constexpr size_t WS_H = 13u << 20;
constexpr size_t WS_PART = 28u << 20;
constexpr size_t WS_FLAG = 37u << 20;
constexpr int FL_P = 0, FL_O = 256, FL_H = 272, FL_Q = 280, FL_KV = 344, FL_S = 345, FL_N = 353;
constexpr size_t WS_HSA = WS_FLAG + 65536, WS_HSB = WS_HSA + 4096, WS_HSC = WS_HSB + 4096;
constexpr int COORD_BLOCK = 64;

#define STAMP(kid, idx) do {} while (0)
DI unsigned rfl(unsigned x) { return __builtin_amdgcn_readfirstlane(x); }
DI unsigned cvtpk(float lo, float hi) {
  f32x2 v = {lo, hi};
  bf16x2_t b = __builtin_convertvector(v, bf16x2_t);
  return __builtin_bit_cast(unsigned, b);
}
DI float bf_lo(unsigned u) { return __uint_as_float(u << 16); }
DI float bf_hi(unsigned u) { return __uint_as_float(u & 0xffff0000u); }
DI float bf_round(float x) { return bf_lo(cvtpk(x, x)); }

template <int IMM>
DI void glds16(unsigned voff, const void* sbase, unsigned ldsaddr) {
  asm volatile("s_mov_b32 m0, %2\n\ts_nop 0\n\tglobal_load_lds_dwordx4 %0, %1 offset:%3"
               :: "v"(voff), "s"(sbase), "s"(ldsaddr), "i"(IMM) : "memory");
}
template <int IMM>
DI void glds16_dev(unsigned voff, const void* sbase, unsigned ldsaddr) {
  asm volatile("s_mov_b32 m0, %2\n\ts_nop 0\n\tglobal_load_lds_dwordx4 %0, %1 offset:%3 sc1"
               :: "v"(voff), "s"(sbase), "s"(ldsaddr), "i"(IMM) : "memory");
}
DI void set_m0(unsigned ldsaddr) { asm volatile("s_mov_b32 m0, %0\n\ts_nop 0" :: "s"(ldsaddr) : "memory"); }
template <int IMM> DI void gl16(unsigned voff, const void* sbase) {
  asm volatile("global_load_lds_dwordx4 %0, %1 offset:%2" :: "v"(voff), "s"(sbase), "i"(IMM) : "memory");
}
template <int IMM> DI void gl16_dev(unsigned voff, const void* sbase) {
  asm volatile("global_load_lds_dwordx4 %0, %1 offset:%2 sc1" :: "v"(voff), "s"(sbase), "i"(IMM) : "memory");
}
DI void peek_dev(const void* word, unsigned ldsaddr) {
  asm volatile("s_mov_b32 m0, %2\n\ts_nop 0\n\tglobal_load_lds_dword %0, %1 sc1" :: "v"(0u), "s"(word), "s"(ldsaddr) : "memory");
}
DI void store16_dev(void* p, u32x4 v) {
  asm volatile("global_store_dwordx4 %0, %1, off sc1\n\ts_nop 1" :: "v"(p), "v"(v) : "memory");
}
DI void l2_touch(const void* p, unsigned ldsaddr) {
  asm volatile("s_mov_b32 m0, %1\n\ts_nop 0\n\tglobal_load_lds_dword %0, off" :: "v"(p), "s"(ldsaddr) : "memory");
}
#define QSLP 8
#define WSLP QSLP
constexpr unsigned SPIN_CAP = 1u << 20;
typedef __attribute__((address_space(1))) unsigned gu32;
#define RLX_AGENT __ATOMIC_RELAXED, __HIP_MEMORY_SCOPE_AGENT
DI void wait_ge(const unsigned* word, unsigned want) {
#pragma unroll 1
  for (unsigned spins = 0; spins < SPIN_CAP; ++spins) {
    if (rfl(__hip_atomic_load((gu32*)word, RLX_AGENT)) >= want) break;
    __builtin_amdgcn_s_sleep(WSLP);
  }
  __builtin_amdgcn_fence(__ATOMIC_ACQUIRE, "wavefront");
}
DI void publish(unsigned* word, int l) {
  asm volatile("s_waitcnt vmcnt(0)" ::: "memory");
  if (l == 0) __hip_atomic_fetch_add((gu32*)word, 1u, RLX_AGENT);
}
DI bf16x8 load16_dev(const char* p) {
  typedef __attribute__((address_space(1))) unsigned long long gu64;
  const unsigned long long a = __hip_atomic_load((gu64*)p, RLX_AGENT), b = __hip_atomic_load((gu64*)p + 1, RLX_AGENT);
  return __builtin_bit_cast(bf16x8, (u32x4){(unsigned)a, (unsigned)(a >> 32), (unsigned)b, (unsigned)(b >> 32)});
}
DI float relu_s(float a) { return __builtin_amdgcn_fmed3f(a, 0.0f, 3.4028234663852886e38f); }
DI bf16x8 lds_b128(const LAS char* p) { return *(const LAS bf16x8*)p; }
DI s16x4 lds_tr(const LAS char* p) {
  return __builtin_amdgcn_ds_read_tr16_b64_v4i16((LAS s16x4*)p);
}
DI bf16x8 cat8(s16x4 lo, s16x4 hi) { return __builtin_shufflevector(lo, hi, 0, 1, 2, 3, 4, 5, 6, 7); }

DI float half_sum(float v) {
  v += __uint_as_float(__builtin_amdgcn_update_dpp(0u, __float_as_uint(v), 0xB1, 0xf, 0xf, true));
  v += __uint_as_float(__builtin_amdgcn_update_dpp(0u, __float_as_uint(v), 0x4E, 0xf, 0xf, true));
  v += __uint_as_float(__builtin_amdgcn_update_dpp(0u, __float_as_uint(v), 0x141, 0xf, 0xf, true));
  v += __uint_as_float(__builtin_amdgcn_update_dpp(0u, __float_as_uint(v), 0x140, 0xf, 0xf, true));
  auto r = __builtin_amdgcn_permlane16_swap(__float_as_uint(v), __float_as_uint(v), false, false);
  return __uint_as_float(r[0]) + __uint_as_float(r[1]);
}
DI unsigned row_stats(const u32x4 (&v)[4]) {
  f32x2 e[16], s2 = {0.f, 0.f};
#pragma unroll
  for (int i = 0; i < 16; ++i) { e[i] = (f32x2){bf_lo(v[i >> 2][i & 3]), bf_hi(v[i >> 2][i & 3])}; s2 += e[i]; }
  const float mean = half_sum(s2[0] + s2[1]) * (1.0f / DM);
  f32x2 q2 = {0.f, 0.f};
#pragma unroll
  for (int i = 0; i < 16; ++i) { const f32x2 d = e[i] - mean; q2 += d * d; }
  const float q = half_sum(q2[0] + q2[1]);
  const float den = __builtin_sqrtf(bf_round(bf_round(q * (1.0f / DM)) + 1e-5f));
  return cvtpk(-mean, den);
}

constexpr int G1_STAGE = 53248, G1_BOFF = 32768;
enum { G1_FFN = 5, G1_Q = 4, G1_KV = 2 };

constexpr int G1_ST = 87040;
constexpr int G1_CF = 53248;
constexpr int AT_RDY = 89088;
constexpr int G1_HS = 159744;
constexpr int G1_CS = 81920;
constexpr int ESTART = 3, PSTART = 9;

DI float xhalf_max(float m) {
  auto rr = __builtin_amdgcn_permlane32_swap(__float_as_uint(m), __float_as_uint(m), false, false);
  return __builtin_fmaxf(__uint_as_float(rr[0]), __uint_as_float(rr[1]));
}
DI float xhalf_sum(float m) {
  auto rr = __builtin_amdgcn_permlane32_swap(__float_as_uint(m), __float_as_uint(m), false, false);
  return __uint_as_float(rr[0]) + __uint_as_float(rr[1]);
}
template <int NP>
DI void g1_load(char* ws, const u16* x, const u16* wsrc, const u16* wv, const u16* b1, int cg, int mtile, char* smem, int lw, int l) {
  constexpr int LD = (NP == G1_FFN) ? FF : (NP == G1_Q) ? DM : 32;
  const unsigned lds0 = (unsigned)(size_t)(LAS char*)smem;
  const char* abase = (const char*)(x + (size_t)(mtile * 256 + lw * 64) * DM);
  const unsigned ave = (unsigned)((l >> 3) * 2048 + (((l & 7) ^ (l >> 4)) << 4)), avo = ave ^ 64u;
  const unsigned bvoff = (unsigned)((lw * 16 + (l >> 2)) * LD * 2 + (((l & 3) ^ ((l >> 5) << 1)) << 4));
  const int krot = NP == G1_Q ? (mtile & 3) * 4 : 0;
  unsigned va[8], vb[NP];
#pragma unroll
  for (int i = 0; i < 8; ++i) va[i] = ((i & 1) ? avo : ave) + i * 16384 - (i - 4) * 1024;
#pragma unroll
  for (int p = 0; p < NP; ++p) {
    int cb = (NP == G1_FFN) ? (cg - 9) * 5 + p : (NP == G1_Q) ? cg * 4 + p : 0;
    cb = cb > 111 ? 111 : cb;
    vb[p] = bvoff + cb * 64 - (p - 2) * 1024 + 4096;
  }
  unsigned bias[NP];
#pragma unroll
  for (int p = 0; p < NP; ++p) {
    int cb = (cg - 9) * 5 + p; cb = cb > 111 ? 111 : cb;
    bias[p] = (NP == G1_FFN) ? (unsigned)b1[cb * 32 + (l & 31)] : 0u;
  }
  float cs[NP];
#pragma unroll
  for (int p = 0; p < NP; ++p) cs[p] = 0.f;
  const LAS char* cb0 = (const LAS char*)smem + G1_BOFF + lw * (NP * 1024) + (8 * (l >> 5) + ((l & 15) >> 2)) * 64 + (((l >> 4) ^ (l >> 5)) & 1) * 32 + (l & 3) * 8;
  u32x2 t0[NP], t1[NP];
  auto cs_read = [&](int slot, int p) {
    t0[p] = __builtin_bit_cast(u32x2, lds_tr(cb0 + slot * G1_STAGE + p * 1024));
    t1[p] = __builtin_bit_cast(u32x2, lds_tr(cb0 + slot * G1_STAGE + p * 1024 + 256));
    SBAR();
  };
  auto cs_add = [&](int p) {
    float a, b, c, d;
    asm volatile("v_lshlrev_b32 %1, 16, %5\n\tv_and_b32 %2, 0xffff0000, %5\n\tv_lshlrev_b32 %3, 16, %6\n\tv_and_b32 %4, 0xffff0000, %6\n\t"
                 "v_add_f32 %1, %1, %2\n\tv_add_f32 %3, %3, %4\n\t"
                 "v_lshlrev_b32 %2, 16, %7\n\tv_and_b32 %4, 0xffff0000, %7\n\tv_add_f32 %1, %1, %2\n\tv_add_f32 %3, %3, %4\n\t"
                 "v_lshlrev_b32 %2, 16, %8\n\tv_and_b32 %4, 0xffff0000, %8\n\tv_add_f32 %1, %1, %2\n\tv_add_f32 %3, %3, %4\n\t"
                 "v_add_f32 %1, %1, %3\n\tv_add_f32 %0, %0, %1"
                 : "+v"(cs[p]), "=&v"(a), "=&v"(b), "=&v"(c), "=&v"(d) : "v"(t0[p][0]), "v"(t0[p][1]), "v"(t1[p][0]), "v"(t1[p][1]));
    SBAR();
  };
  auto issue = [&](int st, int slot, bool sum, int sslot) {
    const int kt = (st + krot) & 15;
    const char* b0 = (const char*)wsrc + (size_t)kt * (64 * LD * 2) - 4096;
    set_m0(lds0 + slot * G1_STAGE + G1_BOFF + lw * (NP * 1024) + 2048);
    gl16<-2048>(vb[0], b0);
    gl16<-1024>(vb[1], NP == G1_KV ? (const char*)wv + (size_t)kt * (64 * LD * 2) - 4096 : b0);
    if (NP > 2) { gl16<0>(vb[2 % NP], b0); gl16<1024>(vb[3 % NP], b0); }
    if (NP > 4) gl16<2048>(vb[4 % NP], b0);
    const char* a0 = abase + kt * 128;
    set_m0(lds0 + slot * G1_STAGE + lw * 8192 + 4096);
#define AL(i) do { gl16<((i) - 4) * 1024>(va[i], a0); \
      if (sum && (i) < NP) cs_read(sslot, (i) % NP); if (sum && (i) >= 1 && (i) <= NP) cs_add(((i) + NP - 1) % NP); } while (0)
    AL(0); AL(1); AL(2); AL(3); AL(4); AL(5); AL(6); AL(7);
#undef AL
  };
  auto colsum = [&](int slot) {
#pragma unroll
    for (int p = 0; p < NP; ++p) cs_read(slot, p);
#pragma unroll
    for (int p = 0; p < NP; ++p) cs_add(p);
  };
  unsigned* flags = (unsigned*)(ws + WS_FLAG);
  const bool coord = (NP == G1_KV) && blockIdx.x == COORD_BLOCK && lw == 1;
  if (coord) {
#pragma unroll
    for (int i = 0; i < 6; ++i) if (i * 64 + l < FL_N) __hip_atomic_store((gu32*)flags + (i * 64 + l) * 32, 0u, RLX_AGENT);
    store16_dev(ws + WS_HSC + l * 16, (u32x4){0u, 0u, 0u, 0u});
    asm volatile("s_waitcnt vmcnt(0)" ::: "memory");
  }
  issue(0, 0, false, 0); issue(1, 1, false, 0); issue(2, 2, false, 0);
  asm volatile("s_waitcnt vmcnt(%0)\n\ts_barrier" :: "n"(2 * (8 + NP)) : "memory");
  __builtin_amdgcn_s_setprio(2);
  int state = (lw == 0 || coord) ? 0 : 2;
  bool pend = false, first = true;
  u32x4 last = {0u, 0u, 0u, 0u};
  const unsigned found = rfl(*(const LAS unsigned*)((const LAS char*)smem + G1_HS));
  if (lw == 0 && l < 9) {
    const unsigned v = *(const LAS unsigned*)((const LAS char*)smem + G1_HS + 16 + (l & 7) * 4);
    __hip_atomic_store(l < 8 ? (gu32*)(ws + WS_STAT) + mtile * 256 + cg * 8 + l : (gu32*)(ws + WS_HSA) + blockIdx.x, l < 8 ? v : found + 1u, RLX_AGENT);
  }
  colsum(0);
  auto poll_issue = [&]() {
    if (NP == G1_KV && coord) {
      glds16_dev<0>(l * 16, ws + WS_HSA, lds0 + G1_HS + 1024); glds16_dev<0>(l * 16, ws + WS_HSC, lds0 + G1_HS + 2048);
    } else {
      const char* a_ = state == 0 ? ws + WS_HSB + blockIdx.x * 4 : (const char*)(flags + (FL_S + mtile) * 32);
      peek_dev(a_, lds0 + G1_HS + 256);
    }
  };
  auto poll_check = [&]() {
    if (NP == G1_KV && coord) {
      const u32x4 pa = *(const LAS u32x4*)((const LAS char*)smem + G1_HS + 1024 + l * 16), pc = *(const LAS u32x4*)((const LAS char*)smem + G1_HS + 2048 + l * 16);
      if (first || pa[0] != last[0] || pa[1] != last[1] || pa[2] != last[2] || pa[3] != last[3]) store16_dev(ws + WS_HSB + l * 16, pa);
      last = pa; first = false;
      if (__all((pc[0] & pc[1] & pc[2] & pc[3]) == 1u)) state = 2;
    } else {
      const unsigned v = rfl(*(const LAS unsigned*)((const LAS char*)smem + G1_HS + 256));
      if (state == 0) {
        if (v != found) {
          if (l == 0) {
            __hip_atomic_store((gu32*)(ws + WS_HSC) + blockIdx.x, 1u, RLX_AGENT);
            __hip_atomic_fetch_add((gu32*)flags + (FL_S + mtile) * 32, 1u, RLX_AGENT);
          }
          state = 1;
        }
      } else if (v >= 32u) state = 2;
    }
    state = rfl(state);
  };
  int slot = 0;
#pragma unroll 1
  for (int i = 0; i < 13; ++i) {
    asm volatile("s_waitcnt vmcnt(%0)\n\ts_barrier" :: "n"(8 + NP) : "memory");
    if (state < 2 && (i >= ESTART || coord)) {
      if (pend) poll_check();
      pend = state == 0 || (state == 1 && i >= PSTART);
      if (pend) poll_issue();
    }
    const int nslot = (slot == 2) ? 0 : slot + 1;
    issue(i + 3, slot, true, nslot);
    slot = nslot;
  }
#pragma unroll 1
  for (unsigned spins = 0; state < 2 && spins < SPIN_CAP; ++spins) {
    if (!pend) poll_issue();
    asm volatile("s_waitcnt vmcnt(0)" ::: "memory");
    poll_check();
    pend = false;
    __builtin_amdgcn_s_sleep(8);
  }
  asm volatile("s_waitcnt vmcnt(%0)\n\ts_barrier" :: "n"(8 + NP) : "memory");
  if (lw == 0) { glds16_dev<0>(l * 16, ws + WS_STAT + mtile * 1024, lds0 + G1_ST); *(LAS u32x2*)((LAS char*)smem + AT_RDY) = (u32x2){0u, 0u}; }
  colsum(2);
  asm volatile("s_waitcnt vmcnt(0)" ::: "memory");
  colsum(0);
#pragma unroll
  for (int p = 0; p < NP; ++p) {
    const float c = xhalf_sum(cs[p]);
    const unsigned c0 = cvtpk(c, c) & 0xffffu; const float r1 = c - bf_lo(c0);
    const unsigned c1 = cvtpk(r1, r1) & 0xffffu; const float r2 = r1 - bf_lo(c1);
    const unsigned c2 = cvtpk(r2, r2) & 0xffffu;
    if (l < 32) *(LAS u32x4*)((LAS char*)smem + G1_CF + (p * 2 + (l >> 4)) * 1024 + (lw * 16 + (l & 15)) * 16) = (u32x4){c0 | (c1 << 16), c2 | (lw ? 0u : bias[p] << 16), 0u, 0u};
  }
  asm volatile("s_waitcnt lgkmcnt(0)\n\ts_barrier" ::: "memory");
}

template <int NP>
DI void g1_compute(char* __restrict__ ws, const u16* __restrict__ x, int cg, int mtile, char* smem, int w, int l) {
  STAMP(1, 0);
  int g = l >> 4, c16 = l & 15;
  {
    const int row = mtile * 256 + cg * 8 + w * 2 + (l >> 5);
    u32x4 v[4];
#pragma unroll
    for (int i = 0; i < 4; ++i) v[i] = *(const u32x4*)(x + (size_t)row * DM + (i * 32 + (l & 31)) * 8);
    const unsigned found = __hip_atomic_load((gu32*)(ws + WS_HSB) + blockIdx.x, RLX_AGENT);
    SBAR();
    const unsigned rs = row_stats(v);
    if ((l & 31) == 0) *(LAS unsigned*)((LAS char*)smem + G1_HS + 16 + (w * 2 + (l >> 5)) * 4) = rs;
    if (w == 0 && l == 0) *(LAS unsigned*)((LAS char*)smem + G1_HS) = found;
  }
  f32x4 acc[4][NP][2];
#pragma unroll
  for (int m = 0; m < 4; ++m)
#pragma unroll
    for (int p = 0; p < NP; ++p) { acc[m][p][0] = f32x4{}; acc[m][p][1] = f32x4{}; }
  const LAS char* lbase = (const LAS char*)smem;
  int aoff[2], boff[2];
#pragma unroll
  for (int k = 0; k < 2; ++k) {
    aoff[k] = (w * 64 + c16) * 128 + (((4 * k + g) ^ (c16 >> 1)) << 4);
    boff[k] = G1_BOFF + (g >> 1) * (NP * 1024) + (8 * (g & 1) + (c16 >> 2)) * 64 + ((k ^ g) & 1) * 32 + (l & 3) * 8;
  }
  bf16x8 fa0[4], fa1[4], fb0[NP], fb1[NP];
#define G1_RA(FA, base, k) do { _Pragma("unroll") for (int m_ = 0; m_ < 4; ++m_) FA[m_] = lds_b128((base) + aoff[k] + m_ * 2048); } while (0)
#define G1_RB(FB, base, k, t) do { _Pragma("unroll") for (int p_ = 0; p_ < NP; ++p_) \
      FB[p_] = cat8(lds_tr((base) + boff[t] + p_ * 1024 + (k) * (NP * 2048)), lds_tr((base) + boff[t] + p_ * 1024 + (k) * (NP * 2048) + 256)); } while (0)
#define G1_MM(FA, FB, t) do { \
    _Pragma("unroll") for (int m_ = 0; m_ < 4; ++m_) _Pragma("unroll") for (int p_ = 0; p_ < NP; ++p_) { \
      if (NP == G1_KV && p_ == 1) acc[m_][p_][t] = MFMA16(FA[m_], FB[p_], acc[m_][p_][t]); \
      else acc[m_][p_][t] = MFMA16(FB[p_], FA[m_], acc[m_][p_][t]); } \
  } while (0)
#define G1_SCHED(NR) do { \
    _Pragma("unroll") for (int q_ = 0; q_ < (NR); ++q_) { \
      __builtin_amdgcn_sched_group_barrier(0x008, 1, 0); __builtin_amdgcn_sched_group_barrier(0x100, 1, 0); } \
    __builtin_amdgcn_sched_group_barrier(0x008, 4 * NP - (NR), 0); SBAR(); \
  } while (0)

#pragma unroll
  for (int m = 0; m < 4; ++m) fa1[m] = bf16x8{};
#pragma unroll
  for (int p = 0; p < NP; ++p) fb1[p] = bf16x8{};
#define G1_STG(nb) do { \
    asm volatile("s_waitcnt lgkmcnt(0)\n\ts_barrier" ::: "memory"); SBAR(); \
    G1_RB(fb0, nb, 0, 0); G1_RA(fa0, nb, 0); G1_MM(fa1, fb1, 1); G1_SCHED(2 * NP + 4); \
    G1_RB(fb1, nb, 0, 1);                    G1_MM(fa0, fb0, 0); G1_SCHED(2 * NP); \
    G1_RB(fb0, nb, 1, 0); G1_RA(fa1, nb, 1); G1_MM(fa0, fb1, 1); G1_SCHED(2 * NP + 4); \
    G1_RB(fb1, nb, 1, 1);                    G1_MM(fa1, fb0, 0); G1_SCHED(2 * NP); } while (0)
  int slot = 0;
  STAMP(1, 1);
#pragma unroll 1
  for (int i = 0; i < 8; ++i) {
    const int s1 = (slot == 2) ? 0 : slot + 1;
    G1_STG(lbase + slot * G1_STAGE);
    G1_STG(lbase + s1 * G1_STAGE);
    slot = (s1 == 2) ? 0 : s1 + 1;
  }
#undef G1_STG
  STAMP(1, 4);
  __builtin_amdgcn_s_setprio(3);
  asm volatile("" : "+v"(l));
  g = l >> 4; c16 = l & 15;
#define G1_MMA(FA, FB, t) do { \
    _Pragma("unroll") for (int m_ = 0; m_ < 4; ++m_) _Pragma("unroll") for (int p_ = 0; p_ < NP; ++p_) { \
      if (NP == G1_KV && p_ == 1) asm volatile("v_mfma_f32_16x16x32_bf16 %0, %1, %2, %0" : "+v"(acc[m_][p_][t]) : "v"(FA[m_]), "v"(FB[p_])); \
      else asm volatile("v_mfma_f32_16x16x32_bf16 %0, %1, %2, %0" : "+v"(acc[m_][p_][t]) : "v"(FB[p_]), "v"(FA[m_])); } \
  } while (0)
  unsigned st[4];
  {
#pragma unroll
    for (int m = 0; m < 4; ++m) st[m] = *(const LAS unsigned*)(lbase + G1_ST + (w * 64 + m * 16 + c16) * 4);
#pragma unroll
    for (int p = 0; p < NP; ++p) fb0[p] = lds_b128(lbase + G1_CF + p * 2048 + l * 16);
    SBAR();
    G1_MMA(fa1, fb1, 1);
    SBAR();
#pragma unroll
    for (int p = 0; p < NP; ++p) fb1[p] = lds_b128(lbase + G1_CF + p * 2048 + 1024 + l * 16);
#pragma unroll
    for (int m = 0; m < 4; ++m) {
      const unsigned nm = st[m] & 0xffffu;
      fa0[m] = __builtin_bit_cast(bf16x8, (u32x4){nm | (nm << 16), st[m], 0u, 0u});
      PIN(fa0[m]);
    }
    SBAR();
    asm volatile("s_nop 1");
    G1_MMA(fa0, fb0, 0);
    G1_MMA(fa0, fb1, 1);
    asm volatile("s_nop 7");
  }
#undef G1_MMA
  SBAR();
  STAMP(1, 2);

#pragma unroll
  for (int p = 0; p < NP; ++p) {
    const int cb = (NP == G1_FFN) ? (cg - 9) * 5 + p : cg * 4 + p;
    if (NP == G1_KV && p == 1) {
#pragma unroll
      for (int mm = 0; mm < 2; ++mm) {
        const u32x4 s0 = *(const LAS u32x4*)(lbase + G1_ST + (w * 64 + mm * 32 + 4 * g) * 4), s1 = *(const LAS u32x4*)(lbase + G1_ST + (w * 64 + mm * 32 + 16 + 4 * g) * 4);
#pragma unroll
        for (int t = 0; t < 2; ++t) {
          const f32x4 a = acc[2 * mm][p][t], b = acc[2 * mm + 1][p][t];
          const u32x4 o = {cvtpk(a[0] / bf_hi(s0[0]), a[1] / bf_hi(s0[1])), cvtpk(a[2] / bf_hi(s0[2]), a[3] / bf_hi(s0[3])),
                           cvtpk(b[0] / bf_hi(s1[0]), b[1] / bf_hi(s1[1])), cvtpk(b[2] / bf_hi(s1[2]), b[3] / bf_hi(s1[3]))};
          store16_dev(ws + WS_KV + (size_t)(mtile * 8 + w * 2 + mm) * 4096 + 2048 + (g >> 1) * 1024 + ((g & 1) * 32 + t * 16 + c16) * 16, o);
        }
      }
    } else if (NP != G1_FFN || cb < 112) {
#pragma unroll
      for (int m = 0; m < 4; ++m) {
        const int tt = mtile * 8 + w * 2 + (m >> 1);
        const int pos = (NP == G1_KV) ? (c16 & 7) + (m & 1) * 8 + (c16 >> 3) * 16 : (m & 1) * 16 + c16;
        char* dst = (NP == G1_FFN) ? ws + WS_H + (size_t)(cb >> 1) * KT + (size_t)tt * 4096 + ((m & 1) * 2 + (cb & 1)) * 1024 + l * 16
                  : ((NP == G1_Q) ? ws + WS_Q + (size_t)(tt * 32 + cb) * 2048 : ws + WS_KV + (size_t)tt * 4096) + (g >> 1) * 1024 + ((g & 1) * 32 + pos) * 16;
        float sc = 1.0f;
        if (NP != G1_FFN) { sc = 1.0f / bf_hi(st[m]); if (NP == G1_Q) sc *= 0.17677669529663687f * 1.4426950408889634f; }
        u32x4 o;
#pragma unroll
        for (int j = 0; j < 4; ++j) {
          float a = acc[m][p][j >> 1][2 * (j & 1)], b = acc[m][p][j >> 1][2 * (j & 1) + 1];
          if (NP == G1_FFN) { a = relu_s(a); b = relu_s(b); a *= a; b *= b; }
          else { a *= sc; b *= sc; }
          o[j] = cvtpk(a, b);
        }
        store16_dev(dst, o);
      }
    }
  }
  STAMP(1, 3);
#undef G1_RA
#undef G1_RB
#undef G1_MM
#undef G1_SCHED
}

constexpr int AT_NCH = 8;
constexpr int AT_BUF = 32768;
constexpr int AT_BASE = 98304;
constexpr int AT_Q = 65536;
constexpr int AT_DUMP = 90112;
constexpr int MAIN_LDS = 163840;

DI float max16(const f32x16& s) {
  float a = __builtin_fmaxf(s[0], s[1]);
#pragma unroll
  for (int r = 2; r < 16; ++r) a = __builtin_fmaxf(a, s[r]);
  return a;
}
DI bf16x8 pack8(const f32x16& p, int s) {
  u32x4 o;
#pragma unroll
  for (int j = 0; j < 4; ++j) o[j] = cvtpk(p[8 * s + 2 * j], p[8 * s + 2 * j + 1]);
  return __builtin_bit_cast(bf16x8, o);
}

DI void attn_fallback(const char* __restrict__ kv, bf16x8 q0, bf16x8 q1, int l, f32x16& o_out, float& l_out) {
  f32x16 o = {};
  float m = -INFINITY, lsum = 0.f;
  for (int t = 0; t < 64; ++t) {
    const char* p = kv + (size_t)t * 4096 + l * 16;
    const bf16x8 k0 = load16_dev(p), k1 = load16_dev(p + 1024), v0 = load16_dev(p + 2048), v1 = load16_dev(p + 3072);
    f32x16 s = {};
    s = MFMA32(k0, q0, s); s = MFMA32(k1, q1, s);
    const float mn = __builtin_fmaxf(m, xhalf_max(max16(s)));
    const float ms = (mn == -INFINITY) ? 0.f : mn;
    const float alpha = __builtin_amdgcn_exp2f(m - ms);
    m = mn;
    float ps = 0.f;
#pragma unroll
    for (int r = 0; r < 16; ++r) { s[r] = __builtin_amdgcn_exp2f(s[r] - ms); ps += s[r]; }
    lsum = lsum * alpha + ps;
#pragma unroll
    for (int r = 0; r < 16; ++r) o[r] *= alpha;
    o = MFMA32(v0, pack8(s, 0), o); o = MFMA32(v1, pack8(s, 1), o);
  }
  o_out = o;
  l_out = xhalf_sum(lsum);
}

constexpr int G2_SLOT = 32768, G2_BOFF = 16384, G2_PART = 131072;
struct G2Ld {
  const char* hb; const char* ob; const char* wb; const char* wb2; unsigned lds; unsigned voff, vb[4]; int rot, frot;
  DI void init(const char* ws, const u16* w2, const u16* wo, unsigned lds0, int lw, int l) {
    const int xcd = blockIdx.x & 7, idx = blockIdx.x >> 3, kh = xcd & 1;
    const int mt = ((xcd >> 1) & 1) * 8 + (idx & 7), nt = (xcd >> 2) * 4 + (idx >> 3);
    hb = ws + WS_H + (size_t)(mt * 4 + lw) * 4096 + (size_t)kh * (28 * KT);
    ob = ws + WS_O + (size_t)(mt * 4 + lw) * 4096;
    wb = (const char*)(w2 + (size_t)(kh * 1792 + lw * 16) * DM + nt * 128) - 4096;
    wb2 = (const char*)(wo + (size_t)(lw * 16) * DM + nt * 128 + kh * 64) - 4096;
#pragma unroll
    for (int p = 0; p < 4; ++p) vb[p] = (unsigned)((l >> 2) * DM * 2 + (((l & 3) ^ ((l >> 3) & 2)) << 4) + p * 64 - p * 1024 + 4096);
    lds = lds0 + lw * 4096; voff = l * 16;
    frot = (7 * (mt & 7)) >> 1;
    rot = 2 * (mt & 7);
  }
  DI void ffn(int s) const {
    const unsigned d = lds + (s & 3) * G2_SLOT;
    s += frot; s = s >= 28 ? s - 28 : s;
    const char* a = hb + (size_t)s * KT;
    const char* b = wb + (size_t)s * 131072;
    set_m0(d); gl16_dev<0>(voff, a); gl16_dev<1024>(voff, a); gl16_dev<2048>(voff, a); gl16_dev<3072>(voff, a);
    set_m0(d + G2_BOFF); gl16<0>(vb[0], b); gl16<1024>(vb[1], b); gl16<2048>(vb[2], b); gl16<3072>(vb[3], b);
  }
  template <int J> DI void ffn_piece(int s) const {
    const unsigned d = lds + (s & 3) * G2_SLOT;
    s += frot; s = s >= 28 ? s - 28 : s;
    if (J < 4) glds16_dev<(J & 3) * 1024>(voff, hb + (size_t)s * KT, d);
    else glds16<(J & 3) * 1024>(vb[J & 3], wb + (size_t)s * 131072, d + G2_BOFF);
  }
  DI void wo(int s) const {
    const unsigned d = lds + (s & 3) * G2_SLOT;
    const int k = (s - 28 + rot) & 15;
    const char* a = ob + (size_t)k * KT;
    const char* b = wb2 + (size_t)k * 131072;
    set_m0(d); gl16_dev<0>(voff, a); gl16_dev<1024>(voff, a); gl16_dev<2048>(voff, a); gl16_dev<3072>(voff, a);
    set_m0(d + G2_BOFF); gl16<0>(vb[0], b); gl16<1024>(vb[1], b);
  }
};

DI void attn_finish(char* __restrict__ ws, f32x16 o, f32x4 lacc, bf16x8 q0, bf16x8 q1, int l, char* dst) {
  const float l0 = __shfl(lacc[0] + lacc[1], l & 15), l1 = __shfl(lacc[2] + lacc[3], l & 15);
  float lsum = (l & 16) ? l1 : l0;
  bool ok = (lsum >= 9.5367431640625e-07f) && (lsum <= 1.8446744073709552e19f);
#pragma unroll
  for (int r = 0; r < 16; ++r) ok = ok && (__builtin_fabsf(o[r]) <= 3.0e38f);
  if (!__all(ok)) attn_fallback(ws + WS_KV, q0, q1, l, o, lsum);
  const float rl = 1.0f / lsum;
#pragma unroll
  for (int s = 0; s < 2; ++s) {
    u32x4 ov;
#pragma unroll
    for (int j = 0; j < 4; ++j) ov[j] = cvtpk(o[8 * s + 2 * j] * rl, o[8 * s + 2 * j + 1] * rl);
    store16_dev(dst + s * 1024, ov);
  }
}

template <bool LOADER>
DI void attn_phase(char* __restrict__ ws, const u16* w2, const u16* wo, char* smem, const int w, const int l, unsigned* late_pub) {
  const int tt = blockIdx.x >> 2, hg = blockIdx.x & 3;
  const int head = hg * 8 + w, lw = w & 3;
  const unsigned lds0 = (unsigned)(size_t)(LAS char*)smem;
  unsigned* flags = (unsigned*)(ws + WS_FLAG);
  const unsigned voff = l * 16;
  const char* ksrc = ws + WS_KV + (size_t)lw * 4096;
  const unsigned kdst = lds0 + AT_BASE + lw * 4096;
  auto ld = [&](int c, int half) {
    const char* s_ = ksrc + (size_t)c * AT_BUF + half * 16384;
    const unsigned d_ = kdst + (c & 1) * AT_BUF + half * 16384;
    set_m0(d_); gl16_dev<0>(voff, s_); gl16_dev<1024>(voff, s_); gl16_dev<2048>(voff, s_); gl16_dev<3072>(voff, s_);
  };
  G2Ld g2;
  const unsigned tdump = lds0 + AT_DUMP + lw * 256;
  const int xcd = blockIdx.x & 7, idx = blockIdx.x >> 3, gk = xcd & 1;
  const int mt = ((xcd >> 1) & 1) * 8 + (idx & 7), nt = (xcd >> 2) * 4 + (idx >> 3);
  STAMP(2, 0);
  if (LOADER) {
    const unsigned* word = flags + (l == 0 ? FL_KV : FL_Q + (tt >> 3) * 8 + hg * 2 + (l & 1)) * 32;
    const unsigned need = l == 0 ? 32u : 4u;
    bool done = l > 2;
    auto await = [&](unsigned level) {
      if (lw == 0) {
#pragma unroll 1
        for (unsigned spins = 0; spins < SPIN_CAP; ++spins) {
          if (level == 1 ? (bool)rfl(done) : (bool)__all(done)) break;
          if (spins) __builtin_amdgcn_s_sleep(QSLP);
          if (!done) done = __hip_atomic_load((gu32*)word, RLX_AGENT) >= need;
        }
        if (__all(done)) level = 2;
        if (l == 0) __hip_atomic_store((LAS unsigned*)((LAS char*)smem + AT_RDY + 4), level, __ATOMIC_RELAXED, __HIP_MEMORY_SCOPE_WORKGROUP);
      } else {
#pragma unroll 1
        for (unsigned spins = 0; spins < 64u * SPIN_CAP; ++spins) {
          if (rfl(__hip_atomic_load((LAS unsigned*)((LAS char*)smem + AT_RDY + 4), __ATOMIC_RELAXED, __HIP_MEMORY_SCOPE_WORKGROUP)) >= level) break;
          __builtin_amdgcn_s_sleep(1);
        }
      }
      asm volatile("" ::: "memory");
    };
    await(1);
    ld(0, 0); ld(0, 1);
    await(2);
    const char* qs = ws + WS_Q + (size_t)(tt * 32 + hg * 8 + lw * 2) * 2048;
    set_m0(lds0 + AT_Q + lw * 4096); gl16_dev<0>(voff, qs); gl16_dev<1024>(voff, qs); gl16_dev<2048>(voff, qs); gl16_dev<3072>(voff, qs);
    ld(1, 0); ld(1, 1);
    l2_touch(wo, tdump);
    g2.init(ws, w2, wo, lds0, lw, l);
  }
  const unsigned s_lo = (l & 14) == 0 ? 0x3f803f80u : 0u, s_hi = (l & 14) == 2 ? 0x3f803f80u : 0u;
  const bf16x8_t sel = __builtin_bit_cast(bf16x8_t, (u32x4){s_lo, s_lo, s_hi, s_hi});
  const int sidx = (l & 1) ? 0xeeee : 0x4444;
  f32x16 o = {}, sa, sb;
  f32x4 ls = {};
  u32x8 pa, pb = {};
  PIN(o);
  if (LOADER) {
    asm volatile("s_waitcnt vmcnt(9)" ::: "memory");
    if (l == 0) __hip_atomic_fetch_add((LAS unsigned*)((LAS char*)smem + AT_RDY), 1u, __ATOMIC_RELAXED, __HIP_MEMORY_SCOPE_WORKGROUP);
  }
#pragma unroll 1
  for (unsigned spins = 0; spins < 64u * SPIN_CAP; ++spins) {
    if (rfl(__hip_atomic_load((LAS unsigned*)((LAS char*)smem + AT_RDY), __ATOMIC_RELAXED, __HIP_MEMORY_SCOPE_WORKGROUP)) >= 4u) break;
    __builtin_amdgcn_s_sleep(1);
  }
  asm volatile("" ::: "memory");
  STAMP(2, 1);

  const LAS char* qp = (const LAS char*)smem + AT_Q + w * 2048 + l * 16;
  const bf16x8 q0 = lds_b128(qp), q1 = lds_b128(qp + 1024);
  const LAS char* kvp = (const LAS char*)smem + AT_BASE + l * 16;
  bf16x8 kx0, kx1, ky0, ky1, va0, va1, vb0, vb1, vc0, vc1, vd0, vd1;
  kx0 = lds_b128(kvp); kx1 = lds_b128(kvp + 1024); ky0 = lds_b128(kvp + 4096); ky1 = lds_b128(kvp + 4096 + 1024);
  va0 = lds_b128(kvp + 2048); va1 = lds_b128(kvp + 3072); vb0 = lds_b128(kvp + 4096 + 2048); vb1 = lds_b128(kvp + 4096 + 3072);
  vd0 = va0; vd1 = va1;
  f32x16 nm;
  {
    sa = MFMA32(kx0, q0, o); sa = MFMA32(kx1, q1, sa);
    const float ma = -xhalf_max(max16(sa));
#pragma unroll
    for (int r = 0; r < 16; ++r) { nm[r] = ma; sa[r] += ma; }
    PIN(nm);
  }
#define EX(S, i) S[i] = __builtin_amdgcn_exp2f(S[i])
#define CV(P, j, S, i) P[j] = cvtpk(S[i], S[(i) + 1])
#define PLO(x) __builtin_bit_cast(bf16x8, __builtin_shufflevector(x, x, 0, 1, 2, 3))
#define PHI(x) __builtin_bit_cast(bf16x8, __builtin_shufflevector(x, x, 4, 5, 6, 7))
#define RSUM(x) ls = __builtin_amdgcn_smfmac_f32_16x16x64_bf16(sel, __builtin_bit_cast(bf16x16_t, x), ls, sidx, 0, 0)
#define STEP(S, P, SN, PP, KN0, KN1, VP0, VP1, KL0, KL1, VL0, VL1, NP) do { \
    SN = MFMA32(KN0, q0, nm); SBAR(); KL0 = lds_b128(NP); EX(S, 0); EX(S, 1); EX(S, 2); EX(S, 3); PIN(S); SBAR(); \
    SN = MFMA32(KN1, q1, SN); SBAR(); KL1 = lds_b128((NP) + 1024); CV(P, 0, S, 0); CV(P, 1, S, 2); EX(S, 4); EX(S, 5); EX(S, 6); EX(S, 7); PIN(S); PIN(P); SBAR(); \
    o = MFMA32(VP0, PLO(PP), o); SBAR(); VL0 = lds_b128((NP) + 2048); CV(P, 2, S, 4); EX(S, 8); CV(P, 3, S, 6); EX(S, 9); EX(S, 10); EX(S, 11); PIN(S); PIN(P); SBAR(); \
    o = MFMA32(VP1, PHI(PP), o); SBAR(); VL1 = lds_b128((NP) + 3072); CV(P, 4, S, 8); CV(P, 5, S, 10); EX(S, 12); EX(S, 13); EX(S, 14); PIN(S); PIN(P); SBAR(); \
    RSUM(PP); SBAR(); EX(S, 15); CV(P, 6, S, 12); CV(P, 7, S, 14); PIN(P); SBAR(); \
  } while (0)
#define XP(on, st, j) do { if (LOADER && (on)) g2.template ffn_piece<j>(st); SBAR(); } while (0)
#define PRIO(n) do { __builtin_amdgcn_s_setprio(n); SBAR(); } while (0)
#define HEAD(cb, on, st) do { \
    PRIO(2); \
    STEP(sa, pa, sb, pb, ky0, ky1, vd0, vd1, kx0, kx1, vc0, vc1, (cb) + 2 * 4096); XP(on, st, 2); \
    STEP(sb, pb, sa, pa, kx0, kx1, va0, va1, ky0, ky1, vd0, vd1, (cb) + 3 * 4096); XP(on, st, 3); \
    PRIO(1); \
    STEP(sa, pa, sb, pb, ky0, ky1, vb0, vb1, kx0, kx1, va0, va1, (cb) + 4 * 4096); XP(on, st, 4); \
    STEP(sb, pb, sa, pa, kx0, kx1, vc0, vc1, ky0, ky1, vb0, vb1, (cb) + 5 * 4096); XP(on, st, 5); \
    STEP(sa, pa, sb, pb, ky0, ky1, vd0, vd1, kx0, kx1, vc0, vc1, (cb) + 6 * 4096); XP(on, st, 6); \
    PRIO(0); \
    STEP(sb, pb, sa, pa, kx0, kx1, va0, va1, ky0, ky1, vd0, vd1, (cb) + 7 * 4096); XP(on, st, 7); \
  } while (0)
#define TAIL(nb, on, st) do { \
    STEP(sa, pa, sb, pb, ky0, ky1, vb0, vb1, kx0, kx1, va0, va1, nb); XP(on, st, 0); \
    STEP(sb, pb, sa, pa, kx0, kx1, vc0, vc1, ky0, ky1, vb0, vb1, (nb) + 4096); XP(on, st, 1); \
  } while (0)
  HEAD(kvp, false, 0);
  if (LOADER) {
#pragma unroll 1
    for (int c = 0; c < 3; ++c) {
      asm volatile("s_waitcnt vmcnt(1) lgkmcnt(0)\n\ts_barrier" ::: "memory");
      if (c == 2 && lw == 0) peek_dev(flags + (FL_H + (mt >> 1)) * 32, lds0 + G1_CS);
      ld(c + 2, 0); ld(c + 2, 1);
      const int j = c * 4 + lw;
      l2_touch(j < 7 ? (const char*)(w2 + (size_t)(gk * 1792 + j * 256 + (mt & 7) * 32 + (l >> 1)) * DM + nt * 128 + (l & 1) * 64)
             : j < 9 ? (const char*)(wo + (size_t)((mt & 7) * 128 + (j - 7) * 64 + l) * DM + nt * 128 + gk * 64) : (const char*)wo, tdump);
      PRIO(3);
      const LAS char* nb = kvp + ((c + 1) & 1) * AT_BUF;
      TAIL(nb, false, 0); HEAD(nb, false, 0);
    }
    asm volatile("s_waitcnt vmcnt(1)" ::: "memory");
    if (lw == 0 && rfl(*(const LAS unsigned*)((const LAS char*)smem + G1_CS)) < 92u) wait_ge(flags + (FL_H + (mt >> 1)) * 32, 92u);
#pragma unroll 1
    for (int c = 3; c < 7; ++c) {
      asm volatile("s_waitcnt vmcnt(8) lgkmcnt(0)\n\ts_barrier" ::: "memory");
      if (c < 6) { ld(c + 2, 0); ld(c + 2, 1); }
      PRIO(3);
      const LAS char* nb = kvp + ((c + 1) & 1) * AT_BUF;
      TAIL(nb, true, c - 3); HEAD(nb, true, c - 3);
    }
  } else {
#pragma unroll 1
    for (int c = 0; c < 7; ++c) {
      asm volatile("s_waitcnt lgkmcnt(0)\n\ts_barrier" ::: "memory");
      if (c == 0 && late_pub) publish(late_pub, l);
      if (!(c & 1)) STAMP(2, 4 + (c >> 1));
      PRIO(3);
      const LAS char* nb = kvp + ((c + 1) & 1) * AT_BUF;
      TAIL(nb, false, 0); HEAD(nb, false, 0);
    }
  }
  TAIL(kvp, false, 0);
#undef HEAD
#undef TAIL
  o = MFMA32(vd0, PLO(pb), o); o = MFMA32(vd1, PHI(pb), o); RSUM(pb);
#undef EX
#undef CV
#undef PLO
#undef PHI
#undef RSUM
#undef STEP
#undef PRIO
#undef XP
  STAMP(2, 2);
  __builtin_amdgcn_s_setprio(0);
  attn_finish(ws, o, ls, q0, q1, l, ws + WS_O + (size_t)(head >> 1) * KT + (size_t)tt * 4096 + (head & 1) * 2048 + l * 16);
  STAMP(2, 3);
}

DI void g2_load(const u16* w2, const u16* wo, char* __restrict__ ws, unsigned lds0, const int lw, const int l) {
  const int xcd = blockIdx.x & 7, idx = blockIdx.x >> 3;
  const int mt = ((xcd >> 1) & 1) * 8 + (idx & 7);
  unsigned* flags = (unsigned*)(ws + WS_FLAG);
  G2Ld g; g.init(ws, w2, wo, lds0, lw, l);
  asm volatile("s_waitcnt vmcnt(10) lgkmcnt(0)\n\ts_barrier" ::: "memory");
  g.ffn(4); g.ffn(5);
  const LAS unsigned* peek = (const LAS unsigned*)(size_t)(lds0 + G2_PART);
#pragma unroll 1
  for (int i = 2; i < 24; ++i) {
    asm volatile("s_waitcnt vmcnt(16)\n\ts_barrier" ::: "memory");
    if (i == 6 && l == 0) __hip_atomic_fetch_add((gu32*)flags + (FL_O + (blockIdx.x >> 4)) * 32, 1u, RLX_AGENT);
    if (i == 20 && lw == 0) peek_dev(flags + (FL_O + mt) * 32, lds0 + G2_PART);
    g.ffn(i + 4);
  }
  if (lw == 0 && rfl(peek[0]) < 128u) wait_ge(flags + (FL_O + mt) * 32, 128u);
  asm volatile("s_waitcnt vmcnt(16)\n\ts_barrier" ::: "memory");
  g.wo(28);
  peek = (const LAS unsigned*)(size_t)(lds0 + G2_BOFF + 2048);
#pragma unroll 1
  for (int i = 25; i < 37; ++i) {
    asm volatile("s_waitcnt vmcnt(12)\n\ts_barrier" ::: "memory");
    if (i == 35 && lw == 0) peek_dev(flags + (FL_P + blockIdx.x) * 32, lds0 + G2_BOFF + 2048);
    g.wo(i + 4);
  }
  auto part = [&]() {
    const char* src = ws + WS_PART + (size_t)(blockIdx.x ^ 1) * 16384 + lw * 4096;
    const unsigned d = lds0 + G2_PART + lw * 4096;
    glds16_dev<0>(g.voff, src, d); glds16_dev<1024>(g.voff, src, d); glds16_dev<2048>(g.voff, src, d); glds16_dev<3072>(g.voff, src, d);
  };
  asm volatile("s_waitcnt vmcnt(12)\n\ts_barrier" ::: "memory");
  const bool early = rfl(peek[0]) >= 4u;
  g.wo(41);
  if (early) part();
#pragma unroll 1
  for (int i = 38; i < 41; ++i) {
    if (early) asm volatile("s_waitcnt vmcnt(16)\n\ts_barrier" ::: "memory");
    else asm volatile("s_waitcnt vmcnt(12)\n\ts_barrier" ::: "memory");
    if (i < 40) g.wo(i + 4);
  }
  if (!early && lw == 0) wait_ge(flags + (FL_P + blockIdx.x) * 32, 4u);
  asm volatile("s_waitcnt vmcnt(6)\n\ts_barrier" ::: "memory");
  if (!early) part();
  asm volatile("s_waitcnt vmcnt(0)\n\ts_barrier" ::: "memory");
}

DI void g2_compute(const u16* __restrict__ x, const u16* __restrict__ b2, char* __restrict__ ws, u16* __restrict__ out,
                   char* smem, const int w, const int l) {
  const int xcd = blockIdx.x & 7, idx = blockIdx.x >> 3;
  const int kh = xcd & 1, mt = ((xcd >> 1) & 1) * 8 + (idx & 7), nt = (xcd >> 2) * 4 + (idx >> 3);
  unsigned* flags = (unsigned*)(ws + WS_FLAG);
  STAMP(3, 0);
  const int g = l >> 4, c16 = l & 15, wm = w & 1, wn = w >> 1;
  f32x4 accg[4][2] = {}, accp[4][2] = {}, accs[4][2] = {};
  const int col0 = nt * 128 + kh * 64 + wn * 32;
  const int row0 = mt * 128 + wm * 64 + c16;
  const int colx = col0 + (g & 1) * 16 + (g >> 1) * 8;
  unsigned stw[4];
  u32x4 xv[4];
  u32x2 bv[2];
#pragma unroll
  for (int m = 0; m < 4; ++m) stw[m] = __hip_atomic_load((gu32*)(ws + WS_STAT) + row0 + m * 16, RLX_AGENT);
#pragma unroll
  for (int m = 0; m < 4; ++m) xv[m] = *(const u32x4*)(x + (size_t)(row0 + m * 16) * DM + colx);
#pragma unroll
  for (int t = 0; t < 2; ++t) bv[t] = *(const u32x2*)(b2 + col0 + 16 * t + 4 * g);
  const LAS char* lbase = (const LAS char*)smem;
  const int ah = wm * 8192 + l * 16;
  const int ao = wm * 8192 + (g >> 1) * 1024 + ((g & 1) * 32 + c16) * 16;
  int bF[2][2], bW[2];
#pragma unroll
  for (int t = 0; t < 2; ++t) {
    const int e = ((t ^ g) & 1) * 32 + (l & 3) * 8;
    bF[0][t] = G2_BOFF + (kh * 2 + wn) * 1024 + (4 * g + (c16 >> 2)) * 64 + e;
    bF[1][t] = G2_BOFF + ((kh ^ 1) * 2 + wn) * 1024 + (4 * g + (c16 >> 2)) * 64 + e;
    bW[t] = G2_BOFF + wn * 1024 + (g >> 1) * 4096 + (4 * (g & 1) + (c16 >> 2)) * 64 + e;
  }
  bf16x8 fa0[4], fb0[4], fa1[4], fb1[4];
#define F_RD(FA, FB, base, k) do { \
    _Pragma("unroll") for (int j_ = 0; j_ < 4; ++j_) { \
      FB[j_] = cat8(lds_tr((base) + bF[j_ >> 1][j_ & 1] + (k) * 8192), lds_tr((base) + bF[j_ >> 1][j_ & 1] + (k) * 8192 + 4096)); \
      FA[j_] = lds_b128((base) + ah + j_ * 2048 + (k) * 1024); } \
  } while (0)
#define F_MM(FA, FB) do { \
    _Pragma("unroll") for (int m_ = 0; m_ < 4; ++m_) { \
      accg[m_][0] = MFMA16(FB[0], FA[m_], accg[m_][0]); accg[m_][1] = MFMA16(FB[1], FA[m_], accg[m_][1]); \
      accp[m_][0] = MFMA16(FB[2], FA[m_], accp[m_][0]); accp[m_][1] = MFMA16(FB[3], FA[m_], accp[m_][1]); } \
  } while (0)
#define W_RD(FA, FB, base, k) do { \
    _Pragma("unroll") for (int j_ = 0; j_ < 2; ++j_) FB[j_] = cat8(lds_tr((base) + bW[j_] + (k) * 8192), lds_tr((base) + bW[j_] + (k) * 8192 + 512)); \
    _Pragma("unroll") for (int j_ = 0; j_ < 4; ++j_) FA[j_] = lds_b128((base) + ao + (j_ >> 1) * 4096 + (j_ & 1) * 256 + (k) * 2048); \
  } while (0)
#define W_MM(FA, FB) do { \
    _Pragma("unroll") for (int m_ = 0; m_ < 4; ++m_) { accs[m_][0] = MFMA16(FB[0], FA[m_], accs[m_][0]); accs[m_][1] = MFMA16(FB[1], FA[m_], accs[m_][1]); } \
  } while (0)
#define G2_SCHED(NG, NR, NM) do { \
    _Pragma("unroll") for (int q_ = 0; q_ < (NG); ++q_) { \
      __builtin_amdgcn_sched_group_barrier(0x008, 1, 0); __builtin_amdgcn_sched_group_barrier(0x100, NR, 0); } \
    __builtin_amdgcn_sched_group_barrier(0x008, (NM) - (NG), 0); SBAR(); \
  } while (0)
#define G2_BAR() do { asm volatile("s_waitcnt lgkmcnt(0)\n\ts_barrier" ::: "memory"); SBAR(); } while (0)
#define F_BODY(nb_) do { \
    F_RD(fa0, fb0, nb_, 0); F_MM(fa1, fb1); G2_SCHED(FNG, FNR, 16); \
    F_RD(fa1, fb1, nb_, 1); F_MM(fa0, fb0); G2_SCHED(FNG, FNR, 16); } while (0)
#define FNG 12
#define FNR 1

  SBAR();
  STAMP(3, 1);
  F_RD(fa0, fb0, lbase, 0); SBAR();
  F_RD(fa1, fb1, lbase, 1); F_MM(fa0, fb0); G2_SCHED(FNG, FNR, 16);
  const LAS char* lhi = lbase + 2 * G2_SLOT;
  F_BODY(lbase + G2_SLOT);
  G2_BAR(); F_BODY(lhi);
  G2_BAR(); F_BODY(lhi + G2_SLOT);
  publish(flags + (FL_O + (blockIdx.x >> 4)) * 32, l); SBAR();
#pragma unroll 1
  for (int i = 0; i < 6; ++i) {
    G2_BAR(); F_BODY(lbase);
    G2_BAR(); F_BODY(lbase + G2_SLOT);
    G2_BAR(); F_BODY(lhi);
    G2_BAR(); F_BODY(lhi + G2_SLOT);
  }
  STAMP(3, 4);
#define W_STEP(i) do { const LAS char* nb_ = lbase + (((i) + 1) & 3) * G2_SLOT; G2_BAR(); \
    W_RD(fa0, fb0, nb_, 0); W_MM(fa1, fb1); G2_SCHED(4, 2, 8); \
    W_RD(fa1, fb1, nb_, 1); W_MM(fa0, fb0); G2_SCHED(4, 2, 8); } while (0)
  char* pdst = ws + WS_PART + (size_t)blockIdx.x * 16384 + w * 4096 + l * 16;
#define G2_SEND(m) do { store16_dev(pdst + (m) * 1024, (u32x4){cvtpk(accp[m][0][0], accp[m][0][1]), cvtpk(accp[m][0][2], accp[m][0][3]), \
                                                               cvtpk(accp[m][1][0], accp[m][1][1]), cvtpk(accp[m][1][2], accp[m][1][3])}); SBAR(); } while (0)
  G2_BAR();
  W_RD(fa0, fb0, lbase, 0); F_MM(fa1, fb1); G2_SCHED(8, 1, 16);
  W_RD(fa1, fb1, lbase, 1); W_MM(fa0, fb0); G2_SCHED(4, 2, 8);
  G2_SEND(0); G2_SEND(1);
  STAMP(3, 5);
  W_STEP(28); G2_SEND(2); G2_SEND(3);
  W_STEP(29); W_STEP(30); W_STEP(31); W_STEP(32);
  publish(flags + (FL_P + (blockIdx.x ^ 1)) * 32, l); SBAR();
  W_STEP(33);
#pragma unroll 1
  for (int i = 0; i < 2; ++i) { W_STEP(34); W_STEP(35); W_STEP(36); W_STEP(37); }
  STAMP(3, 6);
  W_STEP(42);
  W_MM(fa1, fb1);
  STAMP(3, 2);
  {
    const LAS char* xp = (const LAS char*)smem + G2_PART + w * 4096 + l * 16;
#pragma unroll
    for (int m = 0; m < 4; ++m) {
      const float den = bf_hi(stw[m]), rd2 = 1.0f / (den * den);
      const u32x4 tp = *(const LAS u32x4*)(xp + m * 1024);
      u32x4 xx = xv[m];
      { auto r0 = __builtin_amdgcn_permlane16_swap(xx[0], xx[2], false, false); xx[0] = r0[0]; xx[2] = r0[1];
        auto r1 = __builtin_amdgcn_permlane16_swap(xx[1], xx[3], false, false); xx[1] = r1[0]; xx[3] = r1[1]; }
      float r[8];
#pragma unroll
      for (int e = 0; e < 8; ++e) {
        const unsigned xw_ = xx[e >> 1], bw_ = bv[e >> 2][(e >> 1) & 1], pw_ = tp[e >> 1];
        const float z = (accg[m][e >> 2][e & 3] + ((e & 1) ? bf_hi(pw_) : bf_lo(pw_))) * rd2 + ((e & 1) ? bf_hi(bw_) : bf_lo(bw_));
        const float sg = z * __builtin_amdgcn_rcpf(1.0f + __builtin_amdgcn_exp2f(-1.4426950408889634f * z));
        r[e] = ((e & 1) ? bf_hi(xw_) : bf_lo(xw_)) + accs[m][e >> 2][e & 3] * sg;
      }
      auto r0 = __builtin_amdgcn_permlane16_swap(cvtpk(r[0], r[1]), cvtpk(r[4], r[5]), false, false);
      auto r1 = __builtin_amdgcn_permlane16_swap(cvtpk(r[2], r[3]), cvtpk(r[6], r[7]), false, false);
      *(u32x4*)(out + (size_t)(row0 + m * 16) * DM + colx) = (u32x4){r0[0], r1[0], r0[1], r1[1]};
    }
  }
  STAMP(3, 3);
#undef F_RD
#undef F_MM
#undef F_BODY
#undef G2_SCHED
#undef G2_BAR
#undef G2_SEND
#undef W_RD
#undef W_MM
#undef W_STEP
}

__global__ __launch_bounds__(512, 2) void main_kernel(const u16* __restrict__ x, const u16* __restrict__ wq, const u16* __restrict__ wk,
                                                      const u16* __restrict__ wv, const u16* __restrict__ wo, const u16* __restrict__ w1,
                                                      const u16* __restrict__ b1, const u16* __restrict__ w2, const u16* __restrict__ b2,
                                                      char* __restrict__ ws, u16* __restrict__ out) {
  extern __shared__ __attribute__((aligned(16))) char smem[];
  const int l = threadIdx.x & 63, w = rfl(threadIdx.x >> 6);
  const unsigned lds0 = (unsigned)(size_t)(LAS char*)smem;
  const int xcd = blockIdx.x & 7, idx = blockIdx.x >> 3;
  const int mtile = (xcd & 1) * 4 + (idx & 3), cg = (idx >> 2) * 4 + (xcd >> 1);
  unsigned* flags = (unsigned*)(ws + WS_FLAG);
  if (w == 3) {
    unsigned long long pc; unsigned len;
    asm volatile("s_getpc_b64 %0\n.Lmk_code_begin:\n\ts_mov_b32 %1, .Lmk_code_end-.Lmk_code_begin" : "=s"(pc), "=s"(len));
    unsigned off = (unsigned)((l & 31) * 32 + idx) * 128u;
    off = (off < len && len <= 131072u) ? off : 0u;
    l2_touch((const char*)pc + off, lds0 + G1_HS + 3072);
  }
  if (w >= 4) {
    if (cg > 8) g1_load<G1_FFN>(ws, x, w1, nullptr, b1, cg, mtile, smem, w - 4, l);
    else if (cg < 8) g1_load<G1_Q>(ws, x, wq, nullptr, b1, cg, mtile, smem, w - 4, l);
    else g1_load<G1_KV>(ws, x, wk, wv, b1, cg, mtile, smem, w - 4, l);
    attn_phase<true>(ws, w2, wo, smem, w, l, nullptr);
    g2_load(w2, wo, ws, lds0, w - 4, l);
    asm volatile(".Lmk_code_end:");
  } else {
    unsigned* late = nullptr;
    if (cg > 8) { g1_compute<G1_FFN>(ws, x, cg, mtile, smem, w, l); late = flags + (FL_H + mtile) * 32; }
    else if (cg < 8) { g1_compute<G1_Q>(ws, x, cg, mtile, smem, w, l); publish(flags + (FL_Q + mtile * 8 + cg) * 32, l); }
    else { g1_compute<G1_KV>(ws, x, cg, mtile, smem, w, l); publish(flags + FL_KV * 32, l); }
    attn_phase<false>(ws, w2, wo, smem, w, l, late);
    g2_compute(x, b2, ws, out, smem, w, l);
  }
}

extern "C" void kernel_launch(void* const* d_in, const int* in_sizes, int n_in, void* d_out, int out_size, void* d_ws,
                              size_t ws_size, hipStream_t stream) {
  const u16* x = (const u16*)d_in[0];
  const u16* wq = (const u16*)d_in[1];
  const u16* wk = (const u16*)d_in[2];
  const u16* wv = (const u16*)d_in[3];
  const u16* wo = (const u16*)d_in[4];
  const u16* w1 = (const u16*)d_in[6];
  const u16* b1 = (const u16*)d_in[7];
  const u16* w2 = (const u16*)d_in[8];
  const u16* b2 = (const u16*)d_in[9];
  char* ws = (char*)d_ws;
  (void)hipFuncSetAttribute((const void*)main_kernel, hipFuncAttributeMaxDynamicSharedMemorySize, MAIN_LDS);
  main_kernel<<<256, 512, MAIN_LDS, stream>>>(x, wq, wk, wv, wo, w1, b1, w2, b2, ws, (u16*)d_out);
}
```

```cpp
#include <hip/hip_runtime.h>
#include <hip/hip_bf16.h>

typedef short bf16x8 __attribute__((ext_vector_type(8)));
typedef short s16x4 __attribute__((ext_vector_type(4)));
typedef float f32x16 __attribute__((ext_vector_type(16)));
typedef float f32x4 __attribute__((ext_vector_type(4)));
typedef float f32x2 __attribute__((ext_vector_type(2)));
typedef __bf16 bf16x2_t __attribute__((ext_vector_type(2)));
typedef unsigned u32x4 __attribute__((ext_vector_type(4)));
typedef unsigned u32x2 __attribute__((ext_vector_type(2)));
typedef unsigned u32x8 __attribute__((ext_vector_type(8)));
typedef __bf16 bf16x8_t __attribute__((ext_vector_type(8)));
typedef __bf16 bf16x16_t __attribute__((ext_vector_type(16)));
typedef unsigned short u16;
#define LAS __attribute__((address_space(3)))
#define DI __device__ __forceinline__
#define SBAR() __builtin_amdgcn_sched_barrier(0)
#define PIN(x) asm volatile("" : "+v"(x))
#define MFMA16(a, b, c) __builtin_amdgcn_mfma_f32_16x16x32_bf16((a), (b), (c), 0, 0, 0)
#define MFMA32(a, b, c) __builtin_amdgcn_mfma_f32_32x32x16_bf16((a), (b), (c), 0, 0, 0)

constexpr int DM = 1024, FF = 3584;
constexpr size_t KT = 262144;
constexpr size_t WS_STAT = 0;
constexpr size_t WS_Q = 4u << 20;
constexpr size_t WS_KV = 8u << 20;
constexpr size_t WS_O = 9u << 20;
constexpr size_t WS_H = 13u << 20;
constexpr size_t WS_PART = 28u << 20;
constexpr size_t WS_FLAG = 37u << 20;
constexpr int FL_P = 0, FL_O = 256, FL_H = 272, FL_Q = 280, FL_KV = 344, FL_S = 345, FL_N = 353;
constexpr size_t WS_HSA = WS_FLAG + 65536, WS_HSB = WS_HSA + 4096, WS_HSC = WS_HSB + 4096;
constexpr int COORD_BLOCK = 64;

DI unsigned rfl(unsigned x) { return __builtin_amdgcn_readfirstlane(x); }
DI unsigned cvtpk(float lo, float hi) {
  f32x2 v = {lo, hi};
  bf16x2_t b = __builtin_convertvector(v, bf16x2_t);
  return __builtin_bit_cast(unsigned, b);
}
DI float bf_lo(unsigned u) { return __uint_as_float(u << 16); }
DI float bf_hi(unsigned u) { return __uint_as_float(u & 0xffff0000u); }
DI float bf_round(float x) { return bf_lo(cvtpk(x, x)); }

template <int IMM>
DI void glds16(unsigned voff, const void* sbase, unsigned ldsaddr) {
  asm volatile("s_mov_b32 m0, %2\n\ts_nop 0\n\tglobal_load_lds_dwordx4 %0, %1 offset:%3"
               :: "v"(voff), "s"(sbase), "s"(ldsaddr), "i"(IMM) : "memory");
}
template <int IMM>
DI void glds16_dev(unsigned voff, const void* sbase, unsigned ldsaddr) {
  asm volatile("s_mov_b32 m0, %2\n\ts_nop 0\n\tglobal_load_lds_dwordx4 %0, %1 offset:%3 sc1"
               :: "v"(voff), "s"(sbase), "s"(ldsaddr), "i"(IMM) : "memory");
}
DI void set_m0(unsigned ldsaddr) { asm volatile("s_mov_b32 m0, %0\n\ts_nop 0" :: "s"(ldsaddr) : "memory"); }
template <int IMM> DI void gl16(unsigned voff, const void* sbase) {
  asm volatile("global_load_lds_dwordx4 %0, %1 offset:%2" :: "v"(voff), "s"(sbase), "i"(IMM) : "memory");
}
template <int IMM> DI void gl16_dev(unsigned voff, const void* sbase) {
  asm volatile("global_load_lds_dwordx4 %0, %1 offset:%2 sc1" :: "v"(voff), "s"(sbase), "i"(IMM) : "memory");
}
DI void peek_dev(const void* word, unsigned ldsaddr) {
  asm volatile("s_mov_b32 m0, %2\n\ts_nop 0\n\tglobal_load_lds_dword %0, %1 sc1" :: "v"(0u), "s"(word), "s"(ldsaddr) : "memory");
}
DI void store16_dev(void* p, u32x4 v) {
  asm volatile("global_store_dwordx4 %0, %1, off sc1\n\ts_nop 1" :: "v"(p), "v"(v) : "memory");
}
DI void l2_touch(const void* p, unsigned ldsaddr) {
  asm volatile("s_mov_b32 m0, %1\n\ts_nop 0\n\tglobal_load_lds_dword %0, off" :: "v"(p), "s"(ldsaddr) : "memory");
}
constexpr unsigned SPIN_CAP = 1u << 20;
typedef __attribute__((address_space(1))) unsigned gu32;
#define RLX_AGENT __ATOMIC_RELAXED, __HIP_MEMORY_SCOPE_AGENT
DI void wait_ge(const unsigned* word, unsigned want) {
#pragma unroll 1
  for (unsigned spins = 0; spins < SPIN_CAP; ++spins) {
    if (rfl(__hip_atomic_load((gu32*)word, RLX_AGENT)) >= want) break;
    __builtin_amdgcn_s_sleep(8);
  }
  __builtin_amdgcn_fence(__ATOMIC_ACQUIRE, "wavefront");
}
DI void publish(unsigned* word, int l) {
  asm volatile("s_waitcnt vmcnt(0)" ::: "memory");
  if (l == 0) __hip_atomic_fetch_add((gu32*)word, 1u, RLX_AGENT);
}
DI bf16x8 load16_dev(const char* p) {
  typedef __attribute__((address_space(1))) unsigned long long gu64;
  const unsigned long long a = __hip_atomic_load((gu64*)p, RLX_AGENT), b = __hip_atomic_load((gu64*)p + 1, RLX_AGENT);
  return __builtin_bit_cast(bf16x8, (u32x4){(unsigned)a, (unsigned)(a >> 32), (unsigned)b, (unsigned)(b >> 32)});
}
DI float relu_s(float a) { return __builtin_amdgcn_fmed3f(a, 0.0f, 3.4028234663852886e38f); }
DI bf16x8 lds_b128(const LAS char* p) { return *(const LAS bf16x8*)p; }
DI s16x4 lds_tr(const LAS char* p) {
  return __builtin_amdgcn_ds_read_tr16_b64_v4i16((LAS s16x4*)p);
}
DI bf16x8 cat8(s16x4 lo, s16x4 hi) { return __builtin_shufflevector(lo, hi, 0, 1, 2, 3, 4, 5, 6, 7); }

DI float half_sum(float v) {
  v += __uint_as_float(__builtin_amdgcn_update_dpp(0u, __float_as_uint(v), 0xB1, 0xf, 0xf, true));
  v += __uint_as_float(__builtin_amdgcn_update_dpp(0u, __float_as_uint(v), 0x4E, 0xf, 0xf, true));
  v += __uint_as_float(__builtin_amdgcn_update_dpp(0u, __float_as_uint(v), 0x141, 0xf, 0xf, true));
  v += __uint_as_float(__builtin_amdgcn_update_dpp(0u, __float_as_uint(v), 0x140, 0xf, 0xf, true));
  auto r = __builtin_amdgcn_permlane16_swap(__float_as_uint(v), __float_as_uint(v), false, false);
  return __uint_as_float(r[0]) + __uint_as_float(r[1]);
}
DI unsigned row_stats(const u32x4 (&v)[4]) {
  f32x2 e[16], s2 = {0.f, 0.f};
#pragma unroll
  for (int i = 0; i < 16; ++i) { e[i] = (f32x2){bf_lo(v[i >> 2][i & 3]), bf_hi(v[i >> 2][i & 3])}; s2 += e[i]; }
  const float mean = half_sum(s2[0] + s2[1]) * (1.0f / DM);
  f32x2 q2 = {0.f, 0.f};
#pragma unroll
  for (int i = 0; i < 16; ++i) { const f32x2 d = e[i] - mean; q2 += d * d; }
  const float q = half_sum(q2[0] + q2[1]);
  const float den = __builtin_sqrtf(bf_round(bf_round(q * (1.0f / DM)) + 1e-5f));
  return cvtpk(-mean, den);
}

constexpr int G1_STAGE = 53248, G1_BOFF = 32768;
enum { G1_FFN = 5, G1_Q = 4, G1_KV = 2 };

constexpr int G1_ST = 87040;
constexpr int G1_CF = 53248;
constexpr int AT_RDY = 89088;
constexpr int G1_HS = 159744;
constexpr int G1_CS = 81920;
constexpr int ESTART = 3, PSTART = 9;

DI float xhalf_max(float m) {
  auto rr = __builtin_amdgcn_permlane32_swap(__float_as_uint(m), __float_as_uint(m), false, false);
  return __builtin_fmaxf(__uint_as_float(rr[0]), __uint_as_float(rr[1]));
}
DI float xhalf_sum(float m) {
  auto rr = __builtin_amdgcn_permlane32_swap(__float_as_uint(m), __float_as_uint(m), false, false);
  return __uint_as_float(rr[0]) + __uint_as_float(rr[1]);
}
template <int NP>
DI void g1_load(char* ws, const u16* x, const u16* wsrc, const u16* wv, const u16* b1, int cg, int mtile, char* smem, int lw, int l) {
  constexpr int LD = (NP == G1_FFN) ? FF : (NP == G1_Q) ? DM : 32;
  const unsigned lds0 = (unsigned)(size_t)(LAS char*)smem;
  const char* abase = (const char*)(x + (size_t)(mtile * 256 + lw * 64) * DM);
  const unsigned ave = (unsigned)((l >> 3) * 2048 + (((l & 7) ^ (l >> 4)) << 4)), avo = ave ^ 64u;
  const unsigned bvoff = (unsigned)((lw * 16 + (l >> 2)) * LD * 2 + (((l & 3) ^ ((l >> 5) << 1)) << 4));
  const int krot = NP == G1_Q ? (mtile & 3) * 4 : 0;
  unsigned va[8], vb[NP];
#pragma unroll
  for (int i = 0; i < 8; ++i) va[i] = ((i & 1) ? avo : ave) + i * 16384 - (i - 4) * 1024;
#pragma unroll
  for (int p = 0; p < NP; ++p) {
    int cb = (NP == G1_FFN) ? (cg - 9) * 5 + p : (NP == G1_Q) ? cg * 4 + p : 0;
    cb = cb > 111 ? 111 : cb;
    vb[p] = bvoff + cb * 64 - (p - 2) * 1024 + 4096;
  }
  unsigned bias[NP];
#pragma unroll
  for (int p = 0; p < NP; ++p) {
    int cb = (cg - 9) * 5 + p; cb = cb > 111 ? 111 : cb;
    bias[p] = (NP == G1_FFN) ? (unsigned)b1[cb * 32 + (l & 31)] : 0u;
  }
  float cs[NP];
#pragma unroll
  for (int p = 0; p < NP; ++p) cs[p] = 0.f;
  const LAS char* cb0 = (const LAS char*)smem + G1_BOFF + lw * (NP * 1024) + (8 * (l >> 5) + ((l & 15) >> 2)) * 64 + (((l >> 4) ^ (l >> 5)) & 1) * 32 + (l & 3) * 8;
  u32x2 t0[NP], t1[NP];
  auto cs_read = [&](int slot, int p) {
    t0[p] = __builtin_bit_cast(u32x2, lds_tr(cb0 + slot * G1_STAGE + p * 1024));
    t1[p] = __builtin_bit_cast(u32x2, lds_tr(cb0 + slot * G1_STAGE + p * 1024 + 256));
    SBAR();
  };
  auto cs_add = [&](int p) {
    float a, b, c, d;
    asm volatile("v_lshlrev_b32 %1, 16, %5\n\tv_and_b32 %2, 0xffff0000, %5\n\tv_lshlrev_b32 %3, 16, %6\n\tv_and_b32 %4, 0xffff0000, %6\n\t"
                 "v_add_f32 %1, %1, %2\n\tv_add_f32 %3, %3, %4\n\t"
                 "v_lshlrev_b32 %2, 16, %7\n\tv_and_b32 %4, 0xffff0000, %7\n\tv_add_f32 %1, %1, %2\n\tv_add_f32 %3, %3, %4\n\t"
                 "v_lshlrev_b32 %2, 16, %8\n\tv_and_b32 %4, 0xffff0000, %8\n\tv_add_f32 %1, %1, %2\n\tv_add_f32 %3, %3, %4\n\t"
                 "v_add_f32 %1, %1, %3\n\tv_add_f32 %0, %0, %1"
                 : "+v"(cs[p]), "=&v"(a), "=&v"(b), "=&v"(c), "=&v"(d) : "v"(t0[p][0]), "v"(t0[p][1]), "v"(t1[p][0]), "v"(t1[p][1]));
    SBAR();
  };
  auto issue = [&](int st, int slot, bool sum, int sslot) {
    const int kt = (st + krot) & 15;
    const char* b0 = (const char*)wsrc + (size_t)kt * (64 * LD * 2) - 4096;
    set_m0(lds0 + slot * G1_STAGE + G1_BOFF + lw * (NP * 1024) + 2048);
    gl16<-2048>(vb[0], b0);
    gl16<-1024>(vb[1], NP == G1_KV ? (const char*)wv + (size_t)kt * (64 * LD * 2) - 4096 : b0);
    if (NP > 2) { gl16<0>(vb[2 % NP], b0); gl16<1024>(vb[3 % NP], b0); }
    if (NP > 4) gl16<2048>(vb[4 % NP], b0);
    const char* a0 = abase + kt * 128;
    set_m0(lds0 + slot * G1_STAGE + lw * 8192 + 4096);
#define AL(i) do { gl16<((i) - 4) * 1024>(va[i], a0); \
      if (sum && (i) < NP) cs_read(sslot, (i) % NP); if (sum && (i) >= 1 && (i) <= NP) cs_add(((i) + NP - 1) % NP); } while (0)
    AL(0); AL(1); AL(2); AL(3); AL(4); AL(5); AL(6); AL(7);
#undef AL
  };
  auto colsum = [&](int slot) {
#pragma unroll
    for (int p = 0; p < NP; ++p) cs_read(slot, p);
#pragma unroll
    for (int p = 0; p < NP; ++p) cs_add(p);
  };
  unsigned* flags = (unsigned*)(ws + WS_FLAG);
  const bool coord = (NP == G1_KV) && blockIdx.x == COORD_BLOCK && lw == 1;
  if (coord) {
#pragma unroll
    for (int i = 0; i < 6; ++i) if (i * 64 + l < FL_N) __hip_atomic_store((gu32*)flags + (i * 64 + l) * 32, 0u, RLX_AGENT);
    store16_dev(ws + WS_HSC + l * 16, (u32x4){0u, 0u, 0u, 0u});
    asm volatile("s_waitcnt vmcnt(0)" ::: "memory");
  }
  issue(0, 0, false, 0); issue(1, 1, false, 0); issue(2, 2, false, 0);
  asm volatile("s_waitcnt vmcnt(%0)\n\ts_barrier" :: "n"(2 * (8 + NP)) : "memory");
  __builtin_amdgcn_s_setprio(2);
  int state = (lw == 0 || coord) ? 0 : 2;
  bool pend = false, first = true;
  u32x4 last = {0u, 0u, 0u, 0u};
  const unsigned found = rfl(*(const LAS unsigned*)((const LAS char*)smem + G1_HS));
  if (lw == 0 && l < 9) {
    const unsigned v = *(const LAS unsigned*)((const LAS char*)smem + G1_HS + 16 + (l & 7) * 4);
    __hip_atomic_store(l < 8 ? (gu32*)(ws + WS_STAT) + mtile * 256 + cg * 8 + l : (gu32*)(ws + WS_HSA) + blockIdx.x, l < 8 ? v : found + 1u, RLX_AGENT);
  }
  colsum(0);
  auto poll_issue = [&]() {
    if (NP == G1_KV && coord) {
      glds16_dev<0>(l * 16, ws + WS_HSA, lds0 + G1_HS + 1024); glds16_dev<0>(l * 16, ws + WS_HSC, lds0 + G1_HS + 2048);
    } else {
      const char* a_ = state == 0 ? ws + WS_HSB + blockIdx.x * 4 : (const char*)(flags + (FL_S + mtile) * 32);
      peek_dev(a_, lds0 + G1_HS + 256);
    }
  };
  auto poll_check = [&]() {
    if (NP == G1_KV && coord) {
      const u32x4 pa = *(const LAS u32x4*)((const LAS char*)smem + G1_HS + 1024 + l * 16), pc = *(const LAS u32x4*)((const LAS char*)smem + G1_HS + 2048 + l * 16);
      if (first || pa[0] != last[0] || pa[1] != last[1] || pa[2] != last[2] || pa[3] != last[3]) store16_dev(ws + WS_HSB + l * 16, pa);
      last = pa; first = false;
      if (__all((pc[0] & pc[1] & pc[2] & pc[3]) == 1u)) state = 2;
    } else {
      const unsigned v = rfl(*(const LAS unsigned*)((const LAS char*)smem + G1_HS + 256));
      if (state == 0) {
        if (v != found) {
          if (l == 0) {
            __hip_atomic_store((gu32*)(ws + WS_HSC) + blockIdx.x, 1u, RLX_AGENT);
            __hip_atomic_fetch_add((gu32*)flags + (FL_S + mtile) * 32, 1u, RLX_AGENT);
          }
          state = 1;
        }
      } else if (v >= 32u) state = 2;
    }
    state = rfl(state);
  };
  int slot = 0;
#pragma unroll 1
  for (int i = 0; i < 13; ++i) {
    asm volatile("s_waitcnt vmcnt(%0)\n\ts_barrier" :: "n"(8 + NP) : "memory");
    if (state < 2 && (i >= ESTART || coord)) {
      if (pend) poll_check();
      pend = state == 0 || (state == 1 && i >= PSTART);
      if (pend) poll_issue();
    }
    const int nslot = (slot == 2) ? 0 : slot + 1;
    issue(i + 3, slot, true, nslot);
    slot = nslot;
  }
#pragma unroll 1
  for (unsigned spins = 0; state < 2 && spins < SPIN_CAP; ++spins) {
    if (!pend) poll_issue();
    asm volatile("s_waitcnt vmcnt(0)" ::: "memory");
    poll_check();
    pend = false;
    __builtin_amdgcn_s_sleep(8);
  }
  asm volatile("s_waitcnt vmcnt(%0)\n\ts_barrier" :: "n"(8 + NP) : "memory");
  if (lw == 0) { glds16_dev<0>(l * 16, ws + WS_STAT + mtile * 1024, lds0 + G1_ST); *(LAS u32x2*)((LAS char*)smem + AT_RDY) = (u32x2){0u, 0u}; }
  colsum(2);
  asm volatile("s_waitcnt vmcnt(0)" ::: "memory");
  colsum(0);
#pragma unroll
  for (int p = 0; p < NP; ++p) {
    const float c = xhalf_sum(cs[p]);
    const unsigned c0 = cvtpk(c, c) & 0xffffu; const float r1 = c - bf_lo(c0);
    const unsigned c1 = cvtpk(r1, r1) & 0xffffu; const float r2 = r1 - bf_lo(c1);
    const unsigned c2 = cvtpk(r2, r2) & 0xffffu;
    if (l < 32) *(LAS u32x4*)((LAS char*)smem + G1_CF + (p * 2 + (l >> 4)) * 1024 + (lw * 16 + (l & 15)) * 16) = (u32x4){c0 | (c1 << 16), c2 | (lw ? 0u : bias[p] << 16), 0u, 0u};
  }
  asm volatile("s_waitcnt lgkmcnt(0)\n\ts_barrier" ::: "memory");
}

template <int NP>
DI void g1_compute(char* __restrict__ ws, const u16* __restrict__ x, int cg, int mtile, char* smem, int w, int l) {
  int g = l >> 4, c16 = l & 15;
  {
    const int row = mtile * 256 + cg * 8 + w * 2 + (l >> 5);
    u32x4 v[4];
#pragma unroll
    for (int i = 0; i < 4; ++i) v[i] = *(const u32x4*)(x + (size_t)row * DM + (i * 32 + (l & 31)) * 8);
    const unsigned found = __hip_atomic_load((gu32*)(ws + WS_HSB) + blockIdx.x, RLX_AGENT);
    SBAR();
    const unsigned rs = row_stats(v);
    if ((l & 31) == 0) *(LAS unsigned*)((LAS char*)smem + G1_HS + 16 + (w * 2 + (l >> 5)) * 4) = rs;
    if (w == 0 && l == 0) *(LAS unsigned*)((LAS char*)smem + G1_HS) = found;
  }
  f32x4 acc[4][NP][2];
#pragma unroll
  for (int m = 0; m < 4; ++m)
#pragma unroll
    for (int p = 0; p < NP; ++p) { acc[m][p][0] = f32x4{}; acc[m][p][1] = f32x4{}; }
  const LAS char* lbase = (const LAS char*)smem;
  int aoff[2], boff[2];
#pragma unroll
  for (int k = 0; k < 2; ++k) {
    aoff[k] = (w * 64 + c16) * 128 + (((4 * k + g) ^ (c16 >> 1)) << 4);
    boff[k] = G1_BOFF + (g >> 1) * (NP * 1024) + (8 * (g & 1) + (c16 >> 2)) * 64 + ((k ^ g) & 1) * 32 + (l & 3) * 8;
  }
  bf16x8 fa0[4], fa1[4], fb0[NP], fb1[NP];
#define G1_RA(FA, base, k) do { _Pragma("unroll") for (int m_ = 0; m_ < 4; ++m_) FA[m_] = lds_b128((base) + aoff[k] + m_ * 2048); } while (0)
#define G1_RB(FB, base, k, t) do { _Pragma("unroll") for (int p_ = 0; p_ < NP; ++p_) \
      FB[p_] = cat8(lds_tr((base) + boff[t] + p_ * 1024 + (k) * (NP * 2048)), lds_tr((base) + boff[t] + p_ * 1024 + (k) * (NP * 2048) + 256)); } while (0)
#define G1_MM(FA, FB, t) do { \
    _Pragma("unroll") for (int m_ = 0; m_ < 4; ++m_) _Pragma("unroll") for (int p_ = 0; p_ < NP; ++p_) { \
      if (NP == G1_KV && p_ == 1) acc[m_][p_][t] = MFMA16(FA[m_], FB[p_], acc[m_][p_][t]); \
      else acc[m_][p_][t] = MFMA16(FB[p_], FA[m_], acc[m_][p_][t]); } \
  } while (0)
#define G1_SCHED(NR) do { \
    _Pragma("unroll") for (int q_ = 0; q_ < (NR); ++q_) { \
      __builtin_amdgcn_sched_group_barrier(0x008, 1, 0); __builtin_amdgcn_sched_group_barrier(0x100, 1, 0); } \
    __builtin_amdgcn_sched_group_barrier(0x008, 4 * NP - (NR), 0); SBAR(); \
  } while (0)

#pragma unroll
  for (int m = 0; m < 4; ++m) fa1[m] = bf16x8{};
#pragma unroll
  for (int p = 0; p < NP; ++p) fb1[p] = bf16x8{};
#define G1_STG(nb) do { \
    asm volatile("s_waitcnt lgkmcnt(0)\n\ts_barrier" ::: "memory"); SBAR(); \
    G1_RB(fb0, nb, 0, 0); G1_RA(fa0, nb, 0); G1_MM(fa1, fb1, 1); G1_SCHED(2 * NP + 4); \
    G1_RB(fb1, nb, 0, 1);                    G1_MM(fa0, fb0, 0); G1_SCHED(2 * NP); \
    G1_RB(fb0, nb, 1, 0); G1_RA(fa1, nb, 1); G1_MM(fa0, fb1, 1); G1_SCHED(2 * NP + 4); \
    G1_RB(fb1, nb, 1, 1);                    G1_MM(fa1, fb0, 0); G1_SCHED(2 * NP); } while (0)
  int slot = 0;
#pragma unroll 1
  for (int i = 0; i < 8; ++i) {
    const int s1 = (slot == 2) ? 0 : slot + 1;
    G1_STG(lbase + slot * G1_STAGE);
    G1_STG(lbase + s1 * G1_STAGE);
    slot = (s1 == 2) ? 0 : s1 + 1;
  }
#undef G1_STG
  __builtin_amdgcn_s_setprio(3);
  asm volatile("" : "+v"(l));
  g = l >> 4; c16 = l & 15;
#define G1_MMA(FA, FB, t) do { \
    _Pragma("unroll") for (int m_ = 0; m_ < 4; ++m_) _Pragma("unroll") for (int p_ = 0; p_ < NP; ++p_) { \
      if (NP == G1_KV && p_ == 1) asm volatile("v_mfma_f32_16x16x32_bf16 %0, %1, %2, %0" : "+v"(acc[m_][p_][t]) : "v"(FA[m_]), "v"(FB[p_])); \
      else asm volatile("v_mfma_f32_16x16x32_bf16 %0, %1, %2, %0" : "+v"(acc[m_][p_][t]) : "v"(FB[p_]), "v"(FA[m_])); } \
  } while (0)
  unsigned st[4];
  {
#pragma unroll
    for (int m = 0; m < 4; ++m) st[m] = *(const LAS unsigned*)(lbase + G1_ST + (w * 64 + m * 16 + c16) * 4);
#pragma unroll
    for (int p = 0; p < NP; ++p) fb0[p] = lds_b128(lbase + G1_CF + p * 2048 + l * 16);
    SBAR();
    G1_MMA(fa1, fb1, 1);
    SBAR();
#pragma unroll
    for (int p = 0; p < NP; ++p) fb1[p] = lds_b128(lbase + G1_CF + p * 2048 + 1024 + l * 16);
#pragma unroll
    for (int m = 0; m < 4; ++m) {
      const unsigned nm = st[m] & 0xffffu;
      fa0[m] = __builtin_bit_cast(bf16x8, (u32x4){nm | (nm << 16), st[m], 0u, 0u});
      PIN(fa0[m]);
    }
    SBAR();
    asm volatile("s_nop 1");
    G1_MMA(fa0, fb0, 0);
    G1_MMA(fa0, fb1, 1);
    asm volatile("s_nop 7");
  }
#undef G1_MMA
  SBAR();

#pragma unroll
  for (int p = 0; p < NP; ++p) {
    const int cb = (NP == G1_FFN) ? (cg - 9) * 5 + p : cg * 4 + p;
    if (NP == G1_KV && p == 1) {
#pragma unroll
      for (int mm = 0; mm < 2; ++mm) {
        const u32x4 s0 = *(const LAS u32x4*)(lbase + G1_ST + (w * 64 + mm * 32 + 4 * g) * 4), s1 = *(const LAS u32x4*)(lbase + G1_ST + (w * 64 + mm * 32 + 16 + 4 * g) * 4);
#pragma unroll
        for (int t = 0; t < 2; ++t) {
          const f32x4 a = acc[2 * mm][p][t], b = acc[2 * mm + 1][p][t];
          const u32x4 o = {cvtpk(a[0] / bf_hi(s0[0]), a[1] / bf_hi(s0[1])), cvtpk(a[2] / bf_hi(s0[2]), a[3] / bf_hi(s0[3])),
                           cvtpk(b[0] / bf_hi(s1[0]), b[1] / bf_hi(s1[1])), cvtpk(b[2] / bf_hi(s1[2]), b[3] / bf_hi(s1[3]))};
          store16_dev(ws + WS_KV + (size_t)(mtile * 8 + w * 2 + mm) * 4096 + 2048 + (g >> 1) * 1024 + ((g & 1) * 32 + t * 16 + c16) * 16, o);
        }
      }
    } else if (NP != G1_FFN || cb < 112) {
#pragma unroll
      for (int m = 0; m < 4; ++m) {
        const int tt = mtile * 8 + w * 2 + (m >> 1);
        const int pos = (NP == G1_KV) ? (c16 & 7) + (m & 1) * 8 + (c16 >> 3) * 16 : (m & 1) * 16 + c16;
        char* dst = (NP == G1_FFN) ? ws + WS_H + (size_t)(cb >> 1) * KT + (size_t)tt * 4096 + ((m & 1) * 2 + (cb & 1)) * 1024 + l * 16
                  : ((NP == G1_Q) ? ws + WS_Q + (size_t)(tt * 32 + cb) * 2048 : ws + WS_KV + (size_t)tt * 4096) + (g >> 1) * 1024 + ((g & 1) * 32 + pos) * 16;
        float sc = 1.0f;
        if (NP != G1_FFN) { sc = 1.0f / bf_hi(st[m]); if (NP == G1_Q) sc *= 0.17677669529663687f * 1.4426950408889634f; }
        u32x4 o;
#pragma unroll
        for (int j = 0; j < 4; ++j) {
          float a = acc[m][p][j >> 1][2 * (j & 1)], b = acc[m][p][j >> 1][2 * (j & 1) + 1];
          if (NP == G1_FFN) { a = relu_s(a); b = relu_s(b); a *= a; b *= b; }
          else { a *= sc; b *= sc; }
          o[j] = cvtpk(a, b);
        }
        store16_dev(dst, o);
      }
    }
  }
#undef G1_RA
#undef G1_RB
#undef G1_MM
#undef G1_SCHED
}

constexpr int AT_BUF = 32768;
constexpr int AT_BASE = 98304;
constexpr int AT_Q = 65536;
constexpr int AT_DUMP = 90112;
constexpr int MAIN_LDS = 163840;

DI float max16(const f32x16& s) {
  float a = __builtin_fmaxf(s[0], s[1]);
#pragma unroll
  for (int r = 2; r < 16; ++r) a = __builtin_fmaxf(a, s[r]);
  return a;
}
DI bf16x8 pack8(const f32x16& p, int s) {
  u32x4 o;
#pragma unroll
  for (int j = 0; j < 4; ++j) o[j] = cvtpk(p[8 * s + 2 * j], p[8 * s + 2 * j + 1]);
  return __builtin_bit_cast(bf16x8, o);
}

DI void attn_fallback(const char* __restrict__ kv, bf16x8 q0, bf16x8 q1, int l, f32x16& o_out, float& l_out) {
  f32x16 o = {};
  float m = -INFINITY, lsum = 0.f;
  for (int t = 0; t < 64; ++t) {
    const char* p = kv + (size_t)t * 4096 + l * 16;
    const bf16x8 k0 = load16_dev(p), k1 = load16_dev(p + 1024), v0 = load16_dev(p + 2048), v1 = load16_dev(p + 3072);
    f32x16 s = {};
    s = MFMA32(k0, q0, s); s = MFMA32(k1, q1, s);
    const float mn = __builtin_fmaxf(m, xhalf_max(max16(s)));
    const float ms = (mn == -INFINITY) ? 0.f : mn;
    const float alpha = __builtin_amdgcn_exp2f(m - ms);
    m = mn;
    float ps = 0.f;
#pragma unroll
    for (int r = 0; r < 16; ++r) { s[r] = __builtin_amdgcn_exp2f(s[r] - ms); ps += s[r]; }
    lsum = lsum * alpha + ps;
#pragma unroll
    for (int r = 0; r < 16; ++r) o[r] *= alpha;
    o = MFMA32(v0, pack8(s, 0), o); o = MFMA32(v1, pack8(s, 1), o);
  }
  o_out = o;
  l_out = xhalf_sum(lsum);
}

constexpr int G2_SLOT = 32768, G2_BOFF = 16384, G2_PART = 131072;
struct G2Ld {
  const char* hb; const char* ob; const char* wb; const char* wb2; unsigned lds; unsigned voff, vb[4]; int rot, frot;
  DI void init(const char* ws, const u16* w2, const u16* wo, unsigned lds0, int lw, int l) {
    const int xcd = blockIdx.x & 7, idx = blockIdx.x >> 3, kh = xcd & 1;
    const int mt = ((xcd >> 1) & 1) * 8 + (idx & 7), nt = (xcd >> 2) * 4 + (idx >> 3);
    hb = ws + WS_H + (size_t)(mt * 4 + lw) * 4096 + (size_t)kh * (28 * KT);
    ob = ws + WS_O + (size_t)(mt * 4 + lw) * 4096;
    wb = (const char*)(w2 + (size_t)(kh * 1792 + lw * 16) * DM + nt * 128) - 4096;
    wb2 = (const char*)(wo + (size_t)(lw * 16) * DM + nt * 128 + kh * 64) - 4096;
#pragma unroll
    for (int p = 0; p < 4; ++p) vb[p] = (unsigned)((l >> 2) * DM * 2 + (((l & 3) ^ ((l >> 3) & 2)) << 4) + p * 64 - p * 1024 + 4096);
    lds = lds0 + lw * 4096; voff = l * 16;
    frot = (7 * (mt & 7)) >> 1;
    rot = 2 * (mt & 7);
  }
  DI void ffn(int s) const {
    const unsigned d = lds + (s & 3) * G2_SLOT;
    s += frot; s = s >= 28 ? s - 28 : s;
    const char* a = hb + (size_t)s * KT;
    const char* b = wb + (size_t)s * 131072;
    set_m0(d); gl16_dev<0>(voff, a); gl16_dev<1024>(voff, a); gl16_dev<2048>(voff, a); gl16_dev<3072>(voff, a);
    set_m0(d + G2_BOFF); gl16<0>(vb[0], b); gl16<1024>(vb[1], b); gl16<2048>(vb[2], b); gl16<3072>(vb[3], b);
  }
  template <int J> DI void ffn_piece(int s) const {
    const unsigned d = lds + (s & 3) * G2_SLOT;
    s += frot; s = s >= 28 ? s - 28 : s;
    if (J < 4) glds16_dev<(J & 3) * 1024>(voff, hb + (size_t)s * KT, d);
    else glds16<(J & 3) * 1024>(vb[J & 3], wb + (size_t)s * 131072, d + G2_BOFF);
  }
  DI void wo(int s) const {
    const unsigned d = lds + (s & 3) * G2_SLOT;
    const int k = (s - 28 + rot) & 15;
    const char* a = ob + (size_t)k * KT;
    const char* b = wb2 + (size_t)k * 131072;
    set_m0(d); gl16_dev<0>(voff, a); gl16_dev<1024>(voff, a); gl16_dev<2048>(voff, a); gl16_dev<3072>(voff, a);
    set_m0(d + G2_BOFF); gl16<0>(vb[0], b); gl16<1024>(vb[1], b);
  }
};

DI void attn_finish(char* __restrict__ ws, f32x16 o, f32x4 lacc, bf16x8 q0, bf16x8 q1, int l, char* dst) {
  const float l0 = __shfl(lacc[0] + lacc[1], l & 15), l1 = __shfl(lacc[2] + lacc[3], l & 15);
  float lsum = (l & 16) ? l1 : l0;
  bool ok = (lsum >= 9.5367431640625e-07f) && (lsum <= 1.8446744073709552e19f);
#pragma unroll
  for (int r = 0; r < 16; ++r) ok = ok && (__builtin_fabsf(o[r]) <= 3.0e38f);
  if (!__all(ok)) attn_fallback(ws + WS_KV, q0, q1, l, o, lsum);
  const float rl = 1.0f / lsum;
#pragma unroll
  for (int s = 0; s < 2; ++s) {
    u32x4 ov;
#pragma unroll
    for (int j = 0; j < 4; ++j) ov[j] = cvtpk(o[8 * s + 2 * j] * rl, o[8 * s + 2 * j + 1] * rl);
    store16_dev(dst + s * 1024, ov);
  }
}

template <bool LOADER>
DI void attn_phase(char* __restrict__ ws, const u16* w2, const u16* wo, char* smem, const int w, const int l, unsigned* late_pub) {
  const int tt = blockIdx.x >> 2, hg = blockIdx.x & 3;
  const int head = hg * 8 + w, lw = w & 3;
  const unsigned lds0 = (unsigned)(size_t)(LAS char*)smem;
  unsigned* flags = (unsigned*)(ws + WS_FLAG);
  const unsigned voff = l * 16;
  const char* ksrc = ws + WS_KV + (size_t)lw * 4096;
  const unsigned kdst = lds0 + AT_BASE + lw * 4096;
  auto ld = [&](int c, int half) {
    const char* s_ = ksrc + (size_t)c * AT_BUF + half * 16384;
    const unsigned d_ = kdst + (c & 1) * AT_BUF + half * 16384;
    set_m0(d_); gl16_dev<0>(voff, s_); gl16_dev<1024>(voff, s_); gl16_dev<2048>(voff, s_); gl16_dev<3072>(voff, s_);
  };
  G2Ld g2;
  const unsigned tdump = lds0 + AT_DUMP + lw * 256;
  const int xcd = blockIdx.x & 7, idx = blockIdx.x >> 3, gk = xcd & 1;
  const int mt = ((xcd >> 1) & 1) * 8 + (idx & 7), nt = (xcd >> 2) * 4 + (idx >> 3);
  if (LOADER) {
    const unsigned* word = flags + (l == 0 ? FL_KV : FL_Q + (tt >> 3) * 8 + hg * 2 + (l & 1)) * 32;
    const unsigned need = l == 0 ? 32u : 4u;
    bool done = l > 2;
    auto await = [&](unsigned level) {
      if (lw == 0) {
#pragma unroll 1
        for (unsigned spins = 0; spins < SPIN_CAP; ++spins) {
          if (level == 1 ? (bool)rfl(done) : (bool)__all(done)) break;
          if (spins) __builtin_amdgcn_s_sleep(8);
          if (!done) done = __hip_atomic_load((gu32*)word, RLX_AGENT) >= need;
        }
        if (__all(done)) level = 2;
        if (l == 0) __hip_atomic_store((LAS unsigned*)((LAS char*)smem + AT_RDY + 4), level, __ATOMIC_RELAXED, __HIP_MEMORY_SCOPE_WORKGROUP);
      } else {
#pragma unroll 1
        for (unsigned spins = 0; spins < 64u * SPIN_CAP; ++spins) {
          if (rfl(__hip_atomic_load((LAS unsigned*)((LAS char*)smem + AT_RDY + 4), __ATOMIC_RELAXED, __HIP_MEMORY_SCOPE_WORKGROUP)) >= level) break;
          __builtin_amdgcn_s_sleep(1);
        }
      }
      asm volatile("" ::: "memory");
    };
    await(1);
    ld(0, 0); ld(0, 1);
    await(2);
    const char* qs = ws + WS_Q + (size_t)(tt * 32 + hg * 8 + lw * 2) * 2048;
    set_m0(lds0 + AT_Q + lw * 4096); gl16_dev<0>(voff, qs); gl16_dev<1024>(voff, qs); gl16_dev<2048>(voff, qs); gl16_dev<3072>(voff, qs);
    ld(1, 0); ld(1, 1);
    l2_touch(wo, tdump);
    g2.init(ws, w2, wo, lds0, lw, l);
  }
  const unsigned s_lo = (l & 14) == 0 ? 0x3f803f80u : 0u, s_hi = (l & 14) == 2 ? 0x3f803f80u : 0u;
  const bf16x8_t sel = __builtin_bit_cast(bf16x8_t, (u32x4){s_lo, s_lo, s_hi, s_hi});
  const int sidx = (l & 1) ? 0xeeee : 0x4444;
  f32x16 o = {}, sa, sb;
  f32x4 ls = {};
  u32x8 pa, pb = {};
  PIN(o);
  if (LOADER) {
    asm volatile("s_waitcnt vmcnt(9)" ::: "memory");
    if (l == 0) __hip_atomic_fetch_add((LAS unsigned*)((LAS char*)smem + AT_RDY), 1u, __ATOMIC_RELAXED, __HIP_MEMORY_SCOPE_WORKGROUP);
  }
#pragma unroll 1
  for (unsigned spins = 0; spins < 64u * SPIN_CAP; ++spins) {
    if (rfl(__hip_atomic_load((LAS unsigned*)((LAS char*)smem + AT_RDY), __ATOMIC_RELAXED, __HIP_MEMORY_SCOPE_WORKGROUP)) >= 4u) break;
    __builtin_amdgcn_s_sleep(1);
  }
  asm volatile("" ::: "memory");

  const LAS char* qp = (const LAS char*)smem + AT_Q + w * 2048 + l * 16;
  const bf16x8 q0 = lds_b128(qp), q1 = lds_b128(qp + 1024);
  const LAS char* kvp = (const LAS char*)smem + AT_BASE + l * 16;
  bf16x8 kx0, kx1, ky0, ky1, va0, va1, vb0, vb1, vc0, vc1, vd0, vd1;
  kx0 = lds_b128(kvp); kx1 = lds_b128(kvp + 1024); ky0 = lds_b128(kvp + 4096); ky1 = lds_b128(kvp + 4096 + 1024);
  va0 = lds_b128(kvp + 2048); va1 = lds_b128(kvp + 3072); vb0 = lds_b128(kvp + 4096 + 2048); vb1 = lds_b128(kvp + 4096 + 3072);
  vd0 = va0; vd1 = va1;
  f32x16 nm;
  {
    sa = MFMA32(kx0, q0, o); sa = MFMA32(kx1, q1, sa);
    const float ma = -xhalf_max(max16(sa));
#pragma unroll
    for (int r = 0; r < 16; ++r) { nm[r] = ma; sa[r] += ma; }
    PIN(nm);
  }
#define EX(S, i) S[i] = __builtin_amdgcn_exp2f(S[i])
#define CV(P, j, S, i) P[j] = cvtpk(S[i], S[(i) + 1])
#define PLO(x) __builtin_bit_cast(bf16x8, __builtin_shufflevector(x, x, 0, 1, 2, 3))
#define PHI(x) __builtin_bit_cast(bf16x8, __builtin_shufflevector(x, x, 4, 5, 6, 7))
#define RSUM(x) ls = __builtin_amdgcn_smfmac_f32_16x16x64_bf16(sel, __builtin_bit_cast(bf16x16_t, x), ls, sidx, 0, 0)
#define STEP(S, P, SN, PP, KN0, KN1, VP0, VP1, KL0, KL1, VL0, VL1, NP) do { \
    SN = MFMA32(KN0, q0, nm); SBAR(); KL0 = lds_b128(NP); EX(S, 0); EX(S, 1); EX(S, 2); EX(S, 3); PIN(S); SBAR(); \
    SN = MFMA32(KN1, q1, SN); SBAR(); KL1 = lds_b128((NP) + 1024); CV(P, 0, S, 0); CV(P, 1, S, 2); EX(S, 4); EX(S, 5); EX(S, 6); EX(S, 7); PIN(S); PIN(P); SBAR(); \
    o = MFMA32(VP0, PLO(PP), o); SBAR(); VL0 = lds_b128((NP) + 2048); CV(P, 2, S, 4); EX(S, 8); CV(P, 3, S, 6); EX(S, 9); EX(S, 10); EX(S, 11); PIN(S); PIN(P); SBAR(); \
    o = MFMA32(VP1, PHI(PP), o); SBAR(); VL1 = lds_b128((NP) + 3072); CV(P, 4, S, 8); CV(P, 5, S, 10); EX(S, 12); EX(S, 13); EX(S, 14); PIN(S); PIN(P); SBAR(); \
    RSUM(PP); SBAR(); EX(S, 15); CV(P, 6, S, 12); CV(P, 7, S, 14); PIN(P); SBAR(); \
  } while (0)
#define XP(on, st, j) do { if (LOADER && (on)) g2.template ffn_piece<j>(st); SBAR(); } while (0)
#define PRIO(n) do { __builtin_amdgcn_s_setprio(n); SBAR(); } while (0)
#define HEAD(cb, on, st) do { \
    PRIO(2); \
    STEP(sa, pa, sb, pb, ky0, ky1, vd0, vd1, kx0, kx1, vc0, vc1, (cb) + 2 * 4096); XP(on, st, 2); \
    STEP(sb, pb, sa, pa, kx0, kx1, va0, va1, ky0, ky1, vd0, vd1, (cb) + 3 * 4096); XP(on, st, 3); \
    PRIO(1); \
    STEP(sa, pa, sb, pb, ky0, ky1, vb0, vb1, kx0, kx1, va0, va1, (cb) + 4 * 4096); XP(on, st, 4); \
    STEP(sb, pb, sa, pa, kx0, kx1, vc0, vc1, ky0, ky1, vb0, vb1, (cb) + 5 * 4096); XP(on, st, 5); \
    STEP(sa, pa, sb, pb, ky0, ky1, vd0, vd1, kx0, kx1, vc0, vc1, (cb) + 6 * 4096); XP(on, st, 6); \
    PRIO(0); \
    STEP(sb, pb, sa, pa, kx0, kx1, va0, va1, ky0, ky1, vd0, vd1, (cb) + 7 * 4096); XP(on, st, 7); \
  } while (0)
#define TAIL(nb, on, st) do { \
    STEP(sa, pa, sb, pb, ky0, ky1, vb0, vb1, kx0, kx1, va0, va1, nb); XP(on, st, 0); \
    STEP(sb, pb, sa, pa, kx0, kx1, vc0, vc1, ky0, ky1, vb0, vb1, (nb) + 4096); XP(on, st, 1); \
  } while (0)
  HEAD(kvp, false, 0);
  if (LOADER) {
#pragma unroll 1
    for (int c = 0; c < 3; ++c) {
      asm volatile("s_waitcnt vmcnt(1) lgkmcnt(0)\n\ts_barrier" ::: "memory");
      if (c == 2 && lw == 0) peek_dev(flags + (FL_H + (mt >> 1)) * 32, lds0 + G1_CS);
      ld(c + 2, 0); ld(c + 2, 1);
      const int j = c * 4 + lw;
      l2_touch(j < 7 ? (const char*)(w2 + (size_t)(gk * 1792 + j * 256 + (mt & 7) * 32 + (l >> 1)) * DM + nt * 128 + (l & 1) * 64)
             : j < 9 ? (const char*)(wo + (size_t)((mt & 7) * 128 + (j - 7) * 64 + l) * DM + nt * 128 + gk * 64) : (const char*)wo, tdump);
      PRIO(3);
      const LAS char* nb = kvp + ((c + 1) & 1) * AT_BUF;
      TAIL(nb, false, 0); HEAD(nb, false, 0);
    }
    asm volatile("s_waitcnt vmcnt(1)" ::: "memory");
    if (lw == 0 && rfl(*(const LAS unsigned*)((const LAS char*)smem + G1_CS)) < 92u) wait_ge(flags + (FL_H + (mt >> 1)) * 32, 92u);
#pragma unroll 1
    for (int c = 3; c < 7; ++c) {
      asm volatile("s_waitcnt vmcnt(8) lgkmcnt(0)\n\ts_barrier" ::: "memory");
      if (c < 6) { ld(c + 2, 0); ld(c + 2, 1); }
      PRIO(3);
      const LAS char* nb = kvp + ((c + 1) & 1) * AT_BUF;
      TAIL(nb, true, c - 3); HEAD(nb, true, c - 3);
    }
  } else {
#pragma unroll 1
    for (int c = 0; c < 7; ++c) {
      asm volatile("s_waitcnt lgkmcnt(0)\n\ts_barrier" ::: "memory");
      if (c == 0 && late_pub) publish(late_pub, l);
      PRIO(3);
      const LAS char* nb = kvp + ((c + 1) & 1) * AT_BUF;
      TAIL(nb, false, 0); HEAD(nb, false, 0);
    }
  }
  TAIL(kvp, false, 0);
#undef HEAD
#undef TAIL
  o = MFMA32(vd0, PLO(pb), o); o = MFMA32(vd1, PHI(pb), o); RSUM(pb);
#undef EX
#undef CV
#undef PLO
#undef PHI
#undef RSUM
#undef STEP
#undef PRIO
#undef XP
  __builtin_amdgcn_s_setprio(0);
  attn_finish(ws, o, ls, q0, q1, l, ws + WS_O + (size_t)(head >> 1) * KT + (size_t)tt * 4096 + (head & 1) * 2048 + l * 16);
}

DI void g2_load(const u16* w2, const u16* wo, char* __restrict__ ws, unsigned lds0, const int lw, const int l) {
  const int xcd = blockIdx.x & 7, idx = blockIdx.x >> 3;
  const int mt = ((xcd >> 1) & 1) * 8 + (idx & 7);
  unsigned* flags = (unsigned*)(ws + WS_FLAG);
  G2Ld g; g.init(ws, w2, wo, lds0, lw, l);
  asm volatile("s_waitcnt vmcnt(10) lgkmcnt(0)\n\ts_barrier" ::: "memory");
  g.ffn(4); g.ffn(5);
  const LAS unsigned* peek = (const LAS unsigned*)(size_t)(lds0 + G2_PART);
#pragma unroll 1
  for (int i = 2; i < 24; ++i) {
    asm volatile("s_waitcnt vmcnt(16)\n\ts_barrier" ::: "memory");
    if (i == 6 && l == 0) __hip_atomic_fetch_add((gu32*)flags + (FL_O + (blockIdx.x >> 4)) * 32, 1u, RLX_AGENT);
    if (i == 20 && lw == 0) peek_dev(flags + (FL_O + mt) * 32, lds0 + G2_PART);
    g.ffn(i + 4);
  }
  if (lw == 0 && rfl(peek[0]) < 128u) wait_ge(flags + (FL_O + mt) * 32, 128u);
  asm volatile("s_waitcnt vmcnt(16)\n\ts_barrier" ::: "memory");
  g.wo(28);
  peek = (const LAS unsigned*)(size_t)(lds0 + G2_BOFF + 2048);
#pragma unroll 1
  for (int i = 25; i < 37; ++i) {
    asm volatile("s_waitcnt vmcnt(12)\n\ts_barrier" ::: "memory");
    if (i == 35 && lw == 0) peek_dev(flags + (FL_P + blockIdx.x) * 32, lds0 + G2_BOFF + 2048);
    g.wo(i + 4);
  }
  auto part = [&]() {
    const char* src = ws + WS_PART + (size_t)(blockIdx.x ^ 1) * 16384 + lw * 4096;
    const unsigned d = lds0 + G2_PART + lw * 4096;
    glds16_dev<0>(g.voff, src, d); glds16_dev<1024>(g.voff, src, d); glds16_dev<2048>(g.voff, src, d); glds16_dev<3072>(g.voff, src, d);
  };
  asm volatile("s_waitcnt vmcnt(12)\n\ts_barrier" ::: "memory");
  const bool early = rfl(peek[0]) >= 4u;
  g.wo(41);
  if (early) part();
#pragma unroll 1
  for (int i = 38; i < 41; ++i) {
    if (early) asm volatile("s_waitcnt vmcnt(16)\n\ts_barrier" ::: "memory");
    else asm volatile("s_waitcnt vmcnt(12)\n\ts_barrier" ::: "memory");
    if (i < 40) g.wo(i + 4);
  }
  if (!early && lw == 0) wait_ge(flags + (FL_P + blockIdx.x) * 32, 4u);
  asm volatile("s_waitcnt vmcnt(6)\n\ts_barrier" ::: "memory");
  if (!early) part();
  asm volatile("s_waitcnt vmcnt(0)\n\ts_barrier" ::: "memory");
}

DI void g2_compute(const u16* __restrict__ x, const u16* __restrict__ b2, char* __restrict__ ws, u16* __restrict__ out,
                   char* smem, const int w, const int l) {
  const int xcd = blockIdx.x & 7, idx = blockIdx.x >> 3;
  const int kh = xcd & 1, mt = ((xcd >> 1) & 1) * 8 + (idx & 7), nt = (xcd >> 2) * 4 + (idx >> 3);
  unsigned* flags = (unsigned*)(ws + WS_FLAG);
  const int g = l >> 4, c16 = l & 15, wm = w & 1, wn = w >> 1;
  f32x4 accg[4][2] = {}, accp[4][2] = {}, accs[4][2] = {};
  const int col0 = nt * 128 + kh * 64 + wn * 32;
  const int row0 = mt * 128 + wm * 64 + c16;
  const int colx = col0 + (g & 1) * 16 + (g >> 1) * 8;
  unsigned stw[4];
  u32x4 xv[4];
  u32x2 bv[2];
#pragma unroll
  for (int m = 0; m < 4; ++m) stw[m] = __hip_atomic_load((gu32*)(ws + WS_STAT) + row0 + m * 16, RLX_AGENT);
#pragma unroll
  for (int m = 0; m < 4; ++m) xv[m] = *(const u32x4*)(x + (size_t)(row0 + m * 16) * DM + colx);
#pragma unroll
  for (int t = 0; t < 2; ++t) bv[t] = *(const u32x2*)(b2 + col0 + 16 * t + 4 * g);
  const LAS char* lbase = (const LAS char*)smem;
  const int ah = wm * 8192 + l * 16;
  const int ao = wm * 8192 + (g >> 1) * 1024 + ((g & 1) * 32 + c16) * 16;
  int bF[2][2], bW[2];
#pragma unroll
  for (int t = 0; t < 2; ++t) {
    const int e = ((t ^ g) & 1) * 32 + (l & 3) * 8;
    bF[0][t] = G2_BOFF + (kh * 2 + wn) * 1024 + (4 * g + (c16 >> 2)) * 64 + e;
    bF[1][t] = G2_BOFF + ((kh ^ 1) * 2 + wn) * 1024 + (4 * g + (c16 >> 2)) * 64 + e;
    bW[t] = G2_BOFF + wn * 1024 + (g >> 1) * 4096 + (4 * (g & 1) + (c16 >> 2)) * 64 + e;
  }
  bf16x8 fa0[4], fb0[4], fa1[4], fb1[4];
#define F_RD(FA, FB, base, k) do { \
    _Pragma("unroll") for (int j_ = 0; j_ < 4; ++j_) { \
      FB[j_] = cat8(lds_tr((base) + bF[j_ >> 1][j_ & 1] + (k) * 8192), lds_tr((base) + bF[j_ >> 1][j_ & 1] + (k) * 8192 + 4096)); \
      FA[j_] = lds_b128((base) + ah + j_ * 2048 + (k) * 1024); } \
  } while (0)
#define F_MM(FA, FB) do { \
    _Pragma("unroll") for (int m_ = 0; m_ < 4; ++m_) { \
      accg[m_][0] = MFMA16(FB[0], FA[m_], accg[m_][0]); accg[m_][1] = MFMA16(FB[1], FA[m_], accg[m_][1]); \
      accp[m_][0] = MFMA16(FB[2], FA[m_], accp[m_][0]); accp[m_][1] = MFMA16(FB[3], FA[m_], accp[m_][1]); } \
  } while (0)
#define W_RD(FA, FB, base, k) do { \
    _Pragma("unroll") for (int j_ = 0; j_ < 2; ++j_) FB[j_] = cat8(lds_tr((base) + bW[j_] + (k) * 8192), lds_tr((base) + bW[j_] + (k) * 8192 + 512)); \
    _Pragma("unroll") for (int j_ = 0; j_ < 4; ++j_) FA[j_] = lds_b128((base) + ao + (j_ >> 1) * 4096 + (j_ & 1) * 256 + (k) * 2048); \
  } while (0)
#define W_MM(FA, FB) do { \
    _Pragma("unroll") for (int m_ = 0; m_ < 4; ++m_) { accs[m_][0] = MFMA16(FB[0], FA[m_], accs[m_][0]); accs[m_][1] = MFMA16(FB[1], FA[m_], accs[m_][1]); } \
  } while (0)
#define G2_SCHED(NG, NR, NM) do { \
    _Pragma("unroll") for (int q_ = 0; q_ < (NG); ++q_) { \
      __builtin_amdgcn_sched_group_barrier(0x008, 1, 0); __builtin_amdgcn_sched_group_barrier(0x100, NR, 0); } \
    __builtin_amdgcn_sched_group_barrier(0x008, (NM) - (NG), 0); SBAR(); \
  } while (0)
#define G2_BAR() do { asm volatile("s_waitcnt lgkmcnt(0)\n\ts_barrier" ::: "memory"); SBAR(); } while (0)
#define F_BODY(nb_) do { \
    F_RD(fa0, fb0, nb_, 0); F_MM(fa1, fb1); G2_SCHED(12, 1, 16); \
    F_RD(fa1, fb1, nb_, 1); F_MM(fa0, fb0); G2_SCHED(12, 1, 16); } while (0)

  SBAR();
  F_RD(fa0, fb0, lbase, 0); SBAR();
  F_RD(fa1, fb1, lbase, 1); F_MM(fa0, fb0); G2_SCHED(12, 1, 16);
  const LAS char* lhi = lbase + 2 * G2_SLOT;
  F_BODY(lbase + G2_SLOT);
  G2_BAR(); F_BODY(lhi);
  G2_BAR(); F_BODY(lhi + G2_SLOT);
  publish(flags + (FL_O + (blockIdx.x >> 4)) * 32, l); SBAR();
#pragma unroll 1
  for (int i = 0; i < 6; ++i) {
    G2_BAR(); F_BODY(lbase);
    G2_BAR(); F_BODY(lbase + G2_SLOT);
    G2_BAR(); F_BODY(lhi);
    G2_BAR(); F_BODY(lhi + G2_SLOT);
  }
#define W_STEP(i) do { const LAS char* nb_ = lbase + (((i) + 1) & 3) * G2_SLOT; G2_BAR(); \
    W_RD(fa0, fb0, nb_, 0); W_MM(fa1, fb1); G2_SCHED(4, 2, 8); \
    W_RD(fa1, fb1, nb_, 1); W_MM(fa0, fb0); G2_SCHED(4, 2, 8); } while (0)
  char* pdst = ws + WS_PART + (size_t)blockIdx.x * 16384 + w * 4096 + l * 16;
#define G2_SEND(m) do { store16_dev(pdst + (m) * 1024, (u32x4){cvtpk(accp[m][0][0], accp[m][0][1]), cvtpk(accp[m][0][2], accp[m][0][3]), \
                                                               cvtpk(accp[m][1][0], accp[m][1][1]), cvtpk(accp[m][1][2], accp[m][1][3])}); SBAR(); } while (0)
  G2_BAR();
  W_RD(fa0, fb0, lbase, 0); F_MM(fa1, fb1); G2_SCHED(8, 1, 16);
  W_RD(fa1, fb1, lbase, 1); W_MM(fa0, fb0); G2_SCHED(4, 2, 8);
  G2_SEND(0); G2_SEND(1);
  W_STEP(28); G2_SEND(2); G2_SEND(3);
  W_STEP(29); W_STEP(30); W_STEP(31); W_STEP(32);
  publish(flags + (FL_P + (blockIdx.x ^ 1)) * 32, l); SBAR();
  W_STEP(33);
#pragma unroll 1
  for (int i = 0; i < 2; ++i) { W_STEP(34); W_STEP(35); W_STEP(36); W_STEP(37); }
  W_STEP(42);
  W_MM(fa1, fb1);
  {
    const LAS char* xp = (const LAS char*)smem + G2_PART + w * 4096 + l * 16;
#pragma unroll
    for (int m = 0; m < 4; ++m) {
      const float den = bf_hi(stw[m]), rd2 = 1.0f / (den * den);
      const u32x4 tp = *(const LAS u32x4*)(xp + m * 1024);
      u32x4 xx = xv[m];
      { auto r0 = __builtin_amdgcn_permlane16_swap(xx[0], xx[2], false, false); xx[0] = r0[0]; xx[2] = r0[1];
        auto r1 = __builtin_amdgcn_permlane16_swap(xx[1], xx[3], false, false); xx[1] = r1[0]; xx[3] = r1[1]; }
      float r[8];
#pragma unroll
      for (int e = 0; e < 8; ++e) {
        const unsigned xw_ = xx[e >> 1], bw_ = bv[e >> 2][(e >> 1) & 1], pw_ = tp[e >> 1];
        const float z = (accg[m][e >> 2][e & 3] + ((e & 1) ? bf_hi(pw_) : bf_lo(pw_))) * rd2 + ((e & 1) ? bf_hi(bw_) : bf_lo(bw_));
        const float sg = z * __builtin_amdgcn_rcpf(1.0f + __builtin_amdgcn_exp2f(-1.4426950408889634f * z));
        r[e] = ((e & 1) ? bf_hi(xw_) : bf_lo(xw_)) + accs[m][e >> 2][e & 3] * sg;
      }
      auto r0 = __builtin_amdgcn_permlane16_swap(cvtpk(r[0], r[1]), cvtpk(r[4], r[5]), false, false);
      auto r1 = __builtin_amdgcn_permlane16_swap(cvtpk(r[2], r[3]), cvtpk(r[6], r[7]), false, false);
      *(u32x4*)(out + (size_t)(row0 + m * 16) * DM + colx) = (u32x4){r0[0], r1[0], r0[1], r1[1]};
    }
  }
#undef F_RD
#undef F_MM
#undef F_BODY
#undef G2_SCHED
#undef G2_BAR
#undef G2_SEND
#undef W_RD
#undef W_MM
#undef W_STEP
}

__global__ __launch_bounds__(512, 2) void main_kernel(const u16* __restrict__ x, const u16* __restrict__ wq, const u16* __restrict__ wk,
                                                      const u16* __restrict__ wv, const u16* __restrict__ wo, const u16* __restrict__ w1,
                                                      const u16* __restrict__ b1, const u16* __restrict__ w2, const u16* __restrict__ b2,
                                                      char* __restrict__ ws, u16* __restrict__ out) {
  extern __shared__ __attribute__((aligned(16))) char smem[];
  const int l = threadIdx.x & 63, w = rfl(threadIdx.x >> 6);
  const unsigned lds0 = (unsigned)(size_t)(LAS char*)smem;
  const int xcd = blockIdx.x & 7, idx = blockIdx.x >> 3;
  const int mtile = (xcd & 1) * 4 + (idx & 3), cg = (idx >> 2) * 4 + (xcd >> 1);
  unsigned* flags = (unsigned*)(ws + WS_FLAG);
  if (w == 3) {
    unsigned long long pc; unsigned len;
    asm volatile("s_getpc_b64 %0\n.Lmk_code_begin:\n\ts_mov_b32 %1, .Lmk_code_end-.Lmk_code_begin" : "=s"(pc), "=s"(len));
    unsigned off = (unsigned)((l & 31) * 32 + idx) * 128u;
    off = (off < len && len <= 131072u) ? off : 0u;
    l2_touch((const char*)pc + off, lds0 + G1_HS + 3072);
  }
  if (w >= 4) {
    if (cg > 8) g1_load<G1_FFN>(ws, x, w1, nullptr, b1, cg, mtile, smem, w - 4, l);
    else if (cg < 8) g1_load<G1_Q>(ws, x, wq, nullptr, b1, cg, mtile, smem, w - 4, l);
    else g1_load<G1_KV>(ws, x, wk, wv, b1, cg, mtile, smem, w - 4, l);
    attn_phase<true>(ws, w2, wo, smem, w, l, nullptr);
    g2_load(w2, wo, ws, lds0, w - 4, l);
    asm volatile(".Lmk_code_end:");
  } else {
    unsigned* late = nullptr;
    if (cg > 8) { g1_compute<G1_FFN>(ws, x, cg, mtile, smem, w, l); late = flags + (FL_H + mtile) * 32; }
    else if (cg < 8) { g1_compute<G1_Q>(ws, x, cg, mtile, smem, w, l); publish(flags + (FL_Q + mtile * 8 + cg) * 32, l); }
    else { g1_compute<G1_KV>(ws, x, cg, mtile, smem, w, l); publish(flags + FL_KV * 32, l); }
    attn_phase<false>(ws, w2, wo, smem, w, l, late);
    g2_compute(x, b2, ws, out, smem, w, l);
  }
}

extern "C" void kernel_launch(void* const* d_in, const int* in_sizes, int n_in, void* d_out, int out_size, void* d_ws,
                              size_t ws_size, hipStream_t stream) {
  const u16* x = (const u16*)d_in[0];
  const u16* wq = (const u16*)d_in[1];
  const u16* wk = (const u16*)d_in[2];
  const u16* wv = (const u16*)d_in[3];
  const u16* wo = (const u16*)d_in[4];
  const u16* w1 = (const u16*)d_in[6];
  const u16* b1 = (const u16*)d_in[7];
  const u16* w2 = (const u16*)d_in[8];
  const u16* b2 = (const u16*)d_in[9];
  char* ws = (char*)d_ws;
  (void)hipFuncSetAttribute((const void*)main_kernel, hipFuncAttributeMaxDynamicSharedMemorySize, MAIN_LDS);
  main_kernel<<<256, 512, MAIN_LDS, stream>>>(x, wq, wk, wv, wo, w1, b1, w2, b2, ws, (u16*)d_out);
}
```
